```python
import torch

def _softmax(x, axis=-1):
    return torch.softmax(x, dim=axis)
def _gelu(x):
    return torch.nn.functional.gelu(x, approximate="tanh")
def _silu(x):
    return torch.nn.functional.silu(x)
def _sigmoid(x):
    return torch.sigmoid(x)
def _layernorm(x, axis=-1, eps=1e-5):
    m = x.mean(dim=axis, keepdim=True)
    v = x.var(dim=axis, keepdim=True, unbiased=False)
    return (x - m) / torch.sqrt(v + eps)
def _scatter_add(tgt, idx, src, axis=0):
    return tgt.clone().index_add_(axis, idx, src)
def _segment_sum(src, seg, n):
    out = torch.zeros((n,) + src.shape[1:], dtype=src.dtype)
    return out.index_add_(0, seg, src)
def _pad(x, pw):
    flat = tuple(v for pair in reversed(pw) for v in pair)
    return torch.nn.functional.pad(x, flat)

kernel_name = 'parallel_sum__mha_block__swiglu_ffn__prompt'

def setup_inputs():

    _w = (lambda a: (lambda t: t.to(torch.bfloat16) if t.is_floating_point() else t)(torch.as_tensor(a)))
    def _A_setup():
        B, S, D, H = 1, 2048, 1024, 32
        return {
            "x": _w((lambda *s: torch.randn(*s, dtype=torch.bfloat16) * 0.05)(B, S, D)),
            "wqkv": _w((lambda *s: torch.randn(*s, dtype=torch.bfloat16) * 0.05)(D, 3 * D)),
            "wo": _w((lambda *s: torch.randn(*s, dtype=torch.bfloat16) * 0.05)(D, D)),
        }
    _A_in = _A_setup()

    def _B_setup():
        B, S, D, F = 1, 2048, 1024, 3584
        return {
            "x": _w((lambda *s: torch.randn(*s, dtype=torch.bfloat16) * 0.05)(B, S, D)),
            "w_gate": _w((lambda *s: torch.randn(*s, dtype=torch.bfloat16) * 0.05)(D, F)),
            "w_up": _w((lambda *s: torch.randn(*s, dtype=torch.bfloat16) * 0.05)(D, F)),
            "w_down": _w((lambda *s: torch.randn(*s, dtype=torch.bfloat16) * 0.05)(F, D)),
        }
    _B_in = _B_setup()

    return {
        "A_x": _A_in["x"],
        "A_wqkv": _A_in["wqkv"],
        "A_wo": _A_in["wo"],
        "B_x": _B_in["x"],
        "B_w_gate": _B_in["w_gate"],
        "B_w_up": _B_in["w_up"],
        "B_w_down": _B_in["w_down"],
    }

def reference(A_x, A_wqkv, A_wo, B_x, B_w_gate, B_w_up, B_w_down):
    def _A_ref(x, wqkv, wo):
        B, S, D = x.shape
        H = 32
        Dh = D // H
        qkv = (lambda x, *d: x.permute(*d))((x @ wqkv).reshape(B, S, 3, H, Dh), 2, 0, 3, 1, 4)
        q, k, v = qkv[0], qkv[1], qkv[2]
        scores = torch.einsum("bhqd,bhkd->bhqk", q, k) * (Dh ** -0.5)
        p = _softmax(scores, axis=-1)
        o = torch.einsum("bhqk,bhkd->bhqd", p, v)
        return (lambda x, *d: x.permute(*d))(o, 0, 2, 1, 3).reshape(B, S, D) @ wo

    def _B_ref(x, w_gate, w_up, w_down):
        g = _silu(x @ w_gate)
        u = x @ w_up
        return (g * u) @ w_down

    x = A_x
    xn = _layernorm(x, axis=-1)
    return x + _A_ref(xn, A_wqkv, A_wo) + _B_ref(xn, B_w_gate, B_w_up, B_w_down)
```

```cpp
#include <hip/hip_runtime.h>
#include <hip/hip_bf16.h>
#include <stdint.h>

typedef unsigned short bf16_t;
typedef short bf16x8 __attribute__((ext_vector_type(8)));
typedef short s16x4 __attribute__((ext_vector_type(4)));
typedef float f32x4 __attribute__((ext_vector_type(4)));
typedef float f32x16 __attribute__((ext_vector_type(16)));
typedef float f32x2 __attribute__((ext_vector_type(2)));
typedef __bf16 bf16x2v __attribute__((ext_vector_type(2)));
typedef unsigned u32x4 __attribute__((ext_vector_type(4)));
typedef unsigned u32x2 __attribute__((ext_vector_type(2)));
#define LAS __attribute__((address_space(3)))
typedef LAS const char* lds_cptr;

constexpr int SEQ = 2048, DM = 1024, NH = 32, FF = 3584, NQKV = 3072, KA = 4608;

constexpr size_t WS_FLAGS = 0;
constexpr size_t WS_XN = 1u << 20;
constexpr size_t WS_Q = WS_XN + (size_t)SEQ * DM * 2;
constexpr size_t WS_K = WS_Q + (size_t)SEQ * DM * 2;
constexpr size_t WS_V = WS_K + (size_t)SEQ * DM * 2;
constexpr size_t WS_AH = WS_V + (size_t)SEQ * DM * 2;
constexpr size_t WS_PART = WS_AH + (size_t)SEQ * KA * 2;

#define SB() __builtin_amdgcn_sched_barrier(0)

__device__ __forceinline__ unsigned cvtpk(float lo, float hi) {
  f32x2 v = {lo, hi};
  bf16x2v b = __builtin_convertvector(v, bf16x2v);
  return __builtin_bit_cast(unsigned, b);
}
__device__ __forceinline__ float bf_lo(unsigned u) { return __uint_as_float(u << 16); }
__device__ __forceinline__ float bf_hi(unsigned u) { return __uint_as_float(u & 0xffff0000u); }
__device__ __forceinline__ float rbf(float x) { return bf_lo(cvtpk(x, x)); }

__device__ __forceinline__ void glds16(const void* g, unsigned lds_dst) {
  unsigned keep;
  asm volatile("s_mov_b32 %0, m0\n\ts_mov_b32 m0, %2\n\ts_nop 0\n\tglobal_load_lds_dwordx4 %1, off\n\ts_mov_b32 m0, %0"
               : "=&s"(keep) : "v"(g), "s"(lds_dst) : "memory");
}
__device__ __forceinline__ void glds16s(const void* sbase, unsigned voff, unsigned lds_dst) {
  unsigned keep;
  asm volatile("s_mov_b32 %0, m0\n\ts_mov_b32 m0, %3\n\ts_nop 0\n\tglobal_load_lds_dwordx4 %1, %2\n\ts_mov_b32 m0, %0"
               : "=&s"(keep) : "v"(voff), "s"(sbase), "s"(lds_dst) : "memory");
}
__device__ __forceinline__ bf16x8 lds_rd128(lds_cptr p) { return *(const LAS bf16x8*)p; }
__device__ __forceinline__ s16x4 lds_tr(lds_cptr p) {
  return __builtin_amdgcn_ds_read_tr16_b64_v4i16((LAS s16x4*)p);
}
__device__ __forceinline__ bf16x8 cat8(s16x4 a, s16x4 b) { return __builtin_shufflevector(a, b, 0, 1, 2, 3, 4, 5, 6, 7); }

__global__ __launch_bounds__(256) void ln_kernel(const bf16_t* __restrict__ x, bf16_t* __restrict__ xn, unsigned* flags) {
  const int lane = threadIdx.x & 63, w = threadIdx.x >> 6;
  const int row = blockIdx.x * 4 + w;
  if (blockIdx.x < 64) flags[blockIdx.x * 256 + threadIdx.x] = 0u;
  const u32x4* xr = (const u32x4*)(x + (size_t)row * DM);
  u32x4 a = xr[lane], b = xr[64 + lane];
  float v[16];
#pragma unroll
  for (int i = 0; i < 4; ++i) { v[2 * i] = bf_lo(a[i]); v[2 * i + 1] = bf_hi(a[i]); v[8 + 2 * i] = bf_lo(b[i]); v[9 + 2 * i] = bf_hi(b[i]); }
  float s = 0.f;
#pragma unroll
  for (int i = 0; i < 16; ++i) s += v[i];
#pragma unroll
  for (int o = 1; o < 64; o <<= 1) s += __shfl_xor(s, o);
  const float mean = s * (1.f / DM);
  float q = 0.f;
#pragma unroll
  for (int i = 0; i < 16; ++i) { const float d = v[i] - mean; q += d * d; }
#pragma unroll
  for (int o = 1; o < 64; o <<= 1) q += __shfl_xor(q, o);
  const float mb = rbf(mean), vb = rbf(q * (1.f / DM));
  const float den = rbf(sqrtf(rbf(vb + 1e-5f)));
  u32x4 oa, ob;
#pragma unroll
  for (int i = 0; i < 4; ++i) {
    oa[i] = cvtpk(rbf(v[2 * i] - mb) / den, rbf(v[2 * i + 1] - mb) / den);
    ob[i] = cvtpk(rbf(v[8 + 2 * i] - mb) / den, rbf(v[9 + 2 * i] - mb) / den);
  }
  u32x4* o = (u32x4*)(xn + (size_t)row * DM);
  o[lane] = oa; o[64 + lane] = ob;
}

constexpr int G1_AOFF = 0, G1_BOFF = 65536, G1_BSLOT = 20480, G1_LDS = 65536 + 4 * 20480;
#define MFMA16(a, b, c) __builtin_amdgcn_mfma_f32_16x16x32_bf16(a, b, c, 0, 0, 0)
#define MFMA32(a, b, c) __builtin_amdgcn_mfma_f32_32x32x16_bf16(a, b, c, 0, 0, 0)
#define WAITBAR(N) do { SB(); asm volatile("s_waitcnt vmcnt(" #N ") lgkmcnt(0)\n\ts_barrier" ::: "memory"); SB(); } while (0)
#define BARRIER() do { SB(); asm volatile("s_barrier" ::: "memory"); SB(); } while (0)

__global__ __launch_bounds__(512, 2) void gemm1_kernel(const bf16_t* __restrict__ xn, const bf16_t* __restrict__ wqkv,
                                                       const bf16_t* __restrict__ wg, const bf16_t* __restrict__ wu,
                                                       bf16_t* __restrict__ Qb, bf16_t* __restrict__ Kb, bf16_t* __restrict__ Vb,
                                                       bf16_t* __restrict__ AH) {
  extern __shared__ __attribute__((aligned(16))) char smem[];
  const int tid = threadIdx.x, lane = tid & 63;
  const int w = __builtin_amdgcn_readfirstlane(tid >> 6);
  const int g = w >> 2, w4 = w & 3, wm = 2 * g + (w4 >> 1), wn = w4 & 1;
  const int bid = blockIdx.x, xcd = bid & 7, jj = bid >> 3;
  const int cg = xcd * 4 + (jj & 3), rt = jj >> 2;
  const int row0 = rt * 256;
  const unsigned lds0 = (unsigned)(uintptr_t)smem;
  const lds_cptr L = (lds_cptr)smem;

  const char* bptr[4]; unsigned bld[4]; unsigned bdst[4];
#pragma unroll
  for (int i = 0; i < 4; ++i) {
    const int b = (i == 0) ? (g ? 16 + w4 : 4 * w4) : 4 * w4 + i;
    const int region = b >= 10 ? 1 : 0;
    const int P = (b - 10 * region) * 64 + lane;
    const int k = P / 20, cp = P - 20 * k;
    const int c8 = (cp ^ (((k >> 3) & 1) << 1)) * 8;
    const bf16_t* mat; int ld, col;
    if (cg <= 8) { mat = wqkv; ld = NQKV; col = 320 * cg + 160 * region + c8; }
    else if (cg == 9) {
      if (c8 < 96) { mat = wqkv; ld = NQKV; col = 2880 + 96 * region + c8; }
      else { mat = region ? wu : wg; ld = FF; col = c8 - 96; }
    } else { mat = region ? wu : wg; ld = FF; col = 64 + 160 * (cg - 10) + c8; }
    bptr[i] = (const char*)(mat + (size_t)k * ld + col);
    bld[i] = (unsigned)ld * 64u;
    if (i > 0) { if (g) bptr[i] += bld[i]; bld[i] *= 2u; }
    bdst[i] = lds0 + G1_BOFF + b * 1024;
  }
  const unsigned avoff = (unsigned)((lane >> 3) * 2048 + (((lane & 7) ^ (lane >> 4)) * 16));
  const char* abase = (const char*)xn + (size_t)(row0 + 32 * w4) * 2048;

#define DMA_B(i, s) do { glds16(bptr[i], bdst[i] + ((s) & 3) * G1_BSLOT); bptr[i] += bld[i]; asm volatile("" : "+v"(bptr[i])); } while (0)
#define DMA_A(hh, t) do { _Pragma("unroll") for (int _i = 0; _i < 4; ++_i) \
    glds16s(abase + (size_t)(hh) * 262144 + _i * 16384 + (t) * 128, (_i & 1) ? (avoff ^ 64u) : avoff, \
            lds0 + G1_AOFF + ((t) & 1) * 32768 + (hh) * 16384 + (4 * w4 + _i) * 1024); } while (0)

  const int fr = lane & 15, fq = lane >> 4;
  const int aoff0 = G1_AOFF + (64 * wm + fr) * 128 + ((fq ^ (fr >> 1)) * 16);
  const int q4 = fr >> 2, p4 = lane & 3, gb = fq & 1;
  const int bbase = G1_BOFF + (8 * fq + q4) * 320 + 8 * p4 + 160 * wn;
  const int boffE = bbase + (wn ? -32 * gb : 32 * gb);
  const int boffO = bbase + (wn ? 32 * gb : -32 * gb);

  f32x4 acc[4][5][2];
#pragma unroll
  for (int m = 0; m < 4; ++m)
#pragma unroll
    for (int j = 0; j < 5; ++j) { acc[m][j][0] = (f32x4){0.f, 0.f, 0.f, 0.f}; acc[m][j][1] = (f32x4){0.f, 0.f, 0.f, 0.f}; }
  bf16x8 af[4], bfr[5][2];

#define LOADF(t, ks, s) do { \
    const lds_cptr pa = L + ((aoff0 ^ ((ks) * 64)) + ((t) & 1) * 32768); \
    const lds_cptr pe = L + (boffE + ((s) & 3) * G1_BSLOT); const lds_cptr po = L + (boffO + ((s) & 3) * G1_BSLOT); \
    _Pragma("unroll") for (int m = 0; m < 4; ++m) af[m] = lds_rd128(pa + m * 2048); \
    _Pragma("unroll") for (int j = 0; j < 5; ++j) { const lds_cptr pp = (j & 1) ? po : pe; \
      _Pragma("unroll") for (int r = 0; r < 2; ++r) \
        bfr[j][r] = cat8(lds_tr(pp + r * 10240 + j * 32), lds_tr(pp + r * 10240 + j * 32 + 1280)); } } while (0)
#define COMPUTE() do { __builtin_amdgcn_s_setprio(1); \
    _Pragma("unroll") for (int j = 0; j < 5; ++j) _Pragma("unroll") for (int r = 0; r < 2; ++r) _Pragma("unroll") for (int m = 0; m < 4; ++m) \
      acc[m][j][r] = MFMA16(bfr[j][r], af[m], acc[m][j][r]); \
    __builtin_amdgcn_s_setprio(0); } while (0)

  if (g == 0) {
    DMA_B(0, 0); DMA_B(1, 0); DMA_B(2, 0); DMA_B(3, 0);
    DMA_A(0, 0);
    DMA_B(0, 1);
    DMA_B(0, 2); DMA_B(1, 2); DMA_B(2, 2); DMA_B(3, 2);
    WAITBAR(5);
  } else {
    DMA_B(0, 0);
    DMA_A(1, 0);
    DMA_B(0, 1); DMA_B(1, 1); DMA_B(2, 1); DMA_B(3, 1);
    DMA_B(0, 2);
    DMA_A(0, 1);
    WAITBAR(9);
    BARRIER();
  }

#define KITER(t, LE, LO, WE, WO) do { \
      \
    LOADF(t, 0, 2 * (t)); \
    if (LE) { if (g == 0) { DMA_B(0, 2 * (t) + 3); DMA_A(1, (t) + 1); } \
              else { DMA_B(0, 2 * (t) + 3); DMA_B(1, 2 * (t) + 3); DMA_B(2, 2 * (t) + 3); DMA_B(3, 2 * (t) + 3); } } \
    WAITBAR(WE); COMPUTE(); BARRIER(); \
      \
    LOADF(t, 1, 2 * (t) + 1); \
    if (LO) { if (g == 0) { DMA_B(0, 2 * (t) + 4); DMA_B(1, 2 * (t) + 4); DMA_B(2, 2 * (t) + 4); DMA_B(3, 2 * (t) + 4); } \
              else { DMA_B(0, 2 * (t) + 4); DMA_A(0, (t) + 2); } } \
    WAITBAR(WO); COMPUTE(); BARRIER(); } while (0)

#pragma unroll 1
  for (int t = 0; t < 14; ++t) KITER(t, true, true, 9, 9);
  KITER(14, true, false, 9, 4);
  KITER(15, false, false, 0, 0);
  if (g == 0) BARRIER();
#undef KITER
#undef LOADF
#undef COMPUTE
#undef DMA_A
#undef DMA_B

  int tz = threadIdx.x; asm volatile("" : "+v"(tz));
  const int fr2 = tz & 15, fq2 = (tz >> 4) & 3;
#define fr fr2
#define fq fq2
  const float qscale = 0.17677669529663687f * 1.4426950408889634f;
#pragma unroll
  for (int j = 0; j < 5; ++j) {
    const int i = 5 * wn + j;
    const bool gu = (cg >= 10) || (cg == 9 && i >= 6);
    if (gu) {
      const int f0 = (cg == 9 ? 16 * i - 96 : 64 + 160 * (cg - 10) + 16 * i) + 4 * fq;
#pragma unroll
      for (int m = 0; m < 4; ++m) {
        const int row = row0 + 64 * wm + 16 * m + fr;
        float hv[4];
#pragma unroll
        for (int r = 0; r < 4; ++r) {
          const float gv = rbf(acc[m][j][0][r]), uv = rbf(acc[m][j][1][r]);
          const float sg = rbf(gv * __builtin_amdgcn_rcpf(1.f + __builtin_amdgcn_exp2f(-1.4426950408889634f * gv)));
          hv[r] = sg * uv;
        }
        u32x2 o = {cvtpk(hv[0], hv[1]), cvtpk(hv[2], hv[3])};
        *(u32x2*)(AH + (size_t)row * KA + DM + f0) = o;
      }
    } else {
#pragma unroll
      for (int r2 = 0; r2 < 2; ++r2) {
        const int col = (cg <= 8 ? 320 * cg + 160 * r2 : 2880 + 96 * r2) + 16 * i;
        const int typ = col >> 10, h = (col & 1023) >> 5, d = (col & 31) + 4 * fq;
        const float sc = typ == 0 ? qscale : 1.f;
#pragma unroll
        for (int m = 0; m < 4; ++m) {
          const int row = row0 + 64 * wm + 16 * m + fr;
          const f32x4 v = acc[m][j][r2] * sc;
          u32x2 o = {cvtpk(v[0], v[1]), cvtpk(v[2], v[3])};
          bf16_t* dst;
          if (typ == 1) dst = Kb + ((((size_t)h * 64 + (row >> 5)) * 2 + (d >> 4)) * 64 + (row & 31) + 32 * ((d >> 3) & 1)) * 8 + (d & 7);
          else dst = (typ == 0 ? Qb : Vb) + ((size_t)h * SEQ + row) * 32 + d;
          *(u32x2*)dst = o;
        }
      }
    }
  }
#undef fr
#undef fq
}

constexpr int AT_STAGE = 32768, AT_NST = 4, AT_FLAG = AT_STAGE * AT_NST, AT_LDS = AT_FLAG + 64;

__device__ __forceinline__ float max16(const f32x16& p) {
  float a = fmaxf(fmaxf(p[0], p[1]), p[2]), b = fmaxf(fmaxf(p[3], p[4]), p[5]);
  a = fmaxf(fmaxf(a, p[6]), p[7]); b = fmaxf(fmaxf(b, p[8]), p[9]);
  a = fmaxf(fmaxf(a, p[10]), p[11]); b = fmaxf(fmaxf(b, p[12]), p[13]);
  a = fmaxf(fmaxf(a, p[14]), p[15]);
  float m = fmaxf(a, b);
  auto rr = __builtin_amdgcn_permlane32_swap(__float_as_uint(m), __float_as_uint(m), false, false);
  return fmaxf(__uint_as_float(rr[0]), __uint_as_float(rr[1]));
}

template <bool SAFE>
__device__ __forceinline__ void attn_pass(const char* kg, const char* vg, char* smem, const bf16x8 q0, const bf16x8 q1,
                                          const bf16x8 sel, f32x16& o, f32x4& l, int lane, int w) {
  const unsigned lds0 = (unsigned)(uintptr_t)smem;
  const lds_cptr L = (lds_cptr)smem;
  const unsigned voff = lane * 16;
#define DMA_ST(st) do { const unsigned d_ = lds0 + ((st) & 3) * AT_STAGE + w * 2048; \
    glds16s(kg + (st) * 16384 + w * 2048, voff, d_); glds16s(kg + (st) * 16384 + w * 2048 + 1024, voff, d_ + 1024); \
    glds16s(vg + (st) * 16384 + w * 2048, voff, d_ + 16384); glds16s(vg + (st) * 16384 + w * 2048 + 1024, voff, d_ + 16384 + 1024); } while (0)
  DMA_ST(0); DMA_ST(1); DMA_ST(2);
  const int koff = lane * 16;
  const int gq = lane >> 4;
  const int voffl = 16384 + (4 * (gq >> 1) + ((lane & 15) >> 2)) * 64 + (gq & 1) * 32 + (lane & 3) * 8;
  f32x16 negm;
  float mrun = 0.f;
#pragma unroll
  for (int r = 0; r < 16; ++r) { o[r] = 0.f; negm[r] = 0.f; }
  l = (f32x4){0.f, 0.f, 0.f, 0.f};
#pragma unroll 1
  for (int st = 0; st < 8; ++st) {
    if (st < 6) WAITBAR(8); else if (st == 6) WAITBAR(4); else WAITBAR(0);
    if (st < 5) DMA_ST(st + 3);
    const lds_cptr kp = L + (st & 3) * AT_STAGE + koff;
    const lds_cptr vp = L + (st & 3) * AT_STAGE + voffl;
    if (!SAFE && st == 0) {
      f32x16 s = MFMA32(lds_rd128(kp), q0, negm);
      s = MFMA32(lds_rd128(kp + 1024), q1, s);
      const float m = max16(s);
#pragma unroll
      for (int r = 0; r < 16; ++r) negm[r] = -m;
    }
#pragma unroll
    for (int u = 0; u < 8; ++u) {
      const bf16x8 k0 = lds_rd128(kp + u * 2048), k1 = lds_rd128(kp + u * 2048 + 1024);
      const bf16x8 v0 = cat8(lds_tr(vp + u * 2048), lds_tr(vp + u * 2048 + 512));
      const bf16x8 v1 = cat8(lds_tr(vp + u * 2048 + 1024), lds_tr(vp + u * 2048 + 1536));
      f32x16 s = MFMA32(k0, q0, negm);
      s = MFMA32(k1, q1, s);
      if (SAFE) {
        const float m = max16(s);
        const float mn = (st == 0 && u == 0) ? m : fmaxf(mrun, m);
        const float alpha = __builtin_amdgcn_exp2f(mrun - mn);
        mrun = mn;
        if (!(st == 0 && u == 0)) {
#pragma unroll
          for (int r = 0; r < 16; ++r) o[r] *= alpha;
          l *= alpha;
        }
#pragma unroll
        for (int r = 0; r < 16; ++r) s[r] -= mn;
      }
      u32x4 pa, pb;
#pragma unroll
      for (int r = 0; r < 4; ++r) {
        pa[r] = cvtpk(__builtin_amdgcn_exp2f(s[2 * r]), __builtin_amdgcn_exp2f(s[2 * r + 1]));
        pb[r] = cvtpk(__builtin_amdgcn_exp2f(s[8 + 2 * r]), __builtin_amdgcn_exp2f(s[9 + 2 * r]));
      }
      const bf16x8 p0 = __builtin_bit_cast(bf16x8, pa), p1 = __builtin_bit_cast(bf16x8, pb);
      o = MFMA32(v0, p0, o);
      l = MFMA16(sel, p0, l);
      o = MFMA32(v1, p1, o);
      l = MFMA16(sel, p1, l);
    }
  }
#undef DMA_ST
}

__global__ __launch_bounds__(512, 2) void attn_kernel(const bf16_t* __restrict__ Qb, const bf16_t* __restrict__ Kb,
                                                      const bf16_t* __restrict__ Vb, bf16_t* __restrict__ AH) {
  extern __shared__ __attribute__((aligned(16))) char smem[];
  const int tid = threadIdx.x, lane = tid & 63;
  const int w = __builtin_amdgcn_readfirstlane(tid >> 6);
  const int bid = blockIdx.x, xcd = bid & 7, jj = bid >> 3;
  const int h = xcd * 4 + (jj >> 3), qb = jj & 7;
  const int qrow = qb * 256 + w * 32 + (lane & 31), hi = lane >> 5;
  const bf16_t* qp = Qb + ((size_t)h * SEQ + qrow) * 32 + 8 * hi;
  const bf16x8 q0 = *(const bf16x8*)qp, q1 = *(const bf16x8*)(qp + 16);
  const char* kg = (const char*)(Kb + (size_t)h * SEQ * 32);
  const char* vg = (const char*)(Vb + (size_t)h * SEQ * 32);
  const short one = (((lane >> 4) & 1) == ((lane >> 2) & 1)) ? (short)0x3f80 : (short)0;
  const bf16x8 sel = {one, one, one, one, one, one, one, one};
  volatile LAS unsigned* flag = (volatile LAS unsigned*)((LAS char*)smem + AT_FLAG);
  if (tid == 0) *flag = 0u;
  f32x16 o; f32x4 l;
  attn_pass<false>(kg, vg, smem, q0, q1, sel, o, l, lane, w);
  bool bad = !(l[0] < 1.0e36f);
#pragma unroll
  for (int r = 0; r < 16; ++r) bad = bad || !(fabsf(o[r]) < 3.0e38f);
  if (__any(bad) && lane == 0) *flag = 1u;
  __syncthreads();
  if (*flag != 0u) {
    __syncthreads();
    attn_pass<true>(kg, vg, smem, q0, q1, sel, o, l, lane, w);
  }
  const float rl = 1.0f / l[0];
  u32x2 pk[4];
#pragma unroll
  for (int k = 0; k < 4; ++k) { pk[k].x = cvtpk(o[4 * k] * rl, o[4 * k + 1] * rl); pk[k].y = cvtpk(o[4 * k + 2] * rl, o[4 * k + 3] * rl); }
  bf16_t* op = AH + (size_t)qrow * KA + h * 32 + 8 * hi;
#pragma unroll
  for (int k = 0; k < 4; k += 2) {
    auto rx = __builtin_amdgcn_permlane32_swap(pk[k].x, pk[k + 1].x, false, false);
    auto ry = __builtin_amdgcn_permlane32_swap(pk[k].y, pk[k + 1].y, false, false);
    u32x4 v = {rx[0], ry[0], rx[1], ry[1]};
    *(u32x4*)(op + 8 * k) = v;
  }
}

constexpr int G2_SLOT = 32768, G2_LDS = 4 * G2_SLOT;
constexpr int G2_NCH = 36;

__global__ __launch_bounds__(512, 2) void gemm2_kernel(const bf16_t* __restrict__ AH, const bf16_t* __restrict__ wo,
                                                       const bf16_t* __restrict__ wd, const bf16_t* __restrict__ x,
                                                       bf16_t* __restrict__ out, float* part, unsigned* flags) {
  extern __shared__ __attribute__((aligned(16))) char smem[];
  const int tid = threadIdx.x, lane = tid & 63;
  const int w = __builtin_amdgcn_readfirstlane(tid >> 6);
  const int g = w >> 2, w4 = w & 3, wm = w4 >> 1, wn = w4 & 1;
  const int bid = blockIdx.x, xcd = bid & 7, jj = bid >> 3;
  const int kh = xcd & 1, rt = (xcd >> 1) * 4 + (jj >> 3), ct = jj & 7;
  const int row0 = rt * 128, col0 = ct * 128;
  const int tile = rt * 8 + ct;
  const unsigned lds0 = (unsigned)(uintptr_t)smem;
  const lds_cptr L = (lds_cptr)smem;

  const unsigned avoff = (unsigned)((lane >> 3) * (KA * 2) + (((lane & 7) ^ (lane >> 4)) * 16));
  const char* abase = (const char*)AH + (size_t)(row0 + 32 * w4) * (KA * 2) + (size_t)kh * (G2_NCH * 128);
  const unsigned bvoff = (unsigned)((lane >> 4) * 2048 + (((lane & 15) ^ ((lane >> 4) << 1)) * 16));
#define DMA_CH(c) do { const int kg_ = kh * (G2_NCH * 64) + (c) * 64; \
    const char* bb_ = (kg_ < DM ? (const char*)wo + (size_t)kg_ * 2048 : (const char*)wd + (size_t)(kg_ - DM) * 2048) + col0 * 2 + w4 * 32768; \
    const unsigned d_ = lds0 + ((c) & 3) * G2_SLOT + w4 * 4096; \
    _Pragma("unroll") for (int _i = 0; _i < 4; ++_i) \
      glds16s(abase + (size_t)_i * (8 * KA * 2) + (c) * 128, (_i & 1) ? (avoff ^ 64u) : avoff, d_ + _i * 1024); \
    _Pragma("unroll") for (int _i = 0; _i < 4; ++_i) \
      glds16s(bb_ + _i * 8192, (_i >> 1) ? (bvoff ^ 128u) : bvoff, d_ + 16384 + _i * 1024); } while (0)

  const int fr = lane & 15, fq = lane >> 4, q4 = fr >> 2, p4 = lane & 3;
  const int aoff0 = (64 * wm + fr) * 128 + ((fq ^ (fr >> 1)) * 16);
  const int boff0 = 16384 + (8 * fq + q4) * 256 + 8 * p4;
  const int bsw = q4 | ((fq & 1) << 2);
  int bo[4];
#pragma unroll
  for (int n = 0; n < 4; ++n) bo[n] = boff0 + (((4 * wn + n) ^ bsw) * 32);

  f32x4 acc[4][4];
#pragma unroll
  for (int m = 0; m < 4; ++m)
#pragma unroll
    for (int n = 0; n < 4; ++n) acc[m][n] = (f32x4){0.f, 0.f, 0.f, 0.f};
  bf16x8 af[2][4], bfr[2][4];

  if (g == 0) { DMA_CH(1); WAITBAR(8); }
  else { DMA_CH(0); DMA_CH(2); WAITBAR(8); BARRIER(); }

#pragma unroll 1
  for (int c = g; c < G2_NCH; c += 2) {
    const lds_cptr sl = L + (c & 3) * G2_SLOT;
#pragma unroll
    for (int ks = 0; ks < 2; ++ks) {
#pragma unroll
      for (int m = 0; m < 4; ++m) af[ks][m] = lds_rd128(sl + (aoff0 ^ (ks * 64)) + m * 2048);
#pragma unroll
      for (int n = 0; n < 4; ++n) bfr[ks][n] = cat8(lds_tr(sl + bo[n] + ks * 8192), lds_tr(sl + bo[n] + ks * 8192 + 1024));
    }
    if (c + 3 < G2_NCH) { DMA_CH(c + 3); WAITBAR(8); }
    else WAITBAR(0);
    __builtin_amdgcn_s_setprio(1);
#pragma unroll
    for (int ks = 0; ks < 2; ++ks)
#pragma unroll
      for (int n = 0; n < 4; ++n)
#pragma unroll
        for (int m = 0; m < 4; ++m) acc[m][n] = MFMA16(bfr[ks][n], af[ks][m], acc[m][n]);
    __builtin_amdgcn_s_setprio(0);
    BARRIER();
  }
  if (g == 0) BARRIER();
#undef DMA_CH

  {
    LAS f32x4* xb = (LAS f32x4*)((LAS char*)smem) + (size_t)w * 512;
    f32x4 s[2][4];
    if (g) {
#pragma unroll
      for (int mm = 0; mm < 2; ++mm)
#pragma unroll
        for (int n = 0; n < 4; ++n) { xb[(mm * 4 + n) * 64 + lane] = acc[mm][n]; s[mm][n] = acc[2 + mm][n]; }
    } else {
#pragma unroll
      for (int mm = 0; mm < 2; ++mm)
#pragma unroll
        for (int n = 0; n < 4; ++n) { xb[(mm * 4 + n) * 64 + lane] = acc[2 + mm][n]; s[mm][n] = acc[mm][n]; }
    }
    __syncthreads();
    LAS f32x4* xr = (LAS f32x4*)((LAS char*)smem) + (size_t)(w ^ 4) * 512;
#pragma unroll
    for (int mm = 0; mm < 2; ++mm)
#pragma unroll
      for (int n = 0; n < 4; ++n) s[mm][n] += xr[(mm * 4 + n) * 64 + lane];
    f32x4 keep[4], send[4];
    if (kh) {
#pragma unroll
      for (int n = 0; n < 4; ++n) { keep[n] = s[1][n]; send[n] = s[0][n]; }
    } else {
#pragma unroll
      for (int n = 0; n < 4; ++n) { keep[n] = s[0][n]; send[n] = s[1][n]; }
    }
    const int pbid = bid ^ 1;
    float* mine = part + (size_t)bid * 8192 + w * 1024;
#pragma unroll
    for (int n = 0; n < 4; ++n) {
      const f32x4 v = send[n];
      asm volatile("global_store_dwordx4 %0, %1, off sc1\n\ts_nop 1" :: "v"(mine + n * 256 + lane * 4), "v"(v) : "memory");
    }
    asm volatile("s_waitcnt vmcnt(0)" ::: "memory");
    __syncthreads();
    if (tid == 0) {
      __hip_atomic_store(flags + bid * 64, 1u, __ATOMIC_RELAXED, __HIP_MEMORY_SCOPE_AGENT);
      unsigned spins = 0;
      while (__hip_atomic_load(flags + pbid * 64, __ATOMIC_RELAXED, __HIP_MEMORY_SCOPE_AGENT) == 0u) {
        __builtin_amdgcn_s_sleep(1);
        if (++spins > (1u << 22)) break;
      }
      __builtin_amdgcn_fence(__ATOMIC_ACQUIRE, "agent");
      asm volatile("s_waitcnt vmcnt(0)" ::: "memory");
    }
    __syncthreads();
    const float* theirs = part + (size_t)pbid * 8192 + w * 1024;
    const int row = row0 + 64 * wm + 16 * (2 * g + kh) + fr;
#pragma unroll
    for (int n = 0; n < 4; ++n) {
      const f32x4 pv = *(const f32x4*)(theirs + n * 256 + lane * 4);
      const f32x4 v = keep[n] + pv;
      const int col = col0 + 64 * wn + 16 * n + 4 * fq;
      const u32x2 xv = *(const u32x2*)(x + (size_t)row * DM + col);
      u32x2 o = {cvtpk(bf_lo(xv.x) + v[0], bf_hi(xv.x) + v[1]), cvtpk(bf_lo(xv.y) + v[2], bf_hi(xv.y) + v[3])};
      *(u32x2*)(out + (size_t)row * DM + col) = o;
    }
  }
  (void)tile;
}

extern "C" void kernel_launch(void* const* d_in, const int* in_sizes, int n_in, void* d_out, int out_size, void* d_ws,
                              size_t ws_size, hipStream_t stream) {
  const bf16_t* x = (const bf16_t*)d_in[0];
  const bf16_t* wqkv = (const bf16_t*)d_in[1];
  const bf16_t* wo = (const bf16_t*)d_in[2];
  const bf16_t* wg = (const bf16_t*)d_in[4];
  const bf16_t* wu = (const bf16_t*)d_in[5];
  const bf16_t* wd = (const bf16_t*)d_in[6];
  char* ws = (char*)d_ws;
  bf16_t* xn = (bf16_t*)(ws + WS_XN);
  bf16_t* Qb = (bf16_t*)(ws + WS_Q);
  bf16_t* Kb = (bf16_t*)(ws + WS_K);
  bf16_t* Vb = (bf16_t*)(ws + WS_V);
  bf16_t* AH = (bf16_t*)(ws + WS_AH);
  float* part = (float*)(ws + WS_PART);
  unsigned* flags = (unsigned*)(ws + WS_FLAGS);
  (void)hipFuncSetAttribute((const void*)gemm1_kernel, hipFuncAttributeMaxDynamicSharedMemorySize, G1_LDS);
  (void)hipFuncSetAttribute((const void*)attn_kernel, hipFuncAttributeMaxDynamicSharedMemorySize, AT_LDS);
  (void)hipFuncSetAttribute((const void*)gemm2_kernel, hipFuncAttributeMaxDynamicSharedMemorySize, G2_LDS);
  ln_kernel<<<SEQ / 4, 256, 0, stream>>>(x, xn, flags);
  gemm1_kernel<<<256, 512, G1_LDS, stream>>>(xn, wqkv, wg, wu, Qb, Kb, Vb, AH);
  attn_kernel<<<256, 512, AT_LDS, stream>>>(Qb, Kb, Vb, AH);
  gemm2_kernel<<<256, 512, G2_LDS, stream>>>(AH, wo, wd, x, (bf16_t*)d_out, part, flags);
}
```

```cpp
#include <hip/hip_runtime.h>
#include <hip/hip_bf16.h>
#include <stdint.h>

typedef unsigned short bf16_t;
typedef short bf16x8 __attribute__((ext_vector_type(8)));
typedef short s16x4 __attribute__((ext_vector_type(4)));
typedef float f32x4 __attribute__((ext_vector_type(4)));
typedef float f32x16 __attribute__((ext_vector_type(16)));
typedef float f32x2 __attribute__((ext_vector_type(2)));
typedef __bf16 bf16x2v __attribute__((ext_vector_type(2)));
typedef unsigned u32x4 __attribute__((ext_vector_type(4)));
typedef unsigned u32x2 __attribute__((ext_vector_type(2)));
#define LAS __attribute__((address_space(3)))
typedef LAS const char* lds_cptr;

constexpr int SEQ = 2048, DM = 1024, NH = 32, FF = 3584, NQKV = 3072, KA = 4608;

constexpr size_t WS_FLAGS = 0;
constexpr size_t WS_XN = 1u << 20;
constexpr size_t WS_Q = WS_XN + (size_t)SEQ * DM * 2;
constexpr size_t WS_K = WS_Q + (size_t)SEQ * DM * 2;
constexpr size_t WS_V = WS_K + (size_t)SEQ * DM * 2;
constexpr size_t WS_AH = WS_V + (size_t)SEQ * DM * 2;
constexpr size_t WS_PART = WS_AH + (size_t)SEQ * KA * 2;

#define SB() __builtin_amdgcn_sched_barrier(0)
#define STAMP(kid, i) do {} while (0)

__device__ __forceinline__ unsigned cvtpk(float lo, float hi) {
  f32x2 v = {lo, hi};
  bf16x2v b = __builtin_convertvector(v, bf16x2v);
  return __builtin_bit_cast(unsigned, b);
}
__device__ __forceinline__ float bf_lo(unsigned u) { return __uint_as_float(u << 16); }
__device__ __forceinline__ float bf_hi(unsigned u) { return __uint_as_float(u & 0xffff0000u); }
__device__ __forceinline__ float rbf(float x) { return bf_lo(cvtpk(x, x)); }

__device__ __forceinline__ void glds16(const void* g, unsigned lds_dst) {
  unsigned keep;
  asm volatile("s_mov_b32 %0, m0\n\ts_mov_b32 m0, %2\n\ts_nop 0\n\tglobal_load_lds_dwordx4 %1, off\n\ts_mov_b32 m0, %0"
               : "=&s"(keep) : "v"(g), "s"(lds_dst) : "memory");
}
__device__ __forceinline__ void glds16s(const void* sbase, unsigned voff, unsigned lds_dst) {
  unsigned keep;
  asm volatile("s_mov_b32 %0, m0\n\ts_mov_b32 m0, %3\n\ts_nop 0\n\tglobal_load_lds_dwordx4 %1, %2\n\ts_mov_b32 m0, %0"
               : "=&s"(keep) : "v"(voff), "s"(sbase), "s"(lds_dst) : "memory");
}
__device__ __forceinline__ bf16x8 lds_rd128(lds_cptr p) { return *(const LAS bf16x8*)p; }
__device__ __forceinline__ s16x4 lds_tr(lds_cptr p) {
  return __builtin_amdgcn_ds_read_tr16_b64_v4i16((LAS s16x4*)p);
}
__device__ __forceinline__ bf16x8 cat8(s16x4 a, s16x4 b) { return __builtin_shufflevector(a, b, 0, 1, 2, 3, 4, 5, 6, 7); }

constexpr int G1_AOFF = 0, G1_BOFF = 65536, G1_BSLOT = 20480, G1_LDS = 65536 + 4 * 20480;
#define MFMA16(a, b, c) __builtin_amdgcn_mfma_f32_16x16x32_bf16(a, b, c, 0, 0, 0)
#define MFMA32(a, b, c) __builtin_amdgcn_mfma_f32_32x32x16_bf16(a, b, c, 0, 0, 0)
#define WAITBAR(N) do { SB(); asm volatile("s_waitcnt vmcnt(" #N ") lgkmcnt(0)\n\ts_barrier" ::: "memory"); SB(); } while (0)
#define BARRIER() do { SB(); asm volatile("s_barrier" ::: "memory"); SB(); } while (0)
__device__ __forceinline__ float dot2(unsigned a, unsigned b, float c) {
  return __builtin_amdgcn_fdot2_f32_bf16(__builtin_bit_cast(bf16x2v, a), __builtin_bit_cast(bf16x2v, b), c, false);
}
__device__ __forceinline__ void glds16p(u32x2 g, unsigned lds_dst) {
  unsigned keep;
  asm volatile("s_mov_b32 %0, m0\n\ts_mov_b32 m0, %2\n\ts_nop 0\n\tglobal_load_lds_dwordx4 %1, off\n\ts_mov_b32 m0, %0"
               : "=&s"(keep) : "v"(g), "s"(lds_dst) : "memory");
}

__global__ __launch_bounds__(512, 2) void gemm1_kernel(const bf16_t* __restrict__ x, const bf16_t* __restrict__ wqkv,
                                                       const bf16_t* __restrict__ wg, const bf16_t* __restrict__ wu,
                                                       bf16_t* __restrict__ Qb, bf16_t* __restrict__ Kb, bf16_t* __restrict__ Vb,
                                                       bf16_t* __restrict__ AH, unsigned* flags) {
  extern __shared__ __attribute__((aligned(16))) char smem[];
  const int tid = threadIdx.x, lane = tid & 63;
  const int w = __builtin_amdgcn_readfirstlane(tid >> 6);
  const int g = w >> 2, w4 = w & 3, wm = 2 * g + (w4 >> 1), wn = w4 & 1;
  STAMP(1, 0);
  const int bid = blockIdx.x, xcd = bid & 7, jj = bid >> 3;
  const int cg = xcd * 4 + (jj & 3), rt = jj >> 2;
  const int row0 = rt * 256;
  const unsigned lds0 = (unsigned)(uintptr_t)smem;
  const lds_cptr L = (lds_cptr)smem;

  const unsigned avoff = (unsigned)((lane >> 3) * 2048 + (((lane & 7) ^ (lane >> 4)) * 16));
  const char* abase = (const char*)x + (size_t)(row0 + 32 * w4) * 2048;
#define DMA_A(hh, t) do { _Pragma("unroll") for (int _i = 0; _i < 4; ++_i) \
    glds16s(abase + (size_t)(hh) * 262144 + _i * 16384 + (t) * 128, (_i & 1) ? (avoff ^ 64u) : avoff, \
            lds0 + G1_AOFF + ((t) & 1) * 32768 + (hh) * 16384 + (4 * w4 + _i) * 1024); } while (0)
  DMA_A(g, 0);

  u32x2 bptr[4]; unsigned bld[4];
  const int b0 = g ? 16 + w4 : 4 * w4;
#pragma unroll
  for (int i = 0; i < 4; ++i) {
    const int b = (i == 0) ? b0 : 4 * w4 + i;
    const int region = b >= 10 ? 1 : 0;
    const int P = (b - 10 * region) * 64 + lane;
    const int k = P / 20, cp = P - 20 * k;
    const int c8 = (cp ^ (((k >> 3) & 1) << 1)) * 8;
    const bool isq = (cg <= 8) || (cg == 9 && c8 < 96);
    const bf16_t* mat = isq ? wqkv : (region ? wu : wg);
    const int ld = isq ? NQKV : FF;
    const int col = c8 + (cg <= 8 ? 320 * cg + 160 * region : (cg == 9 ? (isq ? 2880 + 96 * region : -96) : 64 + 160 * (cg - 10)));
    const uint64_t pa = (uint64_t)(uintptr_t)(mat + (size_t)k * ld + col);
    bptr[i] = (u32x2){(unsigned)pa, (unsigned)(pa >> 32)};
    bld[i] = (unsigned)ld * 64u;
  }
  const unsigned bdst0 = lds0 + G1_BOFF + b0 * 1024, bdst1 = lds0 + G1_BOFF + 4 * w4 * 1024;
#define DMA_B(i, s) glds16p(bptr[i], ((i) == 0 ? bdst0 : bdst1 + (i) * 1024) + ((s) & 3) * G1_BSLOT)
#define ADV_B() do { _Pragma("unroll") for (int _i = 0; _i < 4; ++_i) { unsigned lo_; \
    const bool c_ = __builtin_uadd_overflow(bptr[_i].x, bld[_i], &lo_); bptr[_i].x = lo_; bptr[_i].y += c_ ? 1u : 0u; } } while (0)

  if (g == 0) {
    DMA_B(0, 0); DMA_B(1, 0); DMA_B(2, 0); DMA_B(3, 0); ADV_B();
    DMA_B(0, 1); ADV_B();
    DMA_B(0, 2); DMA_B(1, 2); DMA_B(2, 2); DMA_B(3, 2); ADV_B();
  } else {
    DMA_B(0, 0); ADV_B();
    DMA_B(0, 1); DMA_B(1, 1); DMA_B(2, 1); DMA_B(3, 1); ADV_B();
    DMA_B(0, 2); ADV_B();
    DMA_A(0, 1);
  }
  if (tid < 64) flags[bid * 64 + tid] = 0u;

  const int fr = lane & 15, fq = lane >> 4;
  const int aoffm = G1_AOFF + (64 * wm + fr) * 128 + ((fq ^ (fr >> 1)) * 16);
  const int aoffP = aoffm + 4096 * wn, aoffM = aoffm - 4096 * wn;
  const int q4 = fr >> 2, p4 = lane & 3, gb = fq & 1;
  const int bbase = G1_BOFF + (8 * fq + q4) * 320 + 8 * p4 + 160 * wn;
  const int boffE = bbase + (wn ? -32 * gb : 32 * gb);
  const int boffO = bbase + (wn ? 32 * gb : -32 * gb);

  f32x4 acc[4][5][2];
#pragma unroll
  for (int m = 0; m < 4; ++m)
#pragma unroll
    for (int j = 0; j < 5; ++j) { acc[m][j][0] = (f32x4){0.f, 0.f, 0.f, 0.f}; acc[m][j][1] = (f32x4){0.f, 0.f, 0.f, 0.f}; }
  bf16x8 af[4], bfr[5][2];
  float s1[2] = {0.f, 0.f}, s2[2] = {0.f, 0.f}, cs[5] = {0.f, 0.f, 0.f, 0.f, 0.f};

  if (g == 0) { WAITBAR(5); } else { WAITBAR(9); BARRIER(); }
  STAMP(1, 1);

#define DW(v, i) (__builtin_bit_cast(u32x4, v)[i])
#define COMPUTE() do { __builtin_amdgcn_s_setprio(1); \
    _Pragma("unroll") for (int j = 0; j < 5; ++j) _Pragma("unroll") for (int r = 0; r < 2; ++r) _Pragma("unroll") for (int m = 0; m < 4; ++m) { \
      acc[m][j][r] = MFMA16(bfr[j][r], af[m], acc[m][j][r]); \
      if (r == 1 && j < 4) { const int mm_ = j >> 1; \
        if (j & 1) s2[mm_] = dot2(DW(af[mm_], m), DW(af[mm_], m), s2[mm_]); else s1[mm_] = dot2(DW(af[mm_], m), 0x3f803f80u, s1[mm_]); } \
      SB(); } \
    __builtin_amdgcn_s_setprio(0); } while (0)
#define COLSUM(R) do { _Pragma("unroll") for (int d = 0; d < 4; ++d) _Pragma("unroll") for (int j = 0; j < 5; ++j) \
      cs[j] = dot2(DW(bfr[j][R], d), 0x3f803f80u, cs[j]); } while (0)

#pragma unroll 1
  for (int s = 0; s < 32; ++s) {
    const int par = (s & 1) ^ g;
    {
      const int ax = (s & 1) * 64, as = ((s >> 1) & 1) * 32768, bs = (s & 3) * G1_BSLOT;
      const lds_cptr pP = L + ((aoffP ^ ax) + as), pM = L + ((aoffM ^ ax) + as);
      const lds_cptr pe = L + (boffE + bs), po = L + (boffO + bs);
      af[0] = lds_rd128(pP); af[1] = lds_rd128(pP + 2048); af[2] = lds_rd128(pM + 4096); af[3] = lds_rd128(pM + 6144);
#pragma unroll
      for (int j = 0; j < 5; ++j) { const lds_cptr pp = (j & 1) ? po : pe;
#pragma unroll
        for (int r = 0; r < 2; ++r) bfr[j][r] = cat8(lds_tr(pp + r * 10240 + j * 32), lds_tr(pp + r * 10240 + j * 32 + 1280)); }
    }
    if (s <= 28) {
      DMA_B(0, s + 3);
      if (par == 0) { DMA_A(1 - g, (s >> 1) + 1 + g); }
      else { DMA_B(1, s + 3); DMA_B(2, s + 3); DMA_B(3, s + 3); }
      ADV_B();
    }
    SB();
    if (par) { if (wm & 1) { COLSUM(1); } else { COLSUM(0); } }
    if (s <= 28) { WAITBAR(9); } else if (s == 29) { WAITBAR(4); } else { WAITBAR(0); }
    COMPUTE();
    BARRIER();
  }
  if (g == 0) BARRIER();
  STAMP(1, 2);
#undef COMPUTE
#undef COLSUM
#undef DMA_A
#undef DMA_B
#undef ADV_B

  int tz = threadIdx.x; asm volatile("" : "+v"(tz));
  const int fr2 = tz & 15, fq2 = (tz >> 4) & 3;
  LAS f32x2* rowst = (LAS f32x2*)((LAS char*)smem);
  LAS float* colst = (LAS float*)((LAS char*)smem + 4096);
#pragma unroll
  for (int i = 0; i < 2; ++i) {
    float a = s1[i], b = s2[i];
    a += __shfl_xor(a, 16); b += __shfl_xor(b, 16);
    a += __shfl_xor(a, 32); b += __shfl_xor(b, 32);
    const float mean = a * (1.f / DM);
    const float var = fmaxf(b * (1.f / DM) - mean * mean, 0.f);
    const float den = rbf(sqrtf(rbf(rbf(var) + 1e-5f)));
    if (fq2 == 0) rowst[64 * wm + 16 * (2 * wn + i) + fr2] = (f32x2){rbf(mean), 1.0f / den};
  }
#pragma unroll
  for (int j = 0; j < 5; ++j) {
    float c = cs[j];
    c += __shfl_xor(c, 16); c += __shfl_xor(c, 32);
    if (fq2 == 0) colst[g * 320 + (wm & 1) * 160 + (5 * wn + j) * 16 + fr2] = c;
  }
  __syncthreads();
  f32x2 rs[4];
#pragma unroll
  for (int m = 0; m < 4; ++m) rs[m] = rowst[64 * wm + 16 * (m ^ (2 * wn)) + fr2];

  const float qscale = 0.17677669529663687f * 1.4426950408889634f;
#pragma unroll
  for (int j = 0; j < 5; ++j) {
    const int i = 5 * wn + j;
    const LAS float* cp = colst + i * 16 + 4 * fq2;
    const f32x4 cx = *(const LAS f32x4*)cp + *(const LAS f32x4*)(cp + 320);
    const f32x4 cy = *(const LAS f32x4*)(cp + 160) + *(const LAS f32x4*)(cp + 480);
    const bool gu = (cg >= 10) || (cg == 9 && i >= 6);
    if (gu) {
      const int f0 = (cg == 9 ? 16 * i - 96 : 64 + 160 * (cg - 10) + 16 * i) + 4 * fq2;
#pragma unroll
      for (int m = 0; m < 4; ++m) {
        const int row = row0 + 64 * wm + 16 * (m ^ (2 * wn)) + fr2;
        const float mu = rs[m].x, r1 = rs[m].y, ce = -1.4426950408889634f * r1, r2 = r1 * r1;
        float hv[4];
#pragma unroll
        for (int r = 0; r < 4; ++r) {
          const float tg = acc[m][j][0][r] - mu * cx[r], tu = acc[m][j][1][r] - mu * cy[r];
          const float sg = __builtin_amdgcn_rcpf(1.f + __builtin_amdgcn_exp2f(ce * tg));
          hv[r] = (tg * tu) * (r2 * sg);
        }
        u32x2 o = {cvtpk(hv[0], hv[1]), cvtpk(hv[2], hv[3])};
        *(u32x2*)(AH + (size_t)row * KA + DM + f0) = o;
      }
    } else {
#pragma unroll
      for (int r2 = 0; r2 < 2; ++r2) {
        const int col = (cg <= 8 ? 320 * cg + 160 * r2 : 2880 + 96 * r2) + 16 * i;
        const int typ = col >> 10, h = (col & 1023) >> 5, d = (col & 31) + 4 * fq2;
        const float sc = typ == 0 ? qscale : 1.f;
        const f32x4 cc = r2 ? cy : cx;
#pragma unroll
        for (int m = 0; m < 4; ++m) {
          const int row = row0 + 64 * wm + 16 * (m ^ (2 * wn)) + fr2;
          const f32x4 v = (acc[m][j][r2] - rs[m].x * cc) * (rs[m].y * sc);
          u32x2 o = {cvtpk(v[0], v[1]), cvtpk(v[2], v[3])};
          bf16_t* dst;
          if (typ == 1) dst = Kb + ((((size_t)h * 64 + (row >> 5)) * 2 + (d >> 4)) * 64 + (row & 31) + 32 * ((d >> 3) & 1)) * 8 + (d & 7);
          else dst = (typ == 0 ? Qb : Vb) + ((size_t)h * SEQ + row) * 32 + d;
          *(u32x2*)dst = o;
        }
      }
    }
  }
  STAMP(1, 3);
}

constexpr int AT_STAGE = 32768, AT_FLAG = AT_STAGE * 4, AT_LDS = AT_FLAG + 64;
#define PIN(x) asm volatile("" : "+v"(x))
#define EXP2(x) __builtin_amdgcn_exp2f(x)

__device__ __forceinline__ float max16(const f32x16& p) {
  float a = fmaxf(fmaxf(p[0], p[1]), p[2]), b = fmaxf(fmaxf(p[3], p[4]), p[5]);
  a = fmaxf(fmaxf(a, p[6]), p[7]); b = fmaxf(fmaxf(b, p[8]), p[9]);
  a = fmaxf(fmaxf(a, p[10]), p[11]); b = fmaxf(fmaxf(b, p[12]), p[13]);
  a = fmaxf(fmaxf(a, p[14]), p[15]);
  float m = fmaxf(a, b);
  auto rr = __builtin_amdgcn_permlane32_swap(__float_as_uint(m), __float_as_uint(m), false, false);
  return fmaxf(__uint_as_float(rr[0]), __uint_as_float(rr[1]));
}
__device__ __forceinline__ f32x16 splat16(float v) { f32x16 r;
#pragma unroll
  for (int i = 0; i < 16; ++i) r[i] = v;
  return r; }

__global__ __launch_bounds__(512, 2) void attn_kernel(const bf16_t* __restrict__ Qb, const bf16_t* __restrict__ Kb,
                                                      const bf16_t* __restrict__ Vb, bf16_t* __restrict__ AH) {
  extern __shared__ __attribute__((aligned(16))) char smem[];
  const int tid = threadIdx.x, lane = tid & 63;
  const int w = __builtin_amdgcn_readfirstlane(tid >> 6);
  const int qg = w & 3, kh = w >> 2;
  const int bid = blockIdx.x, xcd = bid & 7, jj = bid >> 3;
  const int h = xcd * 4 + (jj >> 3), qb = jj & 7;
  STAMP(2, 0);
  const unsigned lds0 = (unsigned)(uintptr_t)smem;
  const lds_cptr L = (lds_cptr)smem;
  const int hi = lane >> 5;
  const char* kg = (const char*)(Kb + (size_t)h * SEQ * 32) + kh * 65536 + qg * 2048;
  const char* vg = (const char*)(Vb + (size_t)h * SEQ * 32) + kh * 65536 + qg * 2048;
  const unsigned voff = lane * 16;
#define DMA_ST(st) do { const unsigned d_ = lds0 + ((st) & 3) * AT_STAGE + kh * 8192 + qg * 2048; \
    glds16s(kg + (st) * 8192, voff, d_); glds16s(kg + (st) * 8192 + 1024, voff, d_ + 1024); \
    glds16s(vg + (st) * 8192, voff, d_ + 16384); glds16s(vg + (st) * 8192 + 1024, voff, d_ + 16384 + 1024); } while (0)
  const bf16_t* qp = Qb + ((size_t)h * SEQ + qb * 256 + qg * 64 + (lane & 31)) * 32 + 8 * hi;
  bf16x8 qa0 = *(const bf16x8*)qp, qa1 = *(const bf16x8*)(qp + 16);
  bf16x8 qb0 = *(const bf16x8*)(qp + 1024), qb1 = *(const bf16x8*)(qp + 1024 + 16);
  DMA_ST(0); DMA_ST(1); DMA_ST(2);
  PIN(qa0); PIN(qa1); PIN(qb0); PIN(qb1);
  const short one = (((lane >> 4) & 1) == ((lane >> 2) & 1)) ? (short)0x3f80 : (short)0;
  const bf16x8 sel = {one, one, one, one, one, one, one, one};
  volatile LAS unsigned* flag = (volatile LAS unsigned*)((LAS char*)smem + AT_FLAG);
  if (tid == 0) *flag = 0u;
  const int gq = lane >> 4;
  const int koff = kh * 8192 + lane * 16;
  const int voffl = 16384 + kh * 8192 + (4 * (gq >> 1) + ((lane & 15) >> 2)) * 64 + (gq & 1) * 32 + (lane & 3) * 8;
  const f32x16 zero16 = splat16(0.f);
  f32x16 oA = zero16, oB = zero16;
  f32x4 lA = {0.f, 0.f, 0.f, 0.f}, lB = {0.f, 0.f, 0.f, 0.f};
  float mA, mB;

#define LDK(K0, K1, p) do { K0 = lds_rd128(p); K1 = lds_rd128((p) + 1024); } while (0)
#define LDV(V0, V1, p) do { V0 = cat8(lds_tr(p), lds_tr((p) + 512)); V1 = cat8(lds_tr((p) + 1024), lds_tr((p) + 1536)); } while (0)
  {
    WAITBAR(8);
    bf16x8 kx0, kx1, ky0, ky1, vx0, vx1, vy0, vy1;
    LDK(kx0, kx1, L + koff);
    LDV(vx0, vx1, L + voffl);
    f32x16 sA = MFMA32(kx0, qa0, zero16); sA = MFMA32(kx1, qa1, sA);
    f32x16 sB = MFMA32(kx0, qb0, zero16); sB = MFMA32(kx1, qb1, sB);
    mA = max16(sA); mB = max16(sB);
    f32x16 nA = splat16(-mA), nB = splat16(-mB);
    PIN(nA); PIN(nB);
    sA = MFMA32(kx0, qa0, nA); sA = MFMA32(kx1, qa1, sA);
    u32x4 pA0, pA1, pB0 = {0u, 0u, 0u, 0u}, pB1 = {0u, 0u, 0u, 0u};
    vy0 = vx0; vy1 = vx1;
#define BF(x) __builtin_bit_cast(bf16x8, x)
#define XLOADS(x) x
    STAMP(2, 1);
#define E4(S, i) do { S[i] = EXP2(S[i]); S[i + 1] = EXP2(S[i + 1]); S[i + 2] = EXP2(S[i + 2]); S[i + 3] = EXP2(S[i + 3]); } while (0)
#define SLOT(SC, PC0, PC1, SN, K0, K1, Q0, Q1, NM, PP0, PP1, V0, V1, O, LS, LOADS) do { \
    SN = MFMA32(K0, Q0, NM); E4(SC, 0); PIN(SC); SB(); \
    SN = MFMA32(K1, Q1, SN); E4(SC, 4); PIN(SC); SB(); \
    XLOADS(LOADS); SB(); \
    O = MFMA32(V0, BF(PP0), O); PC0[0] = cvtpk(SC[0], SC[1]); PC0[1] = cvtpk(SC[2], SC[3]); \
      SC[8] = EXP2(SC[8]); SC[9] = EXP2(SC[9]); SC[10] = EXP2(SC[10]); PIN(SC); PIN(PC0); SB(); \
    LS = MFMA16(sel, BF(PP0), LS); PC0[2] = cvtpk(SC[4], SC[5]); PC0[3] = cvtpk(SC[6], SC[7]); SC[11] = EXP2(SC[11]); PIN(SC); PIN(PC0); SB(); \
    O = MFMA32(V1, BF(PP1), O); E4(SC, 12); PIN(SC); SB(); \
    LS = MFMA16(sel, BF(PP1), LS); PC1[0] = cvtpk(SC[8], SC[9]); PC1[1] = cvtpk(SC[10], SC[11]); \
      PC1[2] = cvtpk(SC[12], SC[13]); PC1[3] = cvtpk(SC[14], SC[15]); PIN(PC1); SB(); } while (0)
#define SUB(KC0, KC1, KN0, KN1, VC0, VC1, VP0, VP1, kpn, vpn) do { \
    SLOT(sA, pA0, pA1, sB, KC0, KC1, qb0, qb1, nB, pB0, pB1, VP0, VP1, oB, lB, LDK(KN0, KN1, kpn)); \
    SLOT(sB, pB0, pB1, sA, KN0, KN1, qa0, qa1, nA, pA0, pA1, VC0, VC1, oA, lA, LDV(VP0, VP1, vpn)); } while (0)
#pragma unroll 1
    for (int st = 0; st < 8; ++st) {
      const lds_cptr kp = L + (st & 3) * AT_STAGE + koff, vp = L + (st & 3) * AT_STAGE + voffl;
      const lds_cptr kn = L + ((st + 1) & 3) * AT_STAGE + koff, vn = L + ((st + 1) & 3) * AT_STAGE + voffl;
      SUB(kx0, kx1, ky0, ky1, vx0, vx1, vy0, vy1, kp + 2048, vp + 2048);
      SUB(ky0, ky1, kx0, kx1, vy0, vy1, vx0, vx1, kp + 4096, vp + 4096);
      SUB(kx0, kx1, ky0, ky1, vx0, vx1, vy0, vy1, kp + 6144, vp + 6144);
      if (st < 6) { SB(); asm volatile("s_waitcnt vmcnt(4)\n\ts_barrier" ::: "memory"); SB(); }
      else { SB(); asm volatile("s_waitcnt vmcnt(0)\n\ts_barrier" ::: "memory"); SB(); }
      if (st < 5) DMA_ST(st + 3);
      SUB(ky0, ky1, kx0, kx1, vy0, vy1, vx0, vx1, kn, vn);
    }
    oB = MFMA32(vy0, BF(pB0), oB); lB = MFMA16(sel, BF(pB0), lB);
    oB = MFMA32(vy1, BF(pB1), oB); lB = MFMA16(sel, BF(pB1), lB);
#undef SUB
#undef SLOT
#undef E4
  }
  STAMP(2, 2);
  bool bad = !(lA[0] < 1.0e36f) || !(lB[0] < 1.0e36f);
#pragma unroll
  for (int r = 0; r < 16; ++r) bad = bad || !(fabsf(oA[r]) < 3.0e38f) || !(fabsf(oB[r]) < 3.0e38f);
  if (__any(bad) && lane == 0) *flag = 1u;
  __syncthreads();
  if (*flag != 0u) {
    __syncthreads();
    DMA_ST(0); DMA_ST(1); DMA_ST(2);
    oA = zero16; oB = zero16; lA = (f32x4){0.f, 0.f, 0.f, 0.f}; lB = lA;
    mA = -3.0e38f; mB = -3.0e38f;
#pragma unroll 1
    for (int st = 0; st < 8; ++st) {
      if (st < 6) WAITBAR(8); else if (st == 6) WAITBAR(4); else WAITBAR(0);
      if (st < 5) DMA_ST(st + 3);
#pragma unroll 1
      for (int u = 0; u < 4; ++u) {
        bf16x8 k0, k1, v0, v1;
        LDK(k0, k1, L + (st & 3) * AT_STAGE + koff + u * 2048);
        LDV(v0, v1, L + (st & 3) * AT_STAGE + voffl + u * 2048);
#define SAFE_UNIT(Q0, Q1, M, O, LS) do { \
        f32x16 s_ = MFMA32(k0, Q0, zero16); s_ = MFMA32(k1, Q1, s_); \
        const float mn_ = fmaxf(M, max16(s_)); const float al_ = EXP2(M - mn_); M = mn_; \
        O *= al_; LS *= al_; u32x4 p0_, p1_; \
        _Pragma("unroll") for (int r = 0; r < 4; ++r) { p0_[r] = cvtpk(EXP2(s_[2 * r] - mn_), EXP2(s_[2 * r + 1] - mn_)); \
          p1_[r] = cvtpk(EXP2(s_[8 + 2 * r] - mn_), EXP2(s_[9 + 2 * r] - mn_)); } \
        O = MFMA32(v0, BF(p0_), O); LS = MFMA16(sel, BF(p0_), LS); O = MFMA32(v1, BF(p1_), O); LS = MFMA16(sel, BF(p1_), LS); } while (0)
        SAFE_UNIT(qa0, qa1, mA, oA, lA);
        SAFE_UNIT(qb0, qb1, mB, oB, lB);
#undef SAFE_UNIT
      }
    }
    __syncthreads();
  }
#undef LDK
#undef LDV
#undef DMA_ST
#undef BF
  {
    const float rA = 1.0f / lA[0], rB = 1.0f / lB[0];
    const float eA = mA + __builtin_amdgcn_logf(lA[0]), eB = mB + __builtin_amdgcn_logf(lB[0]);
    oA *= rA; oB *= rB;
    LAS float* xw = (LAS float*)((LAS char*)smem) + w * 1152;
    const LAS float* xr = (const LAS float*)((LAS char*)smem) + (w ^ 4) * 1152;
    f32x16 o; float e;
    if (kh) {
#pragma unroll
      for (int r = 0; r < 4; ++r) *(LAS f32x4*)(xw + r * 256 + lane * 4) = (f32x4){oA[4 * r], oA[4 * r + 1], oA[4 * r + 2], oA[4 * r + 3]};
      xw[1024 + lane] = eA; o = oB; e = eB;
    } else {
#pragma unroll
      for (int r = 0; r < 4; ++r) *(LAS f32x4*)(xw + r * 256 + lane * 4) = (f32x4){oB[4 * r], oB[4 * r + 1], oB[4 * r + 2], oB[4 * r + 3]};
      xw[1024 + lane] = eB; o = oA; e = eA;
    }
    __syncthreads();
    const float e2 = xr[1024 + lane];
    const float em = fmaxf(e, e2);
    float w1 = EXP2(e - em), w2 = EXP2(e2 - em);
    const float rs = 1.0f / (w1 + w2);
    w1 *= rs; w2 *= rs;
    u32x2 pk[4];
#pragma unroll
    for (int r = 0; r < 4; ++r) {
      const f32x4 t = *(const LAS f32x4*)(xr + r * 256 + lane * 4);
      pk[r].x = cvtpk(o[4 * r] * w1 + t[0] * w2, o[4 * r + 1] * w1 + t[1] * w2);
      pk[r].y = cvtpk(o[4 * r + 2] * w1 + t[2] * w2, o[4 * r + 3] * w1 + t[3] * w2);
    }
    const int qrow = qb * 256 + qg * 64 + kh * 32 + (lane & 31);
    bf16_t* op = AH + (size_t)qrow * KA + h * 32 + 8 * hi;
#pragma unroll
    for (int k = 0; k < 4; k += 2) {
      auto rx = __builtin_amdgcn_permlane32_swap(pk[k].x, pk[k + 1].x, false, false);
      auto ry = __builtin_amdgcn_permlane32_swap(pk[k].y, pk[k + 1].y, false, false);
      u32x4 v = {rx[0], ry[0], rx[1], ry[1]};
      *(u32x4*)(op + 8 * k) = v;
    }
  }
  STAMP(2, 3);
}

constexpr int G2_SLOT = 32768, G2_LDS = 4 * G2_SLOT;
constexpr int G2_NCH = 36;

__global__ __launch_bounds__(512, 2) void gemm2_kernel(const bf16_t* __restrict__ AH, const bf16_t* __restrict__ wo,
                                                       const bf16_t* __restrict__ wd, const bf16_t* __restrict__ x,
                                                       bf16_t* __restrict__ out, float* part, unsigned* flags) {
  extern __shared__ __attribute__((aligned(16))) char smem[];
  const int tid = threadIdx.x, lane = tid & 63;
  const int w = __builtin_amdgcn_readfirstlane(tid >> 6);
  const int g = w >> 2, w4 = w & 3, wm = w4 >> 1, wn = w4 & 1;
  const int bid = blockIdx.x, xcd = bid & 7, jj = bid >> 3;
  const int kh = xcd & 1, rt = (xcd >> 1) * 4 + (jj >> 3), ct = jj & 7;
  const int row0 = rt * 128, col0 = ct * 128;
  const int tile = rt * 8 + ct;
  STAMP(3, 0);
  const unsigned lds0 = (unsigned)(uintptr_t)smem;
  const lds_cptr L = (lds_cptr)smem;

  const unsigned avoff = (unsigned)((lane >> 3) * (KA * 2) + (((lane & 7) ^ (lane >> 4)) * 16));
  const char* abase = (const char*)AH + (size_t)(row0 + 32 * w4) * (KA * 2) + (size_t)kh * (G2_NCH * 128);
  const unsigned bvoff = (unsigned)((lane >> 4) * 2048 + (((lane & 15) ^ ((lane >> 4) << 1)) * 16));
#define DMA_CH(c) do { const int kg_ = kh * (G2_NCH * 64) + (c) * 64; \
    const char* bb_ = (kg_ < DM ? (const char*)wo + (size_t)kg_ * 2048 : (const char*)wd + (size_t)(kg_ - DM) * 2048) + col0 * 2 + w4 * 32768; \
    const unsigned d_ = lds0 + ((c) & 3) * G2_SLOT + w4 * 4096; \
    _Pragma("unroll") for (int _i = 0; _i < 4; ++_i) \
      glds16s(abase + (size_t)_i * (8 * KA * 2) + (c) * 128, (_i & 1) ? (avoff ^ 64u) : avoff, d_ + _i * 1024); \
    _Pragma("unroll") for (int _i = 0; _i < 4; ++_i) \
      glds16s(bb_ + _i * 8192, (_i >> 1) ? (bvoff ^ 128u) : bvoff, d_ + 16384 + _i * 1024); } while (0)

  const int fr = lane & 15, fq = lane >> 4, q4 = fr >> 2, p4 = lane & 3;
  const int aoff0 = (64 * wm + fr) * 128 + ((fq ^ (fr >> 1)) * 16);
  const int boff0 = 16384 + (8 * fq + q4) * 256 + 8 * p4;
  const int bsw = q4 | ((fq & 1) << 2);
  int bo[4];
#pragma unroll
  for (int n = 0; n < 4; ++n) bo[n] = boff0 + (((4 * wn + n) ^ bsw) * 32);

  f32x4 acc[4][4];
#pragma unroll
  for (int m = 0; m < 4; ++m)
#pragma unroll
    for (int n = 0; n < 4; ++n) acc[m][n] = (f32x4){0.f, 0.f, 0.f, 0.f};
  bf16x8 af[2][4], bfr[2][4];

  if (g == 0) { DMA_CH(1); WAITBAR(8); }
  else { DMA_CH(0); DMA_CH(2); WAITBAR(8); BARRIER(); }
  STAMP(3, 1);

#pragma unroll 1
  for (int c = g; c < G2_NCH; c += 2) {
    const lds_cptr sl = L + (c & 3) * G2_SLOT;
#pragma unroll
    for (int ks = 0; ks < 2; ++ks) {
#pragma unroll
      for (int m = 0; m < 4; ++m) af[ks][m] = lds_rd128(sl + (aoff0 ^ (ks * 64)) + m * 2048);
#pragma unroll
      for (int n = 0; n < 4; ++n) bfr[ks][n] = cat8(lds_tr(sl + bo[n] + ks * 8192), lds_tr(sl + bo[n] + ks * 8192 + 1024));
    }
    if (c + 3 < G2_NCH) { DMA_CH(c + 3); WAITBAR(8); }
    else WAITBAR(0);
    __builtin_amdgcn_s_setprio(1);
#pragma unroll
    for (int ks = 0; ks < 2; ++ks)
#pragma unroll
      for (int n = 0; n < 4; ++n)
#pragma unroll
        for (int m = 0; m < 4; ++m) acc[m][n] = MFMA16(bfr[ks][n], af[ks][m], acc[m][n]);
    __builtin_amdgcn_s_setprio(0);
    BARRIER();
  }
  if (g == 0) BARRIER();
  STAMP(3, 2);
#undef DMA_CH

  {
    LAS f32x4* xb = (LAS f32x4*)((LAS char*)smem) + (size_t)w * 512;
    f32x4 s[2][4];
    if (g) {
#pragma unroll
      for (int mm = 0; mm < 2; ++mm)
#pragma unroll
        for (int n = 0; n < 4; ++n) { xb[(mm * 4 + n) * 64 + lane] = acc[mm][n]; s[mm][n] = acc[2 + mm][n]; }
    } else {
#pragma unroll
      for (int mm = 0; mm < 2; ++mm)
#pragma unroll
        for (int n = 0; n < 4; ++n) { xb[(mm * 4 + n) * 64 + lane] = acc[2 + mm][n]; s[mm][n] = acc[mm][n]; }
    }
    __syncthreads();
    LAS f32x4* xr = (LAS f32x4*)((LAS char*)smem) + (size_t)(w ^ 4) * 512;
#pragma unroll
    for (int mm = 0; mm < 2; ++mm)
#pragma unroll
      for (int n = 0; n < 4; ++n) s[mm][n] += xr[(mm * 4 + n) * 64 + lane];
    f32x4 keep[4], send[4];
    if (kh) {
#pragma unroll
      for (int n = 0; n < 4; ++n) { keep[n] = s[1][n]; send[n] = s[0][n]; }
    } else {
#pragma unroll
      for (int n = 0; n < 4; ++n) { keep[n] = s[0][n]; send[n] = s[1][n]; }
    }
    const int pbid = bid ^ 1;
    float* mine = part + (size_t)bid * 8192 + w * 1024;
#pragma unroll
    for (int n = 0; n < 4; ++n) {
      const f32x4 v = send[n];
      asm volatile("global_store_dwordx4 %0, %1, off sc1\n\ts_nop 1" :: "v"(mine + n * 256 + lane * 4), "v"(v) : "memory");
    }
    asm volatile("s_waitcnt vmcnt(0)" ::: "memory");
    __syncthreads();
    if (tid == 0) {
      __hip_atomic_store(flags + bid * 64, 1u, __ATOMIC_RELAXED, __HIP_MEMORY_SCOPE_AGENT);
      unsigned spins = 0;
      while (__hip_atomic_load(flags + pbid * 64, __ATOMIC_RELAXED, __HIP_MEMORY_SCOPE_AGENT) == 0u) {
        __builtin_amdgcn_s_sleep(1);
        if (++spins > (1u << 22)) break;
      }
      __builtin_amdgcn_fence(__ATOMIC_ACQUIRE, "agent");
      asm volatile("s_waitcnt vmcnt(0)" ::: "memory");
    }
    __syncthreads();
    STAMP(3, 4);
    const float* theirs = part + (size_t)pbid * 8192 + w * 1024;
    const int row = row0 + 64 * wm + 16 * (2 * g + kh) + fr;
#pragma unroll
    for (int n = 0; n < 4; ++n) {
      const f32x4 pv = *(const f32x4*)(theirs + n * 256 + lane * 4);
      const f32x4 v = keep[n] + pv;
      const int col = col0 + 64 * wn + 16 * n + 4 * fq;
      const u32x2 xv = *(const u32x2*)(x + (size_t)row * DM + col);
      u32x2 o = {cvtpk(bf_lo(xv.x) + v[0], bf_hi(xv.x) + v[1]), cvtpk(bf_lo(xv.y) + v[2], bf_hi(xv.y) + v[3])};
      *(u32x2*)(out + (size_t)row * DM + col) = o;
    }
  }
  STAMP(3, 3);
  (void)tile;
}

extern "C" void kernel_launch(void* const* d_in, const int* in_sizes, int n_in, void* d_out, int out_size, void* d_ws,
                              size_t ws_size, hipStream_t stream) {
  const bf16_t* x = (const bf16_t*)d_in[0];
  const bf16_t* wqkv = (const bf16_t*)d_in[1];
  const bf16_t* wo = (const bf16_t*)d_in[2];
  const bf16_t* wg = (const bf16_t*)d_in[4];
  const bf16_t* wu = (const bf16_t*)d_in[5];
  const bf16_t* wd = (const bf16_t*)d_in[6];
  char* ws = (char*)d_ws;
  bf16_t* Qb = (bf16_t*)(ws + WS_Q);
  bf16_t* Kb = (bf16_t*)(ws + WS_K);
  bf16_t* Vb = (bf16_t*)(ws + WS_V);
  bf16_t* AH = (bf16_t*)(ws + WS_AH);
  float* part = (float*)(ws + WS_PART);
  unsigned* flags = (unsigned*)(ws + WS_FLAGS);
  (void)hipFuncSetAttribute((const void*)gemm1_kernel, hipFuncAttributeMaxDynamicSharedMemorySize, G1_LDS);
  (void)hipFuncSetAttribute((const void*)attn_kernel, hipFuncAttributeMaxDynamicSharedMemorySize, AT_LDS);
  (void)hipFuncSetAttribute((const void*)gemm2_kernel, hipFuncAttributeMaxDynamicSharedMemorySize, G2_LDS);
  gemm1_kernel<<<256, 512, G1_LDS, stream>>>(x, wqkv, wg, wu, Qb, Kb, Vb, AH, flags);
  attn_kernel<<<256, 512, AT_LDS, stream>>>(Qb, Kb, Vb, AH);
  gemm2_kernel<<<256, 512, G2_LDS, stream>>>(AH, wo, wd, x, (bf16_t*)d_out, part, flags);
}
```

```cpp
#include <hip/hip_runtime.h>
#include <hip/hip_bf16.h>
#include <stdint.h>

typedef unsigned short bf16_t;
typedef short bf16x8 __attribute__((ext_vector_type(8)));
typedef short s16x4 __attribute__((ext_vector_type(4)));
typedef float f32x4 __attribute__((ext_vector_type(4)));
typedef float f32x16 __attribute__((ext_vector_type(16)));
typedef float f32x2 __attribute__((ext_vector_type(2)));
typedef __bf16 bf16x2v __attribute__((ext_vector_type(2)));
typedef unsigned u32x4 __attribute__((ext_vector_type(4)));
typedef unsigned u32x2 __attribute__((ext_vector_type(2)));
#define LAS __attribute__((address_space(3)))
typedef LAS const char* lds_cptr;

constexpr int SEQ = 2048, DM = 1024, NH = 32, FF = 3584, NQKV = 3072, KA = 4608;

constexpr size_t WS_FLAGS = 0;
constexpr size_t WS_XN = 1u << 20;
constexpr size_t WS_Q = WS_XN + (size_t)SEQ * DM * 2;
constexpr size_t WS_K = WS_Q + (size_t)SEQ * DM * 2;
constexpr size_t WS_V = WS_K + (size_t)SEQ * DM * 2;
constexpr size_t WS_AH = WS_V + (size_t)SEQ * DM * 2;
constexpr size_t WS_PART = WS_AH + (size_t)SEQ * KA * 2;

#define SB() __builtin_amdgcn_sched_barrier(0)
#define STAMP(kid, i) do {} while (0)

__device__ __forceinline__ unsigned cvtpk(float lo, float hi) {
  f32x2 v = {lo, hi};
  bf16x2v b = __builtin_convertvector(v, bf16x2v);
  return __builtin_bit_cast(unsigned, b);
}
__device__ __forceinline__ float bf_lo(unsigned u) { return __uint_as_float(u << 16); }
__device__ __forceinline__ float bf_hi(unsigned u) { return __uint_as_float(u & 0xffff0000u); }
__device__ __forceinline__ float rbf(float x) { return bf_lo(cvtpk(x, x)); }

__device__ __forceinline__ void glds16(const void* g, unsigned lds_dst) {
  unsigned keep;
  asm volatile("s_mov_b32 %0, m0\n\ts_mov_b32 m0, %2\n\ts_nop 0\n\tglobal_load_lds_dwordx4 %1, off\n\ts_mov_b32 m0, %0"
               : "=&s"(keep) : "v"(g), "s"(lds_dst) : "memory");
}
__device__ __forceinline__ void glds16s(const void* sbase, unsigned voff, unsigned lds_dst) {
  unsigned keep;
  asm volatile("s_mov_b32 %0, m0\n\ts_mov_b32 m0, %3\n\ts_nop 0\n\tglobal_load_lds_dwordx4 %1, %2\n\ts_mov_b32 m0, %0"
               : "=&s"(keep) : "v"(voff), "s"(sbase), "s"(lds_dst) : "memory");
}
__device__ __forceinline__ bf16x8 lds_rd128(lds_cptr p) { return *(const LAS bf16x8*)p; }
__device__ __forceinline__ s16x4 lds_tr(lds_cptr p) {
  return __builtin_amdgcn_ds_read_tr16_b64_v4i16((LAS s16x4*)p);
}
__device__ __forceinline__ bf16x8 cat8(s16x4 a, s16x4 b) { return __builtin_shufflevector(a, b, 0, 1, 2, 3, 4, 5, 6, 7); }

__global__ __launch_bounds__(256) void ln_kernel(const bf16_t* __restrict__ x, bf16_t* __restrict__ xn, unsigned* flags) {
  const int lane = threadIdx.x & 63, w = threadIdx.x >> 6;
  const int row = blockIdx.x * 4 + w;
  const u32x4* xr = (const u32x4*)(x + (size_t)row * DM);
  u32x4 a = xr[lane], b = xr[64 + lane];
  if (blockIdx.x < 64) flags[blockIdx.x * 256 + threadIdx.x] = 0u;
  float v[16];
#pragma unroll
  for (int i = 0; i < 4; ++i) { v[2 * i] = bf_lo(a[i]); v[2 * i + 1] = bf_hi(a[i]); v[8 + 2 * i] = bf_lo(b[i]); v[9 + 2 * i] = bf_hi(b[i]); }
  float s = 0.f;
#pragma unroll
  for (int i = 0; i < 16; ++i) s += v[i];
#pragma unroll
  for (int o = 1; o < 64; o <<= 1) s += __shfl_xor(s, o);
  const float mean = s * (1.f / DM);
  float q = 0.f;
#pragma unroll
  for (int i = 0; i < 16; ++i) { const float d = v[i] - mean; q += d * d; }
#pragma unroll
  for (int o = 1; o < 64; o <<= 1) q += __shfl_xor(q, o);
  const float mb = rbf(mean), vb = rbf(q * (1.f / DM));
  const float den = rbf(sqrtf(rbf(vb + 1e-5f)));
  u32x4 oa, ob;
#pragma unroll
  for (int i = 0; i < 4; ++i) {
    oa[i] = cvtpk(rbf(v[2 * i] - mb) / den, rbf(v[2 * i + 1] - mb) / den);
    ob[i] = cvtpk(rbf(v[8 + 2 * i] - mb) / den, rbf(v[9 + 2 * i] - mb) / den);
  }
  u32x4* o = (u32x4*)(xn + (size_t)row * DM);
  o[lane] = oa; o[64 + lane] = ob;
}

constexpr int G1_AOFF = 0, G1_BOFF = 65536, G1_BREG = 10304, G1_BSLOT = 2 * G1_BREG, G1_LDS = 65536 + 4 * G1_BSLOT;
#define MFMA16(a, b, c) __builtin_amdgcn_mfma_f32_16x16x32_bf16(a, b, c, 0, 0, 0)
#define MFMA32(a, b, c) __builtin_amdgcn_mfma_f32_32x32x16_bf16(a, b, c, 0, 0, 0)
#define WAITBAR(N) do { SB(); asm volatile("s_waitcnt vmcnt(" #N ") lgkmcnt(0)\n\ts_barrier" ::: "memory"); SB(); } while (0)
#define BARRIER() do { SB(); asm volatile("s_barrier" ::: "memory"); SB(); } while (0)
#define MFMA16A(acc, a, b) asm volatile("v_mfma_f32_16x16x32_bf16 %0, %1, %2, %0" : "+v"(acc) : "v"(a), "v"(b))
#define ADV64(p, inc) asm volatile("v_mad_u64_u32 %0, vcc, %1, 1, %0" : "+v"(p) : "v"(inc) : "vcc")

__global__ __launch_bounds__(512, 2) void gemm1_kernel(const bf16_t* __restrict__ x, const bf16_t* __restrict__ wqkv,
                                                       const bf16_t* __restrict__ wg, const bf16_t* __restrict__ wu,
                                                       bf16_t* __restrict__ Qb, bf16_t* __restrict__ Kb, bf16_t* __restrict__ Vb,
                                                       bf16_t* __restrict__ AH) {
  extern __shared__ __attribute__((aligned(16))) char smem[];
  const int tid = threadIdx.x, lane = tid & 63;
  const int w = __builtin_amdgcn_readfirstlane(tid >> 6);
  const int g = w >> 2, w4 = w & 3, wm = 2 * g + (w4 >> 1), wn = w4 & 1;
  STAMP(1, 0);
  const int bid = blockIdx.x, xcd = bid & 7, jj = bid >> 3;
  const int cg = xcd * 4 + (jj & 3), rt = jj >> 2;
  const int row0 = rt * 256;
  const unsigned lds0 = (unsigned)(uintptr_t)smem;
  const lds_cptr L = (lds_cptr)smem;

  unsigned avoff0 = (unsigned)((lane >> 3) * 2048 + (((lane & 7) ^ ((lane >> 4) + 2)) * 16)), avoff1 = avoff0 ^ 64u;
  const char* abase = (const char*)x + (size_t)(row0 + 32 * w4) * 2048;
#define DMA_A(hh, t) do { _Pragma("unroll") for (int _i = 0; _i < 4; ++_i) \
    glds16s(abase + (size_t)(hh) * 262144 + _i * 16384 + (t) * 128, (_i & 1) ? avoff1 : avoff0, \
            lds0 + G1_AOFF + ((t) & 1) * 32768 + (hh) * 16384 + (4 * w4 + _i) * 1024); } while (0)
  DMA_A(g, 0);

  const char* bptr[4]; unsigned bld[4]; unsigned bdst[4];
#pragma unroll
  for (int i = 0; i < 4; ++i) {
    const int b = (i == 0) ? (g ? 16 + w4 : 4 * w4) : 4 * w4 + i;
    const int region = b >= 10 ? 1 : 0, bi = b - 10 * region;
    const int P = bi * 64 + lane;
    const int k = P / 20, c8 = (P - 20 * k) * 8;
    const bool isq = (cg <= 8) || (cg == 9 && c8 < 96);
    const bf16_t* mat = isq ? wqkv : (region ? wu : wg);
    const int ld = isq ? NQKV : FF;
    const int col = c8 + (cg <= 8 ? 320 * cg + 160 * region : (cg == 9 ? (isq ? 2880 + 96 * region : -96) : 64 + 160 * (cg - 10)));
    bptr[i] = (const char*)(mat + (size_t)k * ld + col);
    bld[i] = (unsigned)ld * 64u;
    bdst[i] = lds0 + G1_BOFF + region * G1_BREG + bi * 1024 + (bi >= 5 ? 32 : 0);
  }
#define DMA_B(i, s) glds16(bptr[i], bdst[i] + ((s) & 3) * G1_BSLOT)
#define ADV_B() do { ADV64(bptr[0], bld[0]); ADV64(bptr[1], bld[1]); ADV64(bptr[2], bld[2]); ADV64(bptr[3], bld[3]); } while (0)

  if (g == 0) {
    DMA_B(0, 0); DMA_B(1, 0); DMA_B(2, 0); DMA_B(3, 0); ADV_B();
    DMA_B(0, 1); ADV_B();
    DMA_B(0, 2); DMA_B(1, 2); DMA_B(2, 2); DMA_B(3, 2); ADV_B();
  } else {
    DMA_B(0, 0); ADV_B();
    DMA_B(0, 1); DMA_B(1, 1); DMA_B(2, 1); DMA_B(3, 1); ADV_B();
    DMA_B(0, 2); ADV_B();
    DMA_A(0, 1);
  }

  const int fr = lane & 15, fq = lane >> 4, kq = ((fq & 1) << 1) | (fq >> 1);
  const int aoff0 = G1_AOFF + (64 * wm + fr) * 128 + ((kq ^ (((fr >> 1) + 2) & 7)) * 16);
  lds_cptr pa0 = L + aoff0, pa1 = L + (aoff0 ^ 64);
  lds_cptr pb0 = L + (G1_BOFF + (8 * kq + (fr >> 2)) * 320 + (fq & 1) * 32 + 8 * (lane & 3) + 160 * wn);
  lds_cptr pb1 = pb0 + 2 * G1_BSLOT;
  asm volatile("" : "+v"(pa0), "+v"(pa1), "+v"(pb0), "+v"(pb1), "+v"(avoff0), "+v"(avoff1));

  f32x4 acc[4][5][2];
#pragma unroll
  for (int m = 0; m < 4; ++m)
#pragma unroll
    for (int j = 0; j < 5; ++j) { acc[m][j][0] = (f32x4){0.f, 0.f, 0.f, 0.f}; acc[m][j][1] = (f32x4){0.f, 0.f, 0.f, 0.f};
      asm volatile("" : "+v"(acc[m][j][0]), "+v"(acc[m][j][1])); }
  bf16x8 af[4], bfr[5][2];

  if (g == 0) { WAITBAR(5); } else { WAITBAR(9); BARRIER(); }
  STAMP(1, 1);

#define G1_PRIO 1
#define STEP(U, ISSUE, WN, LASTBAR) do { \
    { const lds_cptr pa = ((U) & 1) ? pa1 : pa0; const lds_cptr pb = (((U) & 2) ? pb1 : pb0) + ((U) & 1) * G1_BSLOT; \
      _Pragma("unroll") for (int m = 0; m < 4; ++m) af[m] = lds_rd128(pa + ((U) >> 1) * 32768 + m * 2048); \
      _Pragma("unroll") for (int j = 0; j < 5; ++j) \
        _Pragma("unroll") for (int r = 0; r < 2; ++r) bfr[j][r] = cat8(lds_tr(pb + r * G1_BREG + j * 32), lds_tr(pb + r * G1_BREG + j * 32 + 1280)); } \
    if (ISSUE) { DMA_B(0, (U) + 3); \
      if (g == ((U) & 1)) { DMA_A(1 - ((U) & 1), 2 * it + ((U) >> 1) + 1 + ((U) & 1)); } \
      else { DMA_B(1, (U) + 3); DMA_B(2, (U) + 3); DMA_B(3, (U) + 3); } } \
    WAITBAR(WN); \
    if (G1_PRIO) __builtin_amdgcn_s_setprio(1); \
    _Pragma("unroll") for (int j = 0; j < 5; ++j) { _Pragma("unroll") for (int r = 0; r < 2; ++r) _Pragma("unroll") for (int m = 0; m < 4; ++m) \
        MFMA16A(acc[m][j][r], bfr[j][r], af[m]); \
      if ((ISSUE) && j < 4) ADV64(bptr[j], bld[j]); } \
    if (G1_PRIO) __builtin_amdgcn_s_setprio(0); \
    if (LASTBAR) BARRIER(); } while (0)
  int it = 0;
#pragma unroll 1
  for (; it < 7; ++it) { STEP(0, true, 9, true); STEP(1, true, 9, true); STEP(2, true, 9, true); STEP(3, true, 9, true); }
  STEP(0, true, 9, true); STEP(1, false, 4, true); STEP(2, false, 0, true); STEP(3, false, 0, g == 0);
#undef STEP
  asm volatile("s_nop 7\n\ts_nop 7" ::: "memory");
  SB();
  STAMP(1, 2);
#undef DMA_A
#undef DMA_B
#undef ADV_B

  int tz = threadIdx.x; asm volatile("" : "+v"(tz));
  const int fr2 = tz & 15, fq2 = (tz >> 4) & 3, sel1 = fq2 & 1, c8 = (fq2 >> 1) * 8;
  const float qscale = 0.17677669529663687f * 1.4426950408889634f;
#define SWAP16(A, B, OUT) do { auto r0_ = __builtin_amdgcn_permlane16_swap(A.x, B.x, false, false); \
    auto r1_ = __builtin_amdgcn_permlane16_swap(A.y, B.y, false, false); OUT = (u32x4){r0_[0], r1_[0], r0_[1], r1_[1]}; } while (0)
#define SILU4(m, j, OUT) do { float hv_[4]; _Pragma("unroll") for (int r = 0; r < 4; ++r) { \
      const float tg_ = acc[m][j][0][r], tu_ = acc[m][j][1][r]; \
      hv_[r] = (tg_ * tu_) * __builtin_amdgcn_rcpf(1.f + __builtin_amdgcn_exp2f(-1.4426950408889634f * tg_)); } \
    OUT = (u32x2){cvtpk(hv_[0], hv_[1]), cvtpk(hv_[2], hv_[3])}; } while (0)
#define PK4(V, SC, OUT) do { const f32x4 v_ = (V) * (SC); OUT = (u32x2){cvtpk(v_[0], v_[1]), cvtpk(v_[2], v_[3])}; } while (0)
  const int rowb = row0 + 64 * wm + fr2;
#define QKV_DST(col, row, dst, mstep) do { const int typ_ = (col) >> 10, h_ = ((col) & 1023) >> 5, d_ = (col) & 31; \
    if (typ_ == 1) { dst = Kb + ((((size_t)h_ * 64 + ((row) >> 5)) * 2 + (d_ >> 4)) * 64 + ((row) & 31) + 32 * ((d_ >> 3) & 1)) * 8; mstep = 128; } \
    else { dst = (typ_ == 0 ? Qb : Vb) + ((size_t)h_ * SEQ + (row)) * 32 + d_; mstep = 512; } } while (0)
  if (cg >= 10) {
    bf16_t* dst = AH + (size_t)rowb * KA + DM + 64 + 160 * (cg - 10) + 80 * wn + c8;
#pragma unroll
    for (int m = 0; m < 4; ++m) {
#pragma unroll
      for (int jp = 0; jp < 2; ++jp) {
        u32x2 a_, b_; u32x4 o; SILU4(m, 2 * jp, a_); SILU4(m, 2 * jp + 1, b_); SWAP16(a_, b_, o);
        *(u32x4*)(dst + (size_t)m * 16 * KA + 32 * jp + 16 * sel1) = o;
      }
      if (m & 1) {
        u32x2 a_, b_; u32x4 o; SILU4(m - 1, 4, a_); SILU4(m, 4, b_); SWAP16(a_, b_, o);
        *(u32x4*)(dst + (size_t)(m - 1 + sel1) * 16 * KA + 64) = o;
      }
    }
  } else if (cg <= 8) {
#pragma unroll
    for (int r2 = 0; r2 < 2; ++r2) {
      const int colw = 320 * cg + 160 * r2 + 80 * wn;
#pragma unroll
      for (int jp = 0; jp < 2; ++jp) {
        const int ca = colw + 32 * jp;
        const float sa = ca < 1024 ? qscale : 1.f, sb = (ca + 16) < 1024 ? qscale : 1.f;
        bf16_t* dst; int mstep; const int col = ca + 16 * sel1 + c8;
        QKV_DST(col, rowb, dst, mstep);
#pragma unroll
        for (int m = 0; m < 4; ++m) {
          u32x2 a_, b_; u32x4 o; PK4(acc[m][2 * jp][r2], sa, a_); PK4(acc[m][2 * jp + 1][r2], sb, b_); SWAP16(a_, b_, o);
          *(u32x4*)(dst + (m & 1) * mstep + (m >> 1) * 1024) = o;
        }
      }
      {
        const int ca = colw + 64;
        const float sa = ca < 1024 ? qscale : 1.f;
        bf16_t* dst; int mstep; const int col = ca + c8;
        QKV_DST(col, rowb, dst, mstep);
#pragma unroll
        for (int mp = 0; mp < 2; ++mp) {
          u32x2 a_, b_; u32x4 o; PK4(acc[2 * mp][4][r2], sa, a_); PK4(acc[2 * mp + 1][4][r2], sa, b_); SWAP16(a_, b_, o);
          *(u32x4*)(dst + sel1 * mstep + mp * 1024) = o;
        }
      }
    }
  } else {
#pragma unroll
    for (int j = 0; j < 5; ++j) {
      const int i = 5 * wn + j;
      if (i >= 6) {
        bf16_t* dst = AH + (size_t)(rowb + 16 * sel1) * KA + DM + 16 * i - 96 + c8;
#pragma unroll
        for (int mp = 0; mp < 2; ++mp) {
          u32x2 a_, b_; u32x4 o; SILU4(2 * mp, j, a_); SILU4(2 * mp + 1, j, b_); SWAP16(a_, b_, o);
          *(u32x4*)(dst + (size_t)mp * 32 * KA) = o;
        }
      } else {
#pragma unroll
        for (int r2 = 0; r2 < 2; ++r2) {
          const int col = 2880 + 96 * r2 + 16 * i + c8;
          bf16_t* dst = Vb + ((size_t)((col & 1023) >> 5) * SEQ + rowb + 16 * sel1) * 32 + (col & 31);
#pragma unroll
          for (int mp = 0; mp < 2; ++mp) {
            u32x2 a_, b_; u32x4 o; PK4(acc[2 * mp][j][r2], 1.f, a_); PK4(acc[2 * mp + 1][j][r2], 1.f, b_); SWAP16(a_, b_, o);
            *(u32x4*)(dst + mp * 1024) = o;
          }
        }
      }
    }
  }
#undef SWAP16
#undef SILU4
#undef PK4
#undef QKV_DST
  STAMP(1, 3);
}

constexpr int AT_STAGE = 32768, AT_FLAG = AT_STAGE * 4, AT_LDS = AT_FLAG + 64;
#define PIN(x) asm volatile("" : "+v"(x))
#define EXP2(x) __builtin_amdgcn_exp2f(x)

__device__ __forceinline__ float max16(const f32x16& p) {
  float a = fmaxf(fmaxf(p[0], p[1]), p[2]), b = fmaxf(fmaxf(p[3], p[4]), p[5]);
  a = fmaxf(fmaxf(a, p[6]), p[7]); b = fmaxf(fmaxf(b, p[8]), p[9]);
  a = fmaxf(fmaxf(a, p[10]), p[11]); b = fmaxf(fmaxf(b, p[12]), p[13]);
  a = fmaxf(fmaxf(a, p[14]), p[15]);
  float m = fmaxf(a, b);
  auto rr = __builtin_amdgcn_permlane32_swap(__float_as_uint(m), __float_as_uint(m), false, false);
  return fmaxf(__uint_as_float(rr[0]), __uint_as_float(rr[1]));
}
__device__ __forceinline__ f32x16 splat16(float v) { f32x16 r;
#pragma unroll
  for (int i = 0; i < 16; ++i) r[i] = v;
  return r; }

__global__ __launch_bounds__(512, 2) void attn_kernel(const bf16_t* __restrict__ Qb, const bf16_t* __restrict__ Kb,
                                                      const bf16_t* __restrict__ Vb, bf16_t* __restrict__ AH) {
  extern __shared__ __attribute__((aligned(16))) char smem[];
  const int tid = threadIdx.x, lane = tid & 63;
  const int w = __builtin_amdgcn_readfirstlane(tid >> 6);
  const int qg = w & 3, kh = w >> 2;
  const int bid = blockIdx.x, xcd = bid & 7, jj = bid >> 3;
  const int h = xcd * 4 + (jj >> 3), qb = jj & 7;
  STAMP(2, 0);
  const unsigned lds0 = (unsigned)(uintptr_t)smem;
  const lds_cptr L = (lds_cptr)smem;
  const int hi = lane >> 5;
  const char* kg = (const char*)(Kb + (size_t)h * SEQ * 32) + kh * 65536 + qg * 2048;
  const char* vg = (const char*)(Vb + (size_t)h * SEQ * 32) + kh * 65536 + qg * 2048;
  const unsigned voff = lane * 16;
#define DMA_ST(st) do { const unsigned d_ = lds0 + ((st) & 3) * AT_STAGE + kh * 8192 + qg * 2048; \
    glds16s(kg + (st) * 8192, voff, d_); glds16s(kg + (st) * 8192 + 1024, voff, d_ + 1024); \
    glds16s(vg + (st) * 8192, voff, d_ + 16384); glds16s(vg + (st) * 8192 + 1024, voff, d_ + 16384 + 1024); } while (0)
  const bf16_t* qp = Qb + ((size_t)h * SEQ + qb * 256 + qg * 64 + (lane & 31)) * 32 + 8 * hi;
  bf16x8 qa0 = *(const bf16x8*)qp, qa1 = *(const bf16x8*)(qp + 16);
  bf16x8 qb0 = *(const bf16x8*)(qp + 1024), qb1 = *(const bf16x8*)(qp + 1024 + 16);
  DMA_ST(0); DMA_ST(1); DMA_ST(2);
  PIN(qa0); PIN(qa1); PIN(qb0); PIN(qb1);
  const short one = (((lane >> 4) & 1) == ((lane >> 2) & 1)) ? (short)0x3f80 : (short)0;
  const bf16x8 sel = {one, one, one, one, one, one, one, one};
  volatile LAS unsigned* flag = (volatile LAS unsigned*)((LAS char*)smem + AT_FLAG);
  if (tid == 0) *flag = 0u;
  const int gq = lane >> 4;
  const int koff = kh * 8192 + lane * 16;
  const int voffl = 16384 + kh * 8192 + (4 * (gq >> 1) + ((lane & 15) >> 2)) * 64 + (gq & 1) * 32 + (lane & 3) * 8;
  const f32x16 zero16 = splat16(0.f);
  f32x16 oA = zero16, oB = zero16;
  f32x4 lA = {0.f, 0.f, 0.f, 0.f}, lB = {0.f, 0.f, 0.f, 0.f};
  float mA, mB;

#define LDK(K0, K1, p) do { K0 = lds_rd128(p); K1 = lds_rd128((p) + 1024); } while (0)
#define LDV(V0, V1, p) do { V0 = cat8(lds_tr(p), lds_tr((p) + 512)); V1 = cat8(lds_tr((p) + 1024), lds_tr((p) + 1536)); } while (0)
  {
    WAITBAR(8);
    bf16x8 kx0, kx1, ky0, ky1, vx0, vx1, vy0, vy1;
    LDK(kx0, kx1, L + koff);
    LDV(vx0, vx1, L + voffl);
    f32x16 sA = MFMA32(kx0, qa0, zero16); sA = MFMA32(kx1, qa1, sA);
    f32x16 sB = MFMA32(kx0, qb0, zero16); sB = MFMA32(kx1, qb1, sB);
    mA = max16(sA); mB = max16(sB);
    f32x16 nA = splat16(-mA), nB = splat16(-mB);
    PIN(nA); PIN(nB);
    sA = MFMA32(kx0, qa0, nA); sA = MFMA32(kx1, qa1, sA);
    u32x4 pA0, pA1, pB0 = {0u, 0u, 0u, 0u}, pB1 = {0u, 0u, 0u, 0u};
    vy0 = vx0; vy1 = vx1;
#define BF(x) __builtin_bit_cast(bf16x8, x)
#define XLOADS(x) x
    STAMP(2, 1);
#define E4(S, i) do { S[i] = EXP2(S[i]); S[i + 1] = EXP2(S[i + 1]); S[i + 2] = EXP2(S[i + 2]); S[i + 3] = EXP2(S[i + 3]); } while (0)
#define SLOT(SC, PC0, PC1, SN, K0, K1, Q0, Q1, NM, PP0, PP1, V0, V1, O, LS, LOADS) do { \
    SN = MFMA32(K0, Q0, NM); E4(SC, 0); PIN(SC); SB(); \
    SN = MFMA32(K1, Q1, SN); E4(SC, 4); PIN(SC); SB(); \
    XLOADS(LOADS); SB(); \
    O = MFMA32(V0, BF(PP0), O); PC0[0] = cvtpk(SC[0], SC[1]); PC0[1] = cvtpk(SC[2], SC[3]); \
      SC[8] = EXP2(SC[8]); SC[9] = EXP2(SC[9]); SC[10] = EXP2(SC[10]); PIN(SC); PIN(PC0); SB(); \
    LS = MFMA16(sel, BF(PP0), LS); PC0[2] = cvtpk(SC[4], SC[5]); PC0[3] = cvtpk(SC[6], SC[7]); SC[11] = EXP2(SC[11]); PIN(SC); PIN(PC0); SB(); \
    O = MFMA32(V1, BF(PP1), O); E4(SC, 12); PIN(SC); SB(); \
    LS = MFMA16(sel, BF(PP1), LS); PC1[0] = cvtpk(SC[8], SC[9]); PC1[1] = cvtpk(SC[10], SC[11]); \
      PC1[2] = cvtpk(SC[12], SC[13]); PC1[3] = cvtpk(SC[14], SC[15]); PIN(PC1); SB(); } while (0)
#define SUB(KC0, KC1, KN0, KN1, VC0, VC1, VP0, VP1, kpn, vpn) do { \
    SLOT(sA, pA0, pA1, sB, KC0, KC1, qb0, qb1, nB, pB0, pB1, VP0, VP1, oB, lB, LDK(KN0, KN1, kpn)); \
    SLOT(sB, pB0, pB1, sA, KN0, KN1, qa0, qa1, nA, pA0, pA1, VC0, VC1, oA, lA, LDV(VP0, VP1, vpn)); } while (0)
#pragma unroll 1
    for (int st = 0; st < 8; ++st) {
      const lds_cptr kp = L + (st & 3) * AT_STAGE + koff, vp = L + (st & 3) * AT_STAGE + voffl;
      const lds_cptr kn = L + ((st + 1) & 3) * AT_STAGE + koff, vn = L + ((st + 1) & 3) * AT_STAGE + voffl;
      SUB(kx0, kx1, ky0, ky1, vx0, vx1, vy0, vy1, kp + 2048, vp + 2048);
      SUB(ky0, ky1, kx0, kx1, vy0, vy1, vx0, vx1, kp + 4096, vp + 4096);
      SUB(kx0, kx1, ky0, ky1, vx0, vx1, vy0, vy1, kp + 6144, vp + 6144);
      if (st < 6) { SB(); asm volatile("s_waitcnt vmcnt(4)\n\ts_barrier" ::: "memory"); SB(); }
      else { SB(); asm volatile("s_waitcnt vmcnt(0)\n\ts_barrier" ::: "memory"); SB(); }
      if (st < 5) DMA_ST(st + 3);
      SUB(ky0, ky1, kx0, kx1, vy0, vy1, vx0, vx1, kn, vn);
    }
    oB = MFMA32(vy0, BF(pB0), oB); lB = MFMA16(sel, BF(pB0), lB);
    oB = MFMA32(vy1, BF(pB1), oB); lB = MFMA16(sel, BF(pB1), lB);
#undef SUB
#undef SLOT
#undef E4
  }
  STAMP(2, 2);
  bool bad = !(lA[0] < 1.0e36f) || !(lB[0] < 1.0e36f);
#pragma unroll
  for (int r = 0; r < 16; ++r) bad = bad || !(fabsf(oA[r]) < 3.0e38f) || !(fabsf(oB[r]) < 3.0e38f);
  if (__any(bad) && lane == 0) *flag = 1u;
  __syncthreads();
  if (*flag != 0u) {
    __syncthreads();
    DMA_ST(0); DMA_ST(1); DMA_ST(2);
    oA = zero16; oB = zero16; lA = (f32x4){0.f, 0.f, 0.f, 0.f}; lB = lA;
    mA = -3.0e38f; mB = -3.0e38f;
#pragma unroll 1
    for (int st = 0; st < 8; ++st) {
      if (st < 6) WAITBAR(8); else if (st == 6) WAITBAR(4); else WAITBAR(0);
      if (st < 5) DMA_ST(st + 3);
#pragma unroll 1
      for (int u = 0; u < 4; ++u) {
        bf16x8 k0, k1, v0, v1;
        LDK(k0, k1, L + (st & 3) * AT_STAGE + koff + u * 2048);
        LDV(v0, v1, L + (st & 3) * AT_STAGE + voffl + u * 2048);
#define SAFE_UNIT(Q0, Q1, M, O, LS) do { \
        f32x16 s_ = MFMA32(k0, Q0, zero16); s_ = MFMA32(k1, Q1, s_); \
        const float mn_ = fmaxf(M, max16(s_)); const float al_ = EXP2(M - mn_); M = mn_; \
        O *= al_; LS *= al_; u32x4 p0_, p1_; \
        _Pragma("unroll") for (int r = 0; r < 4; ++r) { p0_[r] = cvtpk(EXP2(s_[2 * r] - mn_), EXP2(s_[2 * r + 1] - mn_)); \
          p1_[r] = cvtpk(EXP2(s_[8 + 2 * r] - mn_), EXP2(s_[9 + 2 * r] - mn_)); } \
        O = MFMA32(v0, BF(p0_), O); LS = MFMA16(sel, BF(p0_), LS); O = MFMA32(v1, BF(p1_), O); LS = MFMA16(sel, BF(p1_), LS); } while (0)
        SAFE_UNIT(qa0, qa1, mA, oA, lA);
        SAFE_UNIT(qb0, qb1, mB, oB, lB);
#undef SAFE_UNIT
      }
    }
    __syncthreads();
  }
#undef LDK
#undef LDV
#undef DMA_ST
#undef BF
  {
    const float rA = 1.0f / lA[0], rB = 1.0f / lB[0];
    const float eA = mA + __builtin_amdgcn_logf(lA[0]), eB = mB + __builtin_amdgcn_logf(lB[0]);
    oA *= rA; oB *= rB;
    LAS float* xw = (LAS float*)((LAS char*)smem) + w * 1152;
    const LAS float* xr = (const LAS float*)((LAS char*)smem) + (w ^ 4) * 1152;
    f32x16 o; float e;
    if (kh) {
#pragma unroll
      for (int r = 0; r < 4; ++r) *(LAS f32x4*)(xw + r * 256 + lane * 4) = (f32x4){oA[4 * r], oA[4 * r + 1], oA[4 * r + 2], oA[4 * r + 3]};
      xw[1024 + lane] = eA; o = oB; e = eB;
    } else {
#pragma unroll
      for (int r = 0; r < 4; ++r) *(LAS f32x4*)(xw + r * 256 + lane * 4) = (f32x4){oB[4 * r], oB[4 * r + 1], oB[4 * r + 2], oB[4 * r + 3]};
      xw[1024 + lane] = eB; o = oA; e = eA;
    }
    __syncthreads();
    const float e2 = xr[1024 + lane];
    const float em = fmaxf(e, e2);
    float w1 = EXP2(e - em), w2 = EXP2(e2 - em);
    const float rs = 1.0f / (w1 + w2);
    w1 *= rs; w2 *= rs;
    u32x2 pk[4];
#pragma unroll
    for (int r = 0; r < 4; ++r) {
      const f32x4 t = *(const LAS f32x4*)(xr + r * 256 + lane * 4);
      pk[r].x = cvtpk(o[4 * r] * w1 + t[0] * w2, o[4 * r + 1] * w1 + t[1] * w2);
      pk[r].y = cvtpk(o[4 * r + 2] * w1 + t[2] * w2, o[4 * r + 3] * w1 + t[3] * w2);
    }
    const int qrow = qb * 256 + qg * 64 + kh * 32 + (lane & 31);
    bf16_t* op = AH + (size_t)qrow * KA + h * 32 + 8 * hi;
#pragma unroll
    for (int k = 0; k < 4; k += 2) {
      auto rx = __builtin_amdgcn_permlane32_swap(pk[k].x, pk[k + 1].x, false, false);
      auto ry = __builtin_amdgcn_permlane32_swap(pk[k].y, pk[k + 1].y, false, false);
      u32x4 v = {rx[0], ry[0], rx[1], ry[1]};
      *(u32x4*)(op + 8 * k) = v;
    }
  }
  STAMP(2, 3);
}

constexpr int G2_SLOT = 32768, G2_LDS = 4 * G2_SLOT;
constexpr int G2_NCH = 36;

__global__ __launch_bounds__(768, 3) void gemm2_kernel(const bf16_t* __restrict__ AH, const bf16_t* __restrict__ wo,
                                                       const bf16_t* __restrict__ wd, const bf16_t* __restrict__ x,
                                                       bf16_t* __restrict__ out, float* part, unsigned* flags) {
  extern __shared__ __attribute__((aligned(16))) char smem[];
  const int tid = threadIdx.x, lane = tid & 63;
  const int w = __builtin_amdgcn_readfirstlane(tid >> 6);
  const int g = w >> 2, w4 = w & 3, wm = w4 >> 1, wn = w4 & 1;
  const int bid = blockIdx.x, xcd = bid & 7, jj = bid >> 3;
  const int kh = xcd & 1, rt = (xcd >> 1) * 4 + (jj >> 3), ct = jj & 7;
  const int row0 = rt * 128, col0 = ct * 128;
  STAMP(3, 0);
  const unsigned lds0 = (unsigned)(uintptr_t)smem;
  const lds_cptr L = (lds_cptr)smem;

  if (g == 2) {
    const unsigned avoff0 = (unsigned)((lane >> 3) * (KA * 2) + (((lane & 7) ^ ((lane >> 4) + 2)) * 16)), avoff1 = avoff0 ^ 64u;
    const char* abase = (const char*)AH + (size_t)(row0 + 32 * w4) * (KA * 2) + (size_t)kh * (G2_NCH * 128);
    const unsigned bvoff = (unsigned)((lane >> 4) * 2048 + (((lane & 15) ^ ((lane >> 4) << 1) ^ ((w4 & 1) << 3)) * 16));
#define DMA_CH(c) do { const int kg_ = kh * (G2_NCH * 64) + (c) * 64; \
    const char* bb_ = (kg_ < DM ? (const char*)wo + (size_t)kg_ * 2048 : (const char*)wd + (size_t)(kg_ - DM) * 2048) + col0 * 2 + w4 * 32768; \
    const unsigned d_ = lds0 + (2 * ((c) & 1) + (((c) >> 1) & 1)) * G2_SLOT + w4 * 4096; \
    _Pragma("unroll") for (int _i = 0; _i < 4; ++_i) { \
      glds16s(abase + (size_t)_i * (8 * KA * 2) + (c) * 128, (_i & 1) ? avoff1 : avoff0, d_ + _i * 1024); \
      glds16s(bb_ + _i * 8192, bvoff, d_ + 16384 + _i * 1024); } } while (0)
    DMA_CH(0); DMA_CH(1); DMA_CH(2);
    asm volatile("s_waitcnt vmcnt(16)\n\ts_barrier" ::: "memory");
#pragma unroll 1
    for (int i = 0; i < G2_NCH - 3; ++i) {
      DMA_CH(i + 3);
      asm volatile("s_waitcnt vmcnt(16)\n\ts_barrier" ::: "memory");
    }
    asm volatile("s_waitcnt vmcnt(8)\n\ts_barrier" ::: "memory");
    asm volatile("s_waitcnt vmcnt(0)\n\ts_barrier" ::: "memory");
    asm volatile("s_barrier" ::: "memory");
    asm volatile("s_barrier" ::: "memory");
    return;
#undef DMA_CH
  }

  const int fr = lane & 15, fq = lane >> 4, kq = ((fq & 1) << 1) | (fq >> 1), q4 = fr >> 2;
  const int aoff0 = 2 * g * G2_SLOT + (64 * wm + fr) * 128 + ((kq ^ (((fr >> 1) + 2) & 7)) * 16);
  lds_cptr pa0 = L + aoff0, pa1 = L + (aoff0 ^ 64);
  const int boff0 = 2 * g * G2_SLOT + 16384 + (8 * kq + q4) * 256 + 8 * (lane & 3);
  const int bsw = q4 | ((fq & 1) << 2);
  lds_cptr pb[4];
#pragma unroll
  for (int n = 0; n < 4; ++n) pb[n] = L + (boff0 + (((4 * wn + n) ^ bsw) * 32));
  f32x4 acc[4][4];
#pragma unroll
  for (int m = 0; m < 4; ++m)
#pragma unroll
    for (int n = 0; n < 4; ++n) { acc[m][n] = (f32x4){0.f, 0.f, 0.f, 0.f}; asm volatile("" : "+v"(acc[m][n])); }
  bf16x8 af[2][4], bfr[2][4];
  BARRIER();
  if (g == 1) BARRIER();
  STAMP(3, 1);
#define G2STEP(H) do { \
    _Pragma("unroll") for (int ks = 0; ks < 2; ++ks) { \
      _Pragma("unroll") for (int m = 0; m < 4; ++m) af[ks][m] = lds_rd128((ks ? pa1 : pa0) + (H) * G2_SLOT + m * 2048); \
      _Pragma("unroll") for (int n = 0; n < 4; ++n) bfr[ks][n] = cat8(lds_tr(pb[n] + (H) * G2_SLOT + ks * 8192), lds_tr(pb[n] + (H) * G2_SLOT + ks * 8192 + 1024)); } \
    SB(); asm volatile("s_waitcnt lgkmcnt(0)\n\ts_barrier" ::: "memory"); SB(); \
    _Pragma("unroll") for (int ks = 0; ks < 2; ++ks) _Pragma("unroll") for (int n = 0; n < 4; ++n) _Pragma("unroll") for (int m = 0; m < 4; ++m) \
      MFMA16A(acc[m][n], bfr[ks][n], af[ks][m]); \
    BARRIER(); } while (0)
#pragma unroll 1
  for (int c = 0; c < G2_NCH / 4; ++c) { G2STEP(0); G2STEP(1); }
#undef G2STEP
  if (g == 0) BARRIER();
  asm volatile("s_nop 7\n\ts_nop 7" ::: "memory");
  SB();
  STAMP(3, 2);
  int tz = threadIdx.x; asm volatile("" : "+v"(tz));
  const int orow = row0 + 64 * wm + 16 * (2 * g + kh) + (tz & 15), ocol = col0 + 64 * wn + 4 * ((tz >> 4) & 3);
  u32x2 xv[4];
#pragma unroll
  for (int n = 0; n < 4; ++n) xv[n] = *(const u32x2*)(x + (size_t)orow * DM + ocol + 16 * n);

  {
    LAS f32x4* xb = (LAS f32x4*)((LAS char*)smem) + (size_t)w * 512;
    f32x4 s[2][4];
    if (g) {
#pragma unroll
      for (int mm = 0; mm < 2; ++mm)
#pragma unroll
        for (int n = 0; n < 4; ++n) { xb[(mm * 4 + n) * 64 + lane] = acc[mm][n]; s[mm][n] = acc[2 + mm][n]; }
    } else {
#pragma unroll
      for (int mm = 0; mm < 2; ++mm)
#pragma unroll
        for (int n = 0; n < 4; ++n) { xb[(mm * 4 + n) * 64 + lane] = acc[2 + mm][n]; s[mm][n] = acc[mm][n]; }
    }
    SB(); asm volatile("s_waitcnt lgkmcnt(0)\n\ts_barrier" ::: "memory"); SB();
    LAS f32x4* xr = (LAS f32x4*)((LAS char*)smem) + (size_t)(w ^ 4) * 512;
    f32x4 keep[4], send[4];
    if (kh) {
#pragma unroll
      for (int n = 0; n < 4; ++n) { send[n] = s[0][n] + xr[n * 64 + lane]; keep[n] = s[1][n] + xr[(4 + n) * 64 + lane]; }
    } else {
#pragma unroll
      for (int n = 0; n < 4; ++n) { send[n] = s[1][n] + xr[(4 + n) * 64 + lane]; keep[n] = s[0][n] + xr[n * 64 + lane]; }
    }
    const int pbid = bid ^ 1;
    float* mine = part + (size_t)bid * 8192 + w * 1024 + lane * 4;
#pragma unroll
    for (int n = 0; n < 4; ++n)
      asm volatile("global_store_dwordx4 %0, %1, off offset:%2 sc1\n\ts_nop 1" :: "v"(mine), "v"(send[n]), "i"(n * 1024) : "memory");
    asm volatile("s_waitcnt vmcnt(0)" ::: "memory");
    if (lane == 0) __hip_atomic_store(flags + bid * 64 + w, 1u, __ATOMIC_RELAXED, __HIP_MEMORY_SCOPE_AGENT);
    {
      unsigned spins = 0;
      while (__builtin_amdgcn_readfirstlane(__hip_atomic_load(flags + pbid * 64 + w, __ATOMIC_RELAXED, __HIP_MEMORY_SCOPE_AGENT)) == 0u) {
        __builtin_amdgcn_s_sleep(1);
        if (++spins > (1u << 22)) break;
      }
    }
    STAMP(3, 4);
    const float* theirs = part + (size_t)pbid * 8192 + w * 1024 + lane * 4;
    f32x4 pv[4];
    asm volatile("global_load_dwordx4 %0, %4, off sc1\n\tglobal_load_dwordx4 %1, %4, off offset:1024 sc1\n\t"
                 "global_load_dwordx4 %2, %4, off offset:2048 sc1\n\tglobal_load_dwordx4 %3, %4, off offset:3072 sc1\n\t"
                 "s_waitcnt vmcnt(0)"
                 : "=&v"(pv[0]), "=&v"(pv[1]), "=&v"(pv[2]), "=&v"(pv[3]) : "v"(theirs) : "memory");
#pragma unroll
    for (int n = 0; n < 4; ++n) {
      const f32x4 v = keep[n] + pv[n];
      u32x2 o = {cvtpk(bf_lo(xv[n].x) + v[0], bf_hi(xv[n].x) + v[1]), cvtpk(bf_lo(xv[n].y) + v[2], bf_hi(xv[n].y) + v[3])};
      *(u32x2*)(out + (size_t)orow * DM + ocol + 16 * n) = o;
    }
  }
  STAMP(3, 3);
}

extern "C" void kernel_launch(void* const* d_in, const int* in_sizes, int n_in, void* d_out, int out_size, void* d_ws,
                              size_t ws_size, hipStream_t stream) {
  const bf16_t* x = (const bf16_t*)d_in[0];
  const bf16_t* wqkv = (const bf16_t*)d_in[1];
  const bf16_t* wo = (const bf16_t*)d_in[2];
  const bf16_t* wg = (const bf16_t*)d_in[4];
  const bf16_t* wu = (const bf16_t*)d_in[5];
  const bf16_t* wd = (const bf16_t*)d_in[6];
  char* ws = (char*)d_ws;
  bf16_t* xn = (bf16_t*)(ws + WS_XN);
  bf16_t* Qb = (bf16_t*)(ws + WS_Q);
  bf16_t* Kb = (bf16_t*)(ws + WS_K);
  bf16_t* Vb = (bf16_t*)(ws + WS_V);
  bf16_t* AH = (bf16_t*)(ws + WS_AH);
  float* part = (float*)(ws + WS_PART);
  unsigned* flags = (unsigned*)(ws + WS_FLAGS);
  (void)hipFuncSetAttribute((const void*)gemm1_kernel, hipFuncAttributeMaxDynamicSharedMemorySize, G1_LDS);
  (void)hipFuncSetAttribute((const void*)attn_kernel, hipFuncAttributeMaxDynamicSharedMemorySize, AT_LDS);
  (void)hipFuncSetAttribute((const void*)gemm2_kernel, hipFuncAttributeMaxDynamicSharedMemorySize, G2_LDS);
  ln_kernel<<<SEQ / 4, 256, 0, stream>>>(x, xn, flags);
  gemm1_kernel<<<256, 512, G1_LDS, stream>>>(xn, wqkv, wg, wu, Qb, Kb, Vb, AH);
  attn_kernel<<<256, 512, AT_LDS, stream>>>(Qb, Kb, Vb, AH);
  gemm2_kernel<<<256, 768, G2_LDS, stream>>>(AH, wo, wd, x, (bf16_t*)d_out, part, flags);
}
```

```cpp
#define G2_ROT 0
#include <hip/hip_runtime.h>
#include <hip/hip_bf16.h>
#include <stdint.h>

typedef unsigned short bf16_t;
typedef short bf16x8 __attribute__((ext_vector_type(8)));
typedef short s16x4 __attribute__((ext_vector_type(4)));
typedef float f32x4 __attribute__((ext_vector_type(4)));
typedef float f32x16 __attribute__((ext_vector_type(16)));
typedef float f32x2 __attribute__((ext_vector_type(2)));
typedef __bf16 bf16x2v __attribute__((ext_vector_type(2)));
typedef unsigned u32x4 __attribute__((ext_vector_type(4)));
typedef unsigned u32x2 __attribute__((ext_vector_type(2)));
#define LAS __attribute__((address_space(3)))
typedef LAS const char* lds_cptr;

constexpr int SEQ = 2048, DM = 1024, NH = 32, FF = 3584, NQKV = 3072, KA = 4608;

constexpr size_t WS_FLAGS = 0;
constexpr size_t WS_XN = 1u << 20;
constexpr size_t WS_Q = WS_XN + (size_t)SEQ * DM * 2;
constexpr size_t WS_K = WS_Q + (size_t)SEQ * DM * 2;
constexpr size_t WS_V = WS_K + (size_t)SEQ * DM * 2;
constexpr size_t WS_AH = WS_V + (size_t)SEQ * DM * 2;
constexpr size_t WS_PART = WS_AH + (size_t)SEQ * KA * 2;

#define SB() __builtin_amdgcn_sched_barrier(0)
#define STAMP(kid, i) do {} while (0)

__device__ __forceinline__ unsigned cvtpk(float lo, float hi) {
  f32x2 v = {lo, hi};
  bf16x2v b = __builtin_convertvector(v, bf16x2v);
  return __builtin_bit_cast(unsigned, b);
}
__device__ __forceinline__ float bf_lo(unsigned u) { return __uint_as_float(u << 16); }
__device__ __forceinline__ float bf_hi(unsigned u) { return __uint_as_float(u & 0xffff0000u); }
__device__ __forceinline__ float rbf(float x) { return bf_lo(cvtpk(x, x)); }

__device__ __forceinline__ void glds16(const void* g, unsigned lds_dst) {
  unsigned keep;
  asm volatile("s_mov_b32 %0, m0\n\ts_mov_b32 m0, %2\n\ts_nop 0\n\tglobal_load_lds_dwordx4 %1, off\n\ts_mov_b32 m0, %0"
               : "=&s"(keep) : "v"(g), "s"(lds_dst) : "memory");
}
__device__ __forceinline__ void glds16s(const void* sbase, unsigned voff, unsigned lds_dst) {
  unsigned keep;
  asm volatile("s_mov_b32 %0, m0\n\ts_mov_b32 m0, %3\n\ts_nop 0\n\tglobal_load_lds_dwordx4 %1, %2\n\ts_mov_b32 m0, %0"
               : "=&s"(keep) : "v"(voff), "s"(sbase), "s"(lds_dst) : "memory");
}
__device__ __forceinline__ bf16x8 lds_rd128(lds_cptr p) { return *(const LAS bf16x8*)p; }
__device__ __forceinline__ s16x4 lds_tr(lds_cptr p) {
  return __builtin_amdgcn_ds_read_tr16_b64_v4i16((LAS s16x4*)p);
}
__device__ __forceinline__ bf16x8 cat8(s16x4 a, s16x4 b) { return __builtin_shufflevector(a, b, 0, 1, 2, 3, 4, 5, 6, 7); }

__global__ __launch_bounds__(256) void ln_kernel(const bf16_t* __restrict__ x, bf16_t* __restrict__ xn, unsigned* flags) {
  const int lane = threadIdx.x & 63, w = threadIdx.x >> 6;
  const int row = blockIdx.x * 4 + w;
  const u32x4* xr = (const u32x4*)(x + (size_t)row * DM);
  u32x4 a = xr[lane], b = xr[64 + lane];
  if (blockIdx.x < 64) flags[blockIdx.x * 256 + threadIdx.x] = 0u;
  float v[16];
#pragma unroll
  for (int i = 0; i < 4; ++i) { v[2 * i] = bf_lo(a[i]); v[2 * i + 1] = bf_hi(a[i]); v[8 + 2 * i] = bf_lo(b[i]); v[9 + 2 * i] = bf_hi(b[i]); }
  float s = 0.f;
#pragma unroll
  for (int i = 0; i < 16; ++i) s += v[i];
#pragma unroll
  for (int o = 1; o < 64; o <<= 1) s += __shfl_xor(s, o);
  const float mean = s * (1.f / DM);
  float q = 0.f;
#pragma unroll
  for (int i = 0; i < 16; ++i) { const float d = v[i] - mean; q += d * d; }
#pragma unroll
  for (int o = 1; o < 64; o <<= 1) q += __shfl_xor(q, o);
  const float mb = rbf(mean), vb = rbf(q * (1.f / DM));
  const float den = rbf(sqrtf(rbf(vb + 1e-5f)));
  u32x4 oa, ob;
#pragma unroll
  for (int i = 0; i < 4; ++i) {
    oa[i] = cvtpk(rbf(v[2 * i] - mb) / den, rbf(v[2 * i + 1] - mb) / den);
    ob[i] = cvtpk(rbf(v[8 + 2 * i] - mb) / den, rbf(v[9 + 2 * i] - mb) / den);
  }
  u32x4* o = (u32x4*)(xn + (size_t)row * DM);
  o[lane] = oa; o[64 + lane] = ob;
}

constexpr int G1_AOFF = 0, G1_BOFF = 65536, G1_BREG = 10304, G1_BSLOT = 2 * G1_BREG, G1_LDS = 65536 + 4 * G1_BSLOT;
#define MFMA16(a, b, c) __builtin_amdgcn_mfma_f32_16x16x32_bf16(a, b, c, 0, 0, 0)
#define MFMA32(a, b, c) __builtin_amdgcn_mfma_f32_32x32x16_bf16(a, b, c, 0, 0, 0)
#define WAITBAR(N) do { SB(); asm volatile("s_waitcnt vmcnt(" #N ") lgkmcnt(0)\n\ts_barrier" ::: "memory"); SB(); } while (0)
#define BARRIER() do { SB(); asm volatile("s_barrier" ::: "memory"); SB(); } while (0)
#define MFMA16A(acc, a, b) asm volatile("v_mfma_f32_16x16x32_bf16 %0, %1, %2, %0" : "+v"(acc) : "v"(a), "v"(b))
#define ADV64(p, inc) asm volatile("v_mad_u64_u32 %0, vcc, %1, 1, %0" : "+v"(p) : "v"(inc) : "vcc")

__global__ __launch_bounds__(512, 2) void gemm1_kernel(const bf16_t* __restrict__ x, const bf16_t* __restrict__ wqkv,
                                                       const bf16_t* __restrict__ wg, const bf16_t* __restrict__ wu,
                                                       bf16_t* __restrict__ Qb, bf16_t* __restrict__ Kb, bf16_t* __restrict__ Vb,
                                                       bf16_t* __restrict__ AH) {
  extern __shared__ __attribute__((aligned(16))) char smem[];
  const int tid = threadIdx.x, lane = tid & 63;
  const int w = __builtin_amdgcn_readfirstlane(tid >> 6);
  const int g = w >> 2, w4 = w & 3, wm = 2 * g + (w4 >> 1), wn = w4 & 1;
  STAMP(1, 0);
  const int bid = blockIdx.x, xcd = bid & 7, jj = bid >> 3;
  const int cg = xcd * 4 + (jj & 3), rt = jj >> 2;
  const int row0 = rt * 256;
  const unsigned lds0 = (unsigned)(uintptr_t)smem;
  const lds_cptr L = (lds_cptr)smem;

  unsigned avoff0 = (unsigned)((lane >> 3) * 2048 + (((lane & 7) ^ ((lane >> 4) + 2)) * 16)), avoff1 = avoff0 ^ 64u;
  const char* abase = (const char*)x + (size_t)(row0 + 32 * w4) * 2048;
#define DMA_A(hh, t) do { _Pragma("unroll") for (int _i = 0; _i < 4; ++_i) \
    glds16s(abase + (size_t)(hh) * 262144 + _i * 16384 + (t) * 128, (_i & 1) ? avoff1 : avoff0, \
            lds0 + G1_AOFF + ((t) & 1) * 32768 + (hh) * 16384 + (4 * w4 + _i) * 1024); } while (0)
  DMA_A(g, 0);

  const char* bptr[4]; unsigned bld[4]; unsigned bdst[4];
#pragma unroll
  for (int i = 0; i < 4; ++i) {
    const int b = (i == 0) ? (g ? 16 + w4 : 4 * w4) : 4 * w4 + i;
    const int region = b >= 10 ? 1 : 0, bi = b - 10 * region;
    const int P = bi * 64 + lane;
    const int k = P / 20, c8 = (P - 20 * k) * 8;
    const bool isq = (cg <= 8) || (cg == 9 && c8 < 96);
    const bf16_t* mat = isq ? wqkv : (region ? wu : wg);
    const int ld = isq ? NQKV : FF;
    const int col = c8 + (cg <= 8 ? 320 * cg + 160 * region : (cg == 9 ? (isq ? 2880 + 96 * region : -96) : 64 + 160 * (cg - 10)));
    bptr[i] = (const char*)(mat + (size_t)k * ld + col);
    bld[i] = (unsigned)ld * 64u;
    bdst[i] = lds0 + G1_BOFF + region * G1_BREG + bi * 1024 + (bi >= 5 ? 32 : 0);
  }
#define DMA_B(i, s) glds16(bptr[i], bdst[i] + ((s) & 3) * G1_BSLOT)
#define ADV_B() do { ADV64(bptr[0], bld[0]); ADV64(bptr[1], bld[1]); ADV64(bptr[2], bld[2]); ADV64(bptr[3], bld[3]); } while (0)

  if (g == 0) {
    DMA_B(0, 0); DMA_B(1, 0); DMA_B(2, 0); DMA_B(3, 0); ADV_B();
    DMA_B(0, 1); ADV_B();
    DMA_B(0, 2); DMA_B(1, 2); DMA_B(2, 2); DMA_B(3, 2); ADV_B();
  } else {
    DMA_B(0, 0); ADV_B();
    DMA_B(0, 1); DMA_B(1, 1); DMA_B(2, 1); DMA_B(3, 1); ADV_B();
    DMA_B(0, 2); ADV_B();
    DMA_A(0, 1);
  }

  const int fr = lane & 15, fq = lane >> 4, kq = ((fq & 1) << 1) | (fq >> 1);
  const int aoff0 = G1_AOFF + (64 * wm + fr) * 128 + ((kq ^ (((fr >> 1) + 2) & 7)) * 16);
  lds_cptr pa0 = L + aoff0, pa1 = L + (aoff0 ^ 64);
  lds_cptr pb0 = L + (G1_BOFF + (8 * kq + (fr >> 2)) * 320 + (fq & 1) * 32 + 8 * (lane & 3) + 160 * wn);
  lds_cptr pb1 = pb0 + 2 * G1_BSLOT;
  asm volatile("" : "+v"(pa0), "+v"(pa1), "+v"(pb0), "+v"(pb1), "+v"(avoff0), "+v"(avoff1));

  f32x4 acc[4][5][2];
#pragma unroll
  for (int m = 0; m < 4; ++m)
#pragma unroll
    for (int j = 0; j < 5; ++j) { acc[m][j][0] = (f32x4){0.f, 0.f, 0.f, 0.f}; acc[m][j][1] = (f32x4){0.f, 0.f, 0.f, 0.f};
      asm volatile("" : "+v"(acc[m][j][0]), "+v"(acc[m][j][1])); }
  bf16x8 af[4], bfr[5][2];

  if (g == 0) { WAITBAR(5); } else { WAITBAR(9); BARRIER(); }
  STAMP(1, 1);

#define G1_PRIO 1
#define STEP(U, ISSUE, WN, LASTBAR) do { \
    { const lds_cptr pa = ((U) & 1) ? pa1 : pa0; const lds_cptr pb = (((U) & 2) ? pb1 : pb0) + ((U) & 1) * G1_BSLOT; \
      _Pragma("unroll") for (int m = 0; m < 4; ++m) af[m] = lds_rd128(pa + ((U) >> 1) * 32768 + m * 2048); \
      _Pragma("unroll") for (int j = 0; j < 5; ++j) \
        _Pragma("unroll") for (int r = 0; r < 2; ++r) bfr[j][r] = cat8(lds_tr(pb + r * G1_BREG + j * 32), lds_tr(pb + r * G1_BREG + j * 32 + 1280)); } \
    if (ISSUE) { DMA_B(0, (U) + 3); \
      if (g == ((U) & 1)) { DMA_A(1 - ((U) & 1), 2 * it + ((U) >> 1) + 1 + ((U) & 1)); } \
      else { DMA_B(1, (U) + 3); DMA_B(2, (U) + 3); DMA_B(3, (U) + 3); } } \
    WAITBAR(WN); \
    if (G1_PRIO) __builtin_amdgcn_s_setprio(1); \
    _Pragma("unroll") for (int j = 0; j < 5; ++j) { _Pragma("unroll") for (int r = 0; r < 2; ++r) _Pragma("unroll") for (int m = 0; m < 4; ++m) \
        MFMA16A(acc[m][j][r], bfr[j][r], af[m]); \
      if ((ISSUE) && j < 4) ADV64(bptr[j], bld[j]); } \
    if (G1_PRIO) __builtin_amdgcn_s_setprio(0); \
    if (LASTBAR) BARRIER(); } while (0)
  int it = 0;
#pragma unroll 1
  for (; it < 7; ++it) { STEP(0, true, 9, true); STEP(1, true, 9, true); STEP(2, true, 9, true); STEP(3, true, 9, true); }
  STEP(0, true, 9, true); STEP(1, false, 4, true); STEP(2, false, 0, true); STEP(3, false, 0, g == 0);
#undef STEP
  asm volatile("s_nop 7\n\ts_nop 7" ::: "memory");
  SB();
  STAMP(1, 2);
#undef DMA_A
#undef DMA_B
#undef ADV_B

  int tz = threadIdx.x; asm volatile("" : "+v"(tz));
  const int fr2 = tz & 15, fq2 = (tz >> 4) & 3, sel1 = fq2 & 1, c8 = (fq2 >> 1) * 8;
  const float qscale = 0.17677669529663687f * 1.4426950408889634f;
#define SWAP16(A, B, OUT) do { auto r0_ = __builtin_amdgcn_permlane16_swap(A.x, B.x, false, false); \
    auto r1_ = __builtin_amdgcn_permlane16_swap(A.y, B.y, false, false); OUT = (u32x4){r0_[0], r1_[0], r0_[1], r1_[1]}; } while (0)
#define SILU4(m, j, OUT) do { float hv_[4]; _Pragma("unroll") for (int r = 0; r < 4; ++r) { \
      const float tg_ = acc[m][j][0][r], tu_ = acc[m][j][1][r]; \
      hv_[r] = (tg_ * tu_) * __builtin_amdgcn_rcpf(1.f + __builtin_amdgcn_exp2f(-1.4426950408889634f * tg_)); } \
    OUT = (u32x2){cvtpk(hv_[0], hv_[1]), cvtpk(hv_[2], hv_[3])}; } while (0)
#define PK4(V, SC, OUT) do { const f32x4 v_ = (V) * (SC); OUT = (u32x2){cvtpk(v_[0], v_[1]), cvtpk(v_[2], v_[3])}; } while (0)
  const int rowb = row0 + 64 * wm + fr2;
#define QKV_DST(col, row, dst, mstep) do { const int typ_ = (col) >> 10, h_ = ((col) & 1023) >> 5, d_ = (col) & 31; \
    if (typ_ == 1) { dst = Kb + ((((size_t)h_ * 64 + ((row) >> 5)) * 2 + (d_ >> 4)) * 64 + ((row) & 31) + 32 * ((d_ >> 3) & 1)) * 8; mstep = 128; } \
    else { dst = (typ_ == 0 ? Qb : Vb) + ((size_t)h_ * SEQ + (row)) * 32 + d_; mstep = 512; } } while (0)
  if (cg >= 10) {
    bf16_t* dst = AH + (size_t)rowb * KA + DM + 64 + 160 * (cg - 10) + 80 * wn + c8;
#pragma unroll
    for (int m = 0; m < 4; ++m) {
#pragma unroll
      for (int jp = 0; jp < 2; ++jp) {
        u32x2 a_, b_; u32x4 o; SILU4(m, 2 * jp, a_); SILU4(m, 2 * jp + 1, b_); SWAP16(a_, b_, o);
        *(u32x4*)(dst + (size_t)m * 16 * KA + 32 * jp + 16 * sel1) = o;
      }
      if (m & 1) {
        u32x2 a_, b_; u32x4 o; SILU4(m - 1, 4, a_); SILU4(m, 4, b_); SWAP16(a_, b_, o);
        *(u32x4*)(dst + (size_t)(m - 1 + sel1) * 16 * KA + 64) = o;
      }
    }
  } else if (cg <= 8) {
#pragma unroll
    for (int r2 = 0; r2 < 2; ++r2) {
      const int colw = 320 * cg + 160 * r2 + 80 * wn;
#pragma unroll
      for (int jp = 0; jp < 2; ++jp) {
        const int ca = colw + 32 * jp;
        const float sa = ca < 1024 ? qscale : 1.f, sb = (ca + 16) < 1024 ? qscale : 1.f;
        bf16_t* dst; int mstep; const int col = ca + 16 * sel1 + c8;
        QKV_DST(col, rowb, dst, mstep);
#pragma unroll
        for (int m = 0; m < 4; ++m) {
          u32x2 a_, b_; u32x4 o; PK4(acc[m][2 * jp][r2], sa, a_); PK4(acc[m][2 * jp + 1][r2], sb, b_); SWAP16(a_, b_, o);
          *(u32x4*)(dst + (m & 1) * mstep + (m >> 1) * 1024) = o;
        }
      }
      {
        const int ca = colw + 64;
        const float sa = ca < 1024 ? qscale : 1.f;
        bf16_t* dst; int mstep; const int col = ca + c8;
        QKV_DST(col, rowb, dst, mstep);
#pragma unroll
        for (int mp = 0; mp < 2; ++mp) {
          u32x2 a_, b_; u32x4 o; PK4(acc[2 * mp][4][r2], sa, a_); PK4(acc[2 * mp + 1][4][r2], sa, b_); SWAP16(a_, b_, o);
          *(u32x4*)(dst + sel1 * mstep + mp * 1024) = o;
        }
      }
    }
  } else {
#pragma unroll
    for (int j = 0; j < 5; ++j) {
      const int i = 5 * wn + j;
      if (i >= 6) {
        bf16_t* dst = AH + (size_t)(rowb + 16 * sel1) * KA + DM + 16 * i - 96 + c8;
#pragma unroll
        for (int mp = 0; mp < 2; ++mp) {
          u32x2 a_, b_; u32x4 o; SILU4(2 * mp, j, a_); SILU4(2 * mp + 1, j, b_); SWAP16(a_, b_, o);
          *(u32x4*)(dst + (size_t)mp * 32 * KA) = o;
        }
      } else {
#pragma unroll
        for (int r2 = 0; r2 < 2; ++r2) {
          const int col = 2880 + 96 * r2 + 16 * i + c8;
          bf16_t* dst = Vb + ((size_t)((col & 1023) >> 5) * SEQ + rowb + 16 * sel1) * 32 + (col & 31);
#pragma unroll
          for (int mp = 0; mp < 2; ++mp) {
            u32x2 a_, b_; u32x4 o; PK4(acc[2 * mp][j][r2], 1.f, a_); PK4(acc[2 * mp + 1][j][r2], 1.f, b_); SWAP16(a_, b_, o);
            *(u32x4*)(dst + mp * 1024) = o;
          }
        }
      }
    }
  }
#undef SWAP16
#undef SILU4
#undef PK4
#undef QKV_DST
  STAMP(1, 3);
}

constexpr int AT_STAGE = 32768, AT_SCR = AT_STAGE * 4, AT_FLAG = AT_SCR + 1024, AT_LDS = AT_FLAG + 64;
#define PIN(x) asm volatile("" : "+v"(x))
#define EXP2(x) __builtin_amdgcn_exp2f(x)

__device__ __forceinline__ float max16(const f32x16& p) {
  float a = fmaxf(fmaxf(p[0], p[1]), p[2]), b = fmaxf(fmaxf(p[3], p[4]), p[5]);
  a = fmaxf(fmaxf(a, p[6]), p[7]); b = fmaxf(fmaxf(b, p[8]), p[9]);
  a = fmaxf(fmaxf(a, p[10]), p[11]); b = fmaxf(fmaxf(b, p[12]), p[13]);
  a = fmaxf(fmaxf(a, p[14]), p[15]);
  float m = fmaxf(a, b);
  auto rr = __builtin_amdgcn_permlane32_swap(__float_as_uint(m), __float_as_uint(m), false, false);
  return fmaxf(__uint_as_float(rr[0]), __uint_as_float(rr[1]));
}
__device__ __forceinline__ f32x16 splat16(float v) { f32x16 r;
#pragma unroll
  for (int i = 0; i < 16; ++i) r[i] = v;
  return r; }

__global__ __launch_bounds__(512, 2) void attn_kernel(const bf16_t* __restrict__ Qb, const bf16_t* __restrict__ Kb,
                                                      const bf16_t* __restrict__ Vb, bf16_t* __restrict__ AH,
                                                      const bf16_t* __restrict__ wo, const bf16_t* __restrict__ wd) {
  extern __shared__ __attribute__((aligned(16))) char smem[];
  const int tid = threadIdx.x, lane = tid & 63;
  const int w = __builtin_amdgcn_readfirstlane(tid >> 6);
  const int qg = w & 3, kh = w >> 2;
  const int bid = blockIdx.x, xcd = bid & 7, jj = bid >> 3;
  const int h = xcd * 4 + (jj >> 3), qb = jj & 7;
  STAMP(2, 0);
  const unsigned lds0 = (unsigned)(uintptr_t)smem;
  const lds_cptr L = (lds_cptr)smem;
  const int hi = lane >> 5;
  const char* kg = (const char*)(Kb + (size_t)h * SEQ * 32) + kh * 65536 + qg * 2048;
  const char* vg = (const char*)(Vb + (size_t)h * SEQ * 32) + kh * 65536 + qg * 2048;
  const unsigned voff = lane * 16;
#define DMA_ST(st) do { const unsigned d_ = lds0 + ((st) & 3) * AT_STAGE + kh * 8192 + qg * 2048; \
    glds16s(kg + (st) * 8192, voff, d_); glds16s(kg + (st) * 8192 + 1024, voff, d_ + 1024); \
    glds16s(vg + (st) * 8192, voff, d_ + 16384); glds16s(vg + (st) * 8192 + 1024, voff, d_ + 16384 + 1024); } while (0)
  DMA_ST(0);
  const bf16_t* qp = Qb + ((size_t)h * SEQ + qb * 256 + qg * 64 + (lane & 31)) * 32 + 8 * hi;
  bf16x8 qa0 = *(const bf16x8*)qp, qa1 = *(const bf16x8*)(qp + 16);
  bf16x8 qb0 = *(const bf16x8*)(qp + 1024), qb1 = *(const bf16x8*)(qp + 1024 + 16);
  const short one = (((lane >> 4) & 1) == ((lane >> 2) & 1)) ? (short)0x3f80 : (short)0;
  const bf16x8 sel = {one, one, one, one, one, one, one, one};
  volatile LAS unsigned* flag = (volatile LAS unsigned*)((LAS char*)smem + AT_FLAG);
  if (tid == 0) *flag = 0u;
  const int gq = lane >> 4;
  const int koff = kh * 8192 + lane * 16;
  const int voffl = 16384 + kh * 8192 + (4 * (gq >> 1) + ((lane & 15) >> 2)) * 64 + (gq & 1) * 32 + (lane & 3) * 8;
  const f32x16 zero16 = splat16(0.f);
  f32x16 oA = zero16, oB = zero16;
  f32x4 lA = {0.f, 0.f, 0.f, 0.f}, lB = {0.f, 0.f, 0.f, 0.f};
  float mA, mB;
  PIN(qa0); PIN(qa1); PIN(qb0); PIN(qb1);

#define LDK(K0, K1, p) do { K0 = lds_rd128(p); K1 = lds_rd128((p) + 1024); } while (0)
#define LDV(V0, V1, p) do { V0 = cat8(lds_tr(p), lds_tr((p) + 512)); V1 = cat8(lds_tr((p) + 1024), lds_tr((p) + 1536)); } while (0)
#define BF(x) __builtin_bit_cast(bf16x8, x)
  {
    WAITBAR(0);
    bf16x8 kx0, kx1, ky0, ky1, vx0, vx1, vy0, vy1;
    LDK(kx0, kx1, L + koff);
    LDK(ky0, ky1, L + koff + 2048);
    LDV(vy0, vy1, L + voffl);
    DMA_ST(1); DMA_ST(2);
    f32x16 sA = MFMA32(kx0, qa0, zero16); sA = MFMA32(kx1, qa1, sA);
    f32x16 sB = MFMA32(kx0, qb0, zero16); sB = MFMA32(kx1, qb1, sB);
    mA = max16(sA); mB = max16(sB);
    f32x16 nA = splat16(-mA), nB = splat16(-mB);
    PIN(nA); PIN(nB);
    sA = MFMA32(kx0, qa0, nA); sA = MFMA32(kx1, qa1, sA);
    u32x4 pA0, pA1, pB0 = {0u, 0u, 0u, 0u}, pB1 = {0u, 0u, 0u, 0u};
    STAMP(2, 1);
#define E4(S, i) do { S[i] = EXP2(S[i]); S[i + 1] = EXP2(S[i + 1]); S[i + 2] = EXP2(S[i + 2]); S[i + 3] = EXP2(S[i + 3]); } while (0)
#define SLOT(SC, PC0, PC1, SN, K0, K1, Q0, Q1, NM, PP0, PP1, V0, V1, O, LS, LOADS) do { \
    SN = MFMA32(K0, Q0, NM); E4(SC, 0); PIN(SC); SB(); \
    LOADS; SB(); \
    SN = MFMA32(K1, Q1, SN); E4(SC, 4); PIN(SC); SB(); \
    O = MFMA32(V0, BF(PP0), O); PC0[0] = cvtpk(SC[0], SC[1]); PC0[1] = cvtpk(SC[2], SC[3]); \
      SC[8] = EXP2(SC[8]); SC[9] = EXP2(SC[9]); SC[10] = EXP2(SC[10]); PIN(SC); PIN(PC0); SB(); \
    LS = MFMA16(sel, BF(PP0), LS); PC0[2] = cvtpk(SC[4], SC[5]); PC0[3] = cvtpk(SC[6], SC[7]); SC[11] = EXP2(SC[11]); PIN(SC); PIN(PC0); SB(); \
    O = MFMA32(V1, BF(PP1), O); E4(SC, 12); PIN(SC); SB(); \
    LS = MFMA16(sel, BF(PP1), LS); PC1[0] = cvtpk(SC[8], SC[9]); PC1[1] = cvtpk(SC[10], SC[11]); \
      PC1[2] = cvtpk(SC[12], SC[13]); PC1[3] = cvtpk(SC[14], SC[15]); PIN(PC1); SB(); } while (0)
#define SUB(KC0, KC1, KN0, KN1, VC0, VC1, VP0, VP1, vpu, kp2) do { \
    SLOT(sA, pA0, pA1, sB, KC0, KC1, qb0, qb1, nB, pB0, pB1, VP0, VP1, oB, lB, LDV(VC0, VC1, vpu)); \
    SLOT(sB, pB0, pB1, sA, KN0, KN1, qa0, qa1, nA, pA0, pA1, VC0, VC1, oA, lA, LDK(KC0, KC1, kp2)); } while (0)
#pragma unroll 1
    for (int st = 0; st < 8; ++st) {
      const lds_cptr kp = L + (st & 3) * AT_STAGE + koff, vp = L + (st & 3) * AT_STAGE + voffl;
      const lds_cptr kn = L + ((st + 1) & 3) * AT_STAGE + koff;
      SUB(kx0, kx1, ky0, ky1, vx0, vx1, vy0, vy1, vp, kp + 4096);
      SUB(ky0, ky1, kx0, kx1, vy0, vy1, vx0, vx1, vp + 2048, kp + 6144);
      if (st == 0) { SB(); asm volatile("s_waitcnt vmcnt(4)\n\ts_barrier" ::: "memory"); SB(); }
      else if (st < 6) { SB(); asm volatile("s_waitcnt vmcnt(5)\n\ts_barrier" ::: "memory"); SB(); }
      else { SB(); asm volatile("s_waitcnt vmcnt(0)\n\ts_barrier" ::: "memory"); SB(); }
      if (st < 5) {
        const int pc = st * 8 + w, kb = bid * 36 + (pc < 36 ? pc : 35);
        glds16s(kb < 2048 ? (const char*)wo + (size_t)kb * 1024 : (const char*)wd + (size_t)(kb - 2048) * 1024, voff, lds0 + AT_SCR);
        DMA_ST(st + 3);
      }
      SUB(kx0, kx1, ky0, ky1, vx0, vx1, vy0, vy1, vp + 4096, kn);
      SUB(ky0, ky1, kx0, kx1, vy0, vy1, vx0, vx1, vp + 6144, kn + 2048);
    }
    oB = MFMA32(vy0, BF(pB0), oB); lB = MFMA16(sel, BF(pB0), lB);
    oB = MFMA32(vy1, BF(pB1), oB); lB = MFMA16(sel, BF(pB1), lB);
#undef SUB
#undef SLOT
#undef E4
  }
  STAMP(2, 2);
  LAS float* xw = (LAS float*)((LAS char*)smem) + w * 1152;
  const LAS float* xr = (const LAS float*)((LAS char*)smem) + (w ^ 4) * 1152;
  f32x16 o; float e;
#pragma unroll 1
  for (int pass = 0; pass < 2; ++pass) {
    if (pass) {
      __syncthreads();
      DMA_ST(0); DMA_ST(1); DMA_ST(2);
      oA = zero16; oB = zero16; lA = (f32x4){0.f, 0.f, 0.f, 0.f}; lB = lA;
      mA = -3.0e38f; mB = -3.0e38f;
#pragma unroll 1
      for (int st = 0; st < 8; ++st) {
        if (st < 6) WAITBAR(8); else if (st == 6) WAITBAR(4); else WAITBAR(0);
        if (st < 5) DMA_ST(st + 3);
#pragma unroll 1
        for (int u = 0; u < 4; ++u) {
          bf16x8 k0, k1, v0, v1;
          LDK(k0, k1, L + (st & 3) * AT_STAGE + koff + u * 2048);
          LDV(v0, v1, L + (st & 3) * AT_STAGE + voffl + u * 2048);
#define SAFE_UNIT(Q0, Q1, M, O, LS) do { \
          f32x16 s_ = MFMA32(k0, Q0, zero16); s_ = MFMA32(k1, Q1, s_); \
          const float mn_ = fmaxf(M, max16(s_)); const float al_ = EXP2(M - mn_); M = mn_; \
          O *= al_; LS *= al_; u32x4 p0_, p1_; \
          _Pragma("unroll") for (int r = 0; r < 4; ++r) { p0_[r] = cvtpk(EXP2(s_[2 * r] - mn_), EXP2(s_[2 * r + 1] - mn_)); \
            p1_[r] = cvtpk(EXP2(s_[8 + 2 * r] - mn_), EXP2(s_[9 + 2 * r] - mn_)); } \
          O = MFMA32(v0, BF(p0_), O); LS = MFMA16(sel, BF(p0_), LS); O = MFMA32(v1, BF(p1_), O); LS = MFMA16(sel, BF(p1_), LS); } while (0)
          SAFE_UNIT(qa0, qa1, mA, oA, lA);
          SAFE_UNIT(qb0, qb1, mB, oB, lB);
#undef SAFE_UNIT
        }
      }
    }
    float chk = 0.f;
#pragma unroll
    for (int r = 0; r < 16; ++r) { chk = __builtin_fmaf(oA[r], 0.f, chk); chk = __builtin_fmaf(oB[r], 0.f, chk); }
    const bool bad = !(lA[0] < 1.0e36f) || !(lB[0] < 1.0e36f) || !(chk == 0.f);
    if (pass == 0 && __any(bad) && lane == 0) *flag = 1u;
    const float rA = __builtin_amdgcn_rcpf(lA[0]), rB = __builtin_amdgcn_rcpf(lB[0]);
    const float eA = mA + __builtin_amdgcn_logf(lA[0]), eB = mB + __builtin_amdgcn_logf(lB[0]);
    if (kh) {
#pragma unroll
      for (int r = 0; r < 4; ++r) *(LAS f32x4*)(xw + r * 256 + lane * 4) = (f32x4){oA[4 * r], oA[4 * r + 1], oA[4 * r + 2], oA[4 * r + 3]} * rA;
      xw[1024 + lane] = eA; o = oB * rB; e = eB;
    } else {
#pragma unroll
      for (int r = 0; r < 4; ++r) *(LAS f32x4*)(xw + r * 256 + lane * 4) = (f32x4){oB[4 * r], oB[4 * r + 1], oB[4 * r + 2], oB[4 * r + 3]} * rB;
      xw[1024 + lane] = eB; o = oA * rA; e = eA;
    }
    __syncthreads();
    if (pass == 1 || *flag == 0u) break;
  }
#undef LDK
#undef LDV
#undef DMA_ST
#undef BF
  {
    const float e2 = xr[1024 + lane];
    const float em = fmaxf(e, e2);
    float w1 = EXP2(e - em), w2 = EXP2(e2 - em);
    const float rs = __builtin_amdgcn_rcpf(w1 + w2);
    w1 *= rs; w2 *= rs;
    u32x2 pk[4];
#pragma unroll
    for (int r = 0; r < 4; ++r) {
      const f32x4 t = *(const LAS f32x4*)(xr + r * 256 + lane * 4);
      pk[r].x = cvtpk(o[4 * r] * w1 + t[0] * w2, o[4 * r + 1] * w1 + t[1] * w2);
      pk[r].y = cvtpk(o[4 * r + 2] * w1 + t[2] * w2, o[4 * r + 3] * w1 + t[3] * w2);
    }
    const int qrow = qb * 256 + qg * 64 + kh * 32 + (lane & 31);
    bf16_t* op = AH + (size_t)qrow * KA + h * 32 + 8 * hi;
#pragma unroll
    for (int k = 0; k < 4; k += 2) {
      auto rx = __builtin_amdgcn_permlane32_swap(pk[k].x, pk[k + 1].x, false, false);
      auto ry = __builtin_amdgcn_permlane32_swap(pk[k].y, pk[k + 1].y, false, false);
      u32x4 v = {rx[0], ry[0], rx[1], ry[1]};
      *(u32x4*)(op + 8 * k) = v;
    }
  }
  STAMP(2, 3);
}

constexpr int G2_SLOT = 32768, G2_LDS = 4 * G2_SLOT;
constexpr int G2_NCH = 36;

__global__ __launch_bounds__(768, 3) void gemm2_kernel(const bf16_t* __restrict__ AH, const bf16_t* __restrict__ wo,
                                                       const bf16_t* __restrict__ wd, const bf16_t* __restrict__ x,
                                                       bf16_t* __restrict__ out, float* part, unsigned* flags) {
  extern __shared__ __attribute__((aligned(16))) char smem[];
  const int tid = threadIdx.x, lane = tid & 63;
  const int w = __builtin_amdgcn_readfirstlane(tid >> 6);
  const int g = w >> 2, w4 = w & 3, wm = w4 >> 1, wn = w4 & 1;
  const int bid = blockIdx.x, xcd = bid & 7, jj = bid >> 3;
  const int kh = xcd & 1, rt = (xcd >> 1) * 4 + (jj >> 3), ct = jj & 7;
  const int row0 = rt * 128, col0 = ct * 128;
  STAMP(3, 0);
  const unsigned lds0 = (unsigned)(uintptr_t)smem;
  const lds_cptr L = (lds_cptr)smem;

  if (g == 2) {
    const unsigned avoff0 = (unsigned)((lane >> 3) * (KA * 2) + (((lane & 7) ^ ((lane >> 4) + 2)) * 16)), avoff1 = avoff0 ^ 64u;
    const char* abase = (const char*)AH + (size_t)(row0 + 32 * w4) * (KA * 2) + (size_t)kh * (G2_NCH * 128);
    const unsigned bvoff = (unsigned)((lane >> 4) * 2048 + (((lane & 15) ^ ((lane >> 4) << 1) ^ ((w4 & 1) << 3)) * 16));
    const int rot = G2_ROT * (xcd >> 1);
#define DMA_CH(c) do { int cc_ = (c) + rot; if (cc_ >= G2_NCH) cc_ -= G2_NCH; const int kg_ = kh * (G2_NCH * 64) + cc_ * 64; \
    const char* bb_ = (kg_ < DM ? (const char*)wo + (size_t)kg_ * 2048 : (const char*)wd + (size_t)(kg_ - DM) * 2048) + col0 * 2 + w4 * 32768; \
    const unsigned d_ = lds0 + (2 * ((c) & 1) + (((c) >> 1) & 1)) * G2_SLOT + w4 * 4096; \
    _Pragma("unroll") for (int _i = 0; _i < 4; ++_i) { \
      glds16s(abase + (size_t)_i * (8 * KA * 2) + cc_ * 128, (_i & 1) ? avoff1 : avoff0, d_ + _i * 1024); \
      glds16s(bb_ + _i * 8192, bvoff, d_ + 16384 + _i * 1024); } } while (0)
    DMA_CH(0); DMA_CH(1); DMA_CH(2);
    asm volatile("s_waitcnt vmcnt(16)\n\ts_barrier" ::: "memory");
#pragma unroll 1
    for (int i = 0; i < G2_NCH - 3; ++i) {
      DMA_CH(i + 3);
      asm volatile("s_waitcnt vmcnt(16)\n\ts_barrier" ::: "memory");
    }
    asm volatile("s_waitcnt vmcnt(8)\n\ts_barrier" ::: "memory");
    asm volatile("s_waitcnt vmcnt(0)\n\ts_barrier" ::: "memory");
    asm volatile("s_barrier" ::: "memory");
    asm volatile("s_barrier" ::: "memory");
    return;
#undef DMA_CH
  }

  const int fr = lane & 15, fq = lane >> 4, kq = ((fq & 1) << 1) | (fq >> 1), q4 = fr >> 2;
  const int aoff0 = 2 * g * G2_SLOT + (64 * wm + fr) * 128 + ((kq ^ (((fr >> 1) + 2) & 7)) * 16);
  lds_cptr pa0 = L + aoff0, pa1 = L + (aoff0 ^ 64);
  const int boff0 = 2 * g * G2_SLOT + 16384 + (8 * kq + q4) * 256 + 8 * (lane & 3);
  const int bsw = q4 | ((fq & 1) << 2);
  lds_cptr pb[4];
#pragma unroll
  for (int n = 0; n < 4; ++n) pb[n] = L + (boff0 + (((4 * wn + n) ^ bsw) * 32));
  f32x4 acc[4][4];
#pragma unroll
  for (int m = 0; m < 4; ++m)
#pragma unroll
    for (int n = 0; n < 4; ++n) { acc[m][n] = (f32x4){0.f, 0.f, 0.f, 0.f}; asm volatile("" : "+v"(acc[m][n])); }
  bf16x8 af[2][4], bfr[2][4];
  BARRIER();
  if (g == 1) BARRIER();
  STAMP(3, 1);
#define G2STEP(H) do { \
    _Pragma("unroll") for (int ks = 0; ks < 2; ++ks) { \
      _Pragma("unroll") for (int m = 0; m < 4; ++m) af[ks][m] = lds_rd128((ks ? pa1 : pa0) + (H) * G2_SLOT + m * 2048); \
      _Pragma("unroll") for (int n = 0; n < 4; ++n) bfr[ks][n] = cat8(lds_tr(pb[n] + (H) * G2_SLOT + ks * 8192), lds_tr(pb[n] + (H) * G2_SLOT + ks * 8192 + 1024)); } \
    SB(); asm volatile("s_waitcnt lgkmcnt(0)\n\ts_barrier" ::: "memory"); SB(); \
    _Pragma("unroll") for (int ks = 0; ks < 2; ++ks) _Pragma("unroll") for (int n = 0; n < 4; ++n) _Pragma("unroll") for (int m = 0; m < 4; ++m) \
      MFMA16A(acc[m][n], bfr[ks][n], af[ks][m]); \
    BARRIER(); } while (0)
#pragma unroll 1
  for (int c = 0; c < G2_NCH / 4; ++c) { G2STEP(0); G2STEP(1); }
#undef G2STEP
  if (g == 0) BARRIER();
  asm volatile("s_nop 7\n\ts_nop 7" ::: "memory");
  SB();
  STAMP(3, 2);
  int tz = threadIdx.x; asm volatile("" : "+v"(tz));
  const int orow = row0 + 64 * wm + 16 * (2 * g + kh) + (tz & 15), ocol = col0 + 64 * wn + 4 * ((tz >> 4) & 3);
  u32x2 xv[4];
#pragma unroll
  for (int n = 0; n < 4; ++n) xv[n] = *(const u32x2*)(x + (size_t)orow * DM + ocol + 16 * n);

  {
    LAS f32x4* xb = (LAS f32x4*)((LAS char*)smem) + (size_t)w * 512;
    f32x4 s[2][4];
    if (g) {
#pragma unroll
      for (int mm = 0; mm < 2; ++mm)
#pragma unroll
        for (int n = 0; n < 4; ++n) { xb[(mm * 4 + n) * 64 + lane] = acc[mm][n]; s[mm][n] = acc[2 + mm][n]; }
    } else {
#pragma unroll
      for (int mm = 0; mm < 2; ++mm)
#pragma unroll
        for (int n = 0; n < 4; ++n) { xb[(mm * 4 + n) * 64 + lane] = acc[2 + mm][n]; s[mm][n] = acc[mm][n]; }
    }
    SB(); asm volatile("s_waitcnt lgkmcnt(0)\n\ts_barrier" ::: "memory"); SB();
    LAS f32x4* xr = (LAS f32x4*)((LAS char*)smem) + (size_t)(w ^ 4) * 512;
    f32x4 keep[4], send[4];
    if (kh) {
#pragma unroll
      for (int n = 0; n < 4; ++n) { send[n] = s[0][n] + xr[n * 64 + lane]; keep[n] = s[1][n] + xr[(4 + n) * 64 + lane]; }
    } else {
#pragma unroll
      for (int n = 0; n < 4; ++n) { send[n] = s[1][n] + xr[(4 + n) * 64 + lane]; keep[n] = s[0][n] + xr[n * 64 + lane]; }
    }
    const int pbid = bid ^ 1;
    float* mine = part + (size_t)bid * 8192 + w * 1024 + lane * 4;
#pragma unroll
    for (int n = 0; n < 4; ++n)
      asm volatile("global_store_dwordx4 %0, %1, off offset:%2 sc1\n\ts_nop 1" :: "v"(mine), "v"(send[n]), "i"(n * 1024) : "memory");
    asm volatile("s_waitcnt vmcnt(0)" ::: "memory");
    if (lane == 0) __hip_atomic_store(flags + bid * 64 + w, 1u, __ATOMIC_RELAXED, __HIP_MEMORY_SCOPE_AGENT);
    {
      unsigned spins = 0;
      while (__builtin_amdgcn_readfirstlane(__hip_atomic_load(flags + pbid * 64 + w, __ATOMIC_RELAXED, __HIP_MEMORY_SCOPE_AGENT)) == 0u) {
        __builtin_amdgcn_s_sleep(1);
        if (++spins > (1u << 22)) break;
      }
    }
    STAMP(3, 4);
    const float* theirs = part + (size_t)pbid * 8192 + w * 1024 + lane * 4;
    f32x4 pv[4];
    asm volatile("global_load_dwordx4 %0, %4, off sc1\n\tglobal_load_dwordx4 %1, %4, off offset:1024 sc1\n\t"
                 "global_load_dwordx4 %2, %4, off offset:2048 sc1\n\tglobal_load_dwordx4 %3, %4, off offset:3072 sc1\n\t"
                 "s_waitcnt vmcnt(0)"
                 : "=&v"(pv[0]), "=&v"(pv[1]), "=&v"(pv[2]), "=&v"(pv[3]) : "v"(theirs) : "memory");
#pragma unroll
    for (int n = 0; n < 4; ++n) {
      const f32x4 v = keep[n] + pv[n];
      u32x2 o = {cvtpk(bf_lo(xv[n].x) + v[0], bf_hi(xv[n].x) + v[1]), cvtpk(bf_lo(xv[n].y) + v[2], bf_hi(xv[n].y) + v[3])};
      *(u32x2*)(out + (size_t)orow * DM + ocol + 16 * n) = o;
    }
  }
  STAMP(3, 3);
}

extern "C" void kernel_launch(void* const* d_in, const int* in_sizes, int n_in, void* d_out, int out_size, void* d_ws,
                              size_t ws_size, hipStream_t stream) {
  const bf16_t* x = (const bf16_t*)d_in[0];
  const bf16_t* wqkv = (const bf16_t*)d_in[1];
  const bf16_t* wo = (const bf16_t*)d_in[2];
  const bf16_t* wg = (const bf16_t*)d_in[4];
  const bf16_t* wu = (const bf16_t*)d_in[5];
  const bf16_t* wd = (const bf16_t*)d_in[6];
  char* ws = (char*)d_ws;
  bf16_t* xn = (bf16_t*)(ws + WS_XN);
  bf16_t* Qb = (bf16_t*)(ws + WS_Q);
  bf16_t* Kb = (bf16_t*)(ws + WS_K);
  bf16_t* Vb = (bf16_t*)(ws + WS_V);
  bf16_t* AH = (bf16_t*)(ws + WS_AH);
  float* part = (float*)(ws + WS_PART);
  unsigned* flags = (unsigned*)(ws + WS_FLAGS);
  (void)hipFuncSetAttribute((const void*)gemm1_kernel, hipFuncAttributeMaxDynamicSharedMemorySize, G1_LDS);
  (void)hipFuncSetAttribute((const void*)attn_kernel, hipFuncAttributeMaxDynamicSharedMemorySize, AT_LDS);
  (void)hipFuncSetAttribute((const void*)gemm2_kernel, hipFuncAttributeMaxDynamicSharedMemorySize, G2_LDS);
  ln_kernel<<<SEQ / 4, 256, 0, stream>>>(x, xn, flags);
  gemm1_kernel<<<256, 512, G1_LDS, stream>>>(xn, wqkv, wg, wu, Qb, Kb, Vb, AH);
  attn_kernel<<<256, 512, AT_LDS, stream>>>(Qb, Kb, Vb, AH, wo, wd);
  gemm2_kernel<<<256, 768, G2_LDS, stream>>>(AH, wo, wd, x, (bf16_t*)d_out, part, flags);
}
```

```cpp
#include <hip/hip_runtime.h>
#include <hip/hip_bf16.h>
#include <stdint.h>

typedef unsigned short bf16_t;
typedef short bf16x8 __attribute__((ext_vector_type(8)));
typedef short s16x4 __attribute__((ext_vector_type(4)));
typedef float f32x4 __attribute__((ext_vector_type(4)));
typedef float f32x16 __attribute__((ext_vector_type(16)));
typedef float f32x2 __attribute__((ext_vector_type(2)));
typedef __bf16 bf16x2v __attribute__((ext_vector_type(2)));
typedef unsigned u32x4 __attribute__((ext_vector_type(4)));
typedef unsigned u32x2 __attribute__((ext_vector_type(2)));
#define LAS __attribute__((address_space(3)))
typedef LAS const char* lds_cptr;

constexpr int SEQ = 2048, DM = 1024, FF = 3584, NQKV = 3072, KA = 4608;

constexpr size_t WS_FLAGS = 0;
constexpr size_t WS_XN = 1u << 20;
constexpr size_t WS_Q = WS_XN + (size_t)SEQ * DM * 2;
constexpr size_t WS_K = WS_Q + (size_t)SEQ * DM * 2;
constexpr size_t WS_V = WS_K + (size_t)SEQ * DM * 2;
constexpr size_t WS_AH = WS_V + (size_t)SEQ * DM * 2;
constexpr size_t WS_PART = WS_AH + (size_t)SEQ * KA * 2;
constexpr int FA_OFF = 0, FB_OFF = 256, FH_OFF = 512, FX_OFF = 1024;

#define SB() __builtin_amdgcn_sched_barrier(0)
#define STAMP(kid, i) do {} while (0)

__device__ __forceinline__ unsigned cvtpk(float lo, float hi) {
  f32x2 v = {lo, hi};
  bf16x2v b = __builtin_convertvector(v, bf16x2v);
  return __builtin_bit_cast(unsigned, b);
}
__device__ __forceinline__ float bf_lo(unsigned u) { return __uint_as_float(u << 16); }
__device__ __forceinline__ float bf_hi(unsigned u) { return __uint_as_float(u & 0xffff0000u); }
__device__ __forceinline__ float rbf(float x) { return bf_lo(cvtpk(x, x)); }

__device__ __forceinline__ void glds16(const void* g, unsigned lds_dst) {
  unsigned keep;
  asm volatile("s_mov_b32 %0, m0\n\ts_mov_b32 m0, %2\n\ts_nop 0\n\tglobal_load_lds_dwordx4 %1, off\n\ts_mov_b32 m0, %0"
               : "=&s"(keep) : "v"(g), "s"(lds_dst) : "memory");
}
__device__ __forceinline__ void glds16s(const void* sbase, unsigned voff, unsigned lds_dst) {
  unsigned keep;
  asm volatile("s_mov_b32 %0, m0\n\ts_mov_b32 m0, %3\n\ts_nop 0\n\tglobal_load_lds_dwordx4 %1, %2\n\ts_mov_b32 m0, %0"
               : "=&s"(keep) : "v"(voff), "s"(sbase), "s"(lds_dst) : "memory");
}
__device__ __forceinline__ void glds16c(const void* sbase, unsigned voff, unsigned lds_dst) {
  unsigned keep;
  asm volatile("s_mov_b32 %0, m0\n\ts_mov_b32 m0, %3\n\ts_nop 0\n\tglobal_load_lds_dwordx4 %1, %2 sc1\n\ts_mov_b32 m0, %0"
               : "=&s"(keep) : "v"(voff), "s"(sbase), "s"(lds_dst) : "memory");
}
__device__ __forceinline__ void glds4c(const void* g, unsigned lds_dst) {
  unsigned keep;
  asm volatile("s_mov_b32 %0, m0\n\ts_mov_b32 m0, %2\n\ts_nop 0\n\tglobal_load_lds_dword %1, off sc1\n\ts_mov_b32 m0, %0"
               : "=&s"(keep) : "v"(g), "s"(lds_dst) : "memory");
}
__device__ __forceinline__ bf16x8 lds_rd128(lds_cptr p) { return *(const LAS bf16x8*)p; }
__device__ __forceinline__ s16x4 lds_tr(lds_cptr p) { return __builtin_amdgcn_ds_read_tr16_b64_v4i16((LAS s16x4*)p); }
__device__ __forceinline__ bf16x8 cat8(s16x4 a, s16x4 b) { return __builtin_shufflevector(a, b, 0, 1, 2, 3, 4, 5, 6, 7); }
__device__ __forceinline__ void st16wt(void* p, u32x4 v) {
  asm volatile("global_store_dwordx4 %0, %1, off sc1\n\ts_nop 1" :: "v"(p), "v"(v) : "memory");
}
__device__ __forceinline__ void wait_flags(const unsigned* f) {
  unsigned spins = 0;
  while (!__all(__hip_atomic_load(f, __ATOMIC_RELAXED, __HIP_MEMORY_SCOPE_AGENT) != 0u)) {
    __builtin_amdgcn_s_sleep(4);
    if (++spins > (1u << 20)) break;
  }
  asm volatile("" ::: "memory");
}
__device__ __forceinline__ void publish(LAS unsigned* cnt, unsigned* flag, int lane) {
  asm volatile("s_waitcnt vmcnt(0)" ::: "memory");
  if (lane == 0) {
    const unsigned old = __hip_atomic_fetch_add(cnt, 1u, __ATOMIC_RELAXED, __HIP_MEMORY_SCOPE_WORKGROUP);
    if (old == 7u) __hip_atomic_store(flag, 1u, __ATOMIC_RELAXED, __HIP_MEMORY_SCOPE_AGENT);
  }
}

#define MFMA16(a, b, c) __builtin_amdgcn_mfma_f32_16x16x32_bf16(a, b, c, 0, 0, 0)
#define MFMA32(a, b, c) __builtin_amdgcn_mfma_f32_32x32x16_bf16(a, b, c, 0, 0, 0)
#define WAITBAR(N) do { SB(); asm volatile("s_waitcnt vmcnt(" #N ") lgkmcnt(0)\n\ts_barrier" ::: "memory"); SB(); } while (0)
#define VMBAR(N) do { SB(); asm volatile("s_waitcnt vmcnt(" #N ")\n\ts_barrier" ::: "memory"); SB(); } while (0)
#define LGBAR() do { SB(); asm volatile("s_waitcnt lgkmcnt(0)\n\ts_barrier" ::: "memory"); SB(); } while (0)
#define BARRIER() do { SB(); asm volatile("s_barrier" ::: "memory"); SB(); } while (0)
#define MFMA16A(acc, a, b) asm volatile("v_mfma_f32_16x16x32_bf16 %0, %1, %2, %0" : "+v"(acc) : "v"(a), "v"(b))
#define ADV64(p, inc) asm volatile("v_mad_u64_u32 %0, vcc, %1, 1, %0" : "+v"(p) : "v"(inc) : "vcc")
#define PIN(x) asm volatile("" : "+v"(x))
#define EXP2(x) __builtin_amdgcn_exp2f(x)

__global__ __launch_bounds__(256) void ln_kernel(const bf16_t* __restrict__ x, bf16_t* __restrict__ xn, unsigned* flags,
                                                 const bf16_t* __restrict__ wqkv, const bf16_t* __restrict__ wg, const bf16_t* __restrict__ wu) {
  const int lane = threadIdx.x & 63, w = threadIdx.x >> 6;
  const int row = blockIdx.x * 4 + w;
  const u32x4* xr = (const u32x4*)(x + (size_t)row * DM);
  u32x4 a = xr[lane], b = xr[64 + lane];
  {
    __shared__ __attribute__((aligned(16))) char sink[1024];
    const unsigned sd = (unsigned)(uintptr_t)sink;
    const int wv = __builtin_amdgcn_readfirstlane(blockIdx.x * 4 + w);
    const int k = wv >> 1, hf = wv & 1;
    const char* q = (const char*)wqkv + (size_t)k * (NQKV * 2) + hf * 3072;
    const char* gp = (const char*)(hf ? wu : wg) + (size_t)k * (FF * 2);
#pragma unroll
    for (int i = 0; i < 3; ++i) glds16s(q + i * 1024, lane * 16, sd);
#pragma unroll
    for (int i = 0; i < 7; ++i) glds16s(gp + i * 1024, lane * 16, sd);
  }
  if (blockIdx.x < 64) flags[blockIdx.x * 256 + threadIdx.x] = 0u;
  float v[16];
#pragma unroll
  for (int i = 0; i < 4; ++i) { v[2 * i] = bf_lo(a[i]); v[2 * i + 1] = bf_hi(a[i]); v[8 + 2 * i] = bf_lo(b[i]); v[9 + 2 * i] = bf_hi(b[i]); }
  float s = 0.f;
#pragma unroll
  for (int i = 0; i < 16; ++i) s += v[i];
#pragma unroll
  for (int o = 1; o < 64; o <<= 1) s += __shfl_xor(s, o);
  const float mean = s * (1.f / DM);
  float q = 0.f;
#pragma unroll
  for (int i = 0; i < 16; ++i) { const float d = v[i] - mean; q += d * d; }
#pragma unroll
  for (int o = 1; o < 64; o <<= 1) q += __shfl_xor(q, o);
  const float mb = rbf(mean), vb = rbf(q * (1.f / DM));
  const float den = rbf(sqrtf(rbf(vb + 1e-5f)));
  u32x4 oa, ob;
#pragma unroll
  for (int i = 0; i < 4; ++i) {
    oa[i] = cvtpk(rbf(v[2 * i] - mb) / den, rbf(v[2 * i + 1] - mb) / den);
    ob[i] = cvtpk(rbf(v[8 + 2 * i] - mb) / den, rbf(v[9 + 2 * i] - mb) / den);
  }
  u32x4* o = (u32x4*)(xn + (size_t)row * DM);
  o[lane] = oa; o[64 + lane] = ob;
}

constexpr int G1_AOFF = 0, G1_BOFF = 65536, G1_BREG = 10304, G1_BSLOT = 2 * G1_BREG;
constexpr int AT_STAGE = 32768, AT_SCR = 131072, AT_EX = 65536, AT_EE = 132096, AT_HF = 134144;
constexpr int G2_SLOT = 32768, G2_NCH = 36;
constexpr int LDS_CTRL = 148480, LDS_TOTAL = LDS_CTRL + 64;

__device__ __forceinline__ float max16(const f32x16& p) {
  float a = fmaxf(fmaxf(p[0], p[1]), p[2]), b = fmaxf(fmaxf(p[3], p[4]), p[5]);
  a = fmaxf(fmaxf(a, p[6]), p[7]); b = fmaxf(fmaxf(b, p[8]), p[9]);
  a = fmaxf(fmaxf(a, p[10]), p[11]); b = fmaxf(fmaxf(b, p[12]), p[13]);
  a = fmaxf(fmaxf(a, p[14]), p[15]);
  float m = fmaxf(a, b);
  auto rr = __builtin_amdgcn_permlane32_swap(__float_as_uint(m), __float_as_uint(m), false, false);
  return fmaxf(__uint_as_float(rr[0]), __uint_as_float(rr[1]));
}
__device__ __forceinline__ f32x16 splat16(float v) { f32x16 r;
#pragma unroll
  for (int i = 0; i < 16; ++i) r[i] = v;
  return r; }

struct Params {
  const bf16_t *x, *xn, *wqkv, *wg, *wu, *wo, *wd;
  bf16_t *Qb, *Kb, *Vb, *AH, *out;
  float* part; unsigned* flags;
};

template <int PH_LO, int PH_HI>
__global__ __launch_bounds__(512, 2) void fused_kernel(Params P) {
  extern __shared__ __attribute__((aligned(16))) char smem[];
  const int w = __builtin_amdgcn_readfirstlane(threadIdx.x >> 6);
  const int g = w >> 2, w4 = w & 3;
  const int bid = blockIdx.x, xcd = bid & 7, jj = bid >> 3;
  const int cg = xcd * 4 + (jj & 3), rt = jj >> 2;
  const unsigned lds0 = (unsigned)(uintptr_t)smem;
  const lds_cptr L = (lds_cptr)smem;
  LAS unsigned* ctrl = (LAS unsigned*)((LAS char*)smem + LDS_CTRL);
  if (threadIdx.x < 4) ctrl[threadIdx.x] = 0u;

  u32x4 hv[8];
  bf16_t* hp0 = nullptr; bf16_t* hp1 = nullptr;
#define HSTORE(i) st16wt((i) < 4 ? hp0 + (size_t)(i) * 16 * KA : hp1 + (size_t)((i) - 4) * ((w4 & 1) ? 32 * KA : 16 * KA), hv[i])

  if (PH_LO <= 0) {
    const int lane = threadIdx.x & 63;
    const int wm = 2 * g + (w4 >> 1), wn = w4 & 1;
    STAMP(1, 0);
    const int row0 = rt * 256;
    unsigned avoff0 = (unsigned)((lane >> 3) * 2048 + (((lane & 7) ^ ((lane >> 4) + 2)) * 16)), avoff1 = avoff0 ^ 64u;
    const char* abase = (const char*)P.xn + (size_t)(row0 + 32 * w4) * 2048;
#define DMA_A(hh, t) do { _Pragma("unroll") for (int _i = 0; _i < 4; ++_i) \
    glds16s(abase + (size_t)(hh) * 262144 + _i * 16384 + (t) * 128, (_i & 1) ? avoff1 : avoff0, \
            lds0 + G1_AOFF + ((t) & 1) * 32768 + (hh) * 16384 + (4 * w4 + _i) * 1024); } while (0)
    DMA_A(g, 0);
    const char* bptr[4]; unsigned bld[4]; unsigned bdst[4];
#pragma unroll
    for (int i = 0; i < 4; ++i) {
      const int b = (i == 0) ? (g ? 16 + w4 : 4 * w4) : 4 * w4 + i;
      const int region = b >= 10 ? 1 : 0, bi = b - 10 * region;
      const int Pc = bi * 64 + lane;
      const int k = Pc / 20, pc = Pc - 20 * k, f = pc >> 1, e8 = (pc & 1) * 8;
      const bool isq = f >= 8 || (region && (f & 3) == 3);
      const int qf = f >= 8 ? (region ? f - 4 : f - 8) : 2 + (f >> 2);
      const bf16_t* mat = isq ? P.wqkv : (f >= 4 ? P.wu : P.wg);
      const int ld = isq ? NQKV : FF;
      const int col = e8 + (isq ? 96 * cg + 16 * qf : 112 * cg + 64 * region + 16 * (f & 3));
      bptr[i] = (const char*)(mat + (size_t)k * ld + col);
      bld[i] = (unsigned)ld * 64u;
      bdst[i] = lds0 + G1_BOFF + region * G1_BREG + bi * 1024 + (bi >= 5 ? 32 : 0);
    }
#define DMA_B(i, s) glds16(bptr[i], bdst[i] + ((s) & 3) * G1_BSLOT)
#define ADV_B() do { ADV64(bptr[0], bld[0]); ADV64(bptr[1], bld[1]); ADV64(bptr[2], bld[2]); ADV64(bptr[3], bld[3]); } while (0)
    if (g == 0) {
      DMA_B(0, 0); DMA_B(1, 0); DMA_B(2, 0); DMA_B(3, 0); ADV_B();
      DMA_B(0, 1); ADV_B();
      DMA_B(0, 2); DMA_B(1, 2); DMA_B(2, 2); DMA_B(3, 2); ADV_B();
    } else {
      DMA_B(0, 0); ADV_B();
      DMA_B(0, 1); DMA_B(1, 1); DMA_B(2, 1); DMA_B(3, 1); ADV_B();
      DMA_B(0, 2); ADV_B();
      DMA_A(0, 1);
    }
    const int fr = lane & 15, fq = lane >> 4, kq = ((fq & 1) << 1) | (fq >> 1);
    const int aoff0 = G1_AOFF + (64 * wm + fr) * 128 + ((kq ^ (((fr >> 1) + 2) & 7)) * 16);
    lds_cptr pa0 = L + aoff0, pa1 = L + (aoff0 ^ 64);
    lds_cptr pb0 = L + (G1_BOFF + (8 * kq + (fr >> 2)) * 320 + (fq & 1) * 32 + 8 * (lane & 3) + G1_BREG * wn);
    lds_cptr pb1 = pb0 + 2 * G1_BSLOT;
    asm volatile("" : "+v"(pa0), "+v"(pa1), "+v"(pb0), "+v"(pb1), "+v"(avoff0), "+v"(avoff1));
    f32x4 acc[4][10];
#pragma unroll
    for (int m = 0; m < 4; ++m)
#pragma unroll
      for (int j = 0; j < 10; ++j) { acc[m][j] = (f32x4){0.f, 0.f, 0.f, 0.f}; asm volatile("" : "+v"(acc[m][j])); }
    bf16x8 af[4], bfr[10];
    if (g == 0) { WAITBAR(5); } else { WAITBAR(9); BARRIER(); }
    STAMP(1, 1);
#define STEP(U, ISSUE, WN, LASTBAR) do { \
    { const lds_cptr pa = ((U) & 1) ? pa1 : pa0; const lds_cptr pb = (((U) & 2) ? pb1 : pb0) + ((U) & 1) * G1_BSLOT; \
      _Pragma("unroll") for (int m = 0; m < 4; ++m) af[m] = lds_rd128(pa + ((U) >> 1) * 32768 + m * 2048); \
      _Pragma("unroll") for (int j = 0; j < 10; ++j) bfr[j] = cat8(lds_tr(pb + j * 32), lds_tr(pb + j * 32 + 1280)); } \
    if (ISSUE) { DMA_B(0, (U) + 3); \
      if (g == ((U) & 1)) { DMA_A(1 - ((U) & 1), 2 * it + ((U) >> 1) + 1 + ((U) & 1)); } \
      else { DMA_B(1, (U) + 3); DMA_B(2, (U) + 3); DMA_B(3, (U) + 3); } } \
    WAITBAR(WN); \
    _Pragma("unroll") for (int j = 0; j < 10; ++j) { _Pragma("unroll") for (int m = 0; m < 4; ++m) MFMA16A(acc[m][j], bfr[j], af[m]); \
      if ((ISSUE) && (j & 1) && j < 8) ADV64(bptr[j >> 1], bld[j >> 1]); } \
    if (LASTBAR) BARRIER(); } while (0)
    int it = 0;
#pragma unroll 1
    for (; it < 7; ++it) { STEP(0, true, 9, true); STEP(1, true, 9, true); STEP(2, true, 9, true); STEP(3, true, 9, true); }
    STEP(0, true, 9, true); STEP(1, false, 4, true); STEP(2, false, 0, true); STEP(3, false, 0, g == 0);
#undef STEP
    asm volatile("s_nop 7\n\ts_nop 7" ::: "memory");
    SB();
    STAMP(1, 2);
#undef DMA_A
#undef DMA_B
#undef ADV_B
    int tz = threadIdx.x; asm volatile("" : "+v"(tz));
    const int fr2 = tz & 15, fq2 = (tz >> 4) & 3, sel1 = fq2 & 1, c8 = (fq2 >> 1) * 8;
    const float qscale = 0.17677669529663687f * 1.4426950408889634f;
#define SWAP16(A, B, OUT) do { auto r0_ = __builtin_amdgcn_permlane16_swap(A.x, B.x, false, false); \
    auto r1_ = __builtin_amdgcn_permlane16_swap(A.y, B.y, false, false); OUT = (u32x4){r0_[0], r1_[0], r0_[1], r1_[1]}; } while (0)
#define SILU4(m, j, OUT) do { float hv_[4]; _Pragma("unroll") for (int r = 0; r < 4; ++r) { \
      const float tg_ = acc[m][j][r], tu_ = acc[m][(j) + 4][r]; \
      hv_[r] = (tg_ * tu_) * __builtin_amdgcn_rcpf(1.f + __builtin_amdgcn_exp2f(-1.4426950408889634f * tg_)); } \
    OUT = (u32x2){cvtpk(hv_[0], hv_[1]), cvtpk(hv_[2], hv_[3])}; } while (0)
#define PK4(V, SC, OUT) do { const f32x4 v_ = (V) * (SC); OUT = (u32x2){cvtpk(v_[0], v_[1]), cvtpk(v_[2], v_[3])}; } while (0)
    const int rowb = row0 + 64 * wm + fr2;
#define QKV_HEAD(hd, ja, jb) do { const int typ_ = (hd) >> 5, h_ = (hd) & 31; const float sa = typ_ == 0 ? qscale : 1.f; \
      bf16_t* dst; int mstep; \
      if (typ_ == 1) { dst = P.Kb + ((((size_t)h_ * 64 + (rowb >> 5)) * 2 + sel1) * 64 + (rowb & 31) + 4 * c8) * 8; mstep = 128; } \
      else { dst = (typ_ == 0 ? P.Qb : P.Vb) + ((size_t)h_ * SEQ + rowb) * 32 + 16 * sel1 + c8; mstep = 512; } \
      _Pragma("unroll") for (int m = 0; m < 4; ++m) { \
        u32x2 a_, b_; u32x4 o; PK4(acc[m][ja], sa, a_); PK4(acc[m][jb], sa, b_); SWAP16(a_, b_, o); \
        st16wt(dst + (m & 1) * mstep + (m >> 1) * 1024, o); } } while (0)
    __builtin_amdgcn_s_setprio(3);
    QKV_HEAD(3 * cg + 2 * wn, 8, 9);
    if (wn) QKV_HEAD(3 * cg + 1, 3, 7);
    __builtin_amdgcn_s_setprio(0);
    SB();
    bf16_t* hd = P.AH + (size_t)rowb * KA + DM + 112 * cg + 64 * wn + c8;
    hp0 = hd + 16 * sel1;
    hp1 = wn ? hd + 32 + (size_t)sel1 * 16 * KA : hd + 32 + 16 * sel1;
#pragma unroll
    for (int m = 0; m < 4; ++m) { u32x2 a_, b_; SILU4(m, 0, a_); SILU4(m, 1, b_); SWAP16(a_, b_, hv[m]); PIN(hv[m]); }
    STAMP(1, 4);
    publish(ctrl + 0, P.flags + FA_OFF + cg * 8 + rt, tz & 63);
    if (wn == 0) {
#pragma unroll
      for (int m = 0; m < 4; ++m) { u32x2 a_, b_; SILU4(m, 2, a_); SILU4(m, 3, b_); SWAP16(a_, b_, hv[4 + m]); }
    } else {
#pragma unroll
      for (int mp = 0; mp < 2; ++mp) { u32x2 a_, b_; SILU4(2 * mp, 2, a_); SILU4(2 * mp + 1, 2, b_); SWAP16(a_, b_, hv[4 + mp]); }
      hv[6] = hv[4]; hv[7] = hv[5];
    }
    if (PH_HI == 0) { HSTORE(0); HSTORE(1); HSTORE(2); HSTORE(3); HSTORE(4); HSTORE(5); if (wn == 0) { HSTORE(6); HSTORE(7); } }
#undef SWAP16
#undef SILU4
#undef PK4
#undef QKV_HEAD
    if (PH_HI == 0 && threadIdx.x == 0) P.flags[FH_OFF + cg * 8 + rt] = 1u;
    STAMP(1, 3);
  } else {
    __syncthreads();
  }

  const int kh2 = xcd & 1, rt2 = (xcd >> 1) * 4 + (jj >> 3), ct2 = jj & 7;
  int c_issued = 0;
#define G2_DMA(c, avo0, avo1, bvo) do { int cc_ = (c) + (kh2 ? 0 : 16); if (cc_ >= G2_NCH) cc_ -= G2_NCH; const int kg_ = kh2 * (G2_NCH * 64) + cc_ * 64; \
    const char* ab_ = (const char*)P.AH + (size_t)(rt2 * 128 + 32 * w4) * (KA * 2) + kg_ * 2; \
    const char* bb_ = (kg_ < DM ? (const char*)P.wo + (size_t)kg_ * 2048 : (const char*)P.wd + (size_t)(kg_ - DM) * 2048) + ct2 * 256 + w4 * 32768; \
    const unsigned d_ = lds0 + ((c) & 3) * G2_SLOT + w4 * 4096; \
    _Pragma("unroll") for (int _i = 0; _i < 4; ++_i) { \
      glds16c(ab_ + (size_t)_i * (8 * KA * 2), (_i & 1) ? avo1 : avo0, d_ + _i * 1024); \
      glds16s(bb_ + _i * 8192, bvo, d_ + 16384 + _i * 1024); } } while (0)
#define G2_OFFS(lane) const unsigned avo0 = (unsigned)(((lane) >> 3) * (KA * 2) + ((((lane) & 7) ^ (((lane) >> 4) + 2)) * 16)), avo1 = avo0 ^ 64u; \
    const unsigned bvo = (unsigned)(((lane) >> 4) * 2048 + ((((lane) & 15) ^ (((lane) >> 4) << 1) ^ ((w4 & 1) << 3)) * 16))

  if (PH_LO <= 1 && PH_HI >= 1) {
    int tb = threadIdx.x; asm volatile("" : "+v"(tb));
    const int lane = tb & 63;
    const int qg = w4, kh = g;
    const int h = xcd * 4 + (jj >> 3), qb = jj & 7;
    STAMP(2, 0);
    {
      const int cgQ = h / 3, cgK = (32 + h) / 3, cgV = (64 + h) / 3;
      const int fi = lane < 8 ? cgK * 8 + lane : (lane < 16 ? cgV * 8 + lane - 8 : cgQ * 8 + qb);
      volatile LAS unsigned* go = (volatile LAS unsigned*)(ctrl + 3);
      if (w == 0) { wait_flags(P.flags + FA_OFF + fi); if (lane == 0) *go = 1u; }
      else { while (*go == 0u) __builtin_amdgcn_s_sleep(1); asm volatile("" ::: "memory"); }
    }
    STAMP(2, 4);
    const int hi = lane >> 5;
    const char* kg = (const char*)(P.Kb + (size_t)h * SEQ * 32) + qg * 2048;
    const char* vg = (const char*)(P.Vb + (size_t)h * SEQ * 32) + qg * 2048;
    const unsigned voff = lane * 16;
#define DMA_ST(st, KH) do { const unsigned d_ = lds0 + ((st) & 3) * AT_STAGE + (KH) * 8192 + qg * 2048; const int o_ = (KH) * 65536 + (st) * 8192; \
    glds16c(kg + o_, voff, d_); glds16c(kg + o_ + 1024, voff, d_ + 1024); \
    glds16c(vg + o_, voff, d_ + 16384); glds16c(vg + o_ + 1024, voff, d_ + 16384 + 1024); } while (0)
    if (g == 0) { DMA_ST(0, 0); DMA_ST(0, 1); }
    const bf16_t* qp = P.Qb + ((size_t)h * SEQ + qb * 256 + qg * 64 + (lane & 31)) * 32 + 8 * hi;
    bf16x8 qa0, qa1, qb0, qb1;
    asm volatile("global_load_dwordx4 %0, %4, off sc1\n\tglobal_load_dwordx4 %1, %4, off offset:32 sc1\n\t"
                 "global_load_dwordx4 %2, %4, off offset:2048 sc1\n\tglobal_load_dwordx4 %3, %4, off offset:2080 sc1\n\ts_waitcnt vmcnt(0)"
                 : "=&v"(qa0), "=&v"(qa1), "=&v"(qb0), "=&v"(qb1) : "v"(qp) : "memory");
    const short one = (((lane >> 4) & 1) == ((lane >> 2) & 1)) ? (short)0x3f80 : (short)0;
    const bf16x8 sel = {one, one, one, one, one, one, one, one};
    volatile LAS unsigned* flag = (volatile LAS unsigned*)(ctrl + 2);
    const int gq = lane >> 4;
    const int koff = kh * 8192 + lane * 16;
    const int voffl = 16384 + kh * 8192 + (4 * (gq >> 1) + ((lane & 15) >> 2)) * 64 + (gq & 1) * 32 + (lane & 3) * 8;
    const f32x16 zero16 = splat16(0.f);
    f32x16 oA = zero16, oB = zero16;
    f32x4 lA = {0.f, 0.f, 0.f, 0.f}, lB = {0.f, 0.f, 0.f, 0.f};
    float mA, mB;
    const unsigned* hflag = P.flags + FH_OFF + (lane & 31) * 8 + (rt2 >> 1);

#define LDK(K0, K1, p) do { K0 = lds_rd128(p); K1 = lds_rd128((p) + 1024); } while (0)
#define LDV(V0, V1, p) do { V0 = cat8(lds_tr(p), lds_tr((p) + 512)); V1 = cat8(lds_tr((p) + 1024), lds_tr((p) + 1536)); } while (0)
#define BF(x) __builtin_bit_cast(bf16x8, x)
    {
      WAITBAR(0);
      bf16x8 kx0, kx1, ky0, ky1, vx0, vx1, vy0, vy1;
      LDK(kx0, kx1, L + koff);
      LDK(ky0, ky1, L + koff + 2048);
      LDV(vy0, vy1, L + voffl);
      DMA_ST(1, kh); DMA_ST(2, kh);
      f32x16 sA = MFMA32(kx0, qa0, zero16); sA = MFMA32(kx1, qa1, sA);
      f32x16 sB = MFMA32(kx0, qb0, zero16); sB = MFMA32(kx1, qb1, sB);
      mA = max16(sA); mB = max16(sB);
      f32x16 nA = splat16(-mA), nB = splat16(-mB);
      PIN(nA); PIN(nB);
      sA = MFMA32(kx0, qa0, nA); sA = MFMA32(kx1, qa1, sA);
      u32x4 pA0, pA1, pB0 = {0u, 0u, 0u, 0u}, pB1 = {0u, 0u, 0u, 0u};
      STAMP(2, 1);
#define E4(S, i) do { S[i] = EXP2(S[i]); S[i + 1] = EXP2(S[i + 1]); S[i + 2] = EXP2(S[i + 2]); S[i + 3] = EXP2(S[i + 3]); } while (0)
#define SLOT(SC, PC0, PC1, SN, K0, K1, Q0, Q1, NM, PP0, PP1, V0, V1, O, LS, LOADS) do { \
    SN = MFMA32(K0, Q0, NM); SB(); E4(SC, 0); PIN(SC); SB(); \
    SN = MFMA32(K1, Q1, SN); SB(); LOADS; SB(); E4(SC, 4); PIN(SC); SB(); \
    O = MFMA32(V0, BF(PP0), O); SB(); PC0[0] = cvtpk(SC[0], SC[1]); PC0[1] = cvtpk(SC[2], SC[3]); \
      SC[8] = EXP2(SC[8]); SC[9] = EXP2(SC[9]); SC[10] = EXP2(SC[10]); PIN(SC); PIN(PC0); SB(); \
    LS = MFMA16(sel, BF(PP0), LS); SB(); PC0[2] = cvtpk(SC[4], SC[5]); PC0[3] = cvtpk(SC[6], SC[7]); SC[11] = EXP2(SC[11]); PIN(SC); PIN(PC0); SB(); \
    O = MFMA32(V1, BF(PP1), O); SB(); E4(SC, 12); PIN(SC); SB(); \
    LS = MFMA16(sel, BF(PP1), LS); SB(); PC1[0] = cvtpk(SC[8], SC[9]); PC1[1] = cvtpk(SC[10], SC[11]); \
      PC1[2] = cvtpk(SC[12], SC[13]); PC1[3] = cvtpk(SC[14], SC[15]); PIN(PC1); SB(); } while (0)
#define SUB(KC0, KC1, KN0, KN1, VC0, VC1, VP0, VP1, vpu, kp2) do { \
    SLOT(sA, pA0, pA1, sB, KC0, KC1, qb0, qb1, nB, pB0, pB1, VP0, VP1, oB, lB, LDV(VC0, VC1, vpu)); \
    SLOT(sB, pB0, pB1, sA, KN0, KN1, qa0, qa1, nA, pA0, pA1, VC0, VC1, oA, lA, LDK(KC0, KC1, kp2)); } while (0)
#pragma unroll 1
      for (int st = 0; st < 8; ++st) {
        const lds_cptr kp = L + (st & 3) * AT_STAGE + koff, vp = L + (st & 3) * AT_STAGE + voffl;
        const lds_cptr kn = L + ((st + 1) & 3) * AT_STAGE + koff;
        SUB(kx0, kx1, ky0, ky1, vx0, vx1, vy0, vy1, vp, kp + 4096);
        SUB(ky0, ky1, kx0, kx1, vy0, vy1, vx0, vx1, vp + 2048, kp + 6144);
        if (st == 0) { VMBAR(4); }
        else if (PH_LO == 0 && st < 4) { VMBAR(7); }
        else if (st < 6) { VMBAR(5); } else { VMBAR(0); }
        if (st < 5) {
          if (PH_LO == 0) {
            if (st == 0) { HSTORE(0); HSTORE(1); } else if (st == 1) { HSTORE(2); HSTORE(3); } else if (st == 2) { HSTORE(4); HSTORE(5); }
            else if (st == 3) { if ((w4 & 1) == 0) { HSTORE(6); HSTORE(7); } }
            else if (w == 0 && lane == 0)
              __hip_atomic_store(P.flags + FH_OFF + cg * 8 + rt, 1u, __ATOMIC_RELAXED, __HIP_MEMORY_SCOPE_AGENT);
          }
          const int pc = st * 8 + w, kb = bid * 36 + (pc < 36 ? pc : 35);
          glds16s(kb < 2048 ? (const char*)P.wo + (size_t)kb * 1024 : (const char*)P.wd + (size_t)(kb - 2048) * 1024, voff, lds0 + AT_SCR);
          DMA_ST(st + 3, kh);
        } else if (PH_HI >= 2 && g == 1) {
          if (st == 5) glds4c(hflag, lds0 + AT_HF + w * 256);
          if (st == 6) {
            const unsigned hf = *(volatile LAS unsigned*)((LAS char*)smem + AT_HF + w * 256 + lane * 4);
            if (__all(hf != 0u)) { G2_OFFS(lane); G2_DMA(0, avo0, avo1, bvo); G2_DMA(1, avo0, avo1, bvo); c_issued = 2; }
          }
        }
        SUB(kx0, kx1, ky0, ky1, vx0, vx1, vy0, vy1, vp + 4096, kn);
        SUB(ky0, ky1, kx0, kx1, vy0, vy1, vx0, vx1, vp + 6144, kn + 2048);
      }
      oB = MFMA32(vy0, BF(pB0), oB); lB = MFMA16(sel, BF(pB0), lB);
      oB = MFMA32(vy1, BF(pB1), oB); lB = MFMA16(sel, BF(pB1), lB);
#undef SUB
#undef SLOT
#undef E4
    }
    STAMP(2, 2);
    LAS float* xw = (LAS float*)((LAS char*)smem + AT_EX) + w * 1024;
    const LAS float* xr = (const LAS float*)((LAS char*)smem + AT_EX) + (w ^ 4) * 1024;
    LAS float* ew = (LAS float*)((LAS char*)smem + AT_EE) + w * 64;
    const LAS float* er = (const LAS float*)((LAS char*)smem + AT_EE) + (w ^ 4) * 64;
    f32x16 o; float e;
#pragma unroll 1
    for (int pass = 0; pass < 2; ++pass) {
      if (pass) {
        WAITBAR(0);
        c_issued = 0;
        DMA_ST(0, kh); DMA_ST(1, kh); DMA_ST(2, kh);
        oA = zero16; oB = zero16; lA = (f32x4){0.f, 0.f, 0.f, 0.f}; lB = lA;
        mA = -3.0e38f; mB = -3.0e38f;
#pragma unroll 1
        for (int st = 0; st < 8; ++st) {
          if (st < 6) WAITBAR(8); else if (st == 6) WAITBAR(4); else WAITBAR(0);
          if (st < 5) DMA_ST(st + 3, kh);
#pragma unroll 1
          for (int u = 0; u < 4; ++u) {
            bf16x8 k0, k1, v0, v1;
            LDK(k0, k1, L + (st & 3) * AT_STAGE + koff + u * 2048);
            LDV(v0, v1, L + (st & 3) * AT_STAGE + voffl + u * 2048);
#define SAFE_UNIT(Q0, Q1, M, O, LS) do { \
            f32x16 s_ = MFMA32(k0, Q0, zero16); s_ = MFMA32(k1, Q1, s_); \
            const float mn_ = fmaxf(M, max16(s_)); const float al_ = EXP2(M - mn_); M = mn_; \
            O *= al_; LS *= al_; u32x4 p0_, p1_; \
            _Pragma("unroll") for (int r = 0; r < 4; ++r) { p0_[r] = cvtpk(EXP2(s_[2 * r] - mn_), EXP2(s_[2 * r + 1] - mn_)); \
              p1_[r] = cvtpk(EXP2(s_[8 + 2 * r] - mn_), EXP2(s_[9 + 2 * r] - mn_)); } \
            O = MFMA32(v0, BF(p0_), O); LS = MFMA16(sel, BF(p0_), LS); O = MFMA32(v1, BF(p1_), O); LS = MFMA16(sel, BF(p1_), LS); } while (0)
            SAFE_UNIT(qa0, qa1, mA, oA, lA);
            SAFE_UNIT(qb0, qb1, mB, oB, lB);
#undef SAFE_UNIT
          }
        }
      }
      float chk = 0.f;
#pragma unroll
      for (int r = 0; r < 16; ++r) { chk = __builtin_fmaf(oA[r], 0.f, chk); chk = __builtin_fmaf(oB[r], 0.f, chk); }
      const bool bad = !(lA[0] < 1.0e36f) || !(lB[0] < 1.0e36f) || !(chk == 0.f);
      if (pass == 0 && __any(bad) && lane == 0) *flag = 1u;
      const float rA = __builtin_amdgcn_rcpf(lA[0]), rB = __builtin_amdgcn_rcpf(lB[0]);
      const float eA = mA + __builtin_amdgcn_logf(lA[0]), eB = mB + __builtin_amdgcn_logf(lB[0]);
      if (kh) {
#pragma unroll
        for (int r = 0; r < 4; ++r) *(LAS f32x4*)(xw + r * 256 + lane * 4) = (f32x4){oA[4 * r], oA[4 * r + 1], oA[4 * r + 2], oA[4 * r + 3]} * rA;
        ew[lane] = eA; o = oB * rB; e = eB;
      } else {
#pragma unroll
        for (int r = 0; r < 4; ++r) *(LAS f32x4*)(xw + r * 256 + lane * 4) = (f32x4){oB[4 * r], oB[4 * r + 1], oB[4 * r + 2], oB[4 * r + 3]} * rB;
        ew[lane] = eB; o = oA * rA; e = eA;
      }
      LGBAR();
      if (pass == 1 || *flag == 0u) break;
    }
#undef LDK
#undef LDV
#undef DMA_ST
#undef BF
    {
      const float e2 = er[lane];
      const float em = fmaxf(e, e2);
      float w1 = EXP2(e - em), w2 = EXP2(e2 - em);
      const float rs = __builtin_amdgcn_rcpf(w1 + w2);
      w1 *= rs; w2 *= rs;
      u32x2 pk[4];
#pragma unroll
      for (int r = 0; r < 4; ++r) {
        const f32x4 t = *(const LAS f32x4*)(xr + r * 256 + lane * 4);
        pk[r].x = cvtpk(o[4 * r] * w1 + t[0] * w2, o[4 * r + 1] * w1 + t[1] * w2);
        pk[r].y = cvtpk(o[4 * r + 2] * w1 + t[2] * w2, o[4 * r + 3] * w1 + t[3] * w2);
      }
      const int qrow = qb * 256 + qg * 64 + kh * 32 + (lane & 31);
      bf16_t* op = P.AH + (size_t)qrow * KA + h * 32 + 8 * hi;
#pragma unroll
      for (int k = 0; k < 4; k += 2) {
        auto rx = __builtin_amdgcn_permlane32_swap(pk[k].x, pk[k + 1].x, false, false);
        auto ry = __builtin_amdgcn_permlane32_swap(pk[k].y, pk[k + 1].y, false, false);
        st16wt(op + 8 * k, (u32x4){rx[0], ry[0], rx[1], ry[1]});
      }
    }
    LGBAR();
    publish(ctrl + 1, P.flags + FB_OFF + qb * 32 + h, lane);
    STAMP(2, 3);
  }

  if (PH_HI >= 2) {
    int tc = threadIdx.x; asm volatile("" : "+v"(tc));
    const int lane = tc & 63;
    STAMP(3, 0);
    if (g == 1) {
      G2_OFFS(lane);
      if (c_issued < 2) {
        wait_flags(P.flags + FH_OFF + (lane & 31) * 8 + (rt2 >> 1));
        G2_DMA(0, avo0, avo1, bvo); G2_DMA(1, avo0, avo1, bvo);
      }
      G2_DMA(2, avo0, avo1, bvo); G2_DMA(3, avo0, avo1, bvo);
      VMBAR(24);
      VMBAR(16);
#pragma unroll 1
      for (int c = 0; c < G2_NCH - 4; ++c) { G2_DMA(c + 4, avo0, avo1, bvo); VMBAR(16); }
      VMBAR(8);
      VMBAR(0);
    } else {
      const int wm = w4 >> 1, wn = w4 & 1;
      const int fr = lane & 15, fq = lane >> 4, kq = ((fq & 1) << 1) | (fq >> 1), q4 = fr >> 2;
      const int aoff0 = (64 * wm + fr) * 128 + ((kq ^ (((fr >> 1) + 2) & 7)) * 16);
      const int boff0 = 16384 + (8 * kq + q4) * 256 + 8 * (lane & 3);
      const int bsw = q4 | ((fq & 1) << 2);
      lds_cptr pa[2][2], pb[2][4];
#pragma unroll
      for (int hh = 0; hh < 2; ++hh) {
        pa[hh][0] = L + (aoff0 + hh * 2 * G2_SLOT); pa[hh][1] = L + ((aoff0 ^ 64) + hh * 2 * G2_SLOT);
        PIN(pa[hh][0]); PIN(pa[hh][1]);
#pragma unroll
        for (int n = 0; n < 4; ++n) { pb[hh][n] = L + (boff0 + (((4 * wn + n) ^ bsw) * 32) + hh * 2 * G2_SLOT); PIN(pb[hh][n]); }
      }
      f32x4 acc[4][4];
#pragma unroll
      for (int m = 0; m < 4; ++m)
#pragma unroll
        for (int n = 0; n < 4; ++n) { acc[m][n] = (f32x4){0.f, 0.f, 0.f, 0.f}; PIN(acc[m][n]); }
      bf16x8 fa0[2][4], fb0[2][4], fa1[2][4], fb1[2][4];
      const int orow = rt2 * 128 + 64 * wm + 32 * kh2 + fr, ocol = ct2 * 128 + 64 * wn + 4 * fq;
      u32x2 xv[2][4];
#pragma unroll
      for (int mm = 0; mm < 2; ++mm)
#pragma unroll
        for (int n = 0; n < 4; ++n) xv[mm][n] = *(const u32x2*)(P.x + (size_t)(orow + 16 * mm) * DM + ocol + 16 * n);
#define RD(i, SL, FA, FB) do { constexpr int ks_ = (i) / 12, r_ = (i) % 12; \
      if (r_ < 4) FA[ks_][r_ & 3] = lds_rd128(pa[(SL) >> 1][ks_] + ((SL) & 1) * G2_SLOT + r_ * 2048); \
      else { constexpr int n_ = (r_ - 4) >> 1, t_ = (r_ - 4) & 1; \
        const s16x4 h_ = lds_tr(pb[(SL) >> 1][n_ < 0 ? 0 : n_] + ((SL) & 1) * G2_SLOT + ks_ * 8192 + t_ * 1024); \
        if (t_ == 0) { FB[ks_][n_ < 0 ? 0 : n_][0] = h_[0]; FB[ks_][n_ < 0 ? 0 : n_][1] = h_[1]; FB[ks_][n_ < 0 ? 0 : n_][2] = h_[2]; FB[ks_][n_ < 0 ? 0 : n_][3] = h_[3]; } \
        else { FB[ks_][n_ < 0 ? 0 : n_][4] = h_[0]; FB[ks_][n_ < 0 ? 0 : n_][5] = h_[1]; FB[ks_][n_ < 0 ? 0 : n_][6] = h_[2]; FB[ks_][n_ < 0 ? 0 : n_][7] = h_[3]; } } } while (0)
#define MM(i, FA, FB) MFMA16A(acc[(i) & 3][((i) >> 2) & 3], FB[(i) >> 4][((i) >> 2) & 3], FA[(i) >> 4][(i) & 3])
#define MR(i, SL, CA, CB, NA, NB) do { MM(i, CA, CB); RD(i, SL, NA, NB); SB(); } while (0)
#define MR4(i, SL, CA, CB, NA, NB) do { MR(i, SL, CA, CB, NA, NB); MR(i + 1, SL, CA, CB, NA, NB); MR(i + 2, SL, CA, CB, NA, NB); MR(i + 3, SL, CA, CB, NA, NB); } while (0)
#define INTERVAL(SL, CA, CB, NA, NB) do { \
      MR4(0, SL, CA, CB, NA, NB); MR4(4, SL, CA, CB, NA, NB); MR4(8, SL, CA, CB, NA, NB); MR4(12, SL, CA, CB, NA, NB); MR4(16, SL, CA, CB, NA, NB); MR4(20, SL, CA, CB, NA, NB); \
      MM(24, CA, CB); MM(25, CA, CB); MM(26, CA, CB); MM(27, CA, CB); MM(28, CA, CB); MM(29, CA, CB); \
      LGBAR(); MM(30, CA, CB); MM(31, CA, CB); SB(); } while (0)
      BARRIER();
      STAMP(3, 1);
      RD(0, 0, fa0, fb0); RD(1, 0, fa0, fb0); RD(2, 0, fa0, fb0); RD(3, 0, fa0, fb0); RD(4, 0, fa0, fb0); RD(5, 0, fa0, fb0);
      RD(6, 0, fa0, fb0); RD(7, 0, fa0, fb0); RD(8, 0, fa0, fb0); RD(9, 0, fa0, fb0); RD(10, 0, fa0, fb0); RD(11, 0, fa0, fb0);
      RD(12, 0, fa0, fb0); RD(13, 0, fa0, fb0); RD(14, 0, fa0, fb0); RD(15, 0, fa0, fb0); RD(16, 0, fa0, fb0); RD(17, 0, fa0, fb0);
      RD(18, 0, fa0, fb0); RD(19, 0, fa0, fb0); RD(20, 0, fa0, fb0); RD(21, 0, fa0, fb0); RD(22, 0, fa0, fb0); RD(23, 0, fa0, fb0);
      LGBAR();
#pragma unroll 1
      for (int c4 = 0; c4 < 8; ++c4) {
        if (c4 == 3 && kh2 == 0 && w == 0)
          wait_flags(P.flags + FB_OFF + (rt2 >> 1) * 32 + (lane & 31));
        INTERVAL(1, fa0, fb0, fa1, fb1); INTERVAL(2, fa1, fb1, fa0, fb0); INTERVAL(3, fa0, fb0, fa1, fb1); INTERVAL(0, fa1, fb1, fa0, fb0);
      }
      INTERVAL(1, fa0, fb0, fa1, fb1);
      INTERVAL(2, fa1, fb1, fa0, fb0);
      MR4(0, 3, fa0, fb0, fa1, fb1); MR4(4, 3, fa0, fb0, fa1, fb1); MR4(8, 3, fa0, fb0, fa1, fb1); MR4(12, 3, fa0, fb0, fa1, fb1);
      MR4(16, 3, fa0, fb0, fa1, fb1); MR4(20, 3, fa0, fb0, fa1, fb1);
      MM(24, fa0, fb0); MM(25, fa0, fb0); MM(26, fa0, fb0); MM(27, fa0, fb0); MM(28, fa0, fb0); MM(29, fa0, fb0); MM(30, fa0, fb0); MM(31, fa0, fb0);
      SB(); asm volatile("s_waitcnt lgkmcnt(0)" ::: "memory"); SB();
#pragma unroll
      for (int i = 0; i < 32; ++i) MFMA16A(acc[i & 3][(i >> 2) & 3], fb1[i >> 4][(i >> 2) & 3], fa1[i >> 4][i & 3]);
#undef INTERVAL
#undef MR4
#undef MR
#undef MM
#undef RD
      asm volatile("s_nop 7\n\ts_nop 7" ::: "memory");
      SB();
      STAMP(3, 2);
      const int pbid = bid ^ 1;
      float* mine = P.part + (size_t)bid * 8192 + w4 * 2048 + lane * 4;
      f32x4 keep[2][4];
      if (kh2) {
#pragma unroll
        for (int i = 0; i < 8; ++i) { st16wt(mine + i * 256, __builtin_bit_cast(u32x4, acc[i >> 2][i & 3])); keep[i >> 2][i & 3] = acc[2 + (i >> 2)][i & 3]; }
      } else {
#pragma unroll
        for (int i = 0; i < 8; ++i) { st16wt(mine + i * 256, __builtin_bit_cast(u32x4, acc[2 + (i >> 2)][i & 3])); keep[i >> 2][i & 3] = acc[i >> 2][i & 3]; }
      }
      asm volatile("s_waitcnt vmcnt(0)" ::: "memory");
      if (lane == 0) __hip_atomic_store(P.flags + FX_OFF + bid * 16 + w4, 1u, __ATOMIC_RELAXED, __HIP_MEMORY_SCOPE_AGENT);
      {
        unsigned spins = 0;
        while (__builtin_amdgcn_readfirstlane(__hip_atomic_load(P.flags + FX_OFF + pbid * 16 + w4, __ATOMIC_RELAXED, __HIP_MEMORY_SCOPE_AGENT)) == 0u) {
          __builtin_amdgcn_s_sleep(1);
          if (++spins > (1u << 21)) break;
        }
      }
      STAMP(3, 4);
      const float* theirs = P.part + (size_t)pbid * 8192 + w4 * 2048 + lane * 4;
      f32x4 pv[8];
      asm volatile("global_load_dwordx4 %0, %8, off sc1\n\tglobal_load_dwordx4 %1, %8, off offset:1024 sc1\n\t"
                   "global_load_dwordx4 %2, %8, off offset:2048 sc1\n\tglobal_load_dwordx4 %3, %8, off offset:3072 sc1\n\t"
                   "global_load_dwordx4 %4, %9, off sc1\n\tglobal_load_dwordx4 %5, %9, off offset:1024 sc1\n\t"
                   "global_load_dwordx4 %6, %9, off offset:2048 sc1\n\tglobal_load_dwordx4 %7, %9, off offset:3072 sc1\n\t"
                   "s_waitcnt vmcnt(0)"
                   : "=&v"(pv[0]), "=&v"(pv[1]), "=&v"(pv[2]), "=&v"(pv[3]), "=&v"(pv[4]), "=&v"(pv[5]), "=&v"(pv[6]), "=&v"(pv[7])
                   : "v"(theirs), "v"(theirs + 1024) : "memory");
#pragma unroll
      for (int i = 0; i < 8; ++i) {
        const f32x4 v = keep[i >> 2][i & 3] + pv[i];
        const u32x2 xx = xv[i >> 2][i & 3];
        u32x2 o = {cvtpk(bf_lo(xx.x) + v[0], bf_hi(xx.x) + v[1]), cvtpk(bf_lo(xx.y) + v[2], bf_hi(xx.y) + v[3])};
        *(u32x2*)(P.out + (size_t)(orow + 16 * (i >> 2)) * DM + ocol + 16 * (i & 3)) = o;
      }
    }
    STAMP(3, 3);
  }
#undef G2_DMA
#undef G2_OFFS
#undef HSTORE
}

#define NLAUNCH 1

extern "C" void kernel_launch(void* const* d_in, const int* in_sizes, int n_in, void* d_out, int out_size, void* d_ws,
                              size_t ws_size, hipStream_t stream) {
  char* ws = (char*)d_ws;
  Params P;
  P.x = (const bf16_t*)d_in[0]; P.xn = (const bf16_t*)(ws + WS_XN);
  P.wqkv = (const bf16_t*)d_in[1]; P.wo = (const bf16_t*)d_in[2];
  P.wg = (const bf16_t*)d_in[4]; P.wu = (const bf16_t*)d_in[5]; P.wd = (const bf16_t*)d_in[6];
  P.Qb = (bf16_t*)(ws + WS_Q); P.Kb = (bf16_t*)(ws + WS_K); P.Vb = (bf16_t*)(ws + WS_V);
  P.AH = (bf16_t*)(ws + WS_AH); P.out = (bf16_t*)d_out;
  P.part = (float*)(ws + WS_PART); P.flags = (unsigned*)(ws + WS_FLAGS);
  ln_kernel<<<SEQ / 4, 256, 0, stream>>>(P.x, (bf16_t*)(ws + WS_XN), P.flags, P.wqkv, P.wg, P.wu);
  (void)hipFuncSetAttribute((const void*)fused_kernel<0, 2>, hipFuncAttributeMaxDynamicSharedMemorySize, LDS_TOTAL);
  fused_kernel<0, 2><<<256, 512, LDS_TOTAL, stream>>>(P);
}
```

```cpp
#include <hip/hip_runtime.h>
#include <hip/hip_bf16.h>
#include <stdint.h>

typedef unsigned short bf16_t;
typedef short bf16x8 __attribute__((ext_vector_type(8)));
typedef short s16x4 __attribute__((ext_vector_type(4)));
typedef float f32x4 __attribute__((ext_vector_type(4)));
typedef float f32x16 __attribute__((ext_vector_type(16)));
typedef float f32x2 __attribute__((ext_vector_type(2)));
typedef __bf16 bf16x2v __attribute__((ext_vector_type(2)));
typedef unsigned u32x4 __attribute__((ext_vector_type(4)));
typedef unsigned u32x2 __attribute__((ext_vector_type(2)));
#define LAS __attribute__((address_space(3)))
typedef LAS const char* lds_cptr;

constexpr int SEQ = 2048, DM = 1024, FF = 3584, NQKV = 3072, KA = 4608;

constexpr size_t WS_FLAGS = 0;
constexpr size_t WS_XN = 1u << 20;
constexpr size_t WS_Q = WS_XN + (size_t)SEQ * DM * 2;
constexpr size_t WS_K = WS_Q + (size_t)SEQ * DM * 2;
constexpr size_t WS_V = WS_K + (size_t)SEQ * DM * 2;
constexpr size_t WS_AH = WS_V + (size_t)SEQ * DM * 2;
constexpr size_t WS_PART = WS_AH + (size_t)SEQ * KA * 2;
constexpr int FA_OFF = 0, FB_OFF = 256, FH_OFF = 512, FX_OFF = 1024;

#define SB() __builtin_amdgcn_sched_barrier(0)
#define STAMP(kid, i) do {} while (0)

__device__ __forceinline__ unsigned cvtpk(float lo, float hi) {
  f32x2 v = {lo, hi};
  bf16x2v b = __builtin_convertvector(v, bf16x2v);
  return __builtin_bit_cast(unsigned, b);
}
__device__ __forceinline__ float bf_lo(unsigned u) { return __uint_as_float(u << 16); }
__device__ __forceinline__ float bf_hi(unsigned u) { return __uint_as_float(u & 0xffff0000u); }
__device__ __forceinline__ float rbf(float x) { return bf_lo(cvtpk(x, x)); }

__device__ __forceinline__ void glds16(const void* g, unsigned lds_dst) {
  unsigned keep;
  asm volatile("s_mov_b32 %0, m0\n\ts_mov_b32 m0, %2\n\ts_nop 0\n\tglobal_load_lds_dwordx4 %1, off\n\ts_mov_b32 m0, %0"
               : "=&s"(keep) : "v"(g), "s"(lds_dst) : "memory");
}
__device__ __forceinline__ void glds16s(const void* sbase, unsigned voff, unsigned lds_dst) {
  unsigned keep;
  asm volatile("s_mov_b32 %0, m0\n\ts_mov_b32 m0, %3\n\ts_nop 0\n\tglobal_load_lds_dwordx4 %1, %2\n\ts_mov_b32 m0, %0"
               : "=&s"(keep) : "v"(voff), "s"(sbase), "s"(lds_dst) : "memory");
}
__device__ __forceinline__ void glds16c(const void* sbase, unsigned voff, unsigned lds_dst) {
  unsigned keep;
  asm volatile("s_mov_b32 %0, m0\n\ts_mov_b32 m0, %3\n\ts_nop 0\n\tglobal_load_lds_dwordx4 %1, %2 sc1\n\ts_mov_b32 m0, %0"
               : "=&s"(keep) : "v"(voff), "s"(sbase), "s"(lds_dst) : "memory");
}
__device__ __forceinline__ void glds4c(const void* g, unsigned lds_dst) {
  unsigned keep;
  asm volatile("s_mov_b32 %0, m0\n\ts_mov_b32 m0, %2\n\ts_nop 0\n\tglobal_load_lds_dword %1, off sc1\n\ts_mov_b32 m0, %0"
               : "=&s"(keep) : "v"(g), "s"(lds_dst) : "memory");
}
__device__ __forceinline__ bf16x8 lds_rd128(lds_cptr p) { return *(const LAS bf16x8*)p; }
__device__ __forceinline__ s16x4 lds_tr(lds_cptr p) { return __builtin_amdgcn_ds_read_tr16_b64_v4i16((LAS s16x4*)p); }
__device__ __forceinline__ bf16x8 cat8(s16x4 a, s16x4 b) { return __builtin_shufflevector(a, b, 0, 1, 2, 3, 4, 5, 6, 7); }
__device__ __forceinline__ void st16wt(void* p, u32x4 v) {
  asm volatile("global_store_dwordx4 %0, %1, off sc1\n\ts_nop 1" :: "v"(p), "v"(v) : "memory");
}
__device__ __forceinline__ void wait_flags(const unsigned* f) {
  unsigned spins = 0;
  while (!__all(__hip_atomic_load(f, __ATOMIC_RELAXED, __HIP_MEMORY_SCOPE_AGENT) != 0u)) {
    __builtin_amdgcn_s_sleep(4);
    if (++spins > (1u << 20)) break;
  }
  asm volatile("" ::: "memory");
}
__device__ __forceinline__ void publish(LAS unsigned* cnt, unsigned* flag, int lane) {
  asm volatile("s_waitcnt vmcnt(0)" ::: "memory");
  if (lane == 0) {
    const unsigned old = __hip_atomic_fetch_add(cnt, 1u, __ATOMIC_RELAXED, __HIP_MEMORY_SCOPE_WORKGROUP);
    if (old == 7u) __hip_atomic_store(flag, 1u, __ATOMIC_RELAXED, __HIP_MEMORY_SCOPE_AGENT);
  }
}

#define MFMA16(a, b, c) __builtin_amdgcn_mfma_f32_16x16x32_bf16(a, b, c, 0, 0, 0)
#define MFMA32(a, b, c) __builtin_amdgcn_mfma_f32_32x32x16_bf16(a, b, c, 0, 0, 0)
#define WAITBAR(N) do { SB(); asm volatile("s_waitcnt vmcnt(" #N ") lgkmcnt(0)\n\ts_barrier" ::: "memory"); SB(); } while (0)
#define VMBAR(N) do { SB(); asm volatile("s_waitcnt vmcnt(" #N ")\n\ts_barrier" ::: "memory"); SB(); } while (0)
#define LGBAR() do { SB(); asm volatile("s_waitcnt lgkmcnt(0)\n\ts_barrier" ::: "memory"); SB(); } while (0)
#define BARRIER() do { SB(); asm volatile("s_barrier" ::: "memory"); SB(); } while (0)
#define MFMA16A(acc, a, b) asm volatile("v_mfma_f32_16x16x32_bf16 %0, %1, %2, %0" : "+v"(acc) : "v"(a), "v"(b))
#define ADV64(p, inc) asm volatile("v_mad_u64_u32 %0, vcc, %1, 1, %0" : "+v"(p) : "v"(inc) : "vcc")
#define PIN(x) asm volatile("" : "+v"(x))
#define EXP2(x) __builtin_amdgcn_exp2f(x)

__global__ __launch_bounds__(256) void ln_kernel(const bf16_t* __restrict__ x, bf16_t* __restrict__ xn, unsigned* flags,
                                                 const bf16_t* __restrict__ wqkv, const bf16_t* __restrict__ wg, const bf16_t* __restrict__ wu) {
  const int lane = threadIdx.x & 63, w = threadIdx.x >> 6;
  const int row = blockIdx.x * 4 + w;
  const u32x4* xr = (const u32x4*)(x + (size_t)row * DM);
  u32x4 a = xr[lane], b = xr[64 + lane];
  if (blockIdx.x < 64) flags[blockIdx.x * 256 + threadIdx.x] = 0u;
  float v[16];
#pragma unroll
  for (int i = 0; i < 4; ++i) { v[2 * i] = bf_lo(a[i]); v[2 * i + 1] = bf_hi(a[i]); v[8 + 2 * i] = bf_lo(b[i]); v[9 + 2 * i] = bf_hi(b[i]); }
  float s = 0.f;
#pragma unroll
  for (int i = 0; i < 16; ++i) s += v[i];
#pragma unroll
  for (int o = 1; o < 64; o <<= 1) s += __shfl_xor(s, o);
  const float mean = s * (1.f / DM);
  float q = 0.f;
#pragma unroll
  for (int i = 0; i < 16; ++i) { const float d = v[i] - mean; q += d * d; }
#pragma unroll
  for (int o = 1; o < 64; o <<= 1) q += __shfl_xor(q, o);
  const float mb = rbf(mean), vb = rbf(q * (1.f / DM));
  const float den = rbf(sqrtf(rbf(vb + 1e-5f)));
  u32x4 oa, ob;
#pragma unroll
  for (int i = 0; i < 4; ++i) {
    oa[i] = cvtpk(rbf(v[2 * i] - mb) / den, rbf(v[2 * i + 1] - mb) / den);
    ob[i] = cvtpk(rbf(v[8 + 2 * i] - mb) / den, rbf(v[9 + 2 * i] - mb) / den);
  }
  u32x4* o = (u32x4*)(xn + (size_t)row * DM);
  o[lane] = oa; o[64 + lane] = ob;
}

constexpr int G1_AOFF = 0, G1_BOFF = 65536, G1_BREG = 10304, G1_BSLOT = 2 * G1_BREG;
constexpr int AT_STAGE = 32768, AT_EX = 131072, AT_EE = AT_EX + 16384, AT_HF = 150528;
constexpr int G2_SLOT = 32768, G2_NCH = 36;
constexpr int LDS_CTRL = 150016, LDS_SINK = 152576, LDS_TOTAL = LDS_SINK + 1024;

__device__ __forceinline__ float max16(const f32x16& p) {
  float a = fmaxf(fmaxf(p[0], p[1]), p[2]), b = fmaxf(fmaxf(p[3], p[4]), p[5]);
  a = fmaxf(fmaxf(a, p[6]), p[7]); b = fmaxf(fmaxf(b, p[8]), p[9]);
  a = fmaxf(fmaxf(a, p[10]), p[11]); b = fmaxf(fmaxf(b, p[12]), p[13]);
  a = fmaxf(fmaxf(a, p[14]), p[15]);
  float m = fmaxf(a, b);
  auto rr = __builtin_amdgcn_permlane32_swap(__float_as_uint(m), __float_as_uint(m), false, false);
  return fmaxf(__uint_as_float(rr[0]), __uint_as_float(rr[1]));
}
__device__ __forceinline__ f32x16 splat16(float v) { f32x16 r;
#pragma unroll
  for (int i = 0; i < 16; ++i) r[i] = v;
  return r; }

struct Params {
  const bf16_t *x, *xn, *wqkv, *wg, *wu, *wo, *wd;
  bf16_t *Qb, *Kb, *Vb, *AH, *out;
  float* part; unsigned* flags;
};

template <int PH_LO, int PH_HI>
__global__ __launch_bounds__(512, 2) void fused_kernel(Params P) {
  extern __shared__ __attribute__((aligned(16))) char smem[];
  const int w = __builtin_amdgcn_readfirstlane(threadIdx.x >> 6);
  const int g = w >> 2, w4 = w & 3;
  const int bid = blockIdx.x, xcd = bid & 7, jj = bid >> 3;
  const int cg = xcd * 4 + (jj & 3), rt = jj >> 2;
  const unsigned lds0 = (unsigned)(uintptr_t)smem;
  const lds_cptr L = (lds_cptr)smem;
  LAS unsigned* ctrl = (LAS unsigned*)((LAS char*)smem + LDS_CTRL);
  if (threadIdx.x < 4) ctrl[threadIdx.x] = 0u;

  u32x4 hv[8];
  bf16_t* hp0 = nullptr; bf16_t* hp1 = nullptr;
#define HST(p, v) st16wt(p, v)
#define HSTORE(i) HST((i) < 4 ? hp0 + (size_t)(i) * 16 * KA : hp1 + ((w4 & 1) ? (size_t)((i) & 1) * 32 * KA : (size_t)((i) - 4) * 16 * KA), hv[i])

  if (PH_LO <= 0) {
    const int lane = threadIdx.x & 63;
    const int wm = 2 * g + (w4 >> 1), wn = w4 & 1;
    STAMP(1, 0);
    const int row0 = rt * 256;
    unsigned avoff0 = (unsigned)((lane >> 3) * 2048 + (((lane & 7) ^ ((lane >> 4) + 2)) * 16)), avoff1 = avoff0 ^ 64u;
    const char* abase = (const char*)P.xn + (size_t)(row0 + 32 * w4) * 2048;
#define DMA_A(hh, t) do { _Pragma("unroll") for (int _i = 0; _i < 4; ++_i) \
    glds16s(abase + (size_t)(hh) * 262144 + _i * 16384 + (t) * 128, (_i & 1) ? avoff1 : avoff0, \
            lds0 + G1_AOFF + ((t) & 1) * 32768 + (hh) * 16384 + (4 * w4 + _i) * 1024); } while (0)
    DMA_A(g, 0);
    const char* bptr[4]; unsigned bld[4]; unsigned bdst[4];
#pragma unroll
    for (int i = 0; i < 4; ++i) {
      const int b = (i == 0) ? (g ? 16 + w4 : 4 * w4) : 4 * w4 + i;
      const int region = b >= 10 ? 1 : 0, bi = b - 10 * region;
      const int Pc = bi * 64 + lane;
      const int k = Pc / 20, pc = Pc - 20 * k, f = pc >> 1, e8 = (pc & 1) * 8;
      const bool isq = f >= 8 || (region && (f & 3) == 3);
      const int qf = f >= 8 ? (region ? f - 4 : f - 8) : 2 + (f >> 2);
      const bf16_t* mat = isq ? P.wqkv : (f >= 4 ? P.wu : P.wg);
      const int ld = isq ? NQKV : FF;
      const int col = e8 + (isq ? 96 * cg + 16 * qf : 112 * cg + 64 * region + 16 * (f & 3));
      bptr[i] = (const char*)(mat + (size_t)k * ld + col);
      bld[i] = (unsigned)ld * 64u;
      bdst[i] = lds0 + G1_BOFF + region * G1_BREG + bi * 1024 + (bi >= 5 ? 32 : 0);
    }
#define DMA_B(i, s) glds16(bptr[i], bdst[i] + ((s) & 3) * G1_BSLOT)
#define ADV_B() do { ADV64(bptr[0], bld[0]); ADV64(bptr[1], bld[1]); ADV64(bptr[2], bld[2]); ADV64(bptr[3], bld[3]); } while (0)
    if (g == 0) {
      DMA_B(0, 0); DMA_B(1, 0); DMA_B(2, 0); DMA_B(3, 0); ADV_B();
      DMA_B(0, 1); ADV_B();
      DMA_B(0, 2); DMA_B(1, 2); DMA_B(2, 2); DMA_B(3, 2); ADV_B();
    } else {
      DMA_B(0, 0); ADV_B();
      DMA_B(0, 1); DMA_B(1, 1); DMA_B(2, 1); DMA_B(3, 1); ADV_B();
      DMA_B(0, 2); ADV_B();
      DMA_A(0, 1);
    }
    const int fr = lane & 15, fq = lane >> 4, kq = ((fq & 1) << 1) | (fq >> 1);
    const int aoff0 = G1_AOFF + (64 * wm + fr) * 128 + ((kq ^ (((fr >> 1) + 2) & 7)) * 16);
    lds_cptr pa0 = L + aoff0, pa1 = L + (aoff0 ^ 64);
    lds_cptr pb0 = L + (G1_BOFF + (8 * kq + (fr >> 2)) * 320 + (fq & 1) * 32 + 8 * (lane & 3) + G1_BREG * wn);
    lds_cptr pb1 = pb0 + 2 * G1_BSLOT;
    asm volatile("" : "+v"(pa0), "+v"(pa1), "+v"(pb0), "+v"(pb1), "+v"(avoff0), "+v"(avoff1));
    f32x4 acc[4][10];
#pragma unroll
    for (int m = 0; m < 4; ++m)
#pragma unroll
      for (int j = 0; j < 10; ++j) { acc[m][j] = (f32x4){0.f, 0.f, 0.f, 0.f}; asm volatile("" : "+v"(acc[m][j])); }
    bf16x8 af[4], bfr[10];
    if (g == 0) { WAITBAR(5); } else { WAITBAR(9); BARRIER(); }
    STAMP(1, 1);
#define STEP(U, ISSUE, WN, LASTBAR, PF) do { \
    { const lds_cptr pa = ((U) & 1) ? pa1 : pa0; const lds_cptr pb = (((U) & 2) ? pb1 : pb0) + ((U) & 1) * G1_BSLOT; \
      _Pragma("unroll") for (int m = 0; m < 4; ++m) af[m] = lds_rd128(pa + ((U) >> 1) * 32768 + m * 2048); \
      _Pragma("unroll") for (int j = 0; j < 10; ++j) bfr[j] = cat8(lds_tr(pb + j * 32), lds_tr(pb + j * 32 + 1280)); } \
    if (PF) { const int id_ = 4096 + (4 * it + (U)) * 2048 + bid * 8 + w, r_ = id_ / 20, c_ = id_ - 20 * r_; \
      glds16s(c_ < 6 ? (const char*)P.wqkv + (size_t)r_ * 6144 + c_ * 1024 : (const char*)(c_ < 13 ? P.wg : P.wu) + (size_t)r_ * 7168 + (c_ < 13 ? c_ - 6 : c_ - 13) * 1024, \
              pfoff, lds0 + LDS_SINK); } \
    if (ISSUE) { DMA_B(0, (U) + 3); \
      if (g == ((U) & 1)) { DMA_A(1 - ((U) & 1), 2 * it + ((U) >> 1) + 1 + ((U) & 1)); } \
      else { DMA_B(1, (U) + 3); DMA_B(2, (U) + 3); DMA_B(3, (U) + 3); } } \
    WAITBAR(WN); \
    _Pragma("unroll") for (int j = 0; j < 10; ++j) { _Pragma("unroll") for (int m = 0; m < 4; ++m) MFMA16A(acc[m][j], bfr[j], af[m]); \
      if ((ISSUE) && (j & 1) && j < 8) ADV64(bptr[j >> 1], bld[j >> 1]); } \
    if (LASTBAR) BARRIER(); } while (0)
    unsigned pfoff = lane * 16; PIN(pfoff);
    int it = 0;
    STEP(0, true, 10, true, true); STEP(1, true, 11, true, true); STEP(2, true, 11, true, true); STEP(3, true, 11, true, true);
    ++it;
    STEP(0, true, 11, true, true); STEP(1, true, 11, true, true); STEP(2, true, 11, true, true); STEP(3, true, 11, true, true);
    ++it;
#pragma unroll 1
    for (; it < 7; ++it) { STEP(0, true, 9, true, false); STEP(1, true, 9, true, false); STEP(2, true, 9, true, false); STEP(3, true, 9, true, false); }
    STEP(0, true, 9, true, false); STEP(1, false, 4, true, false); STEP(2, false, 0, true, false); STEP(3, false, 0, g == 0, false);
#undef STEP
    asm volatile("s_nop 7\n\ts_nop 7" ::: "memory");
    SB();
    STAMP(1, 2);
#undef DMA_A
#undef DMA_B
#undef ADV_B
    int tz = threadIdx.x; asm volatile("" : "+v"(tz));
    const int fr2 = tz & 15, fq2 = (tz >> 4) & 3, sel1 = fq2 & 1, c8 = (fq2 >> 1) * 8;
    const float qscale = 0.17677669529663687f * 1.4426950408889634f;
#define SWAP16(A, B, OUT) do { auto r0_ = __builtin_amdgcn_permlane16_swap(A.x, B.x, false, false); \
    auto r1_ = __builtin_amdgcn_permlane16_swap(A.y, B.y, false, false); OUT = (u32x4){r0_[0], r1_[0], r0_[1], r1_[1]}; } while (0)
#define SILU4(m, j, OUT) do { float hv_[4]; _Pragma("unroll") for (int r = 0; r < 4; ++r) { \
      const float tg_ = acc[m][j][r], tu_ = acc[m][(j) + 4][r]; \
      hv_[r] = (tg_ * tu_) * __builtin_amdgcn_rcpf(1.f + __builtin_amdgcn_exp2f(-1.4426950408889634f * tg_)); } \
    OUT = (u32x2){cvtpk(hv_[0], hv_[1]), cvtpk(hv_[2], hv_[3])}; } while (0)
#define PK4(V, SC, OUT) do { const f32x4 v_ = (V) * (SC); OUT = (u32x2){cvtpk(v_[0], v_[1]), cvtpk(v_[2], v_[3])}; } while (0)
    const int rowb = row0 + 64 * wm + fr2;
#define QKV_HEAD(hd, ja, jb) do { const int typ_ = (hd) >> 5, h_ = (hd) & 31; const float sa = typ_ == 0 ? qscale : 1.f; \
      bf16_t* dst; int mstep; \
      if (typ_ == 1) { dst = P.Kb + ((((size_t)h_ * 64 + (rowb >> 5)) * 2 + sel1) * 64 + (rowb & 31) + 4 * c8) * 8; mstep = 128; } \
      else { dst = (typ_ == 0 ? P.Qb : P.Vb) + ((size_t)h_ * SEQ + rowb) * 32 + 16 * sel1 + c8; mstep = 512; } \
      _Pragma("unroll") for (int m = 0; m < 4; ++m) { \
        u32x2 a_, b_; u32x4 o; PK4(acc[m][ja], sa, a_); PK4(acc[m][jb], sa, b_); SWAP16(a_, b_, o); \
        st16wt(dst + (m & 1) * mstep + (m >> 1) * 1024, o); } } while (0)
    __builtin_amdgcn_s_setprio(3);
    QKV_HEAD(3 * cg + 2 * wn, 8, 9);
    if (wn) QKV_HEAD(3 * cg + 1, 3, 7);
    __builtin_amdgcn_s_setprio(0);
    SB();
    bf16_t* hd = P.AH + (size_t)rowb * KA + DM + 112 * cg + 64 * wn + c8;
    hp0 = hd + 16 * sel1;
    hp1 = wn ? hd + 32 + (size_t)sel1 * 16 * KA : hd + 32 + 16 * sel1;
#pragma unroll
    for (int m = 0; m < 4; ++m) { u32x2 a_, b_; SILU4(m, 0, a_); SILU4(m, 1, b_); SWAP16(a_, b_, hv[m]); PIN(hv[m]); }
    STAMP(1, 4);
    publish(ctrl + 0, P.flags + FA_OFF + cg * 8 + rt, tz & 63);
    if (wn == 0) {
#pragma unroll
      for (int m = 0; m < 4; ++m) { u32x2 a_, b_; SILU4(m, 2, a_); SILU4(m, 3, b_); SWAP16(a_, b_, hv[4 + m]); }
    } else {
#pragma unroll
      for (int mp = 0; mp < 2; ++mp) { u32x2 a_, b_; SILU4(2 * mp, 2, a_); SILU4(2 * mp + 1, 2, b_); SWAP16(a_, b_, hv[4 + mp]); }
      hv[6] = hv[4]; hv[7] = hv[5];
    }
    if (PH_HI == 0) { HSTORE(0); HSTORE(1); HSTORE(2); HSTORE(3); HSTORE(4); HSTORE(5); if (wn == 0) { HSTORE(6); HSTORE(7); } }
#undef SWAP16
#undef SILU4
#undef PK4
#undef QKV_HEAD
    if (PH_HI == 0 && threadIdx.x == 0) P.flags[FH_OFF + cg * 8 + rt] = 1u;
    STAMP(1, 3);
  } else {
    __syncthreads();
  }

  const int kh2 = xcd & 1, rt2 = (xcd >> 1) * 4 + (jj >> 3), ct2 = jj & 7;
  int c_issued = 0;
#define G2_DMA(c, avo0, avo1, bvo) do { int cc_ = (c) + (kh2 ? 0 : 16); if (cc_ >= G2_NCH) cc_ -= G2_NCH; const int kg_ = kh2 * (G2_NCH * 64) + cc_ * 64; \
    const char* ab_ = (const char*)P.AH + (size_t)(rt2 * 128 + 32 * w4) * (KA * 2) + kg_ * 2; \
    const char* bb_ = (kg_ < DM ? (const char*)P.wo + (size_t)kg_ * 2048 : (const char*)P.wd + (size_t)(kg_ - DM) * 2048) + ct2 * 256 + w4 * 32768; \
    const unsigned d_ = lds0 + ((c) & 3) * G2_SLOT + w4 * 4096; \
    _Pragma("unroll") for (int _i = 0; _i < 4; ++_i) { \
      glds16c(ab_ + (size_t)_i * (8 * KA * 2), (_i & 1) ? avo1 : avo0, d_ + _i * 1024); \
      glds16s(bb_ + _i * 8192, bvo, d_ + 16384 + _i * 1024); } } while (0)
#define G2_OFFS(lane) const unsigned avo0 = (unsigned)(((lane) >> 3) * (KA * 2) + ((((lane) & 7) ^ (((lane) >> 4) + 2)) * 16)), avo1 = avo0 ^ 64u; \
    const unsigned bvo = (unsigned)(((lane) >> 4) * 2048 + ((((lane) & 15) ^ (((lane) >> 4) << 1) ^ ((w4 & 1) << 3)) * 16))

  if (PH_LO <= 1 && PH_HI >= 1) {
    int tb = threadIdx.x; asm volatile("" : "+v"(tb));
    const int lane = tb & 63;
    const int qg = w4, kh = g;
    const int h = xcd * 4 + (jj >> 3), qb = jj & 7;
    STAMP(2, 0);
    {
      const int cgQ = h / 3, cgK = (32 + h) / 3, cgV = (64 + h) / 3;
      const int fi = lane < 8 ? cgK * 8 + lane : (lane < 16 ? cgV * 8 + lane - 8 : cgQ * 8 + qb);
      volatile LAS unsigned* go = (volatile LAS unsigned*)(ctrl + 3);
      if (w == 0) { wait_flags(P.flags + FA_OFF + fi); if (lane == 0) *go = 1u; }
      else { while (*go == 0u) __builtin_amdgcn_s_sleep(1); asm volatile("" ::: "memory"); }
    }
    STAMP(2, 4);
    const int hi = lane >> 5;
    const char* kg = (const char*)(P.Kb + (size_t)h * SEQ * 32) + qg * 2048;
    const char* vg = (const char*)(P.Vb + (size_t)h * SEQ * 32) + qg * 2048;
    const unsigned voff = lane * 16;
#define DMA_ST(st, KH) do { const unsigned d_ = lds0 + ((st) & 3) * AT_STAGE + (KH) * 8192 + qg * 2048; const int o_ = (KH) * 65536 + (st) * 8192; \
    glds16c(kg + o_, voff, d_); glds16c(kg + o_ + 1024, voff, d_ + 1024); \
    glds16c(vg + o_, voff, d_ + 16384); glds16c(vg + o_ + 1024, voff, d_ + 16384 + 1024); } while (0)
    if (g == 0) { DMA_ST(0, 0); DMA_ST(0, 1); }
    const bf16_t* qp = P.Qb + ((size_t)h * SEQ + qb * 256 + qg * 64 + (lane & 31)) * 32 + 8 * hi;
    bf16x8 qa0, qa1, qb0, qb1;
    asm volatile("global_load_dwordx4 %0, %4, off sc1\n\tglobal_load_dwordx4 %1, %4, off offset:32 sc1\n\t"
                 "global_load_dwordx4 %2, %4, off offset:2048 sc1\n\tglobal_load_dwordx4 %3, %4, off offset:2080 sc1\n\ts_waitcnt vmcnt(0)"
                 : "=&v"(qa0), "=&v"(qa1), "=&v"(qb0), "=&v"(qb1) : "v"(qp) : "memory");
    const short one = (((lane >> 4) & 1) == ((lane >> 2) & 1)) ? (short)0x3f80 : (short)0;
    const bf16x8 sel = {one, one, one, one, one, one, one, one};
    volatile LAS unsigned* flag = (volatile LAS unsigned*)(ctrl + 2);
    const int gq = lane >> 4;
    const int koff = kh * 8192 + lane * 16;
    const int voffl = 16384 + kh * 8192 + (4 * (gq >> 1) + ((lane & 15) >> 2)) * 64 + (gq & 1) * 32 + (lane & 3) * 8;
    const f32x16 zero16 = splat16(0.f);
    f32x16 oA = zero16, oB = zero16;
    f32x4 lA = {0.f, 0.f, 0.f, 0.f}, lB = {0.f, 0.f, 0.f, 0.f};
    float mA, mB;
    const unsigned* hflag = P.flags + FH_OFF + (lane & 31) * 8 + (rt2 >> 1);

#define LDK(K0, K1, p) do { K0 = lds_rd128(p); K1 = lds_rd128((p) + 1024); } while (0)
#define LDV(V0, V1, p) do { V0 = cat8(lds_tr(p), lds_tr((p) + 512)); V1 = cat8(lds_tr((p) + 1024), lds_tr((p) + 1536)); } while (0)
#define BF(x) __builtin_bit_cast(bf16x8, x)
    {
      WAITBAR(0);
      bf16x8 kx0, kx1, ky0, ky1, vx0, vx1, vy0, vy1;
      LDK(kx0, kx1, L + koff);
      LDK(ky0, ky1, L + koff + 2048);
      LDV(vy0, vy1, L + voffl);
      DMA_ST(1, kh); DMA_ST(2, kh);
      f32x16 sA = MFMA32(kx0, qa0, zero16); sA = MFMA32(kx1, qa1, sA);
      f32x16 sB = MFMA32(kx0, qb0, zero16); sB = MFMA32(kx1, qb1, sB);
      mA = max16(sA); mB = max16(sB);
      f32x16 nA = splat16(-mA), nB = splat16(-mB);
      PIN(nA); PIN(nB);
      sA = MFMA32(kx0, qa0, nA); sA = MFMA32(kx1, qa1, sA);
      u32x4 pA0, pA1, pB0 = {0u, 0u, 0u, 0u}, pB1 = {0u, 0u, 0u, 0u};
      STAMP(2, 1);
#define E4(S, i) do { S[i] = EXP2(S[i]); S[i + 1] = EXP2(S[i + 1]); S[i + 2] = EXP2(S[i + 2]); S[i + 3] = EXP2(S[i + 3]); } while (0)
#define SLOT(SC, PC0, PC1, SN, K0, K1, Q0, Q1, NM, PP0, PP1, V0, V1, O, LS, LOADS) do { \
    SN = MFMA32(K0, Q0, NM); SB(); E4(SC, 0); PIN(SC); SB(); \
    SN = MFMA32(K1, Q1, SN); SB(); LOADS; SB(); E4(SC, 4); PIN(SC); SB(); \
    O = MFMA32(V0, BF(PP0), O); SB(); PC0[0] = cvtpk(SC[0], SC[1]); PC0[1] = cvtpk(SC[2], SC[3]); \
      SC[8] = EXP2(SC[8]); SC[9] = EXP2(SC[9]); SC[10] = EXP2(SC[10]); PIN(SC); PIN(PC0); SB(); \
    LS = MFMA16(sel, BF(PP0), LS); SB(); PC0[2] = cvtpk(SC[4], SC[5]); PC0[3] = cvtpk(SC[6], SC[7]); SC[11] = EXP2(SC[11]); PIN(SC); PIN(PC0); SB(); \
    O = MFMA32(V1, BF(PP1), O); SB(); E4(SC, 12); PIN(SC); SB(); \
    LS = MFMA16(sel, BF(PP1), LS); SB(); PC1[0] = cvtpk(SC[8], SC[9]); PC1[1] = cvtpk(SC[10], SC[11]); \
      PC1[2] = cvtpk(SC[12], SC[13]); PC1[3] = cvtpk(SC[14], SC[15]); PIN(PC1); SB(); } while (0)
#define SUB(KC0, KC1, KN0, KN1, VC0, VC1, VP0, VP1, vpu, kp2) do { \
    SLOT(sA, pA0, pA1, sB, KC0, KC1, qb0, qb1, nB, pB0, pB1, VP0, VP1, oB, lB, LDV(VC0, VC1, vpu)); \
    SLOT(sB, pB0, pB1, sA, KN0, KN1, qa0, qa1, nA, pA0, pA1, VC0, VC1, oA, lA, LDK(KC0, KC1, kp2)); } while (0)
      lds_cptr kb0 = L + koff, kb1 = L + koff + 2 * AT_STAGE, vb0 = L + voffl, vb1 = L + voffl + 2 * AT_STAGE;
      PIN(kb0); PIN(kb1); PIN(vb0); PIN(vb1);
#define KP(st) ((((st) & 2) ? kb1 : kb0) + ((st) & 1) * AT_STAGE)
#define VP(st) ((((st) & 2) ? vb1 : vb0) + ((st) & 1) * AT_STAGE)
#define HS(st, i) do { if (PH_LO == 0 && (st) < 2) HSTORE((4 * (st) + (i)) & 7); } while (0)
#define STAGE(st, WN, EXTRA) do { \
        SUB(kx0, kx1, ky0, ky1, vx0, vx1, vy0, vy1, VP(st), KP(st) + 4096); \
        HS(st, 0); \
        SUB(ky0, ky1, kx0, kx1, vy0, vy1, vx0, vx1, VP(st) + 2048, KP(st) + 6144); \
        VMBAR(WN); \
        HS(st, 1); \
        EXTRA; \
        if ((st) < 5) {   \
          const int pc = (st) * 8 + w, kb = bid * 36 + (pc < 36 ? pc : 35); \
          glds16s(kb < 2048 ? (const char*)P.wo + (size_t)kb * 1024 : (const char*)P.wd + (size_t)(kb - 2048) * 1024, voff, lds0 + LDS_SINK); \
          DMA_ST((st) + 3, kh); } \
        SUB(kx0, kx1, ky0, ky1, vx0, vx1, vy0, vy1, VP(st) + 4096, KP((st) + 1)); \
        HS(st, 2); \
        SUB(ky0, ky1, kx0, kx1, vy0, vy1, vx0, vx1, VP(st) + 6144, KP((st) + 1) + 2048); \
        HS(st, 3); } while (0)
      if (PH_LO == 0) { STAGE(0, 5, ); STAGE(1, 10, ); STAGE(2, 11, ); } else { STAGE(0, 4, ); STAGE(1, 5, ); STAGE(2, 5, ); }
      STAGE(3, 5, if (PH_LO == 0 && w == 0 && lane == 0)
                    __hip_atomic_store(P.flags + FH_OFF + cg * 8 + rt, 1u, __ATOMIC_RELAXED, __HIP_MEMORY_SCOPE_AGENT));
      STAGE(4, 5, );
      STAGE(5, 5, if (PH_HI >= 2 && g == 1) glds4c(hflag, lds0 + AT_HF + w * 256));
      STAGE(6, 0, if (PH_HI >= 2 && g == 1) {
                    const unsigned hf = *(volatile LAS unsigned*)((LAS char*)smem + AT_HF + w * 256 + lane * 4);
                    if (__all(hf != 0u)) { G2_OFFS(lane); G2_DMA(0, avo0, avo1, bvo); G2_DMA(1, avo0, avo1, bvo); c_issued = 2; } });
      STAGE(7, 0, if (PH_HI >= 2 && c_issued == 2) { G2_OFFS(lane); G2_DMA(2, avo0, avo1, bvo); c_issued = 3; });
#undef STAGE
#undef HS
#undef KP
#undef VP
      oB = MFMA32(vy0, BF(pB0), oB); lB = MFMA16(sel, BF(pB0), lB);
      oB = MFMA32(vy1, BF(pB1), oB); lB = MFMA16(sel, BF(pB1), lB);
#undef SUB
#undef SLOT
#undef E4
    }
    STAMP(2, 2);
    LAS u32x4* xw = (LAS u32x4*)((LAS char*)smem + AT_EX) + w * 128;
    const LAS u32x4* xr = (const LAS u32x4*)((LAS char*)smem + AT_EX) + (w ^ 4) * 128;
    LAS float* ew = (LAS float*)((LAS char*)smem + AT_EE) + w * 64;
    const LAS float* er = (const LAS float*)((LAS char*)smem + AT_EE) + (w ^ 4) * 64;
    f32x16 o; float e;
#pragma unroll 1
    for (int pass = 0; pass < 2; ++pass) {
      if (pass) {
        WAITBAR(0);
        c_issued = 0;
        DMA_ST(0, kh); DMA_ST(1, kh); DMA_ST(2, kh);
        oA = zero16; oB = zero16; lA = (f32x4){0.f, 0.f, 0.f, 0.f}; lB = lA;
        mA = -3.0e38f; mB = -3.0e38f;
#pragma unroll 1
        for (int st = 0; st < 8; ++st) {
          if (st < 6) WAITBAR(8); else if (st == 6) WAITBAR(4); else WAITBAR(0);
          if (st < 5) DMA_ST(st + 3, kh);
#pragma unroll 1
          for (int u = 0; u < 4; ++u) {
            bf16x8 k0, k1, v0, v1;
            LDK(k0, k1, L + (st & 3) * AT_STAGE + koff + u * 2048);
            LDV(v0, v1, L + (st & 3) * AT_STAGE + voffl + u * 2048);
#define SAFE_UNIT(Q0, Q1, M, O, LS) do { \
            f32x16 s_ = MFMA32(k0, Q0, zero16); s_ = MFMA32(k1, Q1, s_); \
            const float mn_ = fmaxf(M, max16(s_)); const float al_ = EXP2(M - mn_); M = mn_; \
            O *= al_; LS *= al_; u32x4 p0_, p1_; \
            _Pragma("unroll") for (int r = 0; r < 4; ++r) { p0_[r] = cvtpk(EXP2(s_[2 * r] - mn_), EXP2(s_[2 * r + 1] - mn_)); \
              p1_[r] = cvtpk(EXP2(s_[8 + 2 * r] - mn_), EXP2(s_[9 + 2 * r] - mn_)); } \
            O = MFMA32(v0, BF(p0_), O); LS = MFMA16(sel, BF(p0_), LS); O = MFMA32(v1, BF(p1_), O); LS = MFMA16(sel, BF(p1_), LS); } while (0)
            SAFE_UNIT(qa0, qa1, mA, oA, lA);
            SAFE_UNIT(qb0, qb1, mB, oB, lB);
#undef SAFE_UNIT
          }
        }
      }
      float chk = 0.f;
#pragma unroll
      for (int r = 0; r < 16; ++r) { chk = __builtin_fmaf(oA[r], 0.f, chk); chk = __builtin_fmaf(oB[r], 0.f, chk); }
      const bool bad = !(lA[0] < 1.0e36f) || !(lB[0] < 1.0e36f) || !(chk == 0.f);
      if (pass == 0 && __any(bad) && lane == 0) *flag = 1u;
      const float rA = __builtin_amdgcn_rcpf(lA[0]), rB = __builtin_amdgcn_rcpf(lB[0]);
      const float eA = mA + __builtin_amdgcn_logf(lA[0]), eB = mB + __builtin_amdgcn_logf(lB[0]);
#define XW(O, R) do { _Pragma("unroll") for (int r = 0; r < 2; ++r) xw[r * 64 + lane] = (u32x4){cvtpk(O[8 * r] * R, O[8 * r + 1] * R), \
          cvtpk(O[8 * r + 2] * R, O[8 * r + 3] * R), cvtpk(O[8 * r + 4] * R, O[8 * r + 5] * R), cvtpk(O[8 * r + 6] * R, O[8 * r + 7] * R)}; } while (0)
      if (kh) { XW(oA, rA); ew[lane] = eA; o = oB * rB; e = eB; }
      else { XW(oB, rB); ew[lane] = eB; o = oA * rA; e = eA; }
#undef XW
      LGBAR();
      if (pass == 1 || *flag == 0u) break;
    }
#undef LDK
#undef LDV
#undef DMA_ST
#undef BF
    {
      const float e2 = er[lane];
      const u32x4 t0 = xr[lane], t1 = xr[64 + lane];
      const float em = fmaxf(e, e2);
      float w1 = EXP2(e - em), w2 = EXP2(e2 - em);
      const float rs = __builtin_amdgcn_rcpf(w1 + w2);
      w1 *= rs; w2 *= rs;
      u32x2 pk[4];
#pragma unroll
      for (int r = 0; r < 4; ++r) {
        const unsigned ta = r < 2 ? t0[2 * r] : t1[2 * r - 4], tb2 = r < 2 ? t0[2 * r + 1] : t1[2 * r - 3];
        pk[r].x = cvtpk(o[4 * r] * w1 + bf_lo(ta) * w2, o[4 * r + 1] * w1 + bf_hi(ta) * w2);
        pk[r].y = cvtpk(o[4 * r + 2] * w1 + bf_lo(tb2) * w2, o[4 * r + 3] * w1 + bf_hi(tb2) * w2);
      }
      const int qrow = qb * 256 + qg * 64 + kh * 32 + (lane & 31);
      bf16_t* op = P.AH + (size_t)qrow * KA + h * 32 + 8 * hi;
#pragma unroll
      for (int k = 0; k < 4; k += 2) {
        auto rx = __builtin_amdgcn_permlane32_swap(pk[k].x, pk[k + 1].x, false, false);
        auto ry = __builtin_amdgcn_permlane32_swap(pk[k].y, pk[k + 1].y, false, false);
        st16wt(op + 8 * k, (u32x4){rx[0], ry[0], rx[1], ry[1]});
      }
    }
    if (PH_HI < 2) publish(ctrl + 1, P.flags + FB_OFF + qb * 32 + h, lane);
    STAMP(2, 3);
  }

  if (PH_HI >= 2) {
    int tc = threadIdx.x; asm volatile("" : "+v"(tc));
    const int lane = tc & 63;
    STAMP(3, 0);
    if (g == 1) {
      G2_OFFS(lane);
      if (c_issued < 3) {
        wait_flags(P.flags + FH_OFF + (lane & 31) * 8 + (rt2 >> 1));
        G2_DMA(0, avo0, avo1, bvo); G2_DMA(1, avo0, avo1, bvo); G2_DMA(2, avo0, avo1, bvo); G2_DMA(3, avo0, avo1, bvo);
        VMBAR(24);
        VMBAR(16);
      } else {
        BARRIER(); G2_DMA(3, avo0, avo1, bvo); BARRIER();
      }
#pragma unroll 1
      for (int c = 0; c < G2_NCH - 4; ++c) { G2_DMA(c + 4, avo0, avo1, bvo); VMBAR(16); }
      VMBAR(8);
      VMBAR(0);
    } else {
      const int wm = w4 >> 1, wn = w4 & 1;
      const int fr = lane & 15, fq = lane >> 4, kq = ((fq & 1) << 1) | (fq >> 1), q4 = fr >> 2;
      const int aoff0 = (64 * wm + fr) * 128 + ((kq ^ (((fr >> 1) + 2) & 7)) * 16);
      const int boff0 = 16384 + (8 * kq + q4) * 256 + 8 * (lane & 3);
      const int bsw = q4 | ((fq & 1) << 2);
      lds_cptr pa[2][2], pb[2][4];
#pragma unroll
      for (int hh = 0; hh < 2; ++hh) {
        pa[hh][0] = L + (aoff0 + hh * 2 * G2_SLOT); pa[hh][1] = L + ((aoff0 ^ 64) + hh * 2 * G2_SLOT);
        PIN(pa[hh][0]); PIN(pa[hh][1]);
#pragma unroll
        for (int n = 0; n < 4; ++n) { pb[hh][n] = L + (boff0 + (((4 * wn + n) ^ bsw) * 32) + hh * 2 * G2_SLOT); PIN(pb[hh][n]); }
      }
      f32x4 acc[4][4];
#pragma unroll
      for (int m = 0; m < 4; ++m)
#pragma unroll
        for (int n = 0; n < 4; ++n) { acc[m][n] = (f32x4){0.f, 0.f, 0.f, 0.f}; PIN(acc[m][n]); }
      bf16x8 fa0[2][4], fb0[2][4], fa1[2][4], fb1[2][4];
      const int orow = rt2 * 128 + 64 * wm + 32 * kh2 + fr, ocol = ct2 * 128 + 64 * wn + 4 * fq;
      u32x2 xv[2][4];
#pragma unroll
      for (int mm = 0; mm < 2; ++mm)
#pragma unroll
        for (int n = 0; n < 4; ++n) xv[mm][n] = *(const u32x2*)(P.x + (size_t)(orow + 16 * mm) * DM + ocol + 16 * n);
#define RD(i, SL, FA, FB) do { constexpr int ks_ = (i) / 12, r_ = (i) % 12; \
      if (r_ < 4) FA[ks_][r_ & 3] = lds_rd128(pa[(SL) >> 1][ks_] + ((SL) & 1) * G2_SLOT + r_ * 2048); \
      else { constexpr int n_ = (r_ - 4) >> 1, t_ = (r_ - 4) & 1; \
        const s16x4 h_ = lds_tr(pb[(SL) >> 1][n_ < 0 ? 0 : n_] + ((SL) & 1) * G2_SLOT + ks_ * 8192 + t_ * 1024); \
        if (t_ == 0) { FB[ks_][n_ < 0 ? 0 : n_][0] = h_[0]; FB[ks_][n_ < 0 ? 0 : n_][1] = h_[1]; FB[ks_][n_ < 0 ? 0 : n_][2] = h_[2]; FB[ks_][n_ < 0 ? 0 : n_][3] = h_[3]; } \
        else { FB[ks_][n_ < 0 ? 0 : n_][4] = h_[0]; FB[ks_][n_ < 0 ? 0 : n_][5] = h_[1]; FB[ks_][n_ < 0 ? 0 : n_][6] = h_[2]; FB[ks_][n_ < 0 ? 0 : n_][7] = h_[3]; } } } while (0)
#define MM(i, FA, FB) MFMA16A(acc[(i) & 3][((i) >> 2) & 3], FB[(i) >> 4][((i) >> 2) & 3], FA[(i) >> 4][(i) & 3])
#define MR(i, SL, CA, CB, NA, NB) do { MM(i, CA, CB); RD(i, SL, NA, NB); SB(); } while (0)
#define MR4(i, SL, CA, CB, NA, NB) do { MR(i, SL, CA, CB, NA, NB); MR(i + 1, SL, CA, CB, NA, NB); MR(i + 2, SL, CA, CB, NA, NB); MR(i + 3, SL, CA, CB, NA, NB); } while (0)
#define INTERVAL(SL, CA, CB, NA, NB) do { \
      MR4(0, SL, CA, CB, NA, NB); MR4(4, SL, CA, CB, NA, NB); MR4(8, SL, CA, CB, NA, NB); MR4(12, SL, CA, CB, NA, NB); MR4(16, SL, CA, CB, NA, NB); MR4(20, SL, CA, CB, NA, NB); \
      MM(24, CA, CB); MM(25, CA, CB); MM(26, CA, CB); MM(27, CA, CB); MM(28, CA, CB); MM(29, CA, CB); \
      LGBAR(); MM(30, CA, CB); MM(31, CA, CB); SB(); } while (0)
      BARRIER();
      STAMP(3, 1);
      RD(0, 0, fa0, fb0); RD(1, 0, fa0, fb0); RD(2, 0, fa0, fb0); RD(3, 0, fa0, fb0); RD(4, 0, fa0, fb0); RD(5, 0, fa0, fb0);
      RD(6, 0, fa0, fb0); RD(7, 0, fa0, fb0); RD(8, 0, fa0, fb0); RD(9, 0, fa0, fb0); RD(10, 0, fa0, fb0); RD(11, 0, fa0, fb0);
      RD(12, 0, fa0, fb0); RD(13, 0, fa0, fb0); RD(14, 0, fa0, fb0); RD(15, 0, fa0, fb0); RD(16, 0, fa0, fb0); RD(17, 0, fa0, fb0);
      RD(18, 0, fa0, fb0); RD(19, 0, fa0, fb0); RD(20, 0, fa0, fb0); RD(21, 0, fa0, fb0); RD(22, 0, fa0, fb0); RD(23, 0, fa0, fb0);
      LGBAR();
#pragma unroll 1
      for (int c4 = 0; c4 < 8; ++c4) {
        if (PH_LO <= 1 && c4 == 1 && w == 0 && lane == 0)
          __hip_atomic_store(P.flags + FB_OFF + (jj & 7) * 32 + xcd * 4 + (jj >> 3), 1u, __ATOMIC_RELAXED, __HIP_MEMORY_SCOPE_AGENT);
        INTERVAL(1, fa0, fb0, fa1, fb1); INTERVAL(2, fa1, fb1, fa0, fb0); INTERVAL(3, fa0, fb0, fa1, fb1);
        if (PH_LO <= 1 && c4 == 0) asm volatile("s_waitcnt vmcnt(0)" ::: "memory");
        if (c4 == 3 && kh2 == 0 && w == 0)
          wait_flags(P.flags + FB_OFF + (rt2 >> 1) * 32 + (lane & 31));
        INTERVAL(0, fa1, fb1, fa0, fb0);
      }
      INTERVAL(1, fa0, fb0, fa1, fb1);
      INTERVAL(2, fa1, fb1, fa0, fb0);
      MR4(0, 3, fa0, fb0, fa1, fb1); MR4(4, 3, fa0, fb0, fa1, fb1); MR4(8, 3, fa0, fb0, fa1, fb1); MR4(12, 3, fa0, fb0, fa1, fb1);
      MR4(16, 3, fa0, fb0, fa1, fb1); MR4(20, 3, fa0, fb0, fa1, fb1);
      MM(24, fa0, fb0); MM(25, fa0, fb0); MM(26, fa0, fb0); MM(27, fa0, fb0); MM(28, fa0, fb0); MM(29, fa0, fb0); MM(30, fa0, fb0); MM(31, fa0, fb0);
      SB(); asm volatile("s_waitcnt lgkmcnt(0)" ::: "memory"); SB();
#pragma unroll
      for (int i = 0; i < 32; ++i) MFMA16A(acc[i & 3][(i >> 2) & 3], fb1[i >> 4][(i >> 2) & 3], fa1[i >> 4][i & 3]);
#undef INTERVAL
#undef MR4
#undef MR
#undef MM
#undef RD
      asm volatile("s_nop 7\n\ts_nop 7" ::: "memory");
      SB();
      STAMP(3, 2);
      const int pbid = bid ^ 1;
      float* mine = P.part + (size_t)bid * 8192 + w4 * 2048 + lane * 4;
      f32x4 keep[2][4];
      if (kh2) {
#pragma unroll
        for (int i = 0; i < 8; ++i) { st16wt(mine + i * 256, __builtin_bit_cast(u32x4, acc[i >> 2][i & 3])); keep[i >> 2][i & 3] = acc[2 + (i >> 2)][i & 3]; }
      } else {
#pragma unroll
        for (int i = 0; i < 8; ++i) { st16wt(mine + i * 256, __builtin_bit_cast(u32x4, acc[2 + (i >> 2)][i & 3])); keep[i >> 2][i & 3] = acc[i >> 2][i & 3]; }
      }
      asm volatile("s_waitcnt vmcnt(0)" ::: "memory");
      if (lane == 0) __hip_atomic_store(P.flags + FX_OFF + bid * 16 + w4, 1u, __ATOMIC_RELAXED, __HIP_MEMORY_SCOPE_AGENT);
      {
        unsigned spins = 0;
        while (__builtin_amdgcn_readfirstlane(__hip_atomic_load(P.flags + FX_OFF + pbid * 16 + w4, __ATOMIC_RELAXED, __HIP_MEMORY_SCOPE_AGENT)) == 0u) {
          __builtin_amdgcn_s_sleep(1);
          if (++spins > (1u << 21)) break;
        }
      }
      STAMP(3, 4);
      const float* theirs = P.part + (size_t)pbid * 8192 + w4 * 2048 + lane * 4;
      f32x4 pv[8];
      asm volatile("global_load_dwordx4 %0, %8, off sc1\n\tglobal_load_dwordx4 %1, %8, off offset:1024 sc1\n\t"
                   "global_load_dwordx4 %2, %8, off offset:2048 sc1\n\tglobal_load_dwordx4 %3, %8, off offset:3072 sc1\n\t"
                   "global_load_dwordx4 %4, %9, off sc1\n\tglobal_load_dwordx4 %5, %9, off offset:1024 sc1\n\t"
                   "global_load_dwordx4 %6, %9, off offset:2048 sc1\n\tglobal_load_dwordx4 %7, %9, off offset:3072 sc1\n\t"
                   "s_waitcnt vmcnt(0)"
                   : "=&v"(pv[0]), "=&v"(pv[1]), "=&v"(pv[2]), "=&v"(pv[3]), "=&v"(pv[4]), "=&v"(pv[5]), "=&v"(pv[6]), "=&v"(pv[7])
                   : "v"(theirs), "v"(theirs + 1024) : "memory");
#pragma unroll
      for (int i = 0; i < 8; ++i) {
        const f32x4 v = keep[i >> 2][i & 3] + pv[i];
        const u32x2 xx = xv[i >> 2][i & 3];
        u32x2 o = {cvtpk(bf_lo(xx.x) + v[0], bf_hi(xx.x) + v[1]), cvtpk(bf_lo(xx.y) + v[2], bf_hi(xx.y) + v[3])};
        *(u32x2*)(P.out + (size_t)(orow + 16 * (i >> 2)) * DM + ocol + 16 * (i & 3)) = o;
      }
    }
    STAMP(3, 3);
  }
#undef G2_DMA
#undef G2_OFFS
#undef HSTORE
}

#define NLAUNCH 1

extern "C" void kernel_launch(void* const* d_in, const int* in_sizes, int n_in, void* d_out, int out_size, void* d_ws,
                              size_t ws_size, hipStream_t stream) {
  char* ws = (char*)d_ws;
  Params P;
  P.x = (const bf16_t*)d_in[0]; P.xn = (const bf16_t*)(ws + WS_XN);
  P.wqkv = (const bf16_t*)d_in[1]; P.wo = (const bf16_t*)d_in[2];
  P.wg = (const bf16_t*)d_in[4]; P.wu = (const bf16_t*)d_in[5]; P.wd = (const bf16_t*)d_in[6];
  P.Qb = (bf16_t*)(ws + WS_Q); P.Kb = (bf16_t*)(ws + WS_K); P.Vb = (bf16_t*)(ws + WS_V);
  P.AH = (bf16_t*)(ws + WS_AH); P.out = (bf16_t*)d_out;
  P.part = (float*)(ws + WS_PART); P.flags = (unsigned*)(ws + WS_FLAGS);
  ln_kernel<<<SEQ / 4, 256, 0, stream>>>(P.x, (bf16_t*)(ws + WS_XN), P.flags, P.wqkv, P.wg, P.wu);
  (void)hipFuncSetAttribute((const void*)fused_kernel<0, 2>, hipFuncAttributeMaxDynamicSharedMemorySize, LDS_TOTAL);
  fused_kernel<0, 2><<<256, 512, LDS_TOTAL, stream>>>(P);
}
```

```cpp
#include <hip/hip_runtime.h>
#include <hip/hip_bf16.h>
#include <stdint.h>

typedef unsigned short bf16_t;
typedef short bf16x8 __attribute__((ext_vector_type(8)));
typedef short s16x4 __attribute__((ext_vector_type(4)));
typedef float f32x4 __attribute__((ext_vector_type(4)));
typedef float f32x16 __attribute__((ext_vector_type(16)));
typedef float f32x2 __attribute__((ext_vector_type(2)));
typedef __bf16 bf16x2v __attribute__((ext_vector_type(2)));
typedef unsigned u32x4 __attribute__((ext_vector_type(4)));
typedef unsigned u32x2 __attribute__((ext_vector_type(2)));
#define LAS __attribute__((address_space(3)))
typedef LAS const char* lds_cptr;

constexpr int SEQ = 2048, DM = 1024, FF = 3584, NQKV = 3072, KA = 4608;

constexpr size_t WS_FLAGS = 0;
constexpr size_t WS_XN = 1u << 20;
constexpr size_t WS_Q = WS_XN + (size_t)SEQ * DM * 2;
constexpr size_t WS_K = WS_Q + (size_t)SEQ * DM * 2;
constexpr size_t WS_V = WS_K + (size_t)SEQ * DM * 2;
constexpr size_t WS_AH = WS_V + (size_t)SEQ * DM * 2;
constexpr size_t WS_PART = WS_AH + (size_t)SEQ * KA * 2;
typedef unsigned long long u64;
constexpr int FL_OFF = 256, FA_OFF = 512, FH_OFF = 768, FB_OFF = 1024, FX_OFF = 1280;

#define SB() __builtin_amdgcn_sched_barrier(0)
#define STAMP(kid, i) do {} while (0)

__device__ __forceinline__ unsigned cvtpk(float lo, float hi) {
  f32x2 v = {lo, hi};
  bf16x2v b = __builtin_convertvector(v, bf16x2v);
  return __builtin_bit_cast(unsigned, b);
}
__device__ __forceinline__ float bf_lo(unsigned u) { return __uint_as_float(u << 16); }
__device__ __forceinline__ float bf_hi(unsigned u) { return __uint_as_float(u & 0xffff0000u); }
__device__ __forceinline__ float rbf(float x) { return bf_lo(cvtpk(x, x)); }

__device__ __forceinline__ void glds16(const void* g, unsigned lds_dst) {
  unsigned keep;
  asm volatile("s_mov_b32 %0, m0\n\ts_mov_b32 m0, %2\n\ts_nop 0\n\tglobal_load_lds_dwordx4 %1, off\n\ts_mov_b32 m0, %0"
               : "=&s"(keep) : "v"(g), "s"(lds_dst) : "memory");
}
__device__ __forceinline__ void glds16s(const void* sbase, unsigned voff, unsigned lds_dst) {
  unsigned keep;
  asm volatile("s_mov_b32 %0, m0\n\ts_mov_b32 m0, %3\n\ts_nop 0\n\tglobal_load_lds_dwordx4 %1, %2\n\ts_mov_b32 m0, %0"
               : "=&s"(keep) : "v"(voff), "s"(sbase), "s"(lds_dst) : "memory");
}
__device__ __forceinline__ void glds16c(const void* sbase, unsigned voff, unsigned lds_dst) {
  unsigned keep;
  asm volatile("s_mov_b32 %0, m0\n\ts_mov_b32 m0, %3\n\ts_nop 0\n\tglobal_load_lds_dwordx4 %1, %2 sc1\n\ts_mov_b32 m0, %0"
               : "=&s"(keep) : "v"(voff), "s"(sbase), "s"(lds_dst) : "memory");
}
__device__ __forceinline__ void glds4c(const void* g, unsigned lds_dst) {
  unsigned keep;
  asm volatile("s_mov_b32 %0, m0\n\ts_mov_b32 m0, %2\n\ts_nop 0\n\tglobal_load_lds_dword %1, off sc1\n\ts_mov_b32 m0, %0"
               : "=&s"(keep) : "v"(g), "s"(lds_dst) : "memory");
}
__device__ __forceinline__ bf16x8 lds_rd128(lds_cptr p) { return *(const LAS bf16x8*)p; }
__device__ __forceinline__ s16x4 lds_tr(lds_cptr p) { return __builtin_amdgcn_ds_read_tr16_b64_v4i16((LAS s16x4*)p); }
__device__ __forceinline__ bf16x8 cat8(s16x4 a, s16x4 b) { return __builtin_shufflevector(a, b, 0, 1, 2, 3, 4, 5, 6, 7); }
__device__ __forceinline__ void st16wt(void* p, u32x4 v) {
  asm volatile("global_store_dwordx4 %0, %1, off sc1\n\ts_nop 1" :: "v"(p), "v"(v) : "memory");
}
__device__ __forceinline__ void wait_flags(const u64* f, u64 tok) {
  unsigned spins = 0;
  while (!__all(__hip_atomic_load(f, __ATOMIC_RELAXED, __HIP_MEMORY_SCOPE_AGENT) == tok)) {
    __builtin_amdgcn_s_sleep(3);
    if (++spins > (1u << 20)) break;
  }
  asm volatile("" ::: "memory");
}
__device__ __forceinline__ void set_flag(u64* f, u64 tok) { __hip_atomic_store(f, tok, __ATOMIC_RELAXED, __HIP_MEMORY_SCOPE_AGENT); }
__device__ __forceinline__ void publish(LAS unsigned* cnt, u64* flag, u64 tok, int lane) {
  asm volatile("s_waitcnt vmcnt(0)" ::: "memory");
  if (lane == 0) {
    const unsigned old = __hip_atomic_fetch_add(cnt, 1u, __ATOMIC_RELAXED, __HIP_MEMORY_SCOPE_WORKGROUP);
    if (old == 7u) set_flag(flag, tok);
  }
}
__device__ __forceinline__ void wg_wait(int w, int lane, const u64* f, u64 tok, volatile LAS unsigned* go) {
  if (w == 0) { wait_flags(f, tok); if (lane == 0) *go = 1u; }
  else { while (*go == 0u) __builtin_amdgcn_s_sleep(1); asm volatile("" ::: "memory"); }
}

#define MFMA16(a, b, c) __builtin_amdgcn_mfma_f32_16x16x32_bf16(a, b, c, 0, 0, 0)
#define MFMA32(a, b, c) __builtin_amdgcn_mfma_f32_32x32x16_bf16(a, b, c, 0, 0, 0)
#define WAITBAR(N) do { SB(); asm volatile("s_waitcnt vmcnt(" #N ") lgkmcnt(0)\n\ts_barrier" ::: "memory"); SB(); } while (0)
#define VMBAR(N) do { SB(); asm volatile("s_waitcnt vmcnt(" #N ")\n\ts_barrier" ::: "memory"); SB(); } while (0)
#define LGBAR() do { SB(); asm volatile("s_waitcnt lgkmcnt(0)\n\ts_barrier" ::: "memory"); SB(); } while (0)
#define BARRIER() do { SB(); asm volatile("s_barrier" ::: "memory"); SB(); } while (0)
#define MFMA16A(acc, a, b) asm volatile("v_mfma_f32_16x16x32_bf16 %0, %1, %2, %0" : "+v"(acc) : "v"(a), "v"(b))
#define MFMA16G(acc, a, b) MFMA16A(acc, a, b)
#define PINACC(x) asm volatile("" : "+v"(x))
#define ADV64(p, inc) asm volatile("v_mad_u64_u32 %0, vcc, %1, 1, %0" : "+v"(p) : "v"(inc) : "vcc")
#define PIN(x) asm volatile("" : "+v"(x))
#define EXP2(x) __builtin_amdgcn_exp2f(x)

constexpr int G1_AOFF = 0, G1_BOFF = 65536, G1_BREG = 10304, G1_BSLOT = 2 * G1_BREG;
constexpr int AT_STAGE = 32768, AT_Q = 98304, AT_EX = 131072, AT_EE = AT_EX + 16384, AT_HF = 150528;
constexpr int G2_SLOT = 32768, G2_NCH = 36;
constexpr int LDS_CTRL = 150016, LDS_SINK = 152576, LDS_TOTAL = LDS_SINK + 1024;

__device__ __forceinline__ float wave_sum(float v) {
#define ROR(n) v += __uint_as_float(__builtin_amdgcn_update_dpp(0u, __float_as_uint(v), 0x120 + (n), 0xf, 0xf, false))
  ROR(8); ROR(4); ROR(2); ROR(1);
#undef ROR
  auto a = __builtin_amdgcn_permlane16_swap(__float_as_uint(v), __float_as_uint(v), false, false);
  v = __uint_as_float(a[0]) + __uint_as_float(a[1]);
  auto b = __builtin_amdgcn_permlane32_swap(__float_as_uint(v), __float_as_uint(v), false, false);
  return __uint_as_float(b[0]) + __uint_as_float(b[1]);
}
__device__ __forceinline__ float max16(const f32x16& p) {
  float a = fmaxf(fmaxf(p[0], p[1]), p[2]), b = fmaxf(fmaxf(p[3], p[4]), p[5]);
  a = fmaxf(fmaxf(a, p[6]), p[7]); b = fmaxf(fmaxf(b, p[8]), p[9]);
  a = fmaxf(fmaxf(a, p[10]), p[11]); b = fmaxf(fmaxf(b, p[12]), p[13]);
  a = fmaxf(fmaxf(a, p[14]), p[15]);
  float m = fmaxf(a, b);
  auto rr = __builtin_amdgcn_permlane32_swap(__float_as_uint(m), __float_as_uint(m), false, false);
  return fmaxf(__uint_as_float(rr[0]), __uint_as_float(rr[1]));
}
__device__ __forceinline__ f32x16 splat16(float v) { f32x16 r;
#pragma unroll
  for (int i = 0; i < 16; ++i) r[i] = v;
  return r; }

struct Params {
  const bf16_t *x, *wqkv, *wg, *wu, *wo, *wd;
  bf16_t *xn, *Qb, *Kb, *Vb, *AH, *out;
  float* part; u64* flags;
};

__global__ __launch_bounds__(512, 2) void fused_kernel(Params P) {
  extern __shared__ __attribute__((aligned(16))) char smem[];
  const int w = __builtin_amdgcn_readfirstlane(threadIdx.x >> 6);
  const int g = w >> 2, w4 = w & 3;
  const int bid = blockIdx.x, xcd = bid & 7, jj = bid >> 3;
  const int cg = xcd * 4 + (jj & 3), rt = jj >> 2;
  const unsigned lds0 = (unsigned)(uintptr_t)smem;
  const lds_cptr L = (lds_cptr)smem;
  LAS unsigned* ctrl = (LAS unsigned*)((LAS char*)smem + LDS_CTRL);
  u64 tok;

  u32x4 hv[8];
  bf16_t* hp0; bf16_t* hp1;
#define HSTORE(i) st16wt((i) < 4 ? hp0 + (size_t)(i) * 16 * KA : hp1 + ((w4 & 1) ? (size_t)((i) & 1) * 32 * KA : (size_t)(((i) - 2) & 3) * 16 * KA), hv[i])
  const int h = xcd * 4 + (jj >> 3), qb = jj & 7;
  const char* kg = (const char*)(P.Kb + (size_t)h * SEQ * 32) + w4 * 2048;
  const char* vg = (const char*)(P.Vb + (size_t)h * SEQ * 32) + w4 * 2048;
#define DMA_ST(st, KH) do { const unsigned d_ = lds0 + ((st) & 3) * AT_STAGE + (KH) * 8192 + w4 * 2048; const int o_ = (KH) * 65536 + (st) * 8192; \
    glds16c(kg + o_, voff, d_); glds16c(kg + o_ + 1024, voff, d_ + 1024); \
    glds16c(vg + o_, voff, d_ + 16384); glds16c(vg + o_ + 1024, voff, d_ + 16384 + 1024); } while (0)

  {
    const int lane = threadIdx.x & 63;
    const int wm = 2 * g + (w4 >> 1), wn = w4 & 1;
    STAMP(1, 0);
    const int row0 = rt * 256;
    u32x4 xa, xb; u64 ep;
    asm volatile("global_load_dwordx4 %0, %3, off\n\tglobal_load_dwordx4 %1, %3, off offset:1024\n\tglobal_load_dwordx2 %2, %4, off sc1"
                 : "=&v"(xa), "=&v"(xb), "=&v"(ep) : "v"(P.x + (size_t)(bid * 8 + w) * DM + lane * 8), "v"(P.flags) : "memory");
    if (threadIdx.x < 8) ctrl[threadIdx.x] = 0u;
    asm volatile("s_waitcnt vmcnt(0) lgkmcnt(0)\n\ts_barrier" : "+v"(xa), "+v"(xb), "+v"(ep) :: "memory");
    STAMP(0, 0);
    tok = (((u64)(unsigned)__builtin_amdgcn_readfirstlane((unsigned)(ep >> 32)) << 32) | (unsigned)__builtin_amdgcn_readfirstlane((unsigned)ep)) + 1ull;
    {
      float v[16];
#pragma unroll
      for (int i = 0; i < 4; ++i) { v[2 * i] = bf_lo(xa[i]); v[2 * i + 1] = bf_hi(xa[i]); v[8 + 2 * i] = bf_lo(xb[i]); v[9 + 2 * i] = bf_hi(xb[i]); }
      float sm = 0.f;
#pragma unroll
      for (int i = 0; i < 16; ++i) sm += v[i];
      const float mean = wave_sum(sm) * (1.f / DM);
      float q = 0.f;
#pragma unroll
      for (int i = 0; i < 16; ++i) { const float d = v[i] - mean; q += d * d; }
      const float mb = rbf(mean), vb = rbf(wave_sum(q) * (1.f / DM));
      const float inv = 1.0f / rbf(sqrtf(rbf(vb + 1e-5f)));
      u32x4 oa, ob;
#pragma unroll
      for (int i = 0; i < 4; ++i) {
        const unsigned da = cvtpk(v[2 * i] - mb, v[2 * i + 1] - mb), db = cvtpk(v[8 + 2 * i] - mb, v[9 + 2 * i] - mb);
        oa[i] = cvtpk(bf_lo(da) * inv, bf_hi(da) * inv);
        ob[i] = cvtpk(bf_lo(db) * inv, bf_hi(db) * inv);
      }
      bf16_t* op = P.xn + (size_t)(bid * 8 + w) * DM + lane * 8;
      st16wt(op, oa); st16wt(op + 512, ob);
      STAMP(0, 4);
      publish(ctrl + 0, P.flags + FL_OFF + bid, tok, lane);
      STAMP(0, 1);
    }
    unsigned avoff0 = (unsigned)((lane >> 3) * 2048 + (((lane & 7) ^ ((lane >> 4) + 2)) * 16)), avoff1 = avoff0 ^ 64u;
    const char* abase = (const char*)P.xn + (size_t)(row0 + 32 * w4) * 2048;
#define DMA_A(hh, t) do { _Pragma("unroll") for (int _i = 0; _i < 4; ++_i) \
    glds16c(abase + (size_t)(hh) * 262144 + _i * 16384 + (t) * 128, (_i & 1) ? avoff1 : avoff0, \
            lds0 + G1_AOFF + ((t) & 1) * 32768 + (hh) * 16384 + (4 * w4 + _i) * 1024); } while (0)
    const char* bptr[4]; unsigned bld[4]; unsigned bdst[4];
#pragma unroll
    for (int i = 0; i < 4; ++i) {
      const int b = (i == 0) ? (g ? 16 + w4 : 4 * w4) : 4 * w4 + i;
      const int region = b >= 10 ? 1 : 0, bi = b - 10 * region;
      const int Pc = bi * 64 + lane;
      const int k = Pc / 20, pc = Pc - 20 * k, f = pc >> 1, e8 = (pc & 1) * 8;
      const bool isq = f >= 8 || (region && (f & 3) == 3);
      const int qf = f >= 8 ? (region ? f - 4 : f - 8) : 2 + (f >> 2);
      const bf16_t* mat = isq ? P.wqkv : (f >= 4 ? P.wu : P.wg);
      const int ld = isq ? NQKV : FF;
      const int col = e8 + (isq ? 96 * cg + 16 * qf : 112 * cg + 64 * region + 16 * (f & 3));
      bptr[i] = (const char*)(mat + (size_t)k * ld + col);
      bld[i] = (unsigned)ld * 64u;
      bdst[i] = lds0 + G1_BOFF + region * G1_BREG + bi * 1024 + (bi >= 5 ? 32 : 0);
    }
#define DMA_B(i, s) glds16(bptr[i], bdst[i] + ((s) & 3) * G1_BSLOT)
#define ADV_B() do { ADV64(bptr[0], bld[0]); ADV64(bptr[1], bld[1]); ADV64(bptr[2], bld[2]); ADV64(bptr[3], bld[3]); } while (0)
    if (g == 0) {
      DMA_B(0, 0); DMA_B(1, 0); DMA_B(2, 0); DMA_B(3, 0); ADV_B();
      DMA_B(0, 1); ADV_B();
      DMA_B(0, 2); DMA_B(1, 2); DMA_B(2, 2); DMA_B(3, 2); ADV_B();
    } else {
      DMA_B(0, 0); ADV_B();
      DMA_B(0, 1); DMA_B(1, 1); DMA_B(2, 1); DMA_B(3, 1); ADV_B();
      DMA_B(0, 2); ADV_B();
    }
    const int fr = lane & 15, fq = lane >> 4, kq = ((fq & 1) << 1) | (fq >> 1);
    const int aoff0 = G1_AOFF + (64 * wm + fr) * 128 + ((kq ^ (((fr >> 1) + 2) & 7)) * 16);
    lds_cptr pa0 = L + aoff0, pa1 = L + (aoff0 ^ 64);
    lds_cptr pb0 = L + (G1_BOFF + (8 * kq + (fr >> 2)) * 320 + (fq & 1) * 32 + 8 * (lane & 3) + G1_BREG * wn);
    lds_cptr pb1 = pb0 + 2 * G1_BSLOT;
    asm volatile("" : "+v"(pa0), "+v"(pa1), "+v"(pb0), "+v"(pb1), "+v"(avoff0), "+v"(avoff1));
    f32x4 acc[4][10];
#pragma unroll
    for (int m = 0; m < 4; ++m)
#pragma unroll
      for (int j = 0; j < 10; ++j) { acc[m][j] = (f32x4){0.f, 0.f, 0.f, 0.f}; PINACC(acc[m][j]); }
    bf16x8 af[4], bfr[10];
    wg_wait(w, lane, P.flags + FL_OFF + rt * 32 + (lane & 31), tok, ctrl + 3);
    STAMP(0, 2);
    DMA_A(g, 0);
    STAMP(0, 3);
    if (g == 0) { WAITBAR(0); } else { DMA_A(0, 1); WAITBAR(4); BARRIER(); }
    STAMP(1, 1);
#define STEP(U, ISSUE, WN, LASTBAR) do { \
    { const lds_cptr pa = ((U) & 1) ? pa1 : pa0; const lds_cptr pb = (((U) & 2) ? pb1 : pb0) + ((U) & 1) * G1_BSLOT; \
      _Pragma("unroll") for (int m = 0; m < 4; ++m) af[m] = lds_rd128(pa + ((U) >> 1) * 32768 + m * 2048); \
      _Pragma("unroll") for (int j = 0; j < 10; ++j) bfr[j] = cat8(lds_tr(pb + j * 32), lds_tr(pb + j * 32 + 1280)); } \
    if (ISSUE) { DMA_B(0, (U) + 3); \
      if (g == ((U) & 1)) { DMA_A(1 - ((U) & 1), 2 * it + ((U) >> 1) + 1 + ((U) & 1)); } \
      else { DMA_B(1, (U) + 3); DMA_B(2, (U) + 3); DMA_B(3, (U) + 3); } } \
    WAITBAR(WN); \
    _Pragma("unroll") for (int j = 0; j < 10; ++j) { _Pragma("unroll") for (int m = 0; m < 4; ++m) MFMA16G(acc[m][j], bfr[j], af[m]); \
      if ((ISSUE) && (j & 1) && j < 8) ADV64(bptr[j >> 1], bld[j >> 1]); } \
    if (LASTBAR) BARRIER(); } while (0)
    int it = 0;
#pragma unroll 1
    for (; it < 7; ++it) { STEP(0, true, 9, true); STEP(1, true, 9, true); STEP(2, true, 9, true); STEP(3, true, 9, true); }
    STEP(0, true, 9, true); STEP(1, false, 4, true); STEP(2, false, 0, true); STEP(3, false, 0, g == 0);
#undef STEP
    asm volatile("s_nop 7\n\ts_nop 7" ::: "memory");
    SB();
    STAMP(1, 2);
#undef DMA_A
#undef DMA_B
#undef ADV_B
    int tz = threadIdx.x; asm volatile("" : "+v"(tz));
    const int fr2 = tz & 15, fq2 = (tz >> 4) & 3, sel1 = fq2 & 1, c8 = (fq2 >> 1) * 8;
    const float qscale = 0.17677669529663687f * 1.4426950408889634f;
#define SWAP16(A, B, OUT) do { auto r0_ = __builtin_amdgcn_permlane16_swap(A.x, B.x, false, false); \
    auto r1_ = __builtin_amdgcn_permlane16_swap(A.y, B.y, false, false); OUT = (u32x4){r0_[0], r1_[0], r0_[1], r1_[1]}; } while (0)
#define SILU4(m, j, OUT) do { float hv_[4]; _Pragma("unroll") for (int r = 0; r < 4; ++r) { \
      const float tg_ = acc[m][j][r], tu_ = acc[m][(j) + 4][r]; \
      hv_[r] = (tg_ * tu_) * __builtin_amdgcn_rcpf(1.f + __builtin_amdgcn_exp2f(-1.4426950408889634f * tg_)); } \
    OUT = (u32x2){cvtpk(hv_[0], hv_[1]), cvtpk(hv_[2], hv_[3])}; } while (0)
#define PK4(V, SC, OUT) do { const f32x4 v_ = (V) * (SC); OUT = (u32x2){cvtpk(v_[0], v_[1]), cvtpk(v_[2], v_[3])}; } while (0)
    const int rowb = row0 + 64 * wm + fr2;
#define QKV_HEAD(hd, ja, jb) do { const int typ_ = (hd) >> 5, h_ = (hd) & 31; const float sa = typ_ == 0 ? qscale : 1.f; \
      bf16_t* dst; int mstep; \
      if (typ_ == 1) { dst = P.Kb + ((((size_t)h_ * 64 + (rowb >> 5)) * 2 + sel1) * 64 + (rowb & 31) + 4 * c8) * 8; mstep = 128; } \
      else { dst = (typ_ == 0 ? P.Qb : P.Vb) + ((size_t)h_ * SEQ + rowb) * 32 + 16 * sel1 + c8; mstep = 512; } \
      _Pragma("unroll") for (int m = 0; m < 4; ++m) { \
        u32x2 a_, b_; u32x4 o; PK4(acc[m][ja], sa, a_); PK4(acc[m][jb], sa, b_); SWAP16(a_, b_, o); \
        st16wt(dst + (m & 1) * mstep + (m >> 1) * 1024, o); } } while (0)
    __builtin_amdgcn_s_setprio(3);
    QKV_HEAD(3 * cg + 2 * wn, 8, 9);
    if (wn) QKV_HEAD(3 * cg + 1, 3, 7);
    __builtin_amdgcn_s_setprio(0);
    SB();
    bf16_t* hd = P.AH + (size_t)rowb * KA + DM + 112 * cg + 64 * wn + c8;
    hp0 = hd + 16 * sel1;
    hp1 = wn ? hd + 32 + (size_t)sel1 * 16 * KA : hd + 32 + 16 * sel1;
#pragma unroll
    for (int m = 0; m < 3; ++m) { u32x2 a_, b_; SILU4(m, 0, a_); SILU4(m, 1, b_); SWAP16(a_, b_, hv[m]); PIN(hv[m]); }
    STAMP(1, 4);
    publish(ctrl + 1, P.flags + FA_OFF + cg * 8 + rt, tok, tz & 63);
    { u32x2 a_, b_; SILU4(3, 0, a_); SILU4(3, 1, b_); SWAP16(a_, b_, hv[3]); PIN(hv[3]); }
    if (wn == 0) {
#pragma unroll
      for (int m = 0; m < 2; ++m) { u32x2 a_, b_; SILU4(m, 2, a_); SILU4(m, 3, b_); SWAP16(a_, b_, hv[6 + m]); PIN(hv[6 + m]); }
    }
    STAMP(1, 3);
    STAMP(2, 0);
    if (g == 0) {
      const int l6 = tz & 63;
      const int cgQ = h / 3, cgK = (32 + h) / 3, cgV = (64 + h) / 3;
      wg_wait(w, l6, P.flags + FA_OFF + (l6 < 8 ? cgK * 8 + l6 : (l6 < 16 ? cgV * 8 + l6 - 8 : cgQ * 8 + qb)), tok, ctrl + 4);
      const unsigned voff = l6 * 16;
      DMA_ST(0, 0); DMA_ST(0, 1);
      const char* qsrc = (const char*)(P.Qb + ((size_t)h * SEQ + qb * 256 + w4 * 64) * 32);
#pragma unroll
      for (int i = 0; i < 4; ++i) glds16c(qsrc + i * 1024, voff, lds0 + AT_Q + w4 * 4096 + i * 1024);
    }
    STAMP(2, 4);
    if (wn == 0) {
#pragma unroll
      for (int m = 2; m < 4; ++m) { u32x2 a_, b_; SILU4(m, 2, a_); SILU4(m, 3, b_); SWAP16(a_, b_, hv[2 + m]); }
    } else {
#pragma unroll
      for (int mp = 0; mp < 2; ++mp) { u32x2 a_, b_; SILU4(2 * mp, 2, a_); SILU4(2 * mp + 1, 2, b_); SWAP16(a_, b_, hv[4 + mp]); }
      hv[6] = hv[4]; hv[7] = hv[5];
    }
#undef SWAP16
#undef SILU4
#undef PK4
#undef QKV_HEAD
  }

  const int kh2 = xcd & 1, rt2 = (xcd >> 1) * 4 + (jj >> 3), ct2 = jj & 7;
  int c_issued = 0;
#define G2_DMA(c, avo0, avo1, bvo) do { int cc_ = (c) + (kh2 ? 0 : 16); if (cc_ >= G2_NCH) cc_ -= G2_NCH; const int kg_ = kh2 * (G2_NCH * 64) + cc_ * 64; \
    const char* ab_ = (const char*)P.AH + (size_t)(rt2 * 128 + 32 * w4) * (KA * 2) + kg_ * 2; \
    const char* bb_ = (kg_ < DM ? (const char*)P.wo + (size_t)kg_ * 2048 : (const char*)P.wd + (size_t)(kg_ - DM) * 2048) + ct2 * 256 + w4 * 32768; \
    const unsigned d_ = lds0 + ((c) & 3) * G2_SLOT + w4 * 4096; \
    _Pragma("unroll") for (int _i = 0; _i < 4; ++_i) { \
      glds16c(ab_ + (size_t)_i * (8 * KA * 2), (_i & 1) ? avo1 : avo0, d_ + _i * 1024); \
      glds16s(bb_ + _i * 8192, bvo, d_ + 16384 + _i * 1024); } } while (0)
#define G2_OFFS(lane) const unsigned avo0 = (unsigned)(((lane) >> 3) * (KA * 2) + ((((lane) & 7) ^ (((lane) >> 4) + 2)) * 16)), avo1 = avo0 ^ 64u; \
    const unsigned bvo = (unsigned)(((lane) >> 4) * 2048 + ((((lane) & 15) ^ (((lane) >> 4) << 1) ^ ((w4 & 1) << 3)) * 16))

  {
    int tb = threadIdx.x; asm volatile("" : "+v"(tb));
    const int lane = tb & 63;
    const int qg = w4, kh = g;
    const int hi = lane >> 5;
    const unsigned voff = lane * 16;
    const short one = (((lane >> 4) & 1) == ((lane >> 2) & 1)) ? (short)0x3f80 : (short)0;
    const bf16x8 sel = {one, one, one, one, one, one, one, one};
    volatile LAS unsigned* flag = (volatile LAS unsigned*)(ctrl + 2);
    const int gq = lane >> 4;
    const int koff = kh * 8192 + lane * 16;
    const int voffl = 16384 + kh * 8192 + (4 * (gq >> 1) + ((lane & 15) >> 2)) * 64 + (gq & 1) * 32 + (lane & 3) * 8;
    const f32x16 zero16 = splat16(0.f);
    f32x16 oA = zero16, oB = zero16;
    f32x4 lA = {0.f, 0.f, 0.f, 0.f}, lB = {0.f, 0.f, 0.f, 0.f};
    float mA, mB;
    const unsigned* hflag = (const unsigned*)(P.flags + FH_OFF + (lane >> 1) * 8 + (rt2 >> 1)) + (lane & 1);
    bf16x8 qa0, qa1, qb0, qb1;

#define LDK(K0, K1, p) do { K0 = lds_rd128(p); K1 = lds_rd128((p) + 1024); } while (0)
#define LDV(V0, V1, p) do { V0 = cat8(lds_tr(p), lds_tr((p) + 512)); V1 = cat8(lds_tr((p) + 1024), lds_tr((p) + 1536)); } while (0)
#define BF(x) __builtin_bit_cast(bf16x8, x)
    {
      WAITBAR(0);
      { const lds_cptr qp = L + AT_Q + qg * 4096 + (lane & 31) * 64 + hi * 16;
        qa0 = lds_rd128(qp); qa1 = lds_rd128(qp + 32); qb0 = lds_rd128(qp + 2048); qb1 = lds_rd128(qp + 2080); }
      bf16x8 kx0, kx1, ky0, ky1, vx0, vx1, vy0, vy1;
      LDK(kx0, kx1, L + koff);
      LDK(ky0, ky1, L + koff + 2048);
      LDV(vy0, vy1, L + voffl);
      DMA_ST(1, kh); DMA_ST(2, kh);
      f32x16 sA = MFMA32(kx0, qa0, zero16); sA = MFMA32(kx1, qa1, sA);
      f32x16 sB = MFMA32(kx0, qb0, zero16); sB = MFMA32(kx1, qb1, sB);
      mA = max16(sA); mB = max16(sB);
      f32x16 nA = splat16(-mA), nB = splat16(-mB);
      PIN(nA); PIN(nB);
      sA = MFMA32(kx0, qa0, nA); sA = MFMA32(kx1, qa1, sA);
      u32x4 pA0, pA1, pB0 = {0u, 0u, 0u, 0u}, pB1 = {0u, 0u, 0u, 0u};
      STAMP(2, 1);
#define E4(S, i) do { S[i] = EXP2(S[i]); S[i + 1] = EXP2(S[i + 1]); S[i + 2] = EXP2(S[i + 2]); S[i + 3] = EXP2(S[i + 3]); } while (0)
#define SLOT(SC, PC0, PC1, SN, K0, K1, Q0, Q1, NM, PP0, PP1, V0, V1, O, LS, LOADS) do { \
    SN = MFMA32(K0, Q0, NM); SB(); E4(SC, 0); PIN(SC); SB(); \
    SN = MFMA32(K1, Q1, SN); SB(); LOADS; SB(); E4(SC, 4); PIN(SC); SB(); \
    O = MFMA32(V0, BF(PP0), O); SB(); PC0[0] = cvtpk(SC[0], SC[1]); PC0[1] = cvtpk(SC[2], SC[3]); \
      SC[8] = EXP2(SC[8]); SC[9] = EXP2(SC[9]); SC[10] = EXP2(SC[10]); PIN(SC); PIN(PC0); SB(); \
    LS = MFMA16(sel, BF(PP0), LS); SB(); PC0[2] = cvtpk(SC[4], SC[5]); PC0[3] = cvtpk(SC[6], SC[7]); SC[11] = EXP2(SC[11]); PIN(SC); PIN(PC0); SB(); \
    O = MFMA32(V1, BF(PP1), O); SB(); E4(SC, 12); PIN(SC); SB(); \
    LS = MFMA16(sel, BF(PP1), LS); SB(); PC1[0] = cvtpk(SC[8], SC[9]); PC1[1] = cvtpk(SC[10], SC[11]); \
      PC1[2] = cvtpk(SC[12], SC[13]); PC1[3] = cvtpk(SC[14], SC[15]); PIN(PC1); SB(); } while (0)
#define SUB(KC0, KC1, KN0, KN1, VC0, VC1, VP0, VP1, vpu, kp2) do { \
    SLOT(sA, pA0, pA1, sB, KC0, KC1, qb0, qb1, nB, pB0, pB1, VP0, VP1, oB, lB, LDV(VC0, VC1, vpu)); \
    SLOT(sB, pB0, pB1, sA, KN0, KN1, qa0, qa1, nA, pA0, pA1, VC0, VC1, oA, lA, LDK(KC0, KC1, kp2)); } while (0)
      lds_cptr kb0 = L + koff, kb1 = L + koff + 2 * AT_STAGE, vb0 = L + voffl, vb1 = L + voffl + 2 * AT_STAGE;
      PIN(kb0); PIN(kb1); PIN(vb0); PIN(vb1);
#define KP(st) ((((st) & 2) ? kb1 : kb0) + ((st) & 1) * AT_STAGE)
#define VP(st) ((((st) & 2) ? vb1 : vb0) + ((st) & 1) * AT_STAGE)
#define HS(st, i) do { if ((st) < 2) HSTORE((4 * (st) + (i)) & 7); } while (0)
#define STAGE(st, WN, EXTRA) do { \
        SUB(kx0, kx1, ky0, ky1, vx0, vx1, vy0, vy1, VP(st), KP(st) + 4096); \
        HS(st, 0); \
        SUB(ky0, ky1, kx0, kx1, vy0, vy1, vx0, vx1, VP(st) + 2048, KP(st) + 6144); \
        VMBAR(WN); \
        HS(st, 1); \
        EXTRA; \
        if ((st) < 5) {   \
          const int pc = (st) * 8 + w, kb = bid * 36 + (pc < 36 ? pc : 35); \
          glds16s(kb < 2048 ? (const char*)P.wo + (size_t)kb * 1024 : (const char*)P.wd + (size_t)(kb - 2048) * 1024, voff, lds0 + LDS_SINK); \
          DMA_ST((st) + 3, kh); } \
        SUB(kx0, kx1, ky0, ky1, vx0, vx1, vy0, vy1, VP(st) + 4096, KP((st) + 1)); \
        HS(st, 2); \
        SUB(ky0, ky1, kx0, kx1, vy0, vy1, vx0, vx1, VP(st) + 6144, KP((st) + 1) + 2048); \
        HS(st, 3); } while (0)
      STAGE(0, 5, ); STAGE(1, 10, ); STAGE(2, 11, );
      STAGE(3, 5, if (w == 0 && lane == 0) set_flag(P.flags + FH_OFF + cg * 8 + rt, tok));
      STAGE(4, 5, );
      STAGE(5, 5, if (g == 1) glds4c(hflag, lds0 + AT_HF + w * 256));
      STAGE(6, 0, if (g == 1) {
                    const unsigned hf = *(volatile LAS unsigned*)((LAS char*)smem + AT_HF + w * 256 + lane * 4);
                    if (__all(hf == ((lane & 1) ? (unsigned)(tok >> 32) : (unsigned)tok))) {
                      G2_OFFS(lane); G2_DMA(0, avo0, avo1, bvo); G2_DMA(1, avo0, avo1, bvo); c_issued = 2; } });
      STAGE(7, 0, if (c_issued == 2) { G2_OFFS(lane); G2_DMA(2, avo0, avo1, bvo); c_issued = 3; });
#undef STAGE
#undef HS
#undef KP
#undef VP
      oB = MFMA32(vy0, BF(pB0), oB); lB = MFMA16(sel, BF(pB0), lB);
      oB = MFMA32(vy1, BF(pB1), oB); lB = MFMA16(sel, BF(pB1), lB);
#undef SUB
#undef SLOT
#undef E4
    }
    STAMP(2, 2);
    LAS u32x4* xw = (LAS u32x4*)((LAS char*)smem + AT_EX) + w * 128;
    const LAS u32x4* xr = (const LAS u32x4*)((LAS char*)smem + AT_EX) + (w ^ 4) * 128;
    LAS float* ew = (LAS float*)((LAS char*)smem + AT_EE) + w * 64;
    const LAS float* er = (const LAS float*)((LAS char*)smem + AT_EE) + (w ^ 4) * 64;
    f32x16 o; float e;
#pragma unroll 1
    for (int pass = 0; pass < 2; ++pass) {
      if (pass) {
        WAITBAR(0);
        c_issued = 0;
        DMA_ST(0, kh); DMA_ST(1, kh); DMA_ST(2, kh);
        oA = zero16; oB = zero16; lA = (f32x4){0.f, 0.f, 0.f, 0.f}; lB = lA;
        mA = -3.0e38f; mB = -3.0e38f;
#pragma unroll 1
        for (int st = 0; st < 8; ++st) {
          if (st < 6) WAITBAR(8); else if (st == 6) WAITBAR(4); else WAITBAR(0);
          if (st < 5) DMA_ST(st + 3, kh);
#pragma unroll 1
          for (int u = 0; u < 4; ++u) {
            bf16x8 k0, k1, v0, v1;
            LDK(k0, k1, L + (st & 3) * AT_STAGE + koff + u * 2048);
            LDV(v0, v1, L + (st & 3) * AT_STAGE + voffl + u * 2048);
#define SAFE_UNIT(Q0, Q1, M, O, LS) do { \
            f32x16 s_ = MFMA32(k0, Q0, zero16); s_ = MFMA32(k1, Q1, s_); \
            const float mn_ = fmaxf(M, max16(s_)); const float al_ = EXP2(M - mn_); M = mn_; \
            O *= al_; LS *= al_; u32x4 p0_, p1_; \
            _Pragma("unroll") for (int r = 0; r < 4; ++r) { p0_[r] = cvtpk(EXP2(s_[2 * r] - mn_), EXP2(s_[2 * r + 1] - mn_)); \
              p1_[r] = cvtpk(EXP2(s_[8 + 2 * r] - mn_), EXP2(s_[9 + 2 * r] - mn_)); } \
            O = MFMA32(v0, BF(p0_), O); LS = MFMA16(sel, BF(p0_), LS); O = MFMA32(v1, BF(p1_), O); LS = MFMA16(sel, BF(p1_), LS); } while (0)
            SAFE_UNIT(qa0, qa1, mA, oA, lA);
            SAFE_UNIT(qb0, qb1, mB, oB, lB);
#undef SAFE_UNIT
          }
        }
      }
      float chk = 0.f;
#pragma unroll
      for (int r = 0; r < 16; ++r) { chk = __builtin_fmaf(oA[r], 0.f, chk); chk = __builtin_fmaf(oB[r], 0.f, chk); }
      const bool bad = !(lA[0] < 1.0e36f) || !(lB[0] < 1.0e36f) || !(chk == 0.f);
      if (pass == 0 && __any(bad) && lane == 0) *flag = 1u;
      const float rA = __builtin_amdgcn_rcpf(lA[0]), rB = __builtin_amdgcn_rcpf(lB[0]);
      const float eA = mA + __builtin_amdgcn_logf(lA[0]), eB = mB + __builtin_amdgcn_logf(lB[0]);
#define XW(O, R) do { _Pragma("unroll") for (int r = 0; r < 2; ++r) xw[r * 64 + lane] = (u32x4){cvtpk(O[8 * r] * R, O[8 * r + 1] * R), \
          cvtpk(O[8 * r + 2] * R, O[8 * r + 3] * R), cvtpk(O[8 * r + 4] * R, O[8 * r + 5] * R), cvtpk(O[8 * r + 6] * R, O[8 * r + 7] * R)}; } while (0)
      if (kh) { XW(oA, rA); ew[lane] = eA; o = oB * rB; e = eB; }
      else { XW(oB, rB); ew[lane] = eB; o = oA * rA; e = eA; }
#undef XW
      LGBAR();
      if (pass == 1 || *flag == 0u) break;
    }
#undef LDK
#undef LDV
#undef BF
    {
      const float e2 = er[lane];
      const u32x4 t0 = xr[lane], t1 = xr[64 + lane];
      const float em = fmaxf(e, e2);
      float w1 = EXP2(e - em), w2 = EXP2(e2 - em);
      const float rs = __builtin_amdgcn_rcpf(w1 + w2);
      w1 *= rs; w2 *= rs;
      u32x2 pk[4];
#pragma unroll
      for (int r = 0; r < 4; ++r) {
        const unsigned ta = r < 2 ? t0[2 * r] : t1[2 * r - 4], tb2 = r < 2 ? t0[2 * r + 1] : t1[2 * r - 3];
        pk[r].x = cvtpk(o[4 * r] * w1 + bf_lo(ta) * w2, o[4 * r + 1] * w1 + bf_hi(ta) * w2);
        pk[r].y = cvtpk(o[4 * r + 2] * w1 + bf_lo(tb2) * w2, o[4 * r + 3] * w1 + bf_hi(tb2) * w2);
      }
      const int qrow = qb * 256 + qg * 64 + kh * 32 + (lane & 31);
      bf16_t* op = P.AH + (size_t)qrow * KA + h * 32 + 8 * hi;
#pragma unroll
      for (int k = 0; k < 4; k += 2) {
        auto rx = __builtin_amdgcn_permlane32_swap(pk[k].x, pk[k + 1].x, false, false);
        auto ry = __builtin_amdgcn_permlane32_swap(pk[k].y, pk[k + 1].y, false, false);
        st16wt(op + 8 * k, (u32x4){rx[0], ry[0], rx[1], ry[1]});
      }
    }
    STAMP(2, 3);
  }

  {
    int tc = threadIdx.x; asm volatile("" : "+v"(tc));
    const int lane = tc & 63;
    STAMP(3, 0);
    if (g == 1) {
      G2_OFFS(lane);
      if (c_issued < 3) {
        wait_flags(P.flags + FH_OFF + (lane & 31) * 8 + (rt2 >> 1), tok);
        G2_DMA(0, avo0, avo1, bvo); G2_DMA(1, avo0, avo1, bvo); G2_DMA(2, avo0, avo1, bvo); G2_DMA(3, avo0, avo1, bvo);
        VMBAR(24);
        VMBAR(16);
      } else {
        BARRIER(); G2_DMA(3, avo0, avo1, bvo); BARRIER();
      }
#pragma unroll 1
      for (int c = 0; c < G2_NCH - 4; ++c) { G2_DMA(c + 4, avo0, avo1, bvo); VMBAR(16); }
      VMBAR(8);
      VMBAR(0);
      if (bid == 0 && w == 4) {
#pragma unroll
        for (int i = 0; i < 4; ++i) wait_flags(P.flags + FA_OFF + i * 64 + lane, tok);
        if (lane == 0) set_flag(P.flags, tok);
      }
    } else {
      const int wm = w4 >> 1, wn = w4 & 1;
      const int fr = lane & 15, fq = lane >> 4, kq = ((fq & 1) << 1) | (fq >> 1), q4 = fr >> 2;
      const int aoff0 = (64 * wm + fr) * 128 + ((kq ^ (((fr >> 1) + 2) & 7)) * 16);
      const int boff0 = 16384 + (8 * kq + q4) * 256 + 8 * (lane & 3);
      const int bsw = q4 | ((fq & 1) << 2);
      lds_cptr pa[2][2], pb[2][4];
#pragma unroll
      for (int hh = 0; hh < 2; ++hh) {
        pa[hh][0] = L + (aoff0 + hh * 2 * G2_SLOT); pa[hh][1] = L + ((aoff0 ^ 64) + hh * 2 * G2_SLOT);
        PIN(pa[hh][0]); PIN(pa[hh][1]);
#pragma unroll
        for (int n = 0; n < 4; ++n) { pb[hh][n] = L + (boff0 + (((4 * wn + n) ^ bsw) * 32) + hh * 2 * G2_SLOT); PIN(pb[hh][n]); }
      }
      f32x4 acc[4][4];
#pragma unroll
      for (int m = 0; m < 4; ++m)
#pragma unroll
        for (int n = 0; n < 4; ++n) { acc[m][n] = (f32x4){0.f, 0.f, 0.f, 0.f}; PIN(acc[m][n]); }
      bf16x8 fa0[2][4], fb0[2][4], fa1[2][4], fb1[2][4];
      const int orow = rt2 * 128 + 64 * wm + 32 * kh2 + fr, ocol = ct2 * 128 + 64 * wn + 4 * fq;
      u32x2 xv[2][4];
#pragma unroll
      for (int mm = 0; mm < 2; ++mm)
#pragma unroll
        for (int n = 0; n < 4; ++n) xv[mm][n] = *(const u32x2*)(P.x + (size_t)(orow + 16 * mm) * DM + ocol + 16 * n);
#define RD(i, SL, FA, FB) do { constexpr int ks_ = (i) / 12, r_ = (i) % 12; \
      if (r_ < 4) FA[ks_][r_ & 3] = lds_rd128(pa[(SL) >> 1][ks_] + ((SL) & 1) * G2_SLOT + r_ * 2048); \
      else { constexpr int n_ = (r_ - 4) >> 1, t_ = (r_ - 4) & 1; \
        const s16x4 h_ = lds_tr(pb[(SL) >> 1][n_ < 0 ? 0 : n_] + ((SL) & 1) * G2_SLOT + ks_ * 8192 + t_ * 1024); \
        if (t_ == 0) { FB[ks_][n_ < 0 ? 0 : n_][0] = h_[0]; FB[ks_][n_ < 0 ? 0 : n_][1] = h_[1]; FB[ks_][n_ < 0 ? 0 : n_][2] = h_[2]; FB[ks_][n_ < 0 ? 0 : n_][3] = h_[3]; } \
        else { FB[ks_][n_ < 0 ? 0 : n_][4] = h_[0]; FB[ks_][n_ < 0 ? 0 : n_][5] = h_[1]; FB[ks_][n_ < 0 ? 0 : n_][6] = h_[2]; FB[ks_][n_ < 0 ? 0 : n_][7] = h_[3]; } } } while (0)
#define MM(i, FA, FB) MFMA16A(acc[(i) & 3][((i) >> 2) & 3], FB[(i) >> 4][((i) >> 2) & 3], FA[(i) >> 4][(i) & 3])
#define MR(i, SL, CA, CB, NA, NB) do { MM(i, CA, CB); RD(i, SL, NA, NB); SB(); } while (0)
#define MR4(i, SL, CA, CB, NA, NB) do { MR(i, SL, CA, CB, NA, NB); MR(i + 1, SL, CA, CB, NA, NB); MR(i + 2, SL, CA, CB, NA, NB); MR(i + 3, SL, CA, CB, NA, NB); } while (0)
#define INTERVAL(SL, CA, CB, NA, NB) do { \
      MR4(0, SL, CA, CB, NA, NB); MR4(4, SL, CA, CB, NA, NB); MR4(8, SL, CA, CB, NA, NB); MR4(12, SL, CA, CB, NA, NB); MR4(16, SL, CA, CB, NA, NB); MR4(20, SL, CA, CB, NA, NB); \
      MM(24, CA, CB); MM(25, CA, CB); MM(26, CA, CB); MM(27, CA, CB); MM(28, CA, CB); MM(29, CA, CB); \
      LGBAR(); MM(30, CA, CB); MM(31, CA, CB); SB(); } while (0)
      BARRIER();
      STAMP(3, 1);
      RD(0, 0, fa0, fb0); RD(1, 0, fa0, fb0); RD(2, 0, fa0, fb0); RD(3, 0, fa0, fb0); RD(4, 0, fa0, fb0); RD(5, 0, fa0, fb0);
      RD(6, 0, fa0, fb0); RD(7, 0, fa0, fb0); RD(8, 0, fa0, fb0); RD(9, 0, fa0, fb0); RD(10, 0, fa0, fb0); RD(11, 0, fa0, fb0);
      RD(12, 0, fa0, fb0); RD(13, 0, fa0, fb0); RD(14, 0, fa0, fb0); RD(15, 0, fa0, fb0); RD(16, 0, fa0, fb0); RD(17, 0, fa0, fb0);
      RD(18, 0, fa0, fb0); RD(19, 0, fa0, fb0); RD(20, 0, fa0, fb0); RD(21, 0, fa0, fb0); RD(22, 0, fa0, fb0); RD(23, 0, fa0, fb0);
      LGBAR();
#pragma unroll 1
      for (int c4 = 0; c4 < 8; ++c4) {
        if (c4 == 1 && w == 0 && lane == 0) set_flag(P.flags + FB_OFF + qb * 32 + h, tok);
        INTERVAL(1, fa0, fb0, fa1, fb1); INTERVAL(2, fa1, fb1, fa0, fb0); INTERVAL(3, fa0, fb0, fa1, fb1);
        if (c4 == 0) asm volatile("s_waitcnt vmcnt(0)" ::: "memory");
        if (c4 == 3 && kh2 == 0 && w == 0)
          wait_flags(P.flags + FB_OFF + (rt2 >> 1) * 32 + (lane & 31), tok);
        INTERVAL(0, fa1, fb1, fa0, fb0);
      }
      INTERVAL(1, fa0, fb0, fa1, fb1);
      INTERVAL(2, fa1, fb1, fa0, fb0);
      MR4(0, 3, fa0, fb0, fa1, fb1); MR4(4, 3, fa0, fb0, fa1, fb1); MR4(8, 3, fa0, fb0, fa1, fb1); MR4(12, 3, fa0, fb0, fa1, fb1);
      MR4(16, 3, fa0, fb0, fa1, fb1); MR4(20, 3, fa0, fb0, fa1, fb1);
      MM(24, fa0, fb0); MM(25, fa0, fb0); MM(26, fa0, fb0); MM(27, fa0, fb0); MM(28, fa0, fb0); MM(29, fa0, fb0); MM(30, fa0, fb0); MM(31, fa0, fb0);
      SB(); asm volatile("s_waitcnt lgkmcnt(0)" ::: "memory"); SB();
#pragma unroll
      for (int i = 0; i < 32; ++i) MFMA16A(acc[i & 3][(i >> 2) & 3], fb1[i >> 4][(i >> 2) & 3], fa1[i >> 4][i & 3]);
#undef INTERVAL
#undef MR4
#undef MR
#undef MM
#undef RD
      asm volatile("s_nop 7\n\ts_nop 7" ::: "memory");
      SB();
      STAMP(3, 2);
      const int pbid = bid ^ 1;
      float* mine = P.part + (size_t)bid * 8192 + w4 * 2048 + lane * 4;
      f32x4 keep[2][4];
      if (kh2) {
#pragma unroll
        for (int i = 0; i < 8; ++i) { st16wt(mine + i * 256, __builtin_bit_cast(u32x4, acc[i >> 2][i & 3])); keep[i >> 2][i & 3] = acc[2 + (i >> 2)][i & 3]; }
      } else {
#pragma unroll
        for (int i = 0; i < 8; ++i) { st16wt(mine + i * 256, __builtin_bit_cast(u32x4, acc[2 + (i >> 2)][i & 3])); keep[i >> 2][i & 3] = acc[i >> 2][i & 3]; }
      }
      asm volatile("s_waitcnt vmcnt(0)" ::: "memory");
      if (lane == 0) set_flag(P.flags + FX_OFF + bid * 4 + w4, tok);
      wait_flags(P.flags + FX_OFF + pbid * 4 + w4, tok);
      STAMP(3, 4);
      const float* theirs = P.part + (size_t)pbid * 8192 + w4 * 2048 + lane * 4;
      f32x4 pv[8];
      asm volatile("global_load_dwordx4 %0, %8, off sc1\n\tglobal_load_dwordx4 %1, %8, off offset:1024 sc1\n\t"
                   "global_load_dwordx4 %2, %8, off offset:2048 sc1\n\tglobal_load_dwordx4 %3, %8, off offset:3072 sc1\n\t"
                   "global_load_dwordx4 %4, %9, off sc1\n\tglobal_load_dwordx4 %5, %9, off offset:1024 sc1\n\t"
                   "global_load_dwordx4 %6, %9, off offset:2048 sc1\n\tglobal_load_dwordx4 %7, %9, off offset:3072 sc1\n\t"
                   "s_waitcnt vmcnt(0)"
                   : "=&v"(pv[0]), "=&v"(pv[1]), "=&v"(pv[2]), "=&v"(pv[3]), "=&v"(pv[4]), "=&v"(pv[5]), "=&v"(pv[6]), "=&v"(pv[7])
                   : "v"(theirs), "v"(theirs + 1024) : "memory");
#pragma unroll
      for (int i = 0; i < 8; ++i) {
        const f32x4 v = keep[i >> 2][i & 3] + pv[i];
        const u32x2 xx = xv[i >> 2][i & 3];
        u32x2 o = {cvtpk(bf_lo(xx.x) + v[0], bf_hi(xx.x) + v[1]), cvtpk(bf_lo(xx.y) + v[2], bf_hi(xx.y) + v[3])};
        *(u32x2*)(P.out + (size_t)(orow + 16 * (i >> 2)) * DM + ocol + 16 * (i & 3)) = o;
      }
    }
    STAMP(3, 3);
  }
#undef G2_DMA
#undef G2_OFFS
#undef HSTORE
#undef DMA_ST
}

extern "C" void kernel_launch(void* const* d_in, const int* in_sizes, int n_in, void* d_out, int out_size, void* d_ws,
                              size_t ws_size, hipStream_t stream) {
  char* ws = (char*)d_ws;
  Params P;
  P.x = (const bf16_t*)d_in[0];
  P.wqkv = (const bf16_t*)d_in[1]; P.wo = (const bf16_t*)d_in[2];
  P.wg = (const bf16_t*)d_in[4]; P.wu = (const bf16_t*)d_in[5]; P.wd = (const bf16_t*)d_in[6];
  P.xn = (bf16_t*)(ws + WS_XN);
  P.Qb = (bf16_t*)(ws + WS_Q); P.Kb = (bf16_t*)(ws + WS_K); P.Vb = (bf16_t*)(ws + WS_V);
  P.AH = (bf16_t*)(ws + WS_AH); P.out = (bf16_t*)d_out;
  P.part = (float*)(ws + WS_PART); P.flags = (u64*)(ws + WS_FLAGS);
  (void)hipFuncSetAttribute((const void*)fused_kernel, hipFuncAttributeMaxDynamicSharedMemorySize, LDS_TOTAL);
  fused_kernel<<<256, 512, LDS_TOTAL, stream>>>(P);
}
```

```cpp
#include <hip/hip_runtime.h>
#include <hip/hip_bf16.h>
#include <stdint.h>

typedef unsigned short bf16_t;
typedef short bf16x8 __attribute__((ext_vector_type(8)));
typedef short s16x4 __attribute__((ext_vector_type(4)));
typedef float f32x4 __attribute__((ext_vector_type(4)));
typedef float f32x16 __attribute__((ext_vector_type(16)));
typedef float f32x2 __attribute__((ext_vector_type(2)));
typedef __bf16 bf16x2v __attribute__((ext_vector_type(2)));
typedef unsigned u32x4 __attribute__((ext_vector_type(4)));
typedef unsigned u32x2 __attribute__((ext_vector_type(2)));
#define LAS __attribute__((address_space(3)))
typedef LAS const char* lds_cptr;

constexpr int SEQ = 2048, DM = 1024, FF = 3584, NQKV = 3072, KA = 4608;

constexpr size_t WS_FLAGS = 0;
constexpr size_t WS_XN = 1u << 20;
constexpr size_t WS_Q = WS_XN + (size_t)SEQ * DM * 2;
constexpr size_t WS_K = WS_Q + (size_t)SEQ * DM * 2;
constexpr size_t WS_V = WS_K + (size_t)SEQ * DM * 2;
constexpr size_t WS_AH = WS_V + (size_t)SEQ * DM * 2;
constexpr size_t WS_PART = WS_AH + (size_t)SEQ * KA * 2;
typedef unsigned long long u64;
constexpr int FL_OFF = 256, FA_OFF = 512, FH_OFF = 768, FB_OFF = 1024, FX_OFF = 1280;

#define SB() __builtin_amdgcn_sched_barrier(0)
#define STAMP(kid, i) do {} while (0)

__device__ __forceinline__ unsigned cvtpk(float lo, float hi) {
  f32x2 v = {lo, hi};
  bf16x2v b = __builtin_convertvector(v, bf16x2v);
  return __builtin_bit_cast(unsigned, b);
}
__device__ __forceinline__ float bf_lo(unsigned u) { return __uint_as_float(u << 16); }
__device__ __forceinline__ float bf_hi(unsigned u) { return __uint_as_float(u & 0xffff0000u); }
__device__ __forceinline__ float rbf(float x) { return bf_lo(cvtpk(x, x)); }

__device__ __forceinline__ void glds16(const void* g, unsigned lds_dst) {
  unsigned keep;
  asm volatile("s_mov_b32 %0, m0\n\ts_mov_b32 m0, %2\n\ts_nop 0\n\tglobal_load_lds_dwordx4 %1, off\n\ts_mov_b32 m0, %0"
               : "=&s"(keep) : "v"(g), "s"(lds_dst) : "memory");
}
__device__ __forceinline__ void glds16s(const void* sbase, unsigned voff, unsigned lds_dst) {
  unsigned keep;
  asm volatile("s_mov_b32 %0, m0\n\ts_mov_b32 m0, %3\n\ts_nop 0\n\tglobal_load_lds_dwordx4 %1, %2\n\ts_mov_b32 m0, %0"
               : "=&s"(keep) : "v"(voff), "s"(sbase), "s"(lds_dst) : "memory");
}
__device__ __forceinline__ void glds16c(const void* sbase, unsigned voff, unsigned lds_dst) {
  unsigned keep;
  asm volatile("s_mov_b32 %0, m0\n\ts_mov_b32 m0, %3\n\ts_nop 0\n\tglobal_load_lds_dwordx4 %1, %2 sc1\n\ts_mov_b32 m0, %0"
               : "=&s"(keep) : "v"(voff), "s"(sbase), "s"(lds_dst) : "memory");
}
__device__ __forceinline__ void glds4c(const void* g, unsigned lds_dst) {
  unsigned keep;
  asm volatile("s_mov_b32 %0, m0\n\ts_mov_b32 m0, %2\n\ts_nop 0\n\tglobal_load_lds_dword %1, off sc1\n\ts_mov_b32 m0, %0"
               : "=&s"(keep) : "v"(g), "s"(lds_dst) : "memory");
}
__device__ __forceinline__ bf16x8 lds_rd128(lds_cptr p) { return *(const LAS bf16x8*)p; }
__device__ __forceinline__ s16x4 lds_tr(lds_cptr p) { return __builtin_amdgcn_ds_read_tr16_b64_v4i16((LAS s16x4*)p); }
__device__ __forceinline__ bf16x8 cat8(s16x4 a, s16x4 b) { return __builtin_shufflevector(a, b, 0, 1, 2, 3, 4, 5, 6, 7); }
__device__ __forceinline__ void st16wt(void* p, u32x4 v) {
  asm volatile("global_store_dwordx4 %0, %1, off sc1\n\ts_nop 1" :: "v"(p), "v"(v) : "memory");
}
__device__ __forceinline__ void wait_flags(const u64* f, u64 tok) {
  unsigned spins = 0;
  while (!__all(__hip_atomic_load(f, __ATOMIC_RELAXED, __HIP_MEMORY_SCOPE_AGENT) == tok)) {
    __builtin_amdgcn_s_sleep(3);
    if (++spins > (1u << 20)) break;
  }
  asm volatile("" ::: "memory");
}
__device__ __forceinline__ void set_flag(u64* f, u64 tok) { __hip_atomic_store(f, tok, __ATOMIC_RELAXED, __HIP_MEMORY_SCOPE_AGENT); }
__device__ __forceinline__ void publish(LAS unsigned* cnt, u64* flag, u64 tok, int lane) {
  asm volatile("s_waitcnt vmcnt(0)" ::: "memory");
  if (lane == 0) {
    const unsigned old = __hip_atomic_fetch_add(cnt, 1u, __ATOMIC_RELAXED, __HIP_MEMORY_SCOPE_WORKGROUP);
    if (old == 7u) set_flag(flag, tok);
  }
}
__device__ __forceinline__ void wg_wait(int w, int lane, const u64* f, u64 tok, volatile LAS unsigned* go) {
  if (w == 0) { wait_flags(f, tok); if (lane == 0) *go = 1u; }
  else { while (*go == 0u) __builtin_amdgcn_s_sleep(1); asm volatile("" ::: "memory"); }
}

#define MFMA16(a, b, c) __builtin_amdgcn_mfma_f32_16x16x32_bf16(a, b, c, 0, 0, 0)
#define MFMA32(a, b, c) __builtin_amdgcn_mfma_f32_32x32x16_bf16(a, b, c, 0, 0, 0)
#define WAITBAR(N) do { SB(); asm volatile("s_waitcnt vmcnt(" #N ") lgkmcnt(0)\n\ts_barrier" ::: "memory"); SB(); } while (0)
#define VMBAR(N) do { SB(); asm volatile("s_waitcnt vmcnt(" #N ")\n\ts_barrier" ::: "memory"); SB(); } while (0)
#define LGBAR() do { SB(); asm volatile("s_waitcnt lgkmcnt(0)\n\ts_barrier" ::: "memory"); SB(); } while (0)
#define BARRIER() do { SB(); asm volatile("s_barrier" ::: "memory"); SB(); } while (0)
#define MFMA16A(acc, a, b) asm volatile("v_mfma_f32_16x16x32_bf16 %0, %1, %2, %0" : "+v"(acc) : "v"(a), "v"(b))
#define MFMA16G(acc, a, b) MFMA16A(acc, a, b)
#define PINACC(x) asm volatile("" : "+v"(x))
#define ADV64(p, inc) asm volatile("v_mad_u64_u32 %0, vcc, %1, 1, %0" : "+v"(p) : "v"(inc) : "vcc")
#define PIN(x) asm volatile("" : "+v"(x))
#define EXP2(x) __builtin_amdgcn_exp2f(x)

constexpr int G1_AOFF = 0, G1_BOFF = 65536, G1_BREG = 10304, G1_BSLOT = 2 * G1_BREG;
constexpr int AT_STAGE = 32768, AT_Q = 98304, AT_EX = 131072, AT_EE = AT_EX + 16384, AT_HF = 150528;
constexpr int G2_SLOT = 32768, G2_NCH = 36;
constexpr int LDS_CTRL = 150016, LDS_SINK = 152576, LDS_TOTAL = LDS_SINK + 1024;

__device__ __forceinline__ float wave_sum(float v) {
#define ROR(n) v += __uint_as_float(__builtin_amdgcn_update_dpp(0u, __float_as_uint(v), 0x120 + (n), 0xf, 0xf, false))
  ROR(8); ROR(4); ROR(2); ROR(1);
#undef ROR
  auto a = __builtin_amdgcn_permlane16_swap(__float_as_uint(v), __float_as_uint(v), false, false);
  v = __uint_as_float(a[0]) + __uint_as_float(a[1]);
  auto b = __builtin_amdgcn_permlane32_swap(__float_as_uint(v), __float_as_uint(v), false, false);
  return __uint_as_float(b[0]) + __uint_as_float(b[1]);
}
__device__ __forceinline__ float max16(const f32x16& p) {
  float a = fmaxf(fmaxf(p[0], p[1]), p[2]), b = fmaxf(fmaxf(p[3], p[4]), p[5]);
  a = fmaxf(fmaxf(a, p[6]), p[7]); b = fmaxf(fmaxf(b, p[8]), p[9]);
  a = fmaxf(fmaxf(a, p[10]), p[11]); b = fmaxf(fmaxf(b, p[12]), p[13]);
  a = fmaxf(fmaxf(a, p[14]), p[15]);
  float m = fmaxf(a, b);
  auto rr = __builtin_amdgcn_permlane32_swap(__float_as_uint(m), __float_as_uint(m), false, false);
  return fmaxf(__uint_as_float(rr[0]), __uint_as_float(rr[1]));
}
__device__ __forceinline__ f32x16 splat16(float v) { f32x16 r;
#pragma unroll
  for (int i = 0; i < 16; ++i) r[i] = v;
  return r; }

struct Params {
  const bf16_t *x, *wqkv, *wg, *wu, *wo, *wd;
  bf16_t *xn, *Qb, *Kb, *Vb, *AH, *out;
  float* part; u64* flags;
};

__global__ __launch_bounds__(512, 2) void fused_kernel(Params P) {
  extern __shared__ __attribute__((aligned(16))) char smem[];
  const int w = __builtin_amdgcn_readfirstlane(threadIdx.x >> 6);
  const int g = w >> 2, w4 = w & 3;
  const int bid = blockIdx.x, xcd = bid & 7, jj = bid >> 3;
  const int cg = xcd * 4 + (jj & 3), rt = jj >> 2;
  const unsigned lds0 = (unsigned)(uintptr_t)smem;
  const lds_cptr L = (lds_cptr)smem;
  LAS unsigned* ctrl = (LAS unsigned*)((LAS char*)smem + LDS_CTRL);
  u64 tok;

  u32x4 hv[8];
  bf16_t* hp0; bf16_t* hp1;
#define HSTORE(i) st16wt((i) < 4 ? hp0 + (size_t)(i) * 16 * KA : hp1 + ((w4 & 1) ? (size_t)((i) & 1) * 32 * KA : (size_t)(((i) - 2) & 3) * 16 * KA), hv[i])
  const int h = xcd * 4 + (jj >> 3), qb = jj & 7;
  const char* kg = (const char*)(P.Kb + (size_t)h * SEQ * 32) + w4 * 2048;
  const char* vg = (const char*)(P.Vb + (size_t)h * SEQ * 32) + w4 * 2048;
#define DMA_ST(st, KH) do { const unsigned d_ = lds0 + ((st) & 3) * AT_STAGE + (KH) * 8192 + w4 * 2048; const int o_ = (KH) * 65536 + (st) * 8192; \
    glds16c(kg + o_, voff, d_); glds16c(kg + o_ + 1024, voff, d_ + 1024); \
    glds16c(vg + o_, voff, d_ + 16384); glds16c(vg + o_ + 1024, voff, d_ + 16384 + 1024); } while (0)

  {
    const int lane = threadIdx.x & 63;
    const int wm = 2 * g + (w4 >> 1), wn = w4 & 1;
    STAMP(1, 0);
    const int row0 = rt * 256;
    u32x4 xa, xb; u64 ep;
    asm volatile("global_load_dwordx4 %0, %3, off\n\tglobal_load_dwordx4 %1, %3, off offset:1024\n\tglobal_load_dwordx2 %2, %4, off sc1"
                 : "=&v"(xa), "=&v"(xb), "=&v"(ep) : "v"(P.x + (size_t)(bid * 8 + w) * DM + lane * 8), "v"(P.flags) : "memory");
    if (threadIdx.x < 8) ctrl[threadIdx.x] = 0u;
    asm volatile("s_waitcnt vmcnt(0) lgkmcnt(0)\n\ts_barrier" : "+v"(xa), "+v"(xb), "+v"(ep) :: "memory");
    STAMP(0, 0);
    tok = (((u64)(unsigned)__builtin_amdgcn_readfirstlane((unsigned)(ep >> 32)) << 32) | (unsigned)__builtin_amdgcn_readfirstlane((unsigned)ep)) + 1ull;
    {
      float v[16];
#pragma unroll
      for (int i = 0; i < 4; ++i) { v[2 * i] = bf_lo(xa[i]); v[2 * i + 1] = bf_hi(xa[i]); v[8 + 2 * i] = bf_lo(xb[i]); v[9 + 2 * i] = bf_hi(xb[i]); }
      float sm = 0.f;
#pragma unroll
      for (int i = 0; i < 16; ++i) sm += v[i];
      const float mean = wave_sum(sm) * (1.f / DM);
      float q = 0.f;
#pragma unroll
      for (int i = 0; i < 16; ++i) { const float d = v[i] - mean; q += d * d; }
      const float mb = rbf(mean), vb = rbf(wave_sum(q) * (1.f / DM));
      const float inv = 1.0f / rbf(sqrtf(rbf(vb + 1e-5f)));
      u32x4 oa, ob;
#pragma unroll
      for (int i = 0; i < 4; ++i) {
        const unsigned da = cvtpk(v[2 * i] - mb, v[2 * i + 1] - mb), db = cvtpk(v[8 + 2 * i] - mb, v[9 + 2 * i] - mb);
        oa[i] = cvtpk(bf_lo(da) * inv, bf_hi(da) * inv);
        ob[i] = cvtpk(bf_lo(db) * inv, bf_hi(db) * inv);
      }
      bf16_t* op = P.xn + (size_t)(bid * 8 + w) * DM + lane * 8;
      st16wt(op, oa); st16wt(op + 512, ob);
      STAMP(0, 4);
      publish(ctrl + 0, P.flags + FL_OFF + bid, tok, lane);
      STAMP(0, 1);
    }
    unsigned avoff0 = (unsigned)((lane >> 3) * 2048 + (((lane & 7) ^ ((lane >> 4) + 2)) * 16)), avoff1 = avoff0 ^ 64u;
    const char* abase = (const char*)P.xn + (size_t)(row0 + 32 * w4) * 2048;
#define DMA_A(hh, t) do { _Pragma("unroll") for (int _i = 0; _i < 4; ++_i) \
    glds16c(abase + (size_t)(hh) * 262144 + _i * 16384 + (t) * 128, (_i & 1) ? avoff1 : avoff0, \
            lds0 + G1_AOFF + ((t) & 1) * 32768 + (hh) * 16384 + (4 * w4 + _i) * 1024); } while (0)
    const char* bptr[4]; unsigned bld[4]; unsigned bdst[4];
#pragma unroll
    for (int i = 0; i < 4; ++i) {
      const int b = (i == 0) ? (g ? 16 + w4 : 4 * w4) : 4 * w4 + i;
      const int region = b >= 10 ? 1 : 0, bi = b - 10 * region;
      const int Pc = bi * 64 + lane;
      const int k = Pc / 20, pc = Pc - 20 * k, f = pc >> 1, e8 = (pc & 1) * 8;
      const bool isq = f >= 8 || (region && (f & 3) == 3);
      const int qf = f >= 8 ? (region ? f - 4 : f - 8) : 2 + (f >> 2);
      const bf16_t* mat = isq ? P.wqkv : (f >= 4 ? P.wu : P.wg);
      const int ld = isq ? NQKV : FF;
      const int col = e8 + (isq ? 96 * cg + 16 * qf : 112 * cg + 64 * region + 16 * (f & 3));
      bptr[i] = (const char*)(mat + (size_t)k * ld + col);
      bld[i] = (unsigned)ld * 64u;
      bdst[i] = lds0 + G1_BOFF + region * G1_BREG + bi * 1024 + (bi >= 5 ? 32 : 0);
    }
#define DMA_B(i, s) glds16(bptr[i], bdst[i] + ((s) & 3) * G1_BSLOT)
#define ADV_B() do { ADV64(bptr[0], bld[0]); ADV64(bptr[1], bld[1]); ADV64(bptr[2], bld[2]); ADV64(bptr[3], bld[3]); } while (0)
    if (g == 0) {
      DMA_B(0, 0); DMA_B(1, 0); DMA_B(2, 0); DMA_B(3, 0); ADV_B();
      DMA_B(0, 1); ADV_B();
      DMA_B(0, 2); DMA_B(1, 2); DMA_B(2, 2); DMA_B(3, 2); ADV_B();
    } else {
      DMA_B(0, 0); ADV_B();
      DMA_B(0, 1); DMA_B(1, 1); DMA_B(2, 1); DMA_B(3, 1); ADV_B();
      DMA_B(0, 2); ADV_B();
    }
    const int fr = lane & 15, fq = lane >> 4, kq = ((fq & 1) << 1) | (fq >> 1);
    const int aoff0 = G1_AOFF + (64 * wm + fr) * 128 + ((kq ^ (((fr >> 1) + 2) & 7)) * 16);
    lds_cptr pa0 = L + aoff0, pa1 = L + (aoff0 ^ 64);
    lds_cptr pb0 = L + (G1_BOFF + (8 * kq + (fr >> 2)) * 320 + (fq & 1) * 32 + 8 * (lane & 3) + G1_BREG * wn);
    lds_cptr pb1 = pb0 + 2 * G1_BSLOT;
    asm volatile("" : "+v"(pa0), "+v"(pa1), "+v"(pb0), "+v"(pb1), "+v"(avoff0), "+v"(avoff1));
    f32x4 acc[4][10];
#pragma unroll
    for (int m = 0; m < 4; ++m)
#pragma unroll
      for (int j = 0; j < 10; ++j) { acc[m][j] = (f32x4){0.f, 0.f, 0.f, 0.f}; PINACC(acc[m][j]); }
    bf16x8 af[4], bfr[10];
    wg_wait(w, lane, P.flags + FL_OFF + rt * 32 + (lane & 31), tok, ctrl + 3);
    STAMP(0, 2);
    DMA_A(g, 0);
    STAMP(0, 3);
    if (g == 0) { WAITBAR(0); } else { DMA_A(0, 1); WAITBAR(4); BARRIER(); }
    STAMP(1, 1);
#define STEP(U, ISSUE, WN, LASTBAR) do { \
    { const lds_cptr pa = ((U) & 1) ? pa1 : pa0; const lds_cptr pb = (((U) & 2) ? pb1 : pb0) + ((U) & 1) * G1_BSLOT; \
      _Pragma("unroll") for (int m = 0; m < 4; ++m) af[m] = lds_rd128(pa + ((U) >> 1) * 32768 + m * 2048); \
      _Pragma("unroll") for (int j = 0; j < 10; ++j) bfr[j] = cat8(lds_tr(pb + j * 32), lds_tr(pb + j * 32 + 1280)); } \
    if (ISSUE) { DMA_B(0, (U) + 3); \
      if (g == ((U) & 1)) { DMA_A(1 - ((U) & 1), 2 * it + ((U) >> 1) + 1 + ((U) & 1)); } \
      else { DMA_B(1, (U) + 3); DMA_B(2, (U) + 3); DMA_B(3, (U) + 3); } } \
    WAITBAR(WN); \
    _Pragma("unroll") for (int j = 0; j < 10; ++j) { _Pragma("unroll") for (int m = 0; m < 4; ++m) MFMA16G(acc[m][j], bfr[j], af[m]); \
      if ((ISSUE) && (j & 1) && j < 8) ADV64(bptr[j >> 1], bld[j >> 1]); } \
    if (LASTBAR) BARRIER(); } while (0)
    int it = 0;
#pragma unroll 1
    for (; it < 7; ++it) { STEP(0, true, 9, true); STEP(1, true, 9, true); STEP(2, true, 9, true); STEP(3, true, 9, true); }
    STEP(0, true, 9, true); STEP(1, false, 4, true); STEP(2, false, 0, true); STEP(3, false, 0, g == 0);
#undef STEP
    asm volatile("s_nop 7\n\ts_nop 7" ::: "memory");
    SB();
    STAMP(1, 2);
#undef DMA_A
#undef DMA_B
#undef ADV_B
    int tz = threadIdx.x; asm volatile("" : "+v"(tz));
    const int fr2 = tz & 15, fq2 = (tz >> 4) & 3, sel1 = fq2 & 1, c8 = (fq2 >> 1) * 8;
    const float qscale = 0.17677669529663687f * 1.4426950408889634f;
#define SWAP16(A, B, OUT) do { auto r0_ = __builtin_amdgcn_permlane16_swap(A.x, B.x, false, false); \
    auto r1_ = __builtin_amdgcn_permlane16_swap(A.y, B.y, false, false); OUT = (u32x4){r0_[0], r1_[0], r0_[1], r1_[1]}; } while (0)
#define SILU4(m, j, OUT) do { float hv_[4]; _Pragma("unroll") for (int r = 0; r < 4; ++r) { \
      const float tg_ = acc[m][j][r], tu_ = acc[m][(j) + 4][r]; \
      hv_[r] = (tg_ * tu_) * __builtin_amdgcn_rcpf(1.f + __builtin_amdgcn_exp2f(-1.4426950408889634f * tg_)); } \
    OUT = (u32x2){cvtpk(hv_[0], hv_[1]), cvtpk(hv_[2], hv_[3])}; } while (0)
#define PK4(V, SC, OUT) do { const f32x4 v_ = (V) * (SC); OUT = (u32x2){cvtpk(v_[0], v_[1]), cvtpk(v_[2], v_[3])}; } while (0)
    const int rowb = row0 + 64 * wm + fr2;
#define QKV_HEAD(hd, ja, jb) do { const int typ_ = (hd) >> 5, h_ = (hd) & 31; const float sa = typ_ == 0 ? qscale : 1.f; \
      bf16_t* dst; int mstep; \
      if (typ_ == 1) { dst = P.Kb + ((((size_t)h_ * 64 + (rowb >> 5)) * 2 + sel1) * 64 + (rowb & 31) + 4 * c8) * 8; mstep = 128; } \
      else { dst = (typ_ == 0 ? P.Qb : P.Vb) + ((size_t)h_ * SEQ + rowb) * 32 + 16 * sel1 + c8; mstep = 512; } \
      _Pragma("unroll") for (int m = 0; m < 4; ++m) { \
        u32x2 a_, b_; u32x4 o; PK4(acc[m][ja], sa, a_); PK4(acc[m][jb], sa, b_); SWAP16(a_, b_, o); \
        st16wt(dst + (m & 1) * mstep + (m >> 1) * 1024, o); } } while (0)
    __builtin_amdgcn_s_setprio(3);
    QKV_HEAD(3 * cg + 2 * wn, 8, 9);
    if (wn) QKV_HEAD(3 * cg + 1, 3, 7);
    __builtin_amdgcn_s_setprio(0);
    SB();
    bf16_t* hd = P.AH + (size_t)rowb * KA + DM + 112 * cg + 64 * wn + c8;
    hp0 = hd + 16 * sel1;
    hp1 = wn ? hd + 32 + (size_t)sel1 * 16 * KA : hd + 32 + 16 * sel1;
#pragma unroll
    for (int m = 0; m < 3; ++m) { u32x2 a_, b_; SILU4(m, 0, a_); SILU4(m, 1, b_); SWAP16(a_, b_, hv[m]); PIN(hv[m]); }
    STAMP(1, 4);
    publish(ctrl + 1, P.flags + FA_OFF + cg * 8 + rt, tok, tz & 63);
    { u32x2 a_, b_; SILU4(3, 0, a_); SILU4(3, 1, b_); SWAP16(a_, b_, hv[3]); PIN(hv[3]); }
    if (wn == 0) {
#pragma unroll
      for (int m = 0; m < 2; ++m) { u32x2 a_, b_; SILU4(m, 2, a_); SILU4(m, 3, b_); SWAP16(a_, b_, hv[6 + m]); PIN(hv[6 + m]); }
    }
    STAMP(1, 3);
    STAMP(2, 0);
    if (g == 0) {
      const int l6 = tz & 63;
      const int cgQ = h / 3, cgK = (32 + h) / 3, cgV = (64 + h) / 3;
      wg_wait(w, l6, P.flags + FA_OFF + (l6 < 8 ? cgK * 8 + l6 : (l6 < 16 ? cgV * 8 + l6 - 8 : cgQ * 8 + qb)), tok, ctrl + 4);
      const unsigned voff = l6 * 16;
      DMA_ST(0, 0); DMA_ST(0, 1);
      const char* qsrc = (const char*)(P.Qb + ((size_t)h * SEQ + qb * 256 + w4 * 64) * 32);
#pragma unroll
      for (int i = 0; i < 4; ++i) glds16c(qsrc + i * 1024, voff, lds0 + AT_Q + w4 * 4096 + i * 1024);
    }
    STAMP(2, 4);
    if (wn == 0) {
#pragma unroll
      for (int m = 2; m < 4; ++m) { u32x2 a_, b_; SILU4(m, 2, a_); SILU4(m, 3, b_); SWAP16(a_, b_, hv[2 + m]); }
    } else {
#pragma unroll
      for (int mp = 0; mp < 2; ++mp) { u32x2 a_, b_; SILU4(2 * mp, 2, a_); SILU4(2 * mp + 1, 2, b_); SWAP16(a_, b_, hv[4 + mp]); }
      hv[6] = hv[4]; hv[7] = hv[5];
    }
#undef SWAP16
#undef SILU4
#undef PK4
#undef QKV_HEAD
  }

  const int kh2 = xcd & 1, rt2 = (xcd >> 1) * 4 + (jj >> 3), ct2 = jj & 7;
  int c_issued = 0;
#define G2_DMA(c, avo0, avo1, bvo) do { int cc_ = (c) + (kh2 ? 0 : 16); if (cc_ >= G2_NCH) cc_ -= G2_NCH; const int kg_ = kh2 * (G2_NCH * 64) + cc_ * 64; \
    const char* ab_ = (const char*)P.AH + (size_t)(rt2 * 128 + 32 * w4) * (KA * 2) + kg_ * 2; \
    const char* bb_ = (kg_ < DM ? (const char*)P.wo + (size_t)kg_ * 2048 : (const char*)P.wd + (size_t)(kg_ - DM) * 2048) + ct2 * 256 + w4 * 32768; \
    const unsigned d_ = lds0 + ((c) & 3) * G2_SLOT + w4 * 4096; \
    _Pragma("unroll") for (int _i = 0; _i < 4; ++_i) { \
      glds16c(ab_ + (size_t)_i * (8 * KA * 2), (_i & 1) ? avo1 : avo0, d_ + _i * 1024); \
      glds16s(bb_ + _i * 8192, bvo, d_ + 16384 + _i * 1024); } } while (0)
#define G2_OFFS(lane) const unsigned avo0 = (unsigned)(((lane) >> 3) * (KA * 2) + ((((lane) & 7) ^ (((lane) >> 4) + 2)) * 16)), avo1 = avo0 ^ 64u; \
    const unsigned bvo = (unsigned)(((lane) >> 4) * 2048 + ((((lane) & 15) ^ (((lane) >> 4) << 1) ^ ((w4 & 1) << 3)) * 16))

  {
    int tb = threadIdx.x; asm volatile("" : "+v"(tb));
    const int lane = tb & 63;
    const int qg = w4, kh = g;
    const int hi = lane >> 5;
    const unsigned voff = lane * 16;
    const short one = (((lane >> 4) & 1) == ((lane >> 2) & 1)) ? (short)0x3f80 : (short)0;
    const bf16x8 sel = {one, one, one, one, one, one, one, one};
    volatile LAS unsigned* flag = (volatile LAS unsigned*)(ctrl + 2);
    const int gq = lane >> 4;
    const int koff = kh * 8192 + lane * 16;
    const int voffl = 16384 + kh * 8192 + (4 * (gq >> 1) + ((lane & 15) >> 2)) * 64 + (gq & 1) * 32 + (lane & 3) * 8;
    const f32x16 zero16 = splat16(0.f);
    f32x16 oA = zero16, oB = zero16;
    f32x4 lA = {0.f, 0.f, 0.f, 0.f}, lB = {0.f, 0.f, 0.f, 0.f};
    float mA, mB;
    const unsigned* hflag = (const unsigned*)(P.flags + FH_OFF + (lane >> 1) * 8 + (rt2 >> 1)) + (lane & 1);
    bf16x8 qa0, qa1, qb0, qb1;

#define LDK(K0, K1, p) do { K0 = lds_rd128(p); K1 = lds_rd128((p) + 1024); } while (0)
#define LDV(V0, V1, p) do { V0 = cat8(lds_tr(p), lds_tr((p) + 512)); V1 = cat8(lds_tr((p) + 1024), lds_tr((p) + 1536)); } while (0)
#define BF(x) __builtin_bit_cast(bf16x8, x)
    {
      WAITBAR(0);
      { const lds_cptr qp = L + AT_Q + qg * 4096 + (lane & 31) * 64 + hi * 16;
        qa0 = lds_rd128(qp); qa1 = lds_rd128(qp + 32); qb0 = lds_rd128(qp + 2048); qb1 = lds_rd128(qp + 2080); }
      bf16x8 kx0, kx1, ky0, ky1, vx0, vx1, vy0, vy1;
      LDK(kx0, kx1, L + koff);
      LDK(ky0, ky1, L + koff + 2048);
      LDV(vy0, vy1, L + voffl);
      DMA_ST(1, kh); DMA_ST(2, kh);
      f32x16 sA = MFMA32(kx0, qa0, zero16); sA = MFMA32(kx1, qa1, sA);
      f32x16 sB = MFMA32(kx0, qb0, zero16); sB = MFMA32(kx1, qb1, sB);
      mA = max16(sA); mB = max16(sB);
      f32x16 nA = splat16(-mA), nB = splat16(-mB);
      PIN(nA); PIN(nB);
      sA = MFMA32(kx0, qa0, nA); sA = MFMA32(kx1, qa1, sA);
      u32x4 pA0, pA1, pB0 = {0u, 0u, 0u, 0u}, pB1 = {0u, 0u, 0u, 0u};
      STAMP(2, 1);
#define E4(S, i) do { S[i] = EXP2(S[i]); S[i + 1] = EXP2(S[i + 1]); S[i + 2] = EXP2(S[i + 2]); S[i + 3] = EXP2(S[i + 3]); } while (0)
#define SLOT(SC, PC0, PC1, SN, K0, K1, Q0, Q1, NM, PP0, PP1, V0, V1, O, LS, LOADS) do { \
    SN = MFMA32(K0, Q0, NM); SB(); E4(SC, 0); PIN(SC); SB(); \
    SN = MFMA32(K1, Q1, SN); SB(); LOADS; SB(); E4(SC, 4); PIN(SC); SB(); \
    O = MFMA32(V0, BF(PP0), O); SB(); PC0[0] = cvtpk(SC[0], SC[1]); PC0[1] = cvtpk(SC[2], SC[3]); \
      SC[8] = EXP2(SC[8]); SC[9] = EXP2(SC[9]); SC[10] = EXP2(SC[10]); PIN(SC); PIN(PC0); SB(); \
    LS = MFMA16(sel, BF(PP0), LS); SB(); PC0[2] = cvtpk(SC[4], SC[5]); PC0[3] = cvtpk(SC[6], SC[7]); SC[11] = EXP2(SC[11]); PIN(SC); PIN(PC0); SB(); \
    O = MFMA32(V1, BF(PP1), O); SB(); E4(SC, 12); PIN(SC); SB(); \
    LS = MFMA16(sel, BF(PP1), LS); SB(); PC1[0] = cvtpk(SC[8], SC[9]); PC1[1] = cvtpk(SC[10], SC[11]); \
      PC1[2] = cvtpk(SC[12], SC[13]); PC1[3] = cvtpk(SC[14], SC[15]); PIN(PC1); SB(); } while (0)
#define SUB(KC0, KC1, KN0, KN1, VC0, VC1, VP0, VP1, vpu, kp2) do { \
    SLOT(sA, pA0, pA1, sB, KC0, KC1, qb0, qb1, nB, pB0, pB1, VP0, VP1, oB, lB, LDV(VC0, VC1, vpu)); \
    SLOT(sB, pB0, pB1, sA, KN0, KN1, qa0, qa1, nA, pA0, pA1, VC0, VC1, oA, lA, LDK(KC0, KC1, kp2)); } while (0)
      lds_cptr kb0 = L + koff, kb1 = L + koff + 2 * AT_STAGE, vb0 = L + voffl, vb1 = L + voffl + 2 * AT_STAGE;
      PIN(kb0); PIN(kb1); PIN(vb0); PIN(vb1);
#define KP(st) ((((st) & 2) ? kb1 : kb0) + ((st) & 1) * AT_STAGE)
#define VP(st) ((((st) & 2) ? vb1 : vb0) + ((st) & 1) * AT_STAGE)
#define HS(st, i) do { if ((st) < 2) HSTORE((4 * (st) + (i)) & 7); } while (0)
#define STAGE(st, WN, EXTRA) do { \
        SUB(kx0, kx1, ky0, ky1, vx0, vx1, vy0, vy1, VP(st), KP(st) + 4096); \
        HS(st, 0); \
        SUB(ky0, ky1, kx0, kx1, vy0, vy1, vx0, vx1, VP(st) + 2048, KP(st) + 6144); \
        VMBAR(WN); \
        HS(st, 1); \
        EXTRA; \
        if ((st) < 5) {   \
          const int pc = (st) * 8 + w, kb = bid * 36 + (pc < 36 ? pc : 35); \
          glds16s(kb < 2048 ? (const char*)P.wo + (size_t)kb * 1024 : (const char*)P.wd + (size_t)(kb - 2048) * 1024, voff, lds0 + LDS_SINK); \
          DMA_ST((st) + 3, kh); } \
        SUB(kx0, kx1, ky0, ky1, vx0, vx1, vy0, vy1, VP(st) + 4096, KP((st) + 1)); \
        HS(st, 2); \
        SUB(ky0, ky1, kx0, kx1, vy0, vy1, vx0, vx1, VP(st) + 6144, KP((st) + 1) + 2048); \
        HS(st, 3); } while (0)
      STAGE(0, 5, ); STAGE(1, 10, ); STAGE(2, 11, );
      STAGE(3, 5, if (w == 0 && lane == 0) set_flag(P.flags + FH_OFF + cg * 8 + rt, tok));
      STAGE(4, 5, );
      STAGE(5, 5, if (g == 1) glds4c(hflag, lds0 + AT_HF + w * 256));
      STAGE(6, 0, if (g == 1) {
                    const unsigned hf = *(volatile LAS unsigned*)((LAS char*)smem + AT_HF + w * 256 + lane * 4);
                    if (__all(hf == ((lane & 1) ? (unsigned)(tok >> 32) : (unsigned)tok))) {
                      G2_OFFS(lane); G2_DMA(0, avo0, avo1, bvo); G2_DMA(1, avo0, avo1, bvo); c_issued = 2; } });
      STAGE(7, 0, if (c_issued == 2) { G2_OFFS(lane); G2_DMA(2, avo0, avo1, bvo); c_issued = 3; });
#undef STAGE
#undef HS
#undef KP
#undef VP
      oB = MFMA32(vy0, BF(pB0), oB); lB = MFMA16(sel, BF(pB0), lB);
      oB = MFMA32(vy1, BF(pB1), oB); lB = MFMA16(sel, BF(pB1), lB);
#undef SUB
#undef SLOT
#undef E4
    }
    STAMP(2, 2);
    LAS u32x4* xw = (LAS u32x4*)((LAS char*)smem + AT_EX) + w4 * 256;
    LAS float* ew = (LAS float*)((LAS char*)smem + AT_EE) + w4 * 128;
    float eA, eB;
#define FIN1(FIRST) do { \
        \
      float chk = 0.f; \
      _Pragma("unroll") for (int r = 0; r < 16; ++r) { chk = __builtin_fmaf(oA[r], 0.f, chk); chk = __builtin_fmaf(oB[r], 0.f, chk); } \
      const bool bad = !(lA[0] < 1.0e36f) || !(lB[0] < 1.0e36f) || !(chk == 0.f); \
      if (FIRST) { if (__any(bad) && lane == 0) *flag = 1u; \
                   if (c_issued == 3 && lane == 0) __hip_atomic_fetch_add(ctrl + 5, 1u, __ATOMIC_RELAXED, __HIP_MEMORY_SCOPE_WORKGROUP); } \
      oA *= __builtin_amdgcn_rcpf(lA[0]); oB *= __builtin_amdgcn_rcpf(lB[0]); \
      eA = mA + __builtin_amdgcn_logf(lA[0]); eB = mB + __builtin_amdgcn_logf(lB[0]);     \
      if (g == 0) { \
        _Pragma("unroll") for (int r = 0; r < 2; ++r) { \
          xw[r * 64 + lane] = (u32x4){cvtpk(oA[8 * r], oA[8 * r + 1]), cvtpk(oA[8 * r + 2], oA[8 * r + 3]), cvtpk(oA[8 * r + 4], oA[8 * r + 5]), cvtpk(oA[8 * r + 6], oA[8 * r + 7])}; \
          xw[128 + r * 64 + lane] = (u32x4){cvtpk(oB[8 * r], oB[8 * r + 1]), cvtpk(oB[8 * r + 2], oB[8 * r + 3]), cvtpk(oB[8 * r + 4], oB[8 * r + 5]), cvtpk(oB[8 * r + 6], oB[8 * r + 7])}; } \
        ew[lane] = eA; ew[64 + lane] = eB; } } while (0)
#define SAFE_UNIT(Q0, Q1, M, O, LS) do { \
            f32x16 s_ = MFMA32(k0, Q0, zero16); s_ = MFMA32(k1, Q1, s_); \
            const float mn_ = fmaxf(M, max16(s_)); const float al_ = EXP2(M - mn_); M = mn_; \
            O *= al_; LS *= al_; u32x4 p0_, p1_; \
            _Pragma("unroll") for (int r = 0; r < 4; ++r) { p0_[r] = cvtpk(EXP2(s_[2 * r] - mn_), EXP2(s_[2 * r + 1] - mn_)); \
              p1_[r] = cvtpk(EXP2(s_[8 + 2 * r] - mn_), EXP2(s_[9 + 2 * r] - mn_)); } \
            O = MFMA32(v0, BF(p0_), O); LS = MFMA16(sel, BF(p0_), LS); O = MFMA32(v1, BF(p1_), O); LS = MFMA16(sel, BF(p1_), LS); } while (0)
#define SAFE_PASS() do { \
        WAITBAR(0); \
        DMA_ST(0, kh); DMA_ST(1, kh); DMA_ST(2, kh); \
        oA = zero16; oB = zero16; lA = (f32x4){0.f, 0.f, 0.f, 0.f}; lB = lA; \
        mA = -3.0e38f; mB = -3.0e38f; \
        _Pragma("unroll 1") for (int st = 0; st < 8; ++st) { \
          if (st < 6) WAITBAR(8); else if (st == 6) WAITBAR(4); else WAITBAR(0); \
          if (st < 5) DMA_ST(st + 3, kh); \
          _Pragma("unroll 1") for (int u = 0; u < 4; ++u) { \
            bf16x8 k0, k1, v0, v1; \
            LDK(k0, k1, L + (st & 3) * AT_STAGE + koff + u * 2048); \
            LDV(v0, v1, L + (st & 3) * AT_STAGE + voffl + u * 2048); \
            SAFE_UNIT(qa0, qa1, mA, oA, lA); \
            SAFE_UNIT(qb0, qb1, mB, oB, lB); } } \
        FIN1(false); LGBAR(); } while (0)
    FIN1(true);
    STAMP(2, 3);

    STAMP(3, 0);
    {
      const int wm = w4 >> 1, wn = w4 & 1;
      const int fr = lane & 15, fq = lane >> 4, kq = ((fq & 1) << 1) | (fq >> 1), q4 = fr >> 2;
      const int aoff0 = (64 * wm + fr) * 128 + ((kq ^ (((fr >> 1) + 2) & 7)) * 16);
      const int boff0 = 16384 + (8 * kq + q4) * 256 + 8 * (lane & 3);
      const int bsw = q4 | ((fq & 1) << 2);
      lds_cptr pa[2][2], pb[2][4];
#pragma unroll
      for (int hh = 0; hh < 2; ++hh) {
        pa[hh][0] = L + (aoff0 + hh * 2 * G2_SLOT); pa[hh][1] = L + ((aoff0 ^ 64) + hh * 2 * G2_SLOT);
        PIN(pa[hh][0]); PIN(pa[hh][1]);
#pragma unroll
        for (int n = 0; n < 4; ++n) { pb[hh][n] = L + (boff0 + (((4 * wn + n) ^ bsw) * 32) + hh * 2 * G2_SLOT); PIN(pb[hh][n]); }
      }
      bf16x8 fa0[2][4], fb0[2][4], fa1[2][4], fb1[2][4];
      const int orow = rt2 * 128 + 64 * wm + 32 * kh2 + fr, ocol = ct2 * 128 + 64 * wn + 4 * fq;
#define RD(i, SL, FA, FB) do { constexpr int ks_ = (i) / 12, r_ = (i) % 12; \
      if (r_ < 4) FA[ks_][r_ & 3] = lds_rd128(pa[(SL) >> 1][ks_] + ((SL) & 1) * G2_SLOT + r_ * 2048); \
      else { constexpr int n_ = (r_ - 4) >> 1, t_ = (r_ - 4) & 1; \
        const s16x4 h_ = lds_tr(pb[(SL) >> 1][n_ < 0 ? 0 : n_] + ((SL) & 1) * G2_SLOT + ks_ * 8192 + t_ * 1024); \
        if (t_ == 0) { FB[ks_][n_ < 0 ? 0 : n_][0] = h_[0]; FB[ks_][n_ < 0 ? 0 : n_][1] = h_[1]; FB[ks_][n_ < 0 ? 0 : n_][2] = h_[2]; FB[ks_][n_ < 0 ? 0 : n_][3] = h_[3]; } \
        else { FB[ks_][n_ < 0 ? 0 : n_][4] = h_[0]; FB[ks_][n_ < 0 ? 0 : n_][5] = h_[1]; FB[ks_][n_ < 0 ? 0 : n_][6] = h_[2]; FB[ks_][n_ < 0 ? 0 : n_][7] = h_[3]; } } } while (0)
#define MM(i, FA, FB) MFMA16A(acc[(i) & 3][((i) >> 2) & 3], FB[(i) >> 4][((i) >> 2) & 3], FA[(i) >> 4][(i) & 3])
#define MR(i, SL, CA, CB, NA, NB) do { MM(i, CA, CB); RD(i, SL, NA, NB); SB(); } while (0)
#define MR4(i, SL, CA, CB, NA, NB) do { MR(i, SL, CA, CB, NA, NB); MR(i + 1, SL, CA, CB, NA, NB); MR(i + 2, SL, CA, CB, NA, NB); MR(i + 3, SL, CA, CB, NA, NB); } while (0)
#define INTERVAL(SL, CA, CB, NA, NB) do { \
      MR4(0, SL, CA, CB, NA, NB); MR4(4, SL, CA, CB, NA, NB); MR4(8, SL, CA, CB, NA, NB); MR4(12, SL, CA, CB, NA, NB); MR4(16, SL, CA, CB, NA, NB); MR4(20, SL, CA, CB, NA, NB); \
      MM(24, CA, CB); MM(25, CA, CB); MM(26, CA, CB); MM(27, CA, CB); MM(28, CA, CB); MM(29, CA, CB); \
      LGBAR(); MM(30, CA, CB); MM(31, CA, CB); SB(); } while (0)
#define RDALL() do { \
      RD(0, 0, fa0, fb0); RD(1, 0, fa0, fb0); RD(2, 0, fa0, fb0); RD(3, 0, fa0, fb0); RD(4, 0, fa0, fb0); RD(5, 0, fa0, fb0); \
      RD(6, 0, fa0, fb0); RD(7, 0, fa0, fb0); RD(8, 0, fa0, fb0); RD(9, 0, fa0, fb0); RD(10, 0, fa0, fb0); RD(11, 0, fa0, fb0); \
      RD(12, 0, fa0, fb0); RD(13, 0, fa0, fb0); RD(14, 0, fa0, fb0); RD(15, 0, fa0, fb0); RD(16, 0, fa0, fb0); RD(17, 0, fa0, fb0); \
      RD(18, 0, fa0, fb0); RD(19, 0, fa0, fb0); RD(20, 0, fa0, fb0); RD(21, 0, fa0, fb0); RD(22, 0, fa0, fb0); RD(23, 0, fa0, fb0); } while (0)
      if (g == 0) RDALL();
      LGBAR();
      const bool ovf = *flag != 0u, fast = !ovf && *(volatile LAS unsigned*)(ctrl + 5) == 4u;
      G2_OFFS(lane);
      if (!fast) {
        if (ovf) SAFE_PASS();
        if (g == 1) {
          wait_flags(P.flags + FH_OFF + (lane & 31) * 8 + (rt2 >> 1), tok);
          G2_DMA(0, avo0, avo1, bvo); G2_DMA(1, avo0, avo1, bvo); G2_DMA(2, avo0, avo1, bvo);
        }
        VMBAR(16);
        if (g == 0) RDALL();
        WAITBAR(8);
      }
#undef RDALL
      if (g == 1) {
#define FIN2(O, E, BL) do { \
      const float e2 = ew[(BL) * 64 + lane]; const u32x4 t0 = xw[(BL) * 128 + lane], t1 = xw[(BL) * 128 + 64 + lane]; \
      const float em = fmaxf(E, e2); float w1 = EXP2(E - em), w2 = EXP2(e2 - em); \
      const float rs = __builtin_amdgcn_rcpf(w1 + w2); w1 *= rs; w2 *= rs; u32x2 pk[4]; \
      _Pragma("unroll") for (int r = 0; r < 4; ++r) { \
        const unsigned ta = r < 2 ? t0[2 * r] : t1[2 * r - 4], tb2 = r < 2 ? t0[2 * r + 1] : t1[2 * r - 3]; \
        pk[r].x = cvtpk(O[4 * r] * w1 + bf_lo(ta) * w2, O[4 * r + 1] * w1 + bf_hi(ta) * w2); \
        pk[r].y = cvtpk(O[4 * r + 2] * w1 + bf_lo(tb2) * w2, O[4 * r + 3] * w1 + bf_hi(tb2) * w2); } \
      bf16_t* op = P.AH + (size_t)(qb * 256 + qg * 64 + (BL) * 32 + (lane & 31)) * KA + h * 32 + 8 * hi; \
      _Pragma("unroll") for (int k = 0; k < 4; k += 2) { \
        auto rx = __builtin_amdgcn_permlane32_swap(pk[k].x, pk[k + 1].x, false, false); \
        auto ry = __builtin_amdgcn_permlane32_swap(pk[k].y, pk[k + 1].y, false, false); \
        st16wt(op + 8 * k, (u32x4){rx[0], ry[0], rx[1], ry[1]}); } } while (0)
      G2_DMA(3, avo0, avo1, bvo); G2_DMA(4, avo0, avo1, bvo); FIN2(oA, eA, 0); FIN2(oB, eB, 1);
#undef FIN2
      }
      SB();
      if (g == 1) {
      VMBAR(20);
      G2_DMA(5, avo0, avo1, bvo); VMBAR(20);
      G2_DMA(6, avo0, avo1, bvo); VMBAR(20);
      G2_DMA(7, avo0, avo1, bvo); VMBAR(16);
      if (w == 4 && lane == 0) set_flag(P.flags + FB_OFF + qb * 32 + h, tok);
#pragma unroll 1
      for (int c = 4; c < G2_NCH - 4; ++c) { G2_DMA(c + 4, avo0, avo1, bvo); VMBAR(16); }
      VMBAR(8);
      VMBAR(0);
      if (bid == 0 && w == 4) {
#pragma unroll
        for (int i = 0; i < 4; ++i) wait_flags(P.flags + FA_OFF + i * 64 + lane, tok);
        if (lane == 0) set_flag(P.flags, tok);
      }
      STAMP(3, 3);
      __builtin_amdgcn_endpgm();
      }
      f32x4 acc[4][4];
#pragma unroll
      for (int m = 0; m < 4; ++m)
#pragma unroll
        for (int n = 0; n < 4; ++n) { acc[m][n] = (f32x4){0.f, 0.f, 0.f, 0.f}; PIN(acc[m][n]); }
      u32x2 xv[2][4];
#pragma unroll
      for (int mm = 0; mm < 2; ++mm)
#pragma unroll
        for (int n = 0; n < 4; ++n) xv[mm][n] = *(const u32x2*)(P.x + (size_t)(orow + 16 * mm) * DM + ocol + 16 * n);
      asm volatile("s_nop 1" ::: "memory");
      STAMP(3, 1);
#pragma unroll 1
      for (int c4 = 0; c4 < 8; ++c4) {
        INTERVAL(1, fa0, fb0, fa1, fb1); INTERVAL(2, fa1, fb1, fa0, fb0); INTERVAL(3, fa0, fb0, fa1, fb1);
        if (c4 == 3 && kh2 == 0 && w == 0)
          wait_flags(P.flags + FB_OFF + (rt2 >> 1) * 32 + (lane & 31), tok);
        INTERVAL(0, fa1, fb1, fa0, fb0);
      }
      INTERVAL(1, fa0, fb0, fa1, fb1);
      INTERVAL(2, fa1, fb1, fa0, fb0);
      MR4(0, 3, fa0, fb0, fa1, fb1); MR4(4, 3, fa0, fb0, fa1, fb1); MR4(8, 3, fa0, fb0, fa1, fb1); MR4(12, 3, fa0, fb0, fa1, fb1);
      MR4(16, 3, fa0, fb0, fa1, fb1); MR4(20, 3, fa0, fb0, fa1, fb1);
      MM(24, fa0, fb0); MM(25, fa0, fb0); MM(26, fa0, fb0); MM(27, fa0, fb0); MM(28, fa0, fb0); MM(29, fa0, fb0); MM(30, fa0, fb0); MM(31, fa0, fb0);
      SB(); asm volatile("s_waitcnt lgkmcnt(0)" ::: "memory"); SB();
#pragma unroll
      for (int i = 0; i < 32; ++i) MFMA16A(acc[i & 3][(i >> 2) & 3], fb1[i >> 4][(i >> 2) & 3], fa1[i >> 4][i & 3]);
#undef INTERVAL
#undef MR4
#undef MR
#undef MM
#undef RD
      asm volatile("s_nop 7\n\ts_nop 7" ::: "memory");
      SB();
      STAMP(3, 2);
      const int pbid = bid ^ 1;
      unsigned* mine = (unsigned*)P.part + (size_t)bid * 4096 + w4 * 1024 + lane * 4;
      f32x4 keep[2][4];
#define PKP(A, B) (u32x4){cvtpk(A[0], A[1]), cvtpk(A[2], A[3]), cvtpk(B[0], B[1]), cvtpk(B[2], B[3])}
      if (kh2) {
#pragma unroll
        for (int i = 0; i < 4; ++i) st16wt(mine + i * 256, PKP(acc[i >> 1][2 * (i & 1)], acc[i >> 1][2 * (i & 1) + 1]));
#pragma unroll
        for (int i = 0; i < 8; ++i) keep[i >> 2][i & 3] = acc[2 + (i >> 2)][i & 3];
      } else {
#pragma unroll
        for (int i = 0; i < 4; ++i) st16wt(mine + i * 256, PKP(acc[2 + (i >> 1)][2 * (i & 1)], acc[2 + (i >> 1)][2 * (i & 1) + 1]));
#pragma unroll
        for (int i = 0; i < 8; ++i) keep[i >> 2][i & 3] = acc[i >> 2][i & 3];
      }
#undef PKP
      asm volatile("s_waitcnt vmcnt(0)" ::: "memory");
      if (lane == 0) set_flag(P.flags + FX_OFF + bid * 4 + w4, tok);
      wait_flags(P.flags + FX_OFF + pbid * 4 + w4, tok);
      STAMP(3, 4);
      const unsigned* theirs = (const unsigned*)P.part + (size_t)pbid * 4096 + w4 * 1024 + lane * 4;
      u32x4 pv[4];
      asm volatile("global_load_dwordx4 %0, %4, off sc1\n\tglobal_load_dwordx4 %1, %4, off offset:1024 sc1\n\t"
                   "global_load_dwordx4 %2, %4, off offset:2048 sc1\n\tglobal_load_dwordx4 %3, %4, off offset:3072 sc1\n\t"
                   "s_waitcnt vmcnt(0)"
                   : "=&v"(pv[0]), "=&v"(pv[1]), "=&v"(pv[2]), "=&v"(pv[3]) : "v"(theirs) : "memory");
#pragma unroll
      for (int i = 0; i < 8; ++i) {
        const f32x4 v = keep[i >> 2][i & 3];
        const unsigned p0 = pv[i >> 1][2 * (i & 1)], p1 = pv[i >> 1][2 * (i & 1) + 1];
        const u32x2 xx = xv[i >> 2][i & 3];
        u32x2 o = {cvtpk(bf_lo(xx.x) + v[0] + bf_lo(p0), bf_hi(xx.x) + v[1] + bf_hi(p0)),
                   cvtpk(bf_lo(xx.y) + v[2] + bf_lo(p1), bf_hi(xx.y) + v[3] + bf_hi(p1))};
        *(u32x2*)(P.out + (size_t)(orow + 16 * (i >> 2)) * DM + ocol + 16 * (i & 3)) = o;
      }
      STAMP(3, 3);
    }
#undef LDK
#undef LDV
#undef BF
#undef FIN1
#undef SAFE_UNIT
#undef SAFE_PASS
  }
#undef G2_DMA
#undef G2_OFFS
#undef HSTORE
#undef DMA_ST
}

extern "C" void kernel_launch(void* const* d_in, const int* in_sizes, int n_in, void* d_out, int out_size, void* d_ws,
                              size_t ws_size, hipStream_t stream) {
  char* ws = (char*)d_ws;
  Params P;
  P.x = (const bf16_t*)d_in[0];
  P.wqkv = (const bf16_t*)d_in[1]; P.wo = (const bf16_t*)d_in[2];
  P.wg = (const bf16_t*)d_in[4]; P.wu = (const bf16_t*)d_in[5]; P.wd = (const bf16_t*)d_in[6];
  P.xn = (bf16_t*)(ws + WS_XN);
  P.Qb = (bf16_t*)(ws + WS_Q); P.Kb = (bf16_t*)(ws + WS_K); P.Vb = (bf16_t*)(ws + WS_V);
  P.AH = (bf16_t*)(ws + WS_AH); P.out = (bf16_t*)d_out;
  P.part = (float*)(ws + WS_PART); P.flags = (u64*)(ws + WS_FLAGS);
  (void)hipFuncSetAttribute((const void*)fused_kernel, hipFuncAttributeMaxDynamicSharedMemorySize, LDS_TOTAL);
  fused_kernel<<<256, 512, LDS_TOTAL, stream>>>(P);
}
```

```cpp
#include <hip/hip_runtime.h>
#include <hip/hip_bf16.h>
#include <stdint.h>

typedef unsigned short bf16_t;
typedef short bf16x8 __attribute__((ext_vector_type(8)));
typedef short s16x4 __attribute__((ext_vector_type(4)));
typedef float f32x4 __attribute__((ext_vector_type(4)));
typedef float f32x16 __attribute__((ext_vector_type(16)));
typedef float f32x2 __attribute__((ext_vector_type(2)));
typedef __bf16 bf16x2v __attribute__((ext_vector_type(2)));
typedef unsigned u32x4 __attribute__((ext_vector_type(4)));
typedef unsigned u32x2 __attribute__((ext_vector_type(2)));
#define LAS __attribute__((address_space(3)))
typedef LAS const char* lds_cptr;

constexpr int SEQ = 2048, DM = 1024, FF = 3584, NQKV = 3072, KA = 4608;

constexpr size_t WS_FLAGS = 0;
constexpr size_t WS_XN = 1u << 20;
constexpr size_t WS_Q = WS_XN + (size_t)SEQ * DM * 2;
constexpr size_t WS_K = WS_Q + (size_t)SEQ * DM * 2;
constexpr size_t WS_V = WS_K + (size_t)SEQ * DM * 2;
constexpr size_t WS_AH = WS_V + (size_t)SEQ * DM * 2;
constexpr size_t WS_PART = WS_AH + (size_t)SEQ * KA * 2;
typedef unsigned long long u64;
constexpr int FL_OFF = 256, FA_OFF = 512, FH_OFF = 768, FB_OFF = 1024, FX_OFF = 1280;

#define SB() __builtin_amdgcn_sched_barrier(0)
#define STAMP(kid, i) do {} while (0)

__device__ __forceinline__ unsigned cvtpk(float lo, float hi) {
  f32x2 v = {lo, hi};
  bf16x2v b = __builtin_convertvector(v, bf16x2v);
  return __builtin_bit_cast(unsigned, b);
}
__device__ __forceinline__ float bf_lo(unsigned u) { return __uint_as_float(u << 16); }
__device__ __forceinline__ float bf_hi(unsigned u) { return __uint_as_float(u & 0xffff0000u); }
__device__ __forceinline__ float rbf(float x) { return bf_lo(cvtpk(x, x)); }

__device__ __forceinline__ void glds16(const void* g, unsigned lds_dst) {
  unsigned keep;
  asm volatile("s_mov_b32 %0, m0\n\ts_mov_b32 m0, %2\n\ts_nop 0\n\tglobal_load_lds_dwordx4 %1, off\n\ts_mov_b32 m0, %0"
               : "=&s"(keep) : "v"(g), "s"(lds_dst) : "memory");
}
__device__ __forceinline__ void glds16s(const void* sbase, unsigned voff, unsigned lds_dst) {
  unsigned keep;
  asm volatile("s_mov_b32 %0, m0\n\ts_mov_b32 m0, %3\n\ts_nop 0\n\tglobal_load_lds_dwordx4 %1, %2\n\ts_mov_b32 m0, %0"
               : "=&s"(keep) : "v"(voff), "s"(sbase), "s"(lds_dst) : "memory");
}
__device__ __forceinline__ void glds16c(const void* sbase, unsigned voff, unsigned lds_dst) {
  unsigned keep;
  asm volatile("s_mov_b32 %0, m0\n\ts_mov_b32 m0, %3\n\ts_nop 0\n\tglobal_load_lds_dwordx4 %1, %2 sc1\n\ts_mov_b32 m0, %0"
               : "=&s"(keep) : "v"(voff), "s"(sbase), "s"(lds_dst) : "memory");
}
__device__ __forceinline__ void glds4c(const void* g, unsigned lds_dst) {
  unsigned keep;
  asm volatile("s_mov_b32 %0, m0\n\ts_mov_b32 m0, %2\n\ts_nop 0\n\tglobal_load_lds_dword %1, off sc1\n\ts_mov_b32 m0, %0"
               : "=&s"(keep) : "v"(g), "s"(lds_dst) : "memory");
}
__device__ __forceinline__ bf16x8 lds_rd128(lds_cptr p) { return *(const LAS bf16x8*)p; }
__device__ __forceinline__ s16x4 lds_tr(lds_cptr p) { return __builtin_amdgcn_ds_read_tr16_b64_v4i16((LAS s16x4*)p); }
__device__ __forceinline__ bf16x8 cat8(s16x4 a, s16x4 b) { return __builtin_shufflevector(a, b, 0, 1, 2, 3, 4, 5, 6, 7); }
__device__ __forceinline__ void st16wt(void* p, u32x4 v) {
  asm volatile("global_store_dwordx4 %0, %1, off sc1\n\ts_nop 1" :: "v"(p), "v"(v) : "memory");
}
__device__ __forceinline__ void wait_flags(const u64* f, u64 tok) {
  unsigned spins = 0;
  while (!__all(__hip_atomic_load(f, __ATOMIC_RELAXED, __HIP_MEMORY_SCOPE_AGENT) == tok)) {
    __builtin_amdgcn_s_sleep(3);
    if (++spins > (1u << 20)) break;
  }
  asm volatile("" ::: "memory");
}
__device__ __forceinline__ void set_flag(u64* f, u64 tok) { __hip_atomic_store(f, tok, __ATOMIC_RELAXED, __HIP_MEMORY_SCOPE_AGENT); }
__device__ __forceinline__ void publish(LAS unsigned* cnt, unsigned nwaves, u64* flag, u64 tok, int lane) {
  asm volatile("s_waitcnt vmcnt(0)" ::: "memory");
  if (lane == 0) {
    const unsigned old = __hip_atomic_fetch_add(cnt, 1u, __ATOMIC_RELAXED, __HIP_MEMORY_SCOPE_WORKGROUP);
    if (old == nwaves - 1u) set_flag(flag, tok);
  }
}
__device__ __forceinline__ void wg_wait(int w, int lane, const u64* f, u64 tok, volatile LAS unsigned* go) {
  if (w == 0) { wait_flags(f, tok); if (lane == 0) *go = 1u; }
  else { while (*go == 0u) __builtin_amdgcn_s_sleep(1); asm volatile("" ::: "memory"); }
}

#define MFMA16(a, b, c) __builtin_amdgcn_mfma_f32_16x16x32_bf16(a, b, c, 0, 0, 0)
#define MFMA32(a, b, c) __builtin_amdgcn_mfma_f32_32x32x16_bf16(a, b, c, 0, 0, 0)
#define WAITBAR(N) do { SB(); asm volatile("s_waitcnt vmcnt(" #N ") lgkmcnt(0)\n\ts_barrier" ::: "memory"); SB(); } while (0)
#define VMBAR(N) do { SB(); asm volatile("s_waitcnt vmcnt(" #N ")\n\ts_barrier" ::: "memory"); SB(); } while (0)
#define LGBAR() do { SB(); asm volatile("s_waitcnt lgkmcnt(0)\n\ts_barrier" ::: "memory"); SB(); } while (0)
#define BARRIER() do { SB(); asm volatile("s_barrier" ::: "memory"); SB(); } while (0)
#define MFMA16A(acc, a, b) asm volatile("v_mfma_f32_16x16x32_bf16 %0, %1, %2, %0" : "+v"(acc) : "v"(a), "v"(b))
#define MFMA16G(acc, a, b) MFMA16A(acc, a, b)
#define PINACC(x) asm volatile("" : "+v"(x))
#define ADV64(p, inc) asm volatile("v_mad_u64_u32 %0, vcc, %1, 1, %0" : "+v"(p) : "v"(inc) : "vcc")
#define PIN(x) asm volatile("" : "+v"(x))
#define EXP2(x) __builtin_amdgcn_exp2f(x)

constexpr int G1_AOFF = 0, G1_BOFF = 65536, G1_BREG = 10304, G1_BSLOT = 2 * G1_BREG;
constexpr int AT_STAGE = 32768, AT_Q = 98304, AT_EX = 131072, AT_EE = AT_EX + 16384, AT_HF = 150528;
constexpr int G2_SLOT = 32768, G2_NCH = 36;
constexpr int LDS_CTRL = 150016, LDS_SINK = 152576, LDS_TOTAL = LDS_SINK + 1024;

__device__ __forceinline__ float wave_sum(float v) {
#define ROR(n) v += __uint_as_float(__builtin_amdgcn_update_dpp(0u, __float_as_uint(v), 0x120 + (n), 0xf, 0xf, false))
  ROR(8); ROR(4); ROR(2); ROR(1);
#undef ROR
  auto a = __builtin_amdgcn_permlane16_swap(__float_as_uint(v), __float_as_uint(v), false, false);
  v = __uint_as_float(a[0]) + __uint_as_float(a[1]);
  auto b = __builtin_amdgcn_permlane32_swap(__float_as_uint(v), __float_as_uint(v), false, false);
  return __uint_as_float(b[0]) + __uint_as_float(b[1]);
}
__device__ __forceinline__ float max16(const f32x16& p) {
  float a = fmaxf(fmaxf(p[0], p[1]), p[2]), b = fmaxf(fmaxf(p[3], p[4]), p[5]);
  a = fmaxf(fmaxf(a, p[6]), p[7]); b = fmaxf(fmaxf(b, p[8]), p[9]);
  a = fmaxf(fmaxf(a, p[10]), p[11]); b = fmaxf(fmaxf(b, p[12]), p[13]);
  a = fmaxf(fmaxf(a, p[14]), p[15]);
  float m = fmaxf(a, b);
  auto rr = __builtin_amdgcn_permlane32_swap(__float_as_uint(m), __float_as_uint(m), false, false);
  return fmaxf(__uint_as_float(rr[0]), __uint_as_float(rr[1]));
}
__device__ __forceinline__ f32x16 splat16(float v) { f32x16 r;
#pragma unroll
  for (int i = 0; i < 16; ++i) r[i] = v;
  return r; }

struct Params {
  const bf16_t *x, *wqkv, *wg, *wu, *wo, *wd;
  bf16_t *xn, *Qb, *Kb, *Vb, *AH, *out;
  float* part; u64* flags;
};

__global__ __launch_bounds__(512, 2) void fused_kernel(Params P) {
  extern __shared__ __attribute__((aligned(16))) char smem[];
  const int w = __builtin_amdgcn_readfirstlane(threadIdx.x >> 6);
  const int g = w >> 2, w4 = w & 3;
  const int bid = blockIdx.x, xcd = bid & 7, jj = bid >> 3;
  const int cg = xcd * 4 + (jj & 3), rt = jj >> 2;
  const unsigned lds0 = (unsigned)(uintptr_t)smem;
  const lds_cptr L = (lds_cptr)smem;
  LAS unsigned* ctrl = (LAS unsigned*)((LAS char*)smem + LDS_CTRL);
  u64 tok = 0;

  u32x4 hv[8];
  bf16_t* hp0; bf16_t* hp1;
#define HSTORE(i) st16wt((i) < 4 ? hp0 + (size_t)(i) * 16 * KA : hp1 + ((w4 & 1) ? (size_t)((i) & 1) * 32 * KA : (size_t)(((i) - 2) & 3) * 16 * KA), hv[i])
  const int h = xcd * 4 + (jj >> 3), qb = jj & 7;
  const char* kg = (const char*)(P.Kb + (size_t)h * SEQ * 32) + w4 * 2048;
  const char* vg = (const char*)(P.Vb + (size_t)h * SEQ * 32) + w4 * 2048;
#define DMA_ST(st, KH) do { const unsigned d_ = lds0 + ((st) & 3) * AT_STAGE + (KH) * 8192 + w4 * 2048; const int o_ = (KH) * 65536 + (st) * 8192; \
    glds16c(kg + o_, voff, d_); glds16c(kg + o_ + 1024, voff, d_ + 1024); \
    glds16c(vg + o_, voff, d_ + 16384); glds16c(vg + o_ + 1024, voff, d_ + 16384 + 1024); } while (0)

  {
    const int lane = threadIdx.x & 63;
    const int wm = 2 * g + (w4 >> 1), wn = w4 & 1;
    STAMP(1, 0);
    const int row0 = rt * 256;
    if (g == 1) {
      if (threadIdx.x < 256 + 8) ctrl[threadIdx.x - 256] = 0u;
      const unsigned pfoff = lane * 16;
      { u64 pc; const unsigned len = 40960u;
        asm volatile("s_getpc_b64 %0" : "=s"(pc));
        const unsigned off = (unsigned)(jj * 4 + w4) * 512u;
        if ((w4 & 1) == 0 && off + 1024u <= len) glds16s((const char*)(pc & ~255ull) + off, pfoff, lds0 + LDS_SINK); }
#pragma unroll 1
      for (int i = 0; i < 20; ++i) {
        const int id = i * 1024 + bid * 4 + w4, r_ = id / 20, c_ = id - 20 * r_;
        glds16s(c_ < 6 ? (const char*)P.wqkv + (size_t)r_ * 6144 + c_ * 1024
                       : (const char*)(c_ < 13 ? P.wg : P.wu) + (size_t)r_ * 7168 + (c_ < 13 ? c_ - 6 : c_ - 13) * 1024, pfoff, lds0 + LDS_SINK);
      }
      LGBAR();
    } else {
      u32x4 xr[4]; u64 ep;
      asm volatile("global_load_dwordx4 %0, %5, off\n\tglobal_load_dwordx4 %1, %5, off offset:1024\n\t"
                   "global_load_dwordx4 %2, %5, off offset:2048\n\tglobal_load_dwordx4 %3, %5, off offset:3072\n\t"
                   "global_load_dwordx2 %4, %6, off sc1\n\ts_waitcnt vmcnt(0)"
                   : "=&v"(xr[0]), "=&v"(xr[1]), "=&v"(xr[2]), "=&v"(xr[3]), "=&v"(ep)
                   : "v"(P.x + (size_t)(bid * 8 + 2 * w4) * DM + lane * 8), "v"(P.flags) : "memory");
      STAMP(0, 0);
      tok = (((u64)(unsigned)__builtin_amdgcn_readfirstlane((unsigned)(ep >> 32)) << 32) | (unsigned)__builtin_amdgcn_readfirstlane((unsigned)ep)) + 1ull;
#pragma unroll
      for (int rr = 0; rr < 2; ++rr) {
        const u32x4 xa = xr[2 * rr], xb = xr[2 * rr + 1];
        float v[16];
#pragma unroll
        for (int i = 0; i < 4; ++i) { v[2 * i] = bf_lo(xa[i]); v[2 * i + 1] = bf_hi(xa[i]); v[8 + 2 * i] = bf_lo(xb[i]); v[9 + 2 * i] = bf_hi(xb[i]); }
        float sm = 0.f;
#pragma unroll
        for (int i = 0; i < 16; ++i) sm += v[i];
        const float mean = wave_sum(sm) * (1.f / DM);
        float q = 0.f;
#pragma unroll
        for (int i = 0; i < 16; ++i) { const float d = v[i] - mean; q += d * d; }
        const float mb = rbf(mean), vb = rbf(wave_sum(q) * (1.f / DM));
        const float inv = 1.0f / rbf(sqrtf(rbf(vb + 1e-5f)));
        u32x4 oa, ob;
#pragma unroll
        for (int i = 0; i < 4; ++i) {
          const unsigned da = cvtpk(v[2 * i] - mb, v[2 * i + 1] - mb), db = cvtpk(v[8 + 2 * i] - mb, v[9 + 2 * i] - mb);
          oa[i] = cvtpk(bf_lo(da) * inv, bf_hi(da) * inv);
          ob[i] = cvtpk(bf_lo(db) * inv, bf_hi(db) * inv);
        }
        bf16_t* op = P.xn + (size_t)(bid * 8 + 2 * w4 + rr) * DM + lane * 8;
        st16wt(op, oa); st16wt(op + 512, ob);
      }
      STAMP(0, 4);
      LGBAR();
      if (w == 0 && lane == 0) { ctrl[6] = (unsigned)tok; ctrl[7] = (unsigned)(tok >> 32); }
      publish(ctrl + 0, 4u, P.flags + FL_OFF + bid, tok, lane);
      STAMP(0, 1);
    }
    unsigned avoff0 = (unsigned)((lane >> 3) * 2048 + (((lane & 7) ^ ((lane >> 4) + 2)) * 16)), avoff1 = avoff0 ^ 64u;
    const char* abase = (const char*)P.xn + (size_t)(row0 + 32 * w4) * 2048;
#define DMA_A(hh, t) do { _Pragma("unroll") for (int _i = 0; _i < 4; ++_i) \
    glds16c(abase + (size_t)(hh) * 262144 + _i * 16384 + (t) * 128, (_i & 1) ? avoff1 : avoff0, \
            lds0 + G1_AOFF + ((t) & 1) * 32768 + (hh) * 16384 + (4 * w4 + _i) * 1024); } while (0)
    const char* bptr[4]; unsigned bld[4]; unsigned bdst[4];
#pragma unroll
    for (int i = 0; i < 4; ++i) {
      const int b = (i == 0) ? (g ? 16 + w4 : 4 * w4) : 4 * w4 + i;
      const int region = b >= 10 ? 1 : 0, bi = b - 10 * region;
      const int Pc = bi * 64 + lane;
      const int k = Pc / 20, pc = Pc - 20 * k, f = pc >> 1, e8 = (pc & 1) * 8;
      const bool isq = f >= 8 || (region && (f & 3) == 3);
      const int qf = f >= 8 ? (region ? f - 4 : f - 8) : 2 + (f >> 2);
      const bf16_t* mat = isq ? P.wqkv : (f >= 4 ? P.wu : P.wg);
      const int ld = isq ? NQKV : FF;
      const int col = e8 + (isq ? 96 * cg + 16 * qf : 112 * cg + 64 * region + 16 * (f & 3));
      bptr[i] = (const char*)(mat + (size_t)k * ld + col);
      bld[i] = (unsigned)ld * 64u;
      bdst[i] = lds0 + G1_BOFF + region * G1_BREG + bi * 1024 + (bi >= 5 ? 32 : 0);
    }
#define DMA_B(i, s) glds16(bptr[i], bdst[i] + ((s) & 3) * G1_BSLOT)
#define ADV_B() do { ADV64(bptr[0], bld[0]); ADV64(bptr[1], bld[1]); ADV64(bptr[2], bld[2]); ADV64(bptr[3], bld[3]); } while (0)
    if (g == 0) {
      DMA_B(0, 0); DMA_B(1, 0); DMA_B(2, 0); DMA_B(3, 0); ADV_B();
      DMA_B(0, 1); ADV_B();
      DMA_B(0, 2); DMA_B(1, 2); DMA_B(2, 2); DMA_B(3, 2); ADV_B();
    } else {
      DMA_B(0, 0); ADV_B();
      DMA_B(0, 1); DMA_B(1, 1); DMA_B(2, 1); DMA_B(3, 1); ADV_B();
      DMA_B(0, 2); ADV_B();
    }
    const int fr = lane & 15, fq = lane >> 4, kq = ((fq & 1) << 1) | (fq >> 1);
    const int aoff0 = G1_AOFF + (64 * wm + fr) * 128 + ((kq ^ (((fr >> 1) + 2) & 7)) * 16);
    lds_cptr pa0 = L + aoff0, pa1 = L + (aoff0 ^ 64);
    lds_cptr pb0 = L + (G1_BOFF + (8 * kq + (fr >> 2)) * 320 + (fq & 1) * 32 + 8 * (lane & 3) + G1_BREG * wn);
    lds_cptr pb1 = pb0 + 2 * G1_BSLOT;
    asm volatile("" : "+v"(pa0), "+v"(pa1), "+v"(pb0), "+v"(pb1), "+v"(avoff0), "+v"(avoff1));
    f32x4 acc[4][10];
#pragma unroll
    for (int m = 0; m < 4; ++m)
#pragma unroll
      for (int j = 0; j < 10; ++j) { acc[m][j] = (f32x4){0.f, 0.f, 0.f, 0.f}; PINACC(acc[m][j]); }
    bf16x8 af[4], bfr[10];
    wg_wait(w, lane, P.flags + FL_OFF + rt * 32 + (lane & 31), tok, ctrl + 3);
    STAMP(0, 2);
    DMA_A(g, 0);
    STAMP(0, 3);
    if (g == 0) { WAITBAR(0); } else { DMA_A(0, 1); WAITBAR(4); BARRIER(); }
    STAMP(1, 1);
#define STEP(U, ISSUE, WN, LASTBAR) do { \
    { const lds_cptr pa = ((U) & 1) ? pa1 : pa0; const lds_cptr pb = (((U) & 2) ? pb1 : pb0) + ((U) & 1) * G1_BSLOT; \
      _Pragma("unroll") for (int m = 0; m < 4; ++m) af[m] = lds_rd128(pa + ((U) >> 1) * 32768 + m * 2048); \
      _Pragma("unroll") for (int j = 0; j < 10; ++j) bfr[j] = cat8(lds_tr(pb + j * 32), lds_tr(pb + j * 32 + 1280)); } \
    if (ISSUE) { DMA_B(0, (U) + 3); \
      if (g == ((U) & 1)) { DMA_A(1 - ((U) & 1), 2 * it + ((U) >> 1) + 1 + ((U) & 1)); } \
      else { DMA_B(1, (U) + 3); DMA_B(2, (U) + 3); DMA_B(3, (U) + 3); } } \
    WAITBAR(WN); \
    _Pragma("unroll") for (int j = 0; j < 10; ++j) { _Pragma("unroll") for (int m = 0; m < 4; ++m) MFMA16G(acc[m][j], bfr[j], af[m]); \
      if ((ISSUE) && (j & 1) && j < 8) ADV64(bptr[j >> 1], bld[j >> 1]); } \
    if (LASTBAR) BARRIER(); } while (0)
    int it = 0;
#pragma unroll 1
    for (; it < 7; ++it) { STEP(0, true, 9, true); STEP(1, true, 9, true); STEP(2, true, 9, true); STEP(3, true, 9, true); }
    STEP(0, true, 9, true); STEP(1, false, 4, true); STEP(2, false, 0, true); STEP(3, false, 0, g == 0);
#undef STEP
    asm volatile("s_nop 7\n\ts_nop 7" ::: "memory");
    SB();
    STAMP(1, 2);
#undef DMA_A
#undef DMA_B
#undef ADV_B
    int tz = threadIdx.x; asm volatile("" : "+v"(tz));
    tok = ((u64)(unsigned)__builtin_amdgcn_readfirstlane(*(volatile LAS unsigned*)(ctrl + 7)) << 32) |
          (unsigned)__builtin_amdgcn_readfirstlane(*(volatile LAS unsigned*)(ctrl + 6));
    const int fr2 = tz & 15, fq2 = (tz >> 4) & 3, sel1 = fq2 & 1, c8 = (fq2 >> 1) * 8;
    const float qscale = 0.17677669529663687f * 1.4426950408889634f;
#define SWAP16(A, B, OUT) do { auto r0_ = __builtin_amdgcn_permlane16_swap(A.x, B.x, false, false); \
    auto r1_ = __builtin_amdgcn_permlane16_swap(A.y, B.y, false, false); OUT = (u32x4){r0_[0], r1_[0], r0_[1], r1_[1]}; } while (0)
#define SILU4(m, j, OUT) do { float hv_[4]; _Pragma("unroll") for (int r = 0; r < 4; ++r) { \
      const float tg_ = acc[m][j][r], tu_ = acc[m][(j) + 4][r]; \
      hv_[r] = (tg_ * tu_) * __builtin_amdgcn_rcpf(1.f + __builtin_amdgcn_exp2f(-1.4426950408889634f * tg_)); } \
    OUT = (u32x2){cvtpk(hv_[0], hv_[1]), cvtpk(hv_[2], hv_[3])}; } while (0)
#define PK4(V, SC, OUT) do { const f32x4 v_ = (V) * (SC); OUT = (u32x2){cvtpk(v_[0], v_[1]), cvtpk(v_[2], v_[3])}; } while (0)
    const int rowb = row0 + 64 * wm + fr2;
#define QKV_HEAD(hd, ja, jb) do { const int typ_ = (hd) >> 5, h_ = (hd) & 31; const float sa = typ_ == 0 ? qscale : 1.f; \
      bf16_t* dst; int mstep; \
      if (typ_ == 1) { dst = P.Kb + ((((size_t)h_ * 64 + (rowb >> 5)) * 2 + sel1) * 64 + (rowb & 31) + 4 * c8) * 8; mstep = 128; } \
      else { dst = (typ_ == 0 ? P.Qb : P.Vb) + ((size_t)h_ * SEQ + rowb) * 32 + 16 * sel1 + c8; mstep = 512; } \
      _Pragma("unroll") for (int m = 0; m < 4; ++m) { \
        u32x2 a_, b_; u32x4 o; PK4(acc[m][ja], sa, a_); PK4(acc[m][jb], sa, b_); SWAP16(a_, b_, o); \
        st16wt(dst + (m & 1) * mstep + (m >> 1) * 1024, o); } } while (0)
    __builtin_amdgcn_s_setprio(3);
    QKV_HEAD(3 * cg + 2 * wn, 8, 9);
    if (wn) QKV_HEAD(3 * cg + 1, 3, 7);
    __builtin_amdgcn_s_setprio(0);
    SB();
    bf16_t* hd = P.AH + (size_t)rowb * KA + DM + 112 * cg + 64 * wn + c8;
    hp0 = hd + 16 * sel1;
    hp1 = wn ? hd + 32 + (size_t)sel1 * 16 * KA : hd + 32 + 16 * sel1;
#pragma unroll
    for (int m = 0; m < 3; ++m) { u32x2 a_, b_; SILU4(m, 0, a_); SILU4(m, 1, b_); SWAP16(a_, b_, hv[m]); PIN(hv[m]); }
    STAMP(1, 4);
    publish(ctrl + 1, 8u, P.flags + FA_OFF + cg * 8 + rt, tok, tz & 63);
    { u32x2 a_, b_; SILU4(3, 0, a_); SILU4(3, 1, b_); SWAP16(a_, b_, hv[3]); PIN(hv[3]); }
    if (wn == 0) {
#pragma unroll
      for (int m = 0; m < 2; ++m) { u32x2 a_, b_; SILU4(m, 2, a_); SILU4(m, 3, b_); SWAP16(a_, b_, hv[6 + m]); PIN(hv[6 + m]); }
    }
    STAMP(1, 3);
    STAMP(2, 0);
    if (g == 0) {
      const int l6 = tz & 63;
      const int cgQ = h / 3, cgK = (32 + h) / 3, cgV = (64 + h) / 3;
      wg_wait(w, l6, P.flags + FA_OFF + (l6 < 8 ? cgK * 8 + l6 : (l6 < 16 ? cgV * 8 + l6 - 8 : cgQ * 8 + qb)), tok, ctrl + 4);
      const unsigned voff = l6 * 16;
      DMA_ST(0, 0); DMA_ST(0, 1);
      const char* qsrc = (const char*)(P.Qb + ((size_t)h * SEQ + qb * 256 + w4 * 64) * 32);
#pragma unroll
      for (int i = 0; i < 4; ++i) glds16c(qsrc + i * 1024, voff, lds0 + AT_Q + w4 * 4096 + i * 1024);
    }
    STAMP(2, 4);
    if (wn == 0) {
#pragma unroll
      for (int m = 2; m < 4; ++m) { u32x2 a_, b_; SILU4(m, 2, a_); SILU4(m, 3, b_); SWAP16(a_, b_, hv[2 + m]); }
    } else {
#pragma unroll
      for (int mp = 0; mp < 2; ++mp) { u32x2 a_, b_; SILU4(2 * mp, 2, a_); SILU4(2 * mp + 1, 2, b_); SWAP16(a_, b_, hv[4 + mp]); }
      hv[6] = hv[4]; hv[7] = hv[5];
    }
#undef SWAP16
#undef SILU4
#undef PK4
#undef QKV_HEAD
  }

  const int kh2 = xcd & 1, rt2 = (xcd >> 1) * 4 + (jj >> 3), ct2 = jj & 7;
  int c_issued = 0;
#define G2_DMA(c, avo0, avo1, bvo) do { int cc_ = (c) + (kh2 ? 0 : 16); if (cc_ >= G2_NCH) cc_ -= G2_NCH; const int kg_ = kh2 * (G2_NCH * 64) + cc_ * 64; \
    const char* ab_ = (const char*)P.AH + (size_t)(rt2 * 128 + 32 * w4) * (KA * 2) + kg_ * 2; \
    const char* bb_ = (kg_ < DM ? (const char*)P.wo + (size_t)kg_ * 2048 : (const char*)P.wd + (size_t)(kg_ - DM) * 2048) + ct2 * 256 + w4 * 32768; \
    const unsigned d_ = lds0 + ((c) & 3) * G2_SLOT + w4 * 4096; \
    _Pragma("unroll") for (int _i = 0; _i < 4; ++_i) { \
      glds16c(ab_ + (size_t)_i * (8 * KA * 2), (_i & 1) ? avo1 : avo0, d_ + _i * 1024); \
      glds16s(bb_ + _i * 8192, bvo, d_ + 16384 + _i * 1024); } } while (0)
#define G2_OFFS(lane) const unsigned avo0 = (unsigned)(((lane) >> 3) * (KA * 2) + ((((lane) & 7) ^ (((lane) >> 4) + 2)) * 16)), avo1 = avo0 ^ 64u; \
    const unsigned bvo = (unsigned)(((lane) >> 4) * 2048 + ((((lane) & 15) ^ (((lane) >> 4) << 1) ^ ((w4 & 1) << 3)) * 16))

  {
    int tb = threadIdx.x; asm volatile("" : "+v"(tb));
    const int lane = tb & 63;
    const int qg = w4, kh = g;
    const int hi = lane >> 5;
    const unsigned voff = lane * 16;
    const short one = (((lane >> 4) & 1) == ((lane >> 2) & 1)) ? (short)0x3f80 : (short)0;
    const bf16x8 sel = {one, one, one, one, one, one, one, one};
    volatile LAS unsigned* flag = (volatile LAS unsigned*)(ctrl + 2);
    const int gq = lane >> 4;
    const int koff = kh * 8192 + lane * 16;
    const int voffl = 16384 + kh * 8192 + (4 * (gq >> 1) + ((lane & 15) >> 2)) * 64 + (gq & 1) * 32 + (lane & 3) * 8;
    const f32x16 zero16 = splat16(0.f);
    f32x16 oA = zero16, oB = zero16;
    f32x4 lA = {0.f, 0.f, 0.f, 0.f}, lB = {0.f, 0.f, 0.f, 0.f};
    float mA, mB;
    const unsigned* hflag = (const unsigned*)(P.flags + FH_OFF + (lane >> 1) * 8 + (rt2 >> 1)) + (lane & 1);
    bf16x8 qa0, qa1, qb0, qb1;

#define LDK(K0, K1, p) do { K0 = lds_rd128(p); K1 = lds_rd128((p) + 1024); } while (0)
#define LDV(V0, V1, p) do { V0 = cat8(lds_tr(p), lds_tr((p) + 512)); V1 = cat8(lds_tr((p) + 1024), lds_tr((p) + 1536)); } while (0)
#define BF(x) __builtin_bit_cast(bf16x8, x)
    {
      WAITBAR(0);
      { const lds_cptr qp = L + AT_Q + qg * 4096 + (lane & 31) * 64 + hi * 16;
        qa0 = lds_rd128(qp); qa1 = lds_rd128(qp + 32); qb0 = lds_rd128(qp + 2048); qb1 = lds_rd128(qp + 2080); }
      bf16x8 kx0, kx1, ky0, ky1, vx0, vx1, vy0, vy1;
      LDK(kx0, kx1, L + koff);
      LDK(ky0, ky1, L + koff + 2048);
      LDV(vy0, vy1, L + voffl);
      DMA_ST(1, kh); DMA_ST(2, kh);
      f32x16 sA = MFMA32(kx0, qa0, zero16); sA = MFMA32(kx1, qa1, sA);
      f32x16 sB = MFMA32(kx0, qb0, zero16); sB = MFMA32(kx1, qb1, sB);
      mA = max16(sA); mB = max16(sB);
      f32x16 nA = splat16(-mA), nB = splat16(-mB);
      PIN(nA); PIN(nB);
      sA = MFMA32(kx0, qa0, nA); sA = MFMA32(kx1, qa1, sA);
      u32x4 pA0, pA1, pB0 = {0u, 0u, 0u, 0u}, pB1 = {0u, 0u, 0u, 0u};
      STAMP(2, 1);
#define E4(S, i) do { S[i] = EXP2(S[i]); S[i + 1] = EXP2(S[i + 1]); S[i + 2] = EXP2(S[i + 2]); S[i + 3] = EXP2(S[i + 3]); } while (0)
#define SLOT(SC, PC0, PC1, SN, K0, K1, Q0, Q1, NM, PP0, PP1, V0, V1, O, LS, LOADS) do { \
    SN = MFMA32(K0, Q0, NM); SB(); E4(SC, 0); PIN(SC); SB(); \
    SN = MFMA32(K1, Q1, SN); SB(); LOADS; SB(); E4(SC, 4); PIN(SC); SB(); \
    O = MFMA32(V0, BF(PP0), O); SB(); PC0[0] = cvtpk(SC[0], SC[1]); PC0[1] = cvtpk(SC[2], SC[3]); \
      SC[8] = EXP2(SC[8]); SC[9] = EXP2(SC[9]); SC[10] = EXP2(SC[10]); PIN(SC); PIN(PC0); SB(); \
    LS = MFMA16(sel, BF(PP0), LS); SB(); PC0[2] = cvtpk(SC[4], SC[5]); PC0[3] = cvtpk(SC[6], SC[7]); SC[11] = EXP2(SC[11]); PIN(SC); PIN(PC0); SB(); \
    O = MFMA32(V1, BF(PP1), O); SB(); E4(SC, 12); PIN(SC); SB(); \
    LS = MFMA16(sel, BF(PP1), LS); SB(); PC1[0] = cvtpk(SC[8], SC[9]); PC1[1] = cvtpk(SC[10], SC[11]); \
      PC1[2] = cvtpk(SC[12], SC[13]); PC1[3] = cvtpk(SC[14], SC[15]); PIN(PC1); SB(); } while (0)
#define SUB(KC0, KC1, KN0, KN1, VC0, VC1, VP0, VP1, vpu, kp2) do { \
    SLOT(sA, pA0, pA1, sB, KC0, KC1, qb0, qb1, nB, pB0, pB1, VP0, VP1, oB, lB, LDV(VC0, VC1, vpu)); \
    SLOT(sB, pB0, pB1, sA, KN0, KN1, qa0, qa1, nA, pA0, pA1, VC0, VC1, oA, lA, LDK(KC0, KC1, kp2)); } while (0)
      lds_cptr kb0 = L + koff, kb1 = L + koff + 2 * AT_STAGE, vb0 = L + voffl, vb1 = L + voffl + 2 * AT_STAGE;
      PIN(kb0); PIN(kb1); PIN(vb0); PIN(vb1);
#define KP(st) ((((st) & 2) ? kb1 : kb0) + ((st) & 1) * AT_STAGE)
#define VP(st) ((((st) & 2) ? vb1 : vb0) + ((st) & 1) * AT_STAGE)
#define HS(st, i) do { if ((st) < 2) HSTORE((4 * (st) + (i)) & 7); } while (0)
#define STAGE(st, WN, EXTRA) do { \
        SUB(kx0, kx1, ky0, ky1, vx0, vx1, vy0, vy1, VP(st), KP(st) + 4096); \
        HS(st, 0); \
        SUB(ky0, ky1, kx0, kx1, vy0, vy1, vx0, vx1, VP(st) + 2048, KP(st) + 6144); \
        VMBAR(WN); \
        HS(st, 1); \
        EXTRA; \
        if ((st) < 5) {   \
          const int pc = (st) * 8 + w, kb = bid * 36 + (pc < 36 ? pc : 35); \
          glds16s(kb < 2048 ? (const char*)P.wo + (size_t)kb * 1024 : (const char*)P.wd + (size_t)(kb - 2048) * 1024, voff, lds0 + LDS_SINK); \
          DMA_ST((st) + 3, kh); } \
        SUB(kx0, kx1, ky0, ky1, vx0, vx1, vy0, vy1, VP(st) + 4096, KP((st) + 1)); \
        HS(st, 2); \
        SUB(ky0, ky1, kx0, kx1, vy0, vy1, vx0, vx1, VP(st) + 6144, KP((st) + 1) + 2048); \
        HS(st, 3); } while (0)
      STAGE(0, 5, ); STAGE(1, 10, ); STAGE(2, 11, );
      STAGE(3, 5, if (w == 0 && lane == 0) set_flag(P.flags + FH_OFF + cg * 8 + rt, tok));
      STAGE(4, 5, );
      STAGE(5, 5, if (g == 1) glds4c(hflag, lds0 + AT_HF + w * 256));
      STAGE(6, 0, if (g == 1) {
                    const unsigned hf = *(volatile LAS unsigned*)((LAS char*)smem + AT_HF + w * 256 + lane * 4);
                    if (__all(hf == ((lane & 1) ? (unsigned)(tok >> 32) : (unsigned)tok))) {
                      G2_OFFS(lane); G2_DMA(0, avo0, avo1, bvo); G2_DMA(1, avo0, avo1, bvo); c_issued = 2; } });
      STAGE(7, 0, if (c_issued == 2) { G2_OFFS(lane); G2_DMA(2, avo0, avo1, bvo); c_issued = 3; });
#undef STAGE
#undef HS
#undef KP
#undef VP
      oB = MFMA32(vy0, BF(pB0), oB); lB = MFMA16(sel, BF(pB0), lB);
      oB = MFMA32(vy1, BF(pB1), oB); lB = MFMA16(sel, BF(pB1), lB);
#undef SUB
#undef SLOT
#undef E4
    }
    STAMP(2, 2);
    LAS u32x4* xw = (LAS u32x4*)((LAS char*)smem + AT_EX) + w4 * 256;
    LAS float* ew = (LAS float*)((LAS char*)smem + AT_EE) + w4 * 128;
    float eA, eB;
#define FIN1(FIRST) do { \
        \
      float chk = 0.f; \
      _Pragma("unroll") for (int r = 0; r < 16; ++r) { chk = __builtin_fmaf(oA[r], 0.f, chk); chk = __builtin_fmaf(oB[r], 0.f, chk); } \
      const bool bad = !(lA[0] < 1.0e36f) || !(lB[0] < 1.0e36f) || !(chk == 0.f); \
      if (FIRST) { if (__any(bad) && lane == 0) *flag = 1u; \
                   if (c_issued == 3 && lane == 0) __hip_atomic_fetch_add(ctrl + 5, 1u, __ATOMIC_RELAXED, __HIP_MEMORY_SCOPE_WORKGROUP); } \
      oA *= __builtin_amdgcn_rcpf(lA[0]); oB *= __builtin_amdgcn_rcpf(lB[0]); \
      eA = mA + __builtin_amdgcn_logf(lA[0]); eB = mB + __builtin_amdgcn_logf(lB[0]);     \
      if (g == 0) { \
        _Pragma("unroll") for (int r = 0; r < 2; ++r) { \
          xw[r * 64 + lane] = (u32x4){cvtpk(oA[8 * r], oA[8 * r + 1]), cvtpk(oA[8 * r + 2], oA[8 * r + 3]), cvtpk(oA[8 * r + 4], oA[8 * r + 5]), cvtpk(oA[8 * r + 6], oA[8 * r + 7])}; \
          xw[128 + r * 64 + lane] = (u32x4){cvtpk(oB[8 * r], oB[8 * r + 1]), cvtpk(oB[8 * r + 2], oB[8 * r + 3]), cvtpk(oB[8 * r + 4], oB[8 * r + 5]), cvtpk(oB[8 * r + 6], oB[8 * r + 7])}; } \
        ew[lane] = eA; ew[64 + lane] = eB; } } while (0)
#define SAFE_UNIT(Q0, Q1, M, O, LS) do { \
            f32x16 s_ = MFMA32(k0, Q0, zero16); s_ = MFMA32(k1, Q1, s_); \
            const float mn_ = fmaxf(M, max16(s_)); const float al_ = EXP2(M - mn_); M = mn_; \
            O *= al_; LS *= al_; u32x4 p0_, p1_; \
            _Pragma("unroll") for (int r = 0; r < 4; ++r) { p0_[r] = cvtpk(EXP2(s_[2 * r] - mn_), EXP2(s_[2 * r + 1] - mn_)); \
              p1_[r] = cvtpk(EXP2(s_[8 + 2 * r] - mn_), EXP2(s_[9 + 2 * r] - mn_)); } \
            O = MFMA32(v0, BF(p0_), O); LS = MFMA16(sel, BF(p0_), LS); O = MFMA32(v1, BF(p1_), O); LS = MFMA16(sel, BF(p1_), LS); } while (0)
#define SAFE_PASS() do { \
        WAITBAR(0); \
        DMA_ST(0, kh); DMA_ST(1, kh); DMA_ST(2, kh); \
        oA = zero16; oB = zero16; lA = (f32x4){0.f, 0.f, 0.f, 0.f}; lB = lA; \
        mA = -3.0e38f; mB = -3.0e38f; \
        _Pragma("unroll 1") for (int st = 0; st < 8; ++st) { \
          if (st < 6) WAITBAR(8); else if (st == 6) WAITBAR(4); else WAITBAR(0); \
          if (st < 5) DMA_ST(st + 3, kh); \
          _Pragma("unroll 1") for (int u = 0; u < 4; ++u) { \
            bf16x8 k0, k1, v0, v1; \
            LDK(k0, k1, L + (st & 3) * AT_STAGE + koff + u * 2048); \
            LDV(v0, v1, L + (st & 3) * AT_STAGE + voffl + u * 2048); \
            SAFE_UNIT(qa0, qa1, mA, oA, lA); \
            SAFE_UNIT(qb0, qb1, mB, oB, lB); } } \
        FIN1(false); LGBAR(); } while (0)
    FIN1(true);
    STAMP(2, 3);

    STAMP(3, 0);
    {
      const int wm = w4 >> 1, wn = w4 & 1;
      const int fr = lane & 15, fq = lane >> 4, kq = ((fq & 1) << 1) | (fq >> 1), q4 = fr >> 2;
      const int aoff0 = (64 * wm + fr) * 128 + ((kq ^ (((fr >> 1) + 2) & 7)) * 16);
      const int boff0 = 16384 + (8 * kq + q4) * 256 + 8 * (lane & 3);
      const int bsw = q4 | ((fq & 1) << 2);
      lds_cptr pa[2][2], pb[2][4];
#pragma unroll
      for (int hh = 0; hh < 2; ++hh) {
        pa[hh][0] = L + (aoff0 + hh * 2 * G2_SLOT); pa[hh][1] = L + ((aoff0 ^ 64) + hh * 2 * G2_SLOT);
        PIN(pa[hh][0]); PIN(pa[hh][1]);
#pragma unroll
        for (int n = 0; n < 4; ++n) { pb[hh][n] = L + (boff0 + (((4 * wn + n) ^ bsw) * 32) + hh * 2 * G2_SLOT); PIN(pb[hh][n]); }
      }
      bf16x8 fa0[2][4], fb0[2][4], fa1[2][4], fb1[2][4];
      const int orow = rt2 * 128 + 64 * wm + 32 * kh2 + fr, ocol = ct2 * 128 + 64 * wn + 4 * fq;
#define RD(i, SL, FA, FB) do { constexpr int ks_ = (i) / 12, r_ = (i) % 12; \
      if (r_ < 4) FA[ks_][r_ & 3] = lds_rd128(pa[(SL) >> 1][ks_] + ((SL) & 1) * G2_SLOT + r_ * 2048); \
      else { constexpr int n_ = (r_ - 4) >> 1, t_ = (r_ - 4) & 1; \
        const s16x4 h_ = lds_tr(pb[(SL) >> 1][n_ < 0 ? 0 : n_] + ((SL) & 1) * G2_SLOT + ks_ * 8192 + t_ * 1024); \
        if (t_ == 0) { FB[ks_][n_ < 0 ? 0 : n_][0] = h_[0]; FB[ks_][n_ < 0 ? 0 : n_][1] = h_[1]; FB[ks_][n_ < 0 ? 0 : n_][2] = h_[2]; FB[ks_][n_ < 0 ? 0 : n_][3] = h_[3]; } \
        else { FB[ks_][n_ < 0 ? 0 : n_][4] = h_[0]; FB[ks_][n_ < 0 ? 0 : n_][5] = h_[1]; FB[ks_][n_ < 0 ? 0 : n_][6] = h_[2]; FB[ks_][n_ < 0 ? 0 : n_][7] = h_[3]; } } } while (0)
#define MM(i, FA, FB) MFMA16A(acc[(i) & 3][((i) >> 2) & 3], FB[(i) >> 4][((i) >> 2) & 3], FA[(i) >> 4][(i) & 3])
#define MR(i, SL, CA, CB, NA, NB) do { MM(i, CA, CB); RD(i, SL, NA, NB); SB(); } while (0)
#define MR4(i, SL, CA, CB, NA, NB) do { MR(i, SL, CA, CB, NA, NB); MR(i + 1, SL, CA, CB, NA, NB); MR(i + 2, SL, CA, CB, NA, NB); MR(i + 3, SL, CA, CB, NA, NB); } while (0)
#define INTERVAL(SL, CA, CB, NA, NB) do { \
      MR4(0, SL, CA, CB, NA, NB); MR4(4, SL, CA, CB, NA, NB); MR4(8, SL, CA, CB, NA, NB); MR4(12, SL, CA, CB, NA, NB); MR4(16, SL, CA, CB, NA, NB); MR4(20, SL, CA, CB, NA, NB); \
      MM(24, CA, CB); MM(25, CA, CB); MM(26, CA, CB); MM(27, CA, CB); MM(28, CA, CB); MM(29, CA, CB); \
      LGBAR(); MM(30, CA, CB); MM(31, CA, CB); SB(); } while (0)
#define RDALL() do { \
      RD(0, 0, fa0, fb0); RD(1, 0, fa0, fb0); RD(2, 0, fa0, fb0); RD(3, 0, fa0, fb0); RD(4, 0, fa0, fb0); RD(5, 0, fa0, fb0); \
      RD(6, 0, fa0, fb0); RD(7, 0, fa0, fb0); RD(8, 0, fa0, fb0); RD(9, 0, fa0, fb0); RD(10, 0, fa0, fb0); RD(11, 0, fa0, fb0); \
      RD(12, 0, fa0, fb0); RD(13, 0, fa0, fb0); RD(14, 0, fa0, fb0); RD(15, 0, fa0, fb0); RD(16, 0, fa0, fb0); RD(17, 0, fa0, fb0); \
      RD(18, 0, fa0, fb0); RD(19, 0, fa0, fb0); RD(20, 0, fa0, fb0); RD(21, 0, fa0, fb0); RD(22, 0, fa0, fb0); RD(23, 0, fa0, fb0); } while (0)
      if (g == 0) RDALL();
      LGBAR();
      const bool ovf = *flag != 0u, fast = !ovf && *(volatile LAS unsigned*)(ctrl + 5) == 4u;
      G2_OFFS(lane);
      if (!fast) {
        if (ovf) SAFE_PASS();
        if (g == 1) {
          wait_flags(P.flags + FH_OFF + (lane & 31) * 8 + (rt2 >> 1), tok);
          G2_DMA(0, avo0, avo1, bvo); G2_DMA(1, avo0, avo1, bvo); G2_DMA(2, avo0, avo1, bvo);
        }
        VMBAR(16);
        if (g == 0) RDALL();
        WAITBAR(8);
      }
#undef RDALL
      if (g == 1) {
#define FIN2(O, E, BL) do { \
      const float e2 = ew[(BL) * 64 + lane]; const u32x4 t0 = xw[(BL) * 128 + lane], t1 = xw[(BL) * 128 + 64 + lane]; \
      const float em = fmaxf(E, e2); float w1 = EXP2(E - em), w2 = EXP2(e2 - em); \
      const float rs = __builtin_amdgcn_rcpf(w1 + w2); w1 *= rs; w2 *= rs; u32x2 pk[4]; \
      _Pragma("unroll") for (int r = 0; r < 4; ++r) { \
        const unsigned ta = r < 2 ? t0[2 * r] : t1[2 * r - 4], tb2 = r < 2 ? t0[2 * r + 1] : t1[2 * r - 3]; \
        pk[r].x = cvtpk(O[4 * r] * w1 + bf_lo(ta) * w2, O[4 * r + 1] * w1 + bf_hi(ta) * w2); \
        pk[r].y = cvtpk(O[4 * r + 2] * w1 + bf_lo(tb2) * w2, O[4 * r + 3] * w1 + bf_hi(tb2) * w2); } \
      bf16_t* op = P.AH + (size_t)(qb * 256 + qg * 64 + (BL) * 32 + (lane & 31)) * KA + h * 32 + 8 * hi; \
      _Pragma("unroll") for (int k = 0; k < 4; k += 2) { \
        auto rx = __builtin_amdgcn_permlane32_swap(pk[k].x, pk[k + 1].x, false, false); \
        auto ry = __builtin_amdgcn_permlane32_swap(pk[k].y, pk[k + 1].y, false, false); \
        st16wt(op + 8 * k, (u32x4){rx[0], ry[0], rx[1], ry[1]}); } } while (0)
      G2_DMA(3, avo0, avo1, bvo); G2_DMA(4, avo0, avo1, bvo); FIN2(oA, eA, 0); FIN2(oB, eB, 1);
#undef FIN2
      }
      SB();
      if (g == 1) {
      VMBAR(20);
      G2_DMA(5, avo0, avo1, bvo); VMBAR(20);
      G2_DMA(6, avo0, avo1, bvo); VMBAR(20);
      G2_DMA(7, avo0, avo1, bvo); VMBAR(16);
      if (w == 4 && lane == 0) set_flag(P.flags + FB_OFF + qb * 32 + h, tok);
#pragma unroll 1
      for (int c = 4; c < G2_NCH - 4; ++c) { G2_DMA(c + 4, avo0, avo1, bvo); VMBAR(16); }
      VMBAR(8);
      VMBAR(0);
      if (bid == 0 && w == 4) {
#pragma unroll
        for (int i = 0; i < 4; ++i) wait_flags(P.flags + FA_OFF + i * 64 + lane, tok);
        if (lane == 0) set_flag(P.flags, tok);
      }
      STAMP(3, 3);
      __builtin_amdgcn_endpgm();
      }
      f32x4 acc[4][4];
#pragma unroll
      for (int m = 0; m < 4; ++m)
#pragma unroll
        for (int n = 0; n < 4; ++n) { acc[m][n] = (f32x4){0.f, 0.f, 0.f, 0.f}; PIN(acc[m][n]); }
      u32x2 xv[2][4];
#pragma unroll
      for (int mm = 0; mm < 2; ++mm)
#pragma unroll
        for (int n = 0; n < 4; ++n) xv[mm][n] = *(const u32x2*)(P.x + (size_t)(orow + 16 * mm) * DM + ocol + 16 * n);
      asm volatile("s_nop 1" ::: "memory");
      STAMP(3, 1);
#pragma unroll 1
      for (int c4 = 0; c4 < 8; ++c4) {
        INTERVAL(1, fa0, fb0, fa1, fb1); INTERVAL(2, fa1, fb1, fa0, fb0); INTERVAL(3, fa0, fb0, fa1, fb1);
        if (c4 == 3 && kh2 == 0 && w == 0)
          wait_flags(P.flags + FB_OFF + (rt2 >> 1) * 32 + (lane & 31), tok);
        INTERVAL(0, fa1, fb1, fa0, fb0);
      }
      INTERVAL(1, fa0, fb0, fa1, fb1);
      INTERVAL(2, fa1, fb1, fa0, fb0);
      MR4(0, 3, fa0, fb0, fa1, fb1); MR4(4, 3, fa0, fb0, fa1, fb1); MR4(8, 3, fa0, fb0, fa1, fb1); MR4(12, 3, fa0, fb0, fa1, fb1);
      MR4(16, 3, fa0, fb0, fa1, fb1); MR4(20, 3, fa0, fb0, fa1, fb1);
      MM(24, fa0, fb0); MM(25, fa0, fb0); MM(26, fa0, fb0); MM(27, fa0, fb0); MM(28, fa0, fb0); MM(29, fa0, fb0); MM(30, fa0, fb0); MM(31, fa0, fb0);
      SB(); asm volatile("s_waitcnt lgkmcnt(0)" ::: "memory"); SB();
#pragma unroll
      for (int i = 0; i < 32; ++i) MFMA16A(acc[i & 3][(i >> 2) & 3], fb1[i >> 4][(i >> 2) & 3], fa1[i >> 4][i & 3]);
#undef INTERVAL
#undef MR4
#undef MR
#undef MM
#undef RD
      asm volatile("s_nop 7\n\ts_nop 7" ::: "memory");
      SB();
      STAMP(3, 2);
      const int pbid = bid ^ 1;
      unsigned* mine = (unsigned*)P.part + (size_t)bid * 4096 + w4 * 1024 + lane * 4;
      f32x4 keep[2][4];
#define PKP(A, B) (u32x4){cvtpk(A[0], A[1]), cvtpk(A[2], A[3]), cvtpk(B[0], B[1]), cvtpk(B[2], B[3])}
      if (kh2) {
#pragma unroll
        for (int i = 0; i < 4; ++i) st16wt(mine + i * 256, PKP(acc[i >> 1][2 * (i & 1)], acc[i >> 1][2 * (i & 1) + 1]));
#pragma unroll
        for (int i = 0; i < 8; ++i) keep[i >> 2][i & 3] = acc[2 + (i >> 2)][i & 3];
      } else {
#pragma unroll
        for (int i = 0; i < 4; ++i) st16wt(mine + i * 256, PKP(acc[2 + (i >> 1)][2 * (i & 1)], acc[2 + (i >> 1)][2 * (i & 1) + 1]));
#pragma unroll
        for (int i = 0; i < 8; ++i) keep[i >> 2][i & 3] = acc[i >> 2][i & 3];
      }
#undef PKP
      asm volatile("s_waitcnt vmcnt(0)" ::: "memory");
      if (lane == 0) set_flag(P.flags + FX_OFF + bid * 4 + w4, tok);
      wait_flags(P.flags + FX_OFF + pbid * 4 + w4, tok);
      STAMP(3, 4);
      const unsigned* theirs = (const unsigned*)P.part + (size_t)pbid * 4096 + w4 * 1024 + lane * 4;
      u32x4 pv[4];
      asm volatile("global_load_dwordx4 %0, %4, off sc1\n\tglobal_load_dwordx4 %1, %4, off offset:1024 sc1\n\t"
                   "global_load_dwordx4 %2, %4, off offset:2048 sc1\n\tglobal_load_dwordx4 %3, %4, off offset:3072 sc1\n\t"
                   "s_waitcnt vmcnt(0)"
                   : "=&v"(pv[0]), "=&v"(pv[1]), "=&v"(pv[2]), "=&v"(pv[3]) : "v"(theirs) : "memory");
#pragma unroll
      for (int i = 0; i < 8; ++i) {
        const f32x4 v = keep[i >> 2][i & 3];
        const unsigned p0 = pv[i >> 1][2 * (i & 1)], p1 = pv[i >> 1][2 * (i & 1) + 1];
        const u32x2 xx = xv[i >> 2][i & 3];
        u32x2 o = {cvtpk(bf_lo(xx.x) + v[0] + bf_lo(p0), bf_hi(xx.x) + v[1] + bf_hi(p0)),
                   cvtpk(bf_lo(xx.y) + v[2] + bf_lo(p1), bf_hi(xx.y) + v[3] + bf_hi(p1))};
        *(u32x2*)(P.out + (size_t)(orow + 16 * (i >> 2)) * DM + ocol + 16 * (i & 3)) = o;
      }
      STAMP(3, 3);
    }
#undef LDK
#undef LDV
#undef BF
#undef FIN1
#undef SAFE_UNIT
#undef SAFE_PASS
  }
#undef G2_DMA
#undef G2_OFFS
#undef HSTORE
#undef DMA_ST
}

extern "C" void kernel_launch(void* const* d_in, const int* in_sizes, int n_in, void* d_out, int out_size, void* d_ws,
                              size_t ws_size, hipStream_t stream) {
  char* ws = (char*)d_ws;
  Params P;
  P.x = (const bf16_t*)d_in[0];
  P.wqkv = (const bf16_t*)d_in[1]; P.wo = (const bf16_t*)d_in[2];
  P.wg = (const bf16_t*)d_in[4]; P.wu = (const bf16_t*)d_in[5]; P.wd = (const bf16_t*)d_in[6];
  P.xn = (bf16_t*)(ws + WS_XN);
  P.Qb = (bf16_t*)(ws + WS_Q); P.Kb = (bf16_t*)(ws + WS_K); P.Vb = (bf16_t*)(ws + WS_V);
  P.AH = (bf16_t*)(ws + WS_AH); P.out = (bf16_t*)d_out;
  P.part = (float*)(ws + WS_PART); P.flags = (u64*)(ws + WS_FLAGS);
  (void)hipFuncSetAttribute((const void*)fused_kernel, hipFuncAttributeMaxDynamicSharedMemorySize, LDS_TOTAL);
  fused_kernel<<<256, 512, LDS_TOTAL, stream>>>(P);
}
```

```cpp
#include <hip/hip_runtime.h>
#include <hip/hip_bf16.h>
#include <stdint.h>

typedef unsigned short bf16_t;
typedef short bf16x8 __attribute__((ext_vector_type(8)));
typedef short s16x4 __attribute__((ext_vector_type(4)));
typedef float f32x4 __attribute__((ext_vector_type(4)));
typedef float f32x16 __attribute__((ext_vector_type(16)));
typedef float f32x2 __attribute__((ext_vector_type(2)));
typedef __bf16 bf16x2v __attribute__((ext_vector_type(2)));
typedef unsigned u32x4 __attribute__((ext_vector_type(4)));
typedef unsigned u32x2 __attribute__((ext_vector_type(2)));
#define LAS __attribute__((address_space(3)))
typedef LAS const char* lds_cptr;

constexpr int SEQ = 2048, DM = 1024, FF = 3584, NQKV = 3072, KA = 4608;

constexpr size_t WS_FLAGS = 0;
constexpr size_t WS_XN = 1u << 20;
constexpr size_t WS_Q = WS_XN + (size_t)SEQ * DM * 2;
constexpr size_t WS_K = WS_Q + (size_t)SEQ * DM * 2;
constexpr size_t WS_V = WS_K + (size_t)SEQ * DM * 2;
constexpr size_t WS_AH = WS_V + (size_t)SEQ * DM * 2;
constexpr size_t WS_PART = WS_AH + (size_t)SEQ * KA * 2;
typedef unsigned long long u64;
constexpr int FL_OFF = 256, FA_OFF = 512, FH_OFF = 768, FB_OFF = 1024, FX_OFF = 1280;

#define SB() __builtin_amdgcn_sched_barrier(0)
#define STAMP(kid, i) do {} while (0)

__device__ __forceinline__ unsigned cvtpk(float lo, float hi) {
  f32x2 v = {lo, hi};
  bf16x2v b = __builtin_convertvector(v, bf16x2v);
  return __builtin_bit_cast(unsigned, b);
}
__device__ __forceinline__ float bf_lo(unsigned u) { return __uint_as_float(u << 16); }
__device__ __forceinline__ float bf_hi(unsigned u) { return __uint_as_float(u & 0xffff0000u); }
__device__ __forceinline__ float rbf(float x) { return bf_lo(cvtpk(x, x)); }

__device__ __forceinline__ void glds16(const void* g, unsigned lds_dst) {
  unsigned keep;
  asm volatile("s_mov_b32 %0, m0\n\ts_mov_b32 m0, %2\n\ts_nop 0\n\tglobal_load_lds_dwordx4 %1, off\n\ts_mov_b32 m0, %0"
               : "=&s"(keep) : "v"(g), "s"(lds_dst) : "memory");
}
__device__ __forceinline__ void glds16s(const void* sbase, unsigned voff, unsigned lds_dst) {
  unsigned keep;
  asm volatile("s_mov_b32 %0, m0\n\ts_mov_b32 m0, %3\n\ts_nop 0\n\tglobal_load_lds_dwordx4 %1, %2\n\ts_mov_b32 m0, %0"
               : "=&s"(keep) : "v"(voff), "s"(sbase), "s"(lds_dst) : "memory");
}
__device__ __forceinline__ void glds16c(const void* sbase, unsigned voff, unsigned lds_dst) {
  unsigned keep;
  asm volatile("s_mov_b32 %0, m0\n\ts_mov_b32 m0, %3\n\ts_nop 0\n\tglobal_load_lds_dwordx4 %1, %2 sc1\n\ts_mov_b32 m0, %0"
               : "=&s"(keep) : "v"(voff), "s"(sbase), "s"(lds_dst) : "memory");
}
__device__ __forceinline__ void glds4c(const void* g, unsigned lds_dst) {
  unsigned keep;
  asm volatile("s_mov_b32 %0, m0\n\ts_mov_b32 m0, %2\n\ts_nop 0\n\tglobal_load_lds_dword %1, off sc1\n\ts_mov_b32 m0, %0"
               : "=&s"(keep) : "v"(g), "s"(lds_dst) : "memory");
}
__device__ __forceinline__ bf16x8 lds_rd128(lds_cptr p) { return *(const LAS bf16x8*)p; }
__device__ __forceinline__ s16x4 lds_tr(lds_cptr p) { return __builtin_amdgcn_ds_read_tr16_b64_v4i16((LAS s16x4*)p); }
__device__ __forceinline__ bf16x8 cat8(s16x4 a, s16x4 b) { return __builtin_shufflevector(a, b, 0, 1, 2, 3, 4, 5, 6, 7); }
__device__ __forceinline__ void st16wt(void* p, u32x4 v) {
  asm volatile("global_store_dwordx4 %0, %1, off sc1\n\ts_nop 1" :: "v"(p), "v"(v) : "memory");
}
__device__ __forceinline__ void wait_flags(const u64* f, u64 tok) {
  unsigned spins = 0;
  while (!__all(__hip_atomic_load(f, __ATOMIC_RELAXED, __HIP_MEMORY_SCOPE_AGENT) == tok)) {
    __builtin_amdgcn_s_sleep(3);
    if (++spins > (1u << 20)) break;
  }
  asm volatile("" ::: "memory");
}
__device__ __forceinline__ void set_flag(u64* f, u64 tok) { __hip_atomic_store(f, tok, __ATOMIC_RELAXED, __HIP_MEMORY_SCOPE_AGENT); }
__device__ __forceinline__ void publish(LAS unsigned* cnt, u64* flag, u64 tok, int lane) {
  asm volatile("s_waitcnt vmcnt(0)" ::: "memory");
  if (lane == 0) {
    const unsigned old = __hip_atomic_fetch_add(cnt, 1u, __ATOMIC_RELAXED, __HIP_MEMORY_SCOPE_WORKGROUP);
    if (old == 7u) set_flag(flag, tok);
  }
}
__device__ __forceinline__ void wg_wait(int w, int lane, const u64* f, u64 tok, volatile LAS unsigned* go) {
  if (w == 0) { wait_flags(f, tok); if (lane == 0) *go = 1u; }
  else { while (*go == 0u) __builtin_amdgcn_s_sleep(1); asm volatile("" ::: "memory"); }
}

#define MFMA16(a, b, c) __builtin_amdgcn_mfma_f32_16x16x32_bf16(a, b, c, 0, 0, 0)
#define MFMA32(a, b, c) __builtin_amdgcn_mfma_f32_32x32x16_bf16(a, b, c, 0, 0, 0)
#define WAITBAR(N) do { SB(); asm volatile("s_waitcnt vmcnt(" #N ") lgkmcnt(0)\n\ts_barrier" ::: "memory"); SB(); } while (0)
#define VMBAR(N) do { SB(); asm volatile("s_waitcnt vmcnt(" #N ")\n\ts_barrier" ::: "memory"); SB(); } while (0)
#define LGBAR() do { SB(); asm volatile("s_waitcnt lgkmcnt(0)\n\ts_barrier" ::: "memory"); SB(); } while (0)
#define BARRIER() do { SB(); asm volatile("s_barrier" ::: "memory"); SB(); } while (0)
#define MFMA16A(acc, a, b) asm volatile("v_mfma_f32_16x16x32_bf16 %0, %1, %2, %0" : "+v"(acc) : "v"(a), "v"(b))
#define MFMA16G(acc, a, b) MFMA16A(acc, a, b)
#define PINACC(x) asm volatile("" : "+v"(x))
#define ADV64(p, inc) asm volatile("v_mad_u64_u32 %0, vcc, %1, 1, %0" : "+v"(p) : "v"(inc) : "vcc")
#define PIN(x) asm volatile("" : "+v"(x))
#define EXP2(x) __builtin_amdgcn_exp2f(x)

constexpr int G1_AOFF = 0, G1_BOFF = 65536, G1_BREG = 10304, G1_BSLOT = 2 * G1_BREG;
constexpr int AT_STAGE = 32768, AT_Q = 98304, AT_EX = 131072, AT_EE = AT_EX + 16384, AT_HF = 150528;
constexpr int G2_SLOT = 49152, G2_NCH = 18;
constexpr int LDS_CTRL = 150016, LDS_SINK = 152576, LDS_TOTAL = LDS_SINK + 1024;

__device__ __forceinline__ float wave_sum(float v) {
#define ROR(n) v += __uint_as_float(__builtin_amdgcn_update_dpp(0u, __float_as_uint(v), 0x120 + (n), 0xf, 0xf, false))
  ROR(8); ROR(4); ROR(2); ROR(1);
#undef ROR
  auto a = __builtin_amdgcn_permlane16_swap(__float_as_uint(v), __float_as_uint(v), false, false);
  v = __uint_as_float(a[0]) + __uint_as_float(a[1]);
  auto b = __builtin_amdgcn_permlane32_swap(__float_as_uint(v), __float_as_uint(v), false, false);
  return __uint_as_float(b[0]) + __uint_as_float(b[1]);
}
__device__ __forceinline__ float max16(const f32x16& p) {
  float a = fmaxf(fmaxf(p[0], p[1]), p[2]), b = fmaxf(fmaxf(p[3], p[4]), p[5]);
  a = fmaxf(fmaxf(a, p[6]), p[7]); b = fmaxf(fmaxf(b, p[8]), p[9]);
  a = fmaxf(fmaxf(a, p[10]), p[11]); b = fmaxf(fmaxf(b, p[12]), p[13]);
  a = fmaxf(fmaxf(a, p[14]), p[15]);
  float m = fmaxf(a, b);
  auto rr = __builtin_amdgcn_permlane32_swap(__float_as_uint(m), __float_as_uint(m), false, false);
  return fmaxf(__uint_as_float(rr[0]), __uint_as_float(rr[1]));
}
__device__ __forceinline__ f32x16 splat16(float v) { f32x16 r;
#pragma unroll
  for (int i = 0; i < 16; ++i) r[i] = v;
  return r; }

struct Params {
  const bf16_t *x, *wqkv, *wg, *wu, *wo, *wd;
  bf16_t *xn, *Qb, *Kb, *Vb, *AH, *out;
  float* part; u64* flags;
};

__global__ __launch_bounds__(512, 2) void fused_kernel(Params P) {
  extern __shared__ __attribute__((aligned(16))) char smem[];
  const int w = __builtin_amdgcn_readfirstlane(threadIdx.x >> 6);
  const int g = w >> 2, w4 = w & 3;
  const int bid = blockIdx.x, xcd = bid & 7, jj = bid >> 3;
  const int cg = xcd * 4 + (jj & 3), rt = jj >> 2;
  const unsigned lds0 = (unsigned)(uintptr_t)smem;
  const lds_cptr L = (lds_cptr)smem;
  LAS unsigned* ctrl = (LAS unsigned*)((LAS char*)smem + LDS_CTRL);
  u64 tok;

  u32x4 hv[8];
  bf16_t* hp0; bf16_t* hp1;
#define HSTORE(i) st16wt((i) < 4 ? hp0 + (size_t)(i) * 16 * KA : hp1 + ((w4 & 1) ? (size_t)((i) & 1) * 32 * KA : (size_t)(((i) - 2) & 3) * 16 * KA), hv[i])
  const int h = xcd * 4 + (jj >> 3), qb = jj & 7;
  const char* kg = (const char*)(P.Kb + (size_t)h * SEQ * 32) + w4 * 2048;
  const char* vg = (const char*)(P.Vb + (size_t)h * SEQ * 32) + w4 * 2048;
#define DMA_ST(st, KH) do { const unsigned d_ = lds0 + ((st) & 3) * AT_STAGE + (KH) * 8192 + w4 * 2048; const int o_ = (KH) * 65536 + (st) * 8192; \
    glds16c(kg + o_, voff, d_); glds16c(kg + o_ + 1024, voff, d_ + 1024); \
    glds16c(vg + o_, voff, d_ + 16384); glds16c(vg + o_ + 1024, voff, d_ + 16384 + 1024); } while (0)

  {
    const int lane = threadIdx.x & 63;
    const int wm = 2 * g + (w4 >> 1), wn = w4 & 1;
    STAMP(1, 0);
    const int row0 = rt * 256;
    u32x4 xa, xb; u64 ep;
    asm volatile("global_load_dwordx4 %0, %3, off\n\tglobal_load_dwordx4 %1, %3, off offset:1024\n\tglobal_load_dwordx2 %2, %4, off sc1"
                 : "=&v"(xa), "=&v"(xb), "=&v"(ep) : "v"(P.x + (size_t)(bid * 8 + w) * DM + lane * 8), "v"(P.flags) : "memory");
    if (threadIdx.x < 8) ctrl[threadIdx.x] = 0u;
    asm volatile("s_waitcnt vmcnt(0) lgkmcnt(0)\n\ts_barrier" : "+v"(xa), "+v"(xb), "+v"(ep) :: "memory");
    STAMP(0, 0);
    tok = (((u64)(unsigned)__builtin_amdgcn_readfirstlane((unsigned)(ep >> 32)) << 32) | (unsigned)__builtin_amdgcn_readfirstlane((unsigned)ep)) + 1ull;
    {
      float v[16];
#pragma unroll
      for (int i = 0; i < 4; ++i) { v[2 * i] = bf_lo(xa[i]); v[2 * i + 1] = bf_hi(xa[i]); v[8 + 2 * i] = bf_lo(xb[i]); v[9 + 2 * i] = bf_hi(xb[i]); }
      float sm = 0.f;
#pragma unroll
      for (int i = 0; i < 16; ++i) sm += v[i];
      const float mean = wave_sum(sm) * (1.f / DM);
      float q = 0.f;
#pragma unroll
      for (int i = 0; i < 16; ++i) { const float d = v[i] - mean; q += d * d; }
      const float mb = rbf(mean), vb = rbf(wave_sum(q) * (1.f / DM));
      const float inv = 1.0f / rbf(sqrtf(rbf(vb + 1e-5f)));
      u32x4 oa, ob;
#pragma unroll
      for (int i = 0; i < 4; ++i) {
        const unsigned da = cvtpk(v[2 * i] - mb, v[2 * i + 1] - mb), db = cvtpk(v[8 + 2 * i] - mb, v[9 + 2 * i] - mb);
        oa[i] = cvtpk(bf_lo(da) * inv, bf_hi(da) * inv);
        ob[i] = cvtpk(bf_lo(db) * inv, bf_hi(db) * inv);
      }
      bf16_t* op = P.xn + (size_t)(bid * 8 + w) * DM + lane * 8;
      st16wt(op, oa); st16wt(op + 512, ob);
      STAMP(0, 4);
      publish(ctrl + 0, P.flags + FL_OFF + bid, tok, lane);
      STAMP(0, 1);
    }
    unsigned avoff0 = (unsigned)((lane >> 3) * 2048 + (((lane & 7) ^ ((lane >> 4) + 2)) * 16)), avoff1 = avoff0 ^ 64u;
    const char* abase = (const char*)P.xn + (size_t)(row0 + 32 * w4) * 2048;
#define DMA_A(hh, t) do { _Pragma("unroll") for (int _i = 0; _i < 4; ++_i) \
    glds16c(abase + (size_t)(hh) * 262144 + _i * 16384 + (t) * 128, (_i & 1) ? avoff1 : avoff0, \
            lds0 + G1_AOFF + ((t) & 1) * 32768 + (hh) * 16384 + (4 * w4 + _i) * 1024); } while (0)
    const char* bptr[4]; unsigned bld[4]; unsigned bdst[4];
#pragma unroll
    for (int i = 0; i < 4; ++i) {
      const int b = (i == 0) ? (g ? 16 + w4 : 4 * w4) : 4 * w4 + i;
      const int region = b >= 10 ? 1 : 0, bi = b - 10 * region;
      const int Pc = bi * 64 + lane;
      const int k = Pc / 20, pc = Pc - 20 * k, f = pc >> 1, e8 = (pc & 1) * 8;
      const bool isq = f >= 8 || (region && (f & 3) == 3);
      const int qf = f >= 8 ? (region ? f - 4 : f - 8) : 2 + (f >> 2);
      const bf16_t* mat = isq ? P.wqkv : (f >= 4 ? P.wu : P.wg);
      const int ld = isq ? NQKV : FF;
      const int col = e8 + (isq ? 96 * cg + 16 * qf : 112 * cg + 64 * region + 16 * (f & 3));
      bptr[i] = (const char*)(mat + (size_t)k * ld + col);
      bld[i] = (unsigned)ld * 64u;
      bdst[i] = lds0 + G1_BOFF + region * G1_BREG + bi * 1024 + (bi >= 5 ? 32 : 0);
    }
#define DMA_B(i, s) glds16(bptr[i], bdst[i] + ((s) & 3) * G1_BSLOT)
#define ADV_B() do { ADV64(bptr[0], bld[0]); ADV64(bptr[1], bld[1]); ADV64(bptr[2], bld[2]); ADV64(bptr[3], bld[3]); } while (0)
    if (g == 0) {
      DMA_B(0, 0); DMA_B(1, 0); DMA_B(2, 0); DMA_B(3, 0); ADV_B();
      DMA_B(0, 1); ADV_B();
      DMA_B(0, 2); DMA_B(1, 2); DMA_B(2, 2); DMA_B(3, 2); ADV_B();
    } else {
      DMA_B(0, 0); ADV_B();
      DMA_B(0, 1); DMA_B(1, 1); DMA_B(2, 1); DMA_B(3, 1); ADV_B();
      DMA_B(0, 2); ADV_B();
    }
    const int fr = lane & 15, fq = lane >> 4, kq = ((fq & 1) << 1) | (fq >> 1);
    const int aoff0 = G1_AOFF + (64 * wm + fr) * 128 + ((kq ^ (((fr >> 1) + 2) & 7)) * 16);
    lds_cptr pa0 = L + aoff0, pa1 = L + (aoff0 ^ 64);
    lds_cptr pb0 = L + (G1_BOFF + (8 * kq + (fr >> 2)) * 320 + (fq & 1) * 32 + 8 * (lane & 3) + G1_BREG * wn);
    lds_cptr pb1 = pb0 + 2 * G1_BSLOT;
    asm volatile("" : "+v"(pa0), "+v"(pa1), "+v"(pb0), "+v"(pb1), "+v"(avoff0), "+v"(avoff1));
    f32x4 acc[4][10];
#pragma unroll
    for (int m = 0; m < 4; ++m)
#pragma unroll
      for (int j = 0; j < 10; ++j) { acc[m][j] = (f32x4){0.f, 0.f, 0.f, 0.f}; PINACC(acc[m][j]); }
    bf16x8 af[4], bfr[10];
    wg_wait(w, lane, P.flags + FL_OFF + rt * 32 + (lane & 31), tok, ctrl + 3);
    STAMP(0, 2);
    DMA_A(g, 0);
    STAMP(0, 3);
    if (g == 0) { WAITBAR(0); } else { DMA_A(0, 1); WAITBAR(4); BARRIER(); }
    STAMP(1, 1);
#define STEP(U, ISSUE, WN, LASTBAR) do { \
    { const lds_cptr pa = ((U) & 1) ? pa1 : pa0; const lds_cptr pb = (((U) & 2) ? pb1 : pb0) + ((U) & 1) * G1_BSLOT; \
      _Pragma("unroll") for (int m = 0; m < 4; ++m) af[m] = lds_rd128(pa + ((U) >> 1) * 32768 + m * 2048); \
      _Pragma("unroll") for (int j = 0; j < 10; ++j) bfr[j] = cat8(lds_tr(pb + j * 32), lds_tr(pb + j * 32 + 1280)); } \
    if (ISSUE) { DMA_B(0, (U) + 3); \
      if (g == ((U) & 1)) { DMA_A(1 - ((U) & 1), 2 * it + ((U) >> 1) + 1 + ((U) & 1)); } \
      else { DMA_B(1, (U) + 3); DMA_B(2, (U) + 3); DMA_B(3, (U) + 3); } } \
    WAITBAR(WN); \
    _Pragma("unroll") for (int j = 0; j < 10; ++j) { _Pragma("unroll") for (int m = 0; m < 4; ++m) MFMA16G(acc[m][j], bfr[j], af[m]); \
      if ((ISSUE) && (j & 1) && j < 8) ADV64(bptr[j >> 1], bld[j >> 1]); } \
    if (LASTBAR) BARRIER(); } while (0)
    int it = 0;
#pragma unroll 1
    for (; it < 7; ++it) { STEP(0, true, 9, true); STEP(1, true, 9, true); STEP(2, true, 9, true); STEP(3, true, 9, true); }
    STEP(0, true, 9, true); STEP(1, false, 4, true); STEP(2, false, 0, true); STEP(3, false, 0, g == 0);
#undef STEP
    asm volatile("s_nop 7\n\ts_nop 7" ::: "memory");
    SB();
    STAMP(1, 2);
#undef DMA_A
#undef DMA_B
#undef ADV_B
    int tz = threadIdx.x; asm volatile("" : "+v"(tz));
    const int fr2 = tz & 15, fq2 = (tz >> 4) & 3, sel1 = fq2 & 1, c8 = (fq2 >> 1) * 8;
    const float qscale = 0.17677669529663687f * 1.4426950408889634f;
#define SWAP16(A, B, OUT) do { auto r0_ = __builtin_amdgcn_permlane16_swap(A.x, B.x, false, false); \
    auto r1_ = __builtin_amdgcn_permlane16_swap(A.y, B.y, false, false); OUT = (u32x4){r0_[0], r1_[0], r0_[1], r1_[1]}; } while (0)
#define SILU4(m, j, OUT) do { float hv_[4]; _Pragma("unroll") for (int r = 0; r < 4; ++r) { \
      const float tg_ = acc[m][j][r], tu_ = acc[m][(j) + 4][r]; \
      hv_[r] = (tg_ * tu_) * __builtin_amdgcn_rcpf(1.f + __builtin_amdgcn_exp2f(-1.4426950408889634f * tg_)); } \
    OUT = (u32x2){cvtpk(hv_[0], hv_[1]), cvtpk(hv_[2], hv_[3])}; } while (0)
#define PK4(V, SC, OUT) do { const f32x4 v_ = (V) * (SC); OUT = (u32x2){cvtpk(v_[0], v_[1]), cvtpk(v_[2], v_[3])}; } while (0)
    const int rowb = row0 + 64 * wm + fr2;
#define QKV_HEAD(hd, ja, jb) do { const int typ_ = (hd) >> 5, h_ = (hd) & 31; const float sa = typ_ == 0 ? qscale : 1.f; \
      bf16_t* dst; int mstep; \
      if (typ_ == 1) { dst = P.Kb + ((((size_t)h_ * 64 + (rowb >> 5)) * 2 + sel1) * 64 + (rowb & 31) + 4 * c8) * 8; mstep = 128; } \
      else { dst = (typ_ == 0 ? P.Qb : P.Vb) + ((size_t)h_ * SEQ + rowb) * 32 + 16 * sel1 + c8; mstep = 512; } \
      _Pragma("unroll") for (int m = 0; m < 4; ++m) { \
        u32x2 a_, b_; u32x4 o; PK4(acc[m][ja], sa, a_); PK4(acc[m][jb], sa, b_); SWAP16(a_, b_, o); \
        st16wt(dst + (m & 1) * mstep + (m >> 1) * 1024, o); } } while (0)
    __builtin_amdgcn_s_setprio(3);
    QKV_HEAD(3 * cg + 2 * wn, 8, 9);
    if (wn) QKV_HEAD(3 * cg + 1, 3, 7);
    __builtin_amdgcn_s_setprio(0);
    SB();
    bf16_t* hd = P.AH + (size_t)rowb * KA + DM + 112 * cg + 64 * wn + c8;
    hp0 = hd + 16 * sel1;
    hp1 = wn ? hd + 32 + (size_t)sel1 * 16 * KA : hd + 32 + 16 * sel1;
#pragma unroll
    for (int m = 0; m < 3; ++m) { u32x2 a_, b_; SILU4(m, 0, a_); SILU4(m, 1, b_); SWAP16(a_, b_, hv[m]); PIN(hv[m]); }
    STAMP(1, 4);
    publish(ctrl + 1, P.flags + FA_OFF + cg * 8 + rt, tok, tz & 63);
    { u32x2 a_, b_; SILU4(3, 0, a_); SILU4(3, 1, b_); SWAP16(a_, b_, hv[3]); PIN(hv[3]); }
    if (wn == 0) {
#pragma unroll
      for (int m = 0; m < 2; ++m) { u32x2 a_, b_; SILU4(m, 2, a_); SILU4(m, 3, b_); SWAP16(a_, b_, hv[6 + m]); PIN(hv[6 + m]); }
    }
    STAMP(1, 3);
    STAMP(2, 0);
    if (g == 0) {
      const int l6 = tz & 63;
      const int cgQ = h / 3, cgK = (32 + h) / 3, cgV = (64 + h) / 3;
      wg_wait(w, l6, P.flags + FA_OFF + (l6 < 8 ? cgK * 8 + l6 : (l6 < 16 ? cgV * 8 + l6 - 8 : cgQ * 8 + qb)), tok, ctrl + 4);
      const unsigned voff = l6 * 16;
      DMA_ST(0, 0); DMA_ST(0, 1);
      const char* qsrc = (const char*)(P.Qb + ((size_t)h * SEQ + qb * 256 + w4 * 64) * 32);
#pragma unroll
      for (int i = 0; i < 4; ++i) glds16c(qsrc + i * 1024, voff, lds0 + AT_Q + w4 * 4096 + i * 1024);
    }
    STAMP(2, 4);
    if (wn == 0) {
#pragma unroll
      for (int m = 2; m < 4; ++m) { u32x2 a_, b_; SILU4(m, 2, a_); SILU4(m, 3, b_); SWAP16(a_, b_, hv[2 + m]); }
    } else {
#pragma unroll
      for (int mp = 0; mp < 2; ++mp) { u32x2 a_, b_; SILU4(2 * mp, 2, a_); SILU4(2 * mp + 1, 2, b_); SWAP16(a_, b_, hv[4 + mp]); }
      hv[6] = hv[4]; hv[7] = hv[5];
    }
#undef SWAP16
#undef SILU4
#undef PK4
#undef QKV_HEAD
  }

  const int k4 = xcd & 3, rt2 = (xcd >> 2) * 4 + (jj >> 3), ct2 = jj & 7;
  int c_issued = 0;
#define G2_K(c) ((c) < 14 ? DM + (14 * k4 + (c)) * 64 : (4 * k4 + (c) - 14) * 64)
#define G2_DMA_A(c, sl) do { \
    const char* ab_ = (const char*)P.AH + (size_t)(rt2 * 256 + 64 * w4) * (KA * 2) + G2_K(c) * 2; \
    const unsigned d_ = lds0 + (sl) * G2_SLOT + w4 * 8192; unsigned keep_; \
    asm volatile("s_mov_b32 %0, m0\n\ts_mov_b32 m0, %7\n\ts_nop 0\n\t" \
                 "global_load_lds_dwordx4 %1, %5 sc1\n\tglobal_load_lds_dwordx4 %2, %5 offset:1024 sc1\n\t" \
                 "global_load_lds_dwordx4 %3, %5 offset:2048 sc1\n\tglobal_load_lds_dwordx4 %4, %5 offset:3072 sc1\n\t" \
                 "s_mov_b32 m0, %8\n\ts_nop 0\n\t" \
                 "global_load_lds_dwordx4 %1, %6 sc1\n\tglobal_load_lds_dwordx4 %2, %6 offset:1024 sc1\n\t" \
                 "global_load_lds_dwordx4 %3, %6 offset:2048 sc1\n\tglobal_load_lds_dwordx4 %4, %6 offset:3072 sc1\n\ts_mov_b32 m0, %0" \
                 : "=&s"(keep_) : "v"(avo[0]), "v"(avo[1]), "v"(avo[2]), "v"(avo[3]), "s"(ab_), "s"(ab_ + 32 * KA * 2), "s"(d_), "s"(d_ + 4096u) : "memory"); } while (0)
#define G2_DMA_B(c, sl) do { const int kg_ = G2_K(c); \
    const char* bb_ = (kg_ < DM ? (const char*)P.wo + (size_t)kg_ * 2048 : (const char*)P.wd + (size_t)(kg_ - DM) * 2048) + ct2 * 256 + w4 * 32768; \
    const unsigned d_ = lds0 + (sl) * G2_SLOT + 32768 + w4 * 4096; unsigned keep_; \
    asm volatile("s_mov_b32 %0, m0\n\ts_mov_b32 m0, %6\n\ts_nop 0\n\t" \
                 "global_load_lds_dwordx4 %1, %5\n\tglobal_load_lds_dwordx4 %2, %5 offset:1024\n\t" \
                 "global_load_lds_dwordx4 %3, %5 offset:2048\n\tglobal_load_lds_dwordx4 %4, %5 offset:3072\n\ts_mov_b32 m0, %0" \
                 : "=&s"(keep_) : "v"(bvo[0]), "v"(bvo[1]), "v"(bvo[2]), "v"(bvo[3]), "s"(bb_), "s"(d_) : "memory"); } while (0)
#define G2_DMA(c, sl) do { G2_DMA_A(c, sl); G2_DMA_B(c, sl); } while (0)
#define G2_OFFS(lane) unsigned avo[4], bvo[4]; _Pragma("unroll") for (int _i = 0; _i < 4; ++_i) { \
    avo[_i] = ((unsigned)(((lane) >> 3) * (KA * 2) + ((((lane) & 7) ^ (((lane) >> 4) + 2)) * 16)) ^ ((_i & 1) * 64u)) + _i * (8 * KA * 2 - 1024); \
    bvo[_i] = (unsigned)(((lane) >> 4) * 2048 + ((((lane) & 15) ^ (((lane) >> 4) << 1) ^ ((w4 & 1) << 3)) * 16)) + _i * (8192 - 1024); }

  {
    int tb = threadIdx.x; asm volatile("" : "+v"(tb));
    const int lane = tb & 63;
    const int qg = w4, kh = g;
    const int hi = lane >> 5;
    const unsigned voff = lane * 16;
    const short one = (((lane >> 4) & 1) == ((lane >> 2) & 1)) ? (short)0x3f80 : (short)0;
    const bf16x8 sel = {one, one, one, one, one, one, one, one};
    volatile LAS unsigned* flag = (volatile LAS unsigned*)(ctrl + 2);
    const int gq = lane >> 4;
    const int koff = kh * 8192 + lane * 16;
    const int voffl = 16384 + kh * 8192 + (4 * (gq >> 1) + ((lane & 15) >> 2)) * 64 + (gq & 1) * 32 + (lane & 3) * 8;
    const f32x16 zero16 = splat16(0.f);
    f32x16 oA = zero16, oB = zero16;
    f32x4 lA = {0.f, 0.f, 0.f, 0.f}, lB = {0.f, 0.f, 0.f, 0.f};
    float mA, mB;
    const unsigned* hflag = (const unsigned*)(P.flags + FH_OFF + (8 * k4 + ((lane >> 1) & 7)) * 8 + rt2) + (lane & 1);
    bf16x8 qa0, qa1, qb0, qb1;

#define LDK(K0, K1, p) do { K0 = lds_rd128(p); K1 = lds_rd128((p) + 1024); } while (0)
#define LDV(V0, V1, p) do { V0 = cat8(lds_tr(p), lds_tr((p) + 512)); V1 = cat8(lds_tr((p) + 1024), lds_tr((p) + 1536)); } while (0)
#define BF(x) __builtin_bit_cast(bf16x8, x)
    {
      WAITBAR(0);
      { const lds_cptr qp = L + AT_Q + qg * 4096 + (lane & 31) * 64 + hi * 16;
        qa0 = lds_rd128(qp); qa1 = lds_rd128(qp + 32); qb0 = lds_rd128(qp + 2048); qb1 = lds_rd128(qp + 2080); }
      bf16x8 kx0, kx1, ky0, ky1, vx0, vx1, vy0, vy1;
      LDK(kx0, kx1, L + koff);
      LDK(ky0, ky1, L + koff + 2048);
      LDV(vy0, vy1, L + voffl);
      DMA_ST(1, kh); DMA_ST(2, kh);
      f32x16 sA = MFMA32(kx0, qa0, zero16); sA = MFMA32(kx1, qa1, sA);
      f32x16 sB = MFMA32(kx0, qb0, zero16); sB = MFMA32(kx1, qb1, sB);
      mA = max16(sA); mB = max16(sB);
      f32x16 nA = splat16(-mA), nB = splat16(-mB);
      PIN(nA); PIN(nB);
      sA = MFMA32(kx0, qa0, nA); sA = MFMA32(kx1, qa1, sA);
      u32x4 pA0, pA1, pB0 = {0u, 0u, 0u, 0u}, pB1 = {0u, 0u, 0u, 0u};
      STAMP(2, 1);
#define E4(S, i) do { S[i] = EXP2(S[i]); S[i + 1] = EXP2(S[i + 1]); S[i + 2] = EXP2(S[i + 2]); S[i + 3] = EXP2(S[i + 3]); } while (0)
#define SLOT(SC, PC0, PC1, SN, K0, K1, Q0, Q1, NM, PP0, PP1, V0, V1, O, LS, LOADS) do { \
    SN = MFMA32(K0, Q0, NM); SB(); E4(SC, 0); PIN(SC); SB(); \
    SN = MFMA32(K1, Q1, SN); SB(); LOADS; SB(); E4(SC, 4); PIN(SC); SB(); \
    O = MFMA32(V0, BF(PP0), O); SB(); PC0[0] = cvtpk(SC[0], SC[1]); PC0[1] = cvtpk(SC[2], SC[3]); \
      SC[8] = EXP2(SC[8]); SC[9] = EXP2(SC[9]); SC[10] = EXP2(SC[10]); PIN(SC); PIN(PC0); SB(); \
    LS = MFMA16(sel, BF(PP0), LS); SB(); PC0[2] = cvtpk(SC[4], SC[5]); PC0[3] = cvtpk(SC[6], SC[7]); SC[11] = EXP2(SC[11]); PIN(SC); PIN(PC0); SB(); \
    O = MFMA32(V1, BF(PP1), O); SB(); E4(SC, 12); PIN(SC); SB(); \
    LS = MFMA16(sel, BF(PP1), LS); SB(); PC1[0] = cvtpk(SC[8], SC[9]); PC1[1] = cvtpk(SC[10], SC[11]); \
      PC1[2] = cvtpk(SC[12], SC[13]); PC1[3] = cvtpk(SC[14], SC[15]); PIN(PC1); SB(); } while (0)
#define SUB(KC0, KC1, KN0, KN1, VC0, VC1, VP0, VP1, vpu, kp2) do { \
    SLOT(sA, pA0, pA1, sB, KC0, KC1, qb0, qb1, nB, pB0, pB1, VP0, VP1, oB, lB, LDV(VC0, VC1, vpu)); \
    SLOT(sB, pB0, pB1, sA, KN0, KN1, qa0, qa1, nA, pA0, pA1, VC0, VC1, oA, lA, LDK(KC0, KC1, kp2)); } while (0)
      lds_cptr kb0 = L + koff, kb1 = L + koff + 2 * AT_STAGE, vb0 = L + voffl, vb1 = L + voffl + 2 * AT_STAGE;
      PIN(kb0); PIN(kb1); PIN(vb0); PIN(vb1);
#define KP(st) ((((st) & 2) ? kb1 : kb0) + ((st) & 1) * AT_STAGE)
#define VP(st) ((((st) & 2) ? vb1 : vb0) + ((st) & 1) * AT_STAGE)
#define HS(st, i) do { if ((st) < 2) HSTORE((4 * (st) + (i)) & 7); } while (0)
#define STAGE(st, WN, EXTRA) do { \
        SUB(kx0, kx1, ky0, ky1, vx0, vx1, vy0, vy1, VP(st), KP(st) + 4096); \
        HS(st, 0); \
        SUB(ky0, ky1, kx0, kx1, vy0, vy1, vx0, vx1, VP(st) + 2048, KP(st) + 6144); \
        VMBAR(WN); \
        HS(st, 1); \
        EXTRA; \
        if ((st) < 5) {   \
          const int pc = (st) * 8 + w, kb = bid * 36 + (pc < 36 ? pc : 35); \
          glds16s(kb < 2048 ? (const char*)P.wo + (size_t)kb * 1024 : (const char*)P.wd + (size_t)(kb - 2048) * 1024, voff, lds0 + LDS_SINK); \
          DMA_ST((st) + 3, kh); } \
        SUB(kx0, kx1, ky0, ky1, vx0, vx1, vy0, vy1, VP(st) + 4096, KP((st) + 1)); \
        HS(st, 2); \
        SUB(ky0, ky1, kx0, kx1, vy0, vy1, vx0, vx1, VP(st) + 6144, KP((st) + 1) + 2048); \
        HS(st, 3); } while (0)
      STAGE(0, 5, ); STAGE(1, 10, ); STAGE(2, 11, );
      STAGE(3, 5, if (w == 0 && lane == 0) set_flag(P.flags + FH_OFF + cg * 8 + rt, tok));
      STAGE(4, 5, );
      STAGE(5, 5, if (g == 1) glds4c(hflag, lds0 + AT_HF + w * 256));
      STAGE(6, 0, if (g == 1) {
                    const unsigned hf = *(volatile LAS unsigned*)((LAS char*)smem + AT_HF + w * 256 + lane * 4);
                    if (__all(hf == ((lane & 1) ? (unsigned)(tok >> 32) : (unsigned)tok))) {
                      G2_OFFS(lane); G2_DMA(0, 0); c_issued = 2; } });
      STAGE(7, 0, if (c_issued == 2) { G2_OFFS(lane); G2_DMA(1, 1); c_issued = 3; });
#undef STAGE
#undef HS
#undef KP
#undef VP
      oB = MFMA32(vy0, BF(pB0), oB); lB = MFMA16(sel, BF(pB0), lB);
      oB = MFMA32(vy1, BF(pB1), oB); lB = MFMA16(sel, BF(pB1), lB);
#undef SUB
#undef SLOT
#undef E4
    }
    STAMP(2, 2);
    LAS u32x4* xw = (LAS u32x4*)((LAS char*)smem + AT_EX) + w4 * 256;
    LAS float* ew = (LAS float*)((LAS char*)smem + AT_EE) + w4 * 128;
    float eA, eB;
#define FIN1(FIRST) do { \
        \
      float chk = 0.f; \
      _Pragma("unroll") for (int r = 0; r < 16; ++r) { chk = __builtin_fmaf(oA[r], 0.f, chk); chk = __builtin_fmaf(oB[r], 0.f, chk); } \
      const bool bad = !(lA[0] < 1.0e36f) || !(lB[0] < 1.0e36f) || !(chk == 0.f); \
      if (FIRST) { if (__any(bad) && lane == 0) *flag = 1u; \
                   if (c_issued == 3 && lane == 0) __hip_atomic_fetch_add(ctrl + 5, 1u, __ATOMIC_RELAXED, __HIP_MEMORY_SCOPE_WORKGROUP); } \
      oA *= __builtin_amdgcn_rcpf(lA[0]); oB *= __builtin_amdgcn_rcpf(lB[0]); \
      eA = mA + __builtin_amdgcn_logf(lA[0]); eB = mB + __builtin_amdgcn_logf(lB[0]);     \
      if (g == 0) { \
        _Pragma("unroll") for (int r = 0; r < 2; ++r) { \
          xw[r * 64 + lane] = (u32x4){cvtpk(oA[8 * r], oA[8 * r + 1]), cvtpk(oA[8 * r + 2], oA[8 * r + 3]), cvtpk(oA[8 * r + 4], oA[8 * r + 5]), cvtpk(oA[8 * r + 6], oA[8 * r + 7])}; \
          xw[128 + r * 64 + lane] = (u32x4){cvtpk(oB[8 * r], oB[8 * r + 1]), cvtpk(oB[8 * r + 2], oB[8 * r + 3]), cvtpk(oB[8 * r + 4], oB[8 * r + 5]), cvtpk(oB[8 * r + 6], oB[8 * r + 7])}; } \
        ew[lane] = eA; ew[64 + lane] = eB; } } while (0)
#define SAFE_UNIT(Q0, Q1, M, O, LS) do { \
            f32x16 s_ = MFMA32(k0, Q0, zero16); s_ = MFMA32(k1, Q1, s_); \
            const float mn_ = fmaxf(M, max16(s_)); const float al_ = EXP2(M - mn_); M = mn_; \
            O *= al_; LS *= al_; u32x4 p0_, p1_; \
            _Pragma("unroll") for (int r = 0; r < 4; ++r) { p0_[r] = cvtpk(EXP2(s_[2 * r] - mn_), EXP2(s_[2 * r + 1] - mn_)); \
              p1_[r] = cvtpk(EXP2(s_[8 + 2 * r] - mn_), EXP2(s_[9 + 2 * r] - mn_)); } \
            O = MFMA32(v0, BF(p0_), O); LS = MFMA16(sel, BF(p0_), LS); O = MFMA32(v1, BF(p1_), O); LS = MFMA16(sel, BF(p1_), LS); } while (0)
#define SAFE_PASS() do { \
        WAITBAR(0); \
        DMA_ST(0, kh); DMA_ST(1, kh); DMA_ST(2, kh); \
        oA = zero16; oB = zero16; lA = (f32x4){0.f, 0.f, 0.f, 0.f}; lB = lA; \
        mA = -3.0e38f; mB = -3.0e38f; \
        _Pragma("unroll 1") for (int st = 0; st < 8; ++st) { \
          if (st < 6) WAITBAR(8); else if (st == 6) WAITBAR(4); else WAITBAR(0); \
          if (st < 5) DMA_ST(st + 3, kh); \
          _Pragma("unroll 1") for (int u = 0; u < 4; ++u) { \
            bf16x8 k0, k1, v0, v1; \
            LDK(k0, k1, L + (st & 3) * AT_STAGE + koff + u * 2048); \
            LDV(v0, v1, L + (st & 3) * AT_STAGE + voffl + u * 2048); \
            SAFE_UNIT(qa0, qa1, mA, oA, lA); \
            SAFE_UNIT(qb0, qb1, mB, oB, lB); } } \
        FIN1(false); LGBAR(); } while (0)
    FIN1(true);
    STAMP(2, 3);

    STAMP(3, 0);
    {
      const int wm = w4 >> 1, wn = w4 & 1;
      const int fr = lane & 15, fq = lane >> 4, kq = ((fq & 1) << 1) | (fq >> 1), q4 = fr >> 2;
      const int aoff0 = (128 * wm + fr) * 128 + ((kq ^ (((fr >> 1) + 2) & 7)) * 16);
      const int boff0 = 32768 + (8 * kq + q4) * 256 + 8 * (lane & 3);
      const int bsw = q4 | ((fq & 1) << 2);
      lds_cptr pa[2], pb[4];
      pa[0] = L + aoff0; pa[1] = L + (aoff0 ^ 64); PIN(pa[0]); PIN(pa[1]);
#pragma unroll
      for (int n = 0; n < 4; ++n) { pb[n] = L + (boff0 + (((4 * wn + n) ^ bsw) * 32)); PIN(pb[n]); }
      bf16x8 fa0[8], fb0[4], fa1[8], fb1[4];
#define RD(i, KS, FA, FB) do { if ((i) < 4) FB[(i) & 3] = cat8(lds_tr(pb[(i) & 3] + (KS) * 8192), lds_tr(pb[(i) & 3] + (KS) * 8192 + 1024)); \
      else FA[((i) - 4) & 7] = lds_rd128(pa[KS] + (((i) - 4) & 7) * 2048); } while (0)
#define RD12(KS, FA, FB) do { RD(0, KS, FA, FB); RD(1, KS, FA, FB); RD(2, KS, FA, FB); RD(3, KS, FA, FB); RD(4, KS, FA, FB); RD(5, KS, FA, FB); \
      RD(6, KS, FA, FB); RD(7, KS, FA, FB); RD(8, KS, FA, FB); RD(9, KS, FA, FB); RD(10, KS, FA, FB); RD(11, KS, FA, FB); } while (0)
      if (g == 0) RD12(0, fa0, fb0);
      LGBAR();
      const bool ovf = *flag != 0u, fast = !ovf && *(volatile LAS unsigned*)(ctrl + 5) == 4u;
      if (!fast) {
        if (ovf) SAFE_PASS();
        if (g == 1) {
          G2_OFFS(lane);
          wait_flags(P.flags + FH_OFF + (8 * k4 + (lane & 7)) * 8 + rt2, tok);
          G2_DMA(0, 0); G2_DMA(1, 1);
        }
        VMBAR(12);
        if (g == 0) RD12(0, fa0, fb0);
        LGBAR();
      }
#undef RD12
      if (g == 1) {
#define FIN2(O, E, BL) do { \
      const float e2 = ew[(BL) * 64 + lane]; const u32x4 t0 = xw[(BL) * 128 + lane], t1 = xw[(BL) * 128 + 64 + lane]; \
      const float em = fmaxf(E, e2); float w1 = EXP2(E - em), w2 = EXP2(e2 - em); \
      const float rs = __builtin_amdgcn_rcpf(w1 + w2); w1 *= rs; w2 *= rs; u32x2 pk[4]; \
      _Pragma("unroll") for (int r = 0; r < 4; ++r) { \
        const unsigned ta = r < 2 ? t0[2 * r] : t1[2 * r - 4], tb2 = r < 2 ? t0[2 * r + 1] : t1[2 * r - 3]; \
        pk[r].x = cvtpk(O[4 * r] * w1 + bf_lo(ta) * w2, O[4 * r + 1] * w1 + bf_hi(ta) * w2); \
        pk[r].y = cvtpk(O[4 * r + 2] * w1 + bf_lo(tb2) * w2, O[4 * r + 3] * w1 + bf_hi(tb2) * w2); } \
      bf16_t* op = P.AH + (size_t)(qb * 256 + qg * 64 + (BL) * 32 + (lane & 31)) * KA + h * 32 + 8 * hi; \
      _Pragma("unroll") for (int k = 0; k < 4; k += 2) { \
        auto rx = __builtin_amdgcn_permlane32_swap(pk[k].x, pk[k + 1].x, false, false); \
        auto ry = __builtin_amdgcn_permlane32_swap(pk[k].y, pk[k + 1].y, false, false); \
        st16wt(op + 8 * k, (u32x4){rx[0], ry[0], rx[1], ry[1]}); } } while (0)
      __builtin_amdgcn_s_setprio(3);
      { G2_OFFS(lane); (void)bvo; G2_DMA_A(2, 2); }
      FIN2(oA, eA, 0); FIN2(oB, eB, 1);
#undef FIN2
      }
      SB();
      if (g == 1) {
      int tl = threadIdx.x; asm volatile("" : "+v"(tl));
      const int ll = tl & 63;
      G2_OFFS(ll);
      WAITBAR(12);
      G2_DMA_B(2, 2); G2_DMA(3, 0); VMBAR(12);
      if (w == 4 && ll == 0) set_flag(P.flags + FB_OFF + qb * 32 + h, tok);
      int sl = 1;
#pragma unroll 1
      for (int i = 2; i < G2_NCH - 2; ++i) {
        const u64* of = P.flags + FB_OFF + rt2 * 32 + 8 * k4;
        if (i == 10) glds4c((const unsigned*)(of + ((ll >> 1) & 7)) + (ll & 1), lds0 + AT_HF + w * 256);
        if (i == 12) {
          const unsigned hf = *(volatile LAS unsigned*)((LAS char*)smem + AT_HF + w * 256 + ll * 4);
          if (!__all(hf == ((ll & 1) ? (unsigned)(tok >> 32) : (unsigned)tok))) wait_flags(of + (ll & 7), tok);
        }
        G2_DMA(i + 2, sl); sl = sl == 2 ? 0 : sl + 1; VMBAR(12);
      }
      VMBAR(0);
      if (bid == 0 && w == 4) {
#pragma unroll
        for (int i = 0; i < 4; ++i) wait_flags(P.flags + FA_OFF + i * 64 + ll, tok);
        if (ll == 0) set_flag(P.flags, tok);
      }
      STAMP(3, 3);
      __builtin_amdgcn_endpgm();
      }
      f32x4 acc[8][4];
#pragma unroll
      for (int m = 0; m < 8; ++m)
#pragma unroll
        for (int n = 0; n < 4; ++n) { acc[m][n] = (f32x4){0.f, 0.f, 0.f, 0.f}; PIN(acc[m][n]); }
      asm volatile("s_nop 1" ::: "memory");
      STAMP(3, 1);
#define MM(i, FA, FB) MFMA16A(acc[(i) >> 2][(i) & 3], FB[(i) & 3], FA[(i) >> 2])
#define MR(i, KS, CA, CB, NA, NB) do { MM(i, CA, CB); RD(i, KS, NA, NB); SB(); } while (0)
#define MR4(i, KS, CA, CB, NA, NB) do { MR(i, KS, CA, CB, NA, NB); MR(i + 1, KS, CA, CB, NA, NB); MR(i + 2, KS, CA, CB, NA, NB); MR(i + 3, KS, CA, CB, NA, NB); } while (0)
#define MM4(i, CA, CB) do { MM(i, CA, CB); MM(i + 1, CA, CB); MM(i + 2, CA, CB); MM(i + 3, CA, CB); } while (0)
#define HALF(KS, CA, CB, NA, NB, END) do { \
      MR4(0, KS, CA, CB, NA, NB); MR4(4, KS, CA, CB, NA, NB); MR4(8, KS, CA, CB, NA, NB); \
      MM4(12, CA, CB); MM4(16, CA, CB); MM4(20, CA, CB); MM4(24, CA, CB); MM(28, CA, CB); MM(29, CA, CB); \
      END; MM(30, CA, CB); MM(31, CA, CB); SB(); } while (0)
#define LGW() do { SB(); asm volatile("s_waitcnt lgkmcnt(0)" ::: "memory"); SB(); } while (0)
      HALF(1, fa0, fb0, fa1, fb1, LGBAR());
      int sl = 0;
#pragma unroll 1
      for (int i = 1; i < G2_NCH; ++i) {
        const int dl = sl == 2 ? -2 * G2_SLOT : G2_SLOT; sl = sl == 2 ? 0 : sl + 1;
        pa[0] += dl; pa[1] += dl; pb[0] += dl; pb[1] += dl; pb[2] += dl; pb[3] += dl;
        PIN(pa[0]); PIN(pa[1]); PIN(pb[0]); PIN(pb[1]); PIN(pb[2]); PIN(pb[3]); SB();
        HALF(0, fa1, fb1, fa0, fb0, LGW());
        HALF(1, fa0, fb0, fa1, fb1, LGW(); if (i < G2_NCH - 1) BARRIER());
      }
      MM4(0, fa1, fb1); MM4(4, fa1, fb1); MM4(8, fa1, fb1); MM4(12, fa1, fb1); MM4(16, fa1, fb1); MM4(20, fa1, fb1); MM4(24, fa1, fb1); MM4(28, fa1, fb1);
#undef HALF
#undef LGW
#undef MR4
#undef MM4
#undef MR
#undef MM
#undef RD
      asm volatile("s_nop 7\n\ts_nop 7" ::: "memory");
      SB();
      STAMP(3, 2);
      int te = threadIdx.x; asm volatile("" : "+v"(te));
      const int le = te & 63;
      unsigned* mine = (unsigned*)P.part + (size_t)bid * 16384 + w4 * 1024 + le * 4;
      f32x4 keep[2][4];
#define PKP(A, B) (u32x4){cvtpk(A[0], A[1]), cvtpk(A[2], A[3]), cvtpk(B[0], B[1]), cvtpk(B[2], B[3])}
#pragma unroll
      for (int q = 0; q < 4; ++q) {
        if (q != k4) {
#pragma unroll
          for (int i = 0; i < 4; ++i) st16wt(mine + q * 4096 + i * 256, PKP(acc[2 * q + (i >> 1)][2 * (i & 1)], acc[2 * q + (i >> 1)][2 * (i & 1) + 1]));
        } else {
#pragma unroll
          for (int i = 0; i < 8; ++i) keep[i >> 2][i & 3] = acc[2 * q + (i >> 2)][i & 3];
        }
      }
#undef PKP
      asm volatile("s_waitcnt vmcnt(0)" ::: "memory");
      if (le == 0) set_flag(P.flags + FX_OFF + bid * 4 + w4, tok);
      const int orow = rt2 * 256 + 128 * wm + 32 * k4 + (le & 15), ocol = ct2 * 128 + 64 * wn + 4 * (le >> 4);
      u32x2 xv[2][4];
#pragma unroll
      for (int mm = 0; mm < 2; ++mm)
#pragma unroll
        for (int n = 0; n < 4; ++n) xv[mm][n] = *(const u32x2*)(P.x + (size_t)(orow + 16 * mm) * DM + ocol + 16 * n);
      wait_flags(P.flags + FX_OFF + (bid ^ (1 + le % 3)) * 4 + w4, tok);
      STAMP(3, 4);
      u32x4 pv[3][4];
      const unsigned* t1 = (const unsigned*)P.part + (size_t)(bid ^ 1) * 16384 + k4 * 4096 + w4 * 1024 + le * 4;
      const unsigned* t2 = (const unsigned*)P.part + (size_t)(bid ^ 2) * 16384 + k4 * 4096 + w4 * 1024 + le * 4;
      const unsigned* t3 = (const unsigned*)P.part + (size_t)(bid ^ 3) * 16384 + k4 * 4096 + w4 * 1024 + le * 4;
      asm volatile("global_load_dwordx4 %0, %12, off sc1\n\tglobal_load_dwordx4 %1, %12, off offset:1024 sc1\n\t"
                   "global_load_dwordx4 %2, %12, off offset:2048 sc1\n\tglobal_load_dwordx4 %3, %12, off offset:3072 sc1\n\t"
                   "global_load_dwordx4 %4, %13, off sc1\n\tglobal_load_dwordx4 %5, %13, off offset:1024 sc1\n\t"
                   "global_load_dwordx4 %6, %13, off offset:2048 sc1\n\tglobal_load_dwordx4 %7, %13, off offset:3072 sc1\n\t"
                   "global_load_dwordx4 %8, %14, off sc1\n\tglobal_load_dwordx4 %9, %14, off offset:1024 sc1\n\t"
                   "global_load_dwordx4 %10, %14, off offset:2048 sc1\n\tglobal_load_dwordx4 %11, %14, off offset:3072 sc1\n\t"
                   "s_waitcnt vmcnt(0)"
                   : "=&v"(pv[0][0]), "=&v"(pv[0][1]), "=&v"(pv[0][2]), "=&v"(pv[0][3]), "=&v"(pv[1][0]), "=&v"(pv[1][1]), "=&v"(pv[1][2]), "=&v"(pv[1][3]),
                     "=&v"(pv[2][0]), "=&v"(pv[2][1]), "=&v"(pv[2][2]), "=&v"(pv[2][3]) : "v"(t1), "v"(t2), "v"(t3) : "memory");
#pragma unroll
      for (int i = 0; i < 8; ++i) {
        f32x4 v = keep[i >> 2][i & 3];
#pragma unroll
        for (int q = 0; q < 3; ++q) {
          const unsigned p0 = pv[q][i >> 1][2 * (i & 1)], p1 = pv[q][i >> 1][2 * (i & 1) + 1];
          v += (f32x4){bf_lo(p0), bf_hi(p0), bf_lo(p1), bf_hi(p1)};
        }
        const u32x2 xx = xv[i >> 2][i & 3];
        u32x2 o = {cvtpk(bf_lo(xx.x) + v[0], bf_hi(xx.x) + v[1]), cvtpk(bf_lo(xx.y) + v[2], bf_hi(xx.y) + v[3])};
        *(u32x2*)(P.out + (size_t)(orow + 16 * (i >> 2)) * DM + ocol + 16 * (i & 3)) = o;
      }
      STAMP(3, 3);
    }
#undef LDK
#undef LDV
#undef BF
#undef FIN1
#undef SAFE_UNIT
#undef SAFE_PASS
  }
#undef G2_DMA
#undef G2_DMA_A
#undef G2_DMA_B
#undef G2_K
#undef G2_OFFS
#undef HSTORE
#undef DMA_ST
}

extern "C" void kernel_launch(void* const* d_in, const int* in_sizes, int n_in, void* d_out, int out_size, void* d_ws,
                              size_t ws_size, hipStream_t stream) {
  char* ws = (char*)d_ws;
  Params P;
  P.x = (const bf16_t*)d_in[0];
  P.wqkv = (const bf16_t*)d_in[1]; P.wo = (const bf16_t*)d_in[2];
  P.wg = (const bf16_t*)d_in[4]; P.wu = (const bf16_t*)d_in[5]; P.wd = (const bf16_t*)d_in[6];
  P.xn = (bf16_t*)(ws + WS_XN);
  P.Qb = (bf16_t*)(ws + WS_Q); P.Kb = (bf16_t*)(ws + WS_K); P.Vb = (bf16_t*)(ws + WS_V);
  P.AH = (bf16_t*)(ws + WS_AH); P.out = (bf16_t*)d_out;
  P.part = (float*)(ws + WS_PART); P.flags = (u64*)(ws + WS_FLAGS);
  (void)hipFuncSetAttribute((const void*)fused_kernel, hipFuncAttributeMaxDynamicSharedMemorySize, LDS_TOTAL);
  fused_kernel<<<256, 512, LDS_TOTAL, stream>>>(P);
}
```

```cpp
#include <hip/hip_runtime.h>
#include <hip/hip_bf16.h>
#include <stdint.h>

typedef unsigned short bf16_t;
typedef short bf16x8 __attribute__((ext_vector_type(8)));
typedef short s16x4 __attribute__((ext_vector_type(4)));
typedef float f32x4 __attribute__((ext_vector_type(4)));
typedef float f32x16 __attribute__((ext_vector_type(16)));
typedef float f32x2 __attribute__((ext_vector_type(2)));
typedef __bf16 bf16x2v __attribute__((ext_vector_type(2)));
typedef unsigned u32x4 __attribute__((ext_vector_type(4)));
typedef unsigned u32x2 __attribute__((ext_vector_type(2)));
#define LAS __attribute__((address_space(3)))
typedef LAS const char* lds_cptr;

constexpr int SEQ = 2048, DM = 1024, FF = 3584, NQKV = 3072;
constexpr int KA = 4608 + 64;
constexpr int HSTR = SEQ * 32 + 128;

constexpr size_t WS_FLAGS = 0;
constexpr size_t WS_XN = 1u << 20;
constexpr size_t WS_Q = WS_XN + (size_t)SEQ * DM * 2;
constexpr size_t WS_K = WS_Q + (size_t)32 * HSTR * 2;
constexpr size_t WS_V = WS_K + (size_t)32 * HSTR * 2;
constexpr size_t WS_AH = WS_V + (size_t)32 * HSTR * 2;
constexpr size_t WS_PART = WS_AH + (size_t)SEQ * KA * 2;
typedef unsigned long long u64;
constexpr int FL_OFF = 256, FA_OFF = 512, FH_OFF = 768, FB_OFF = 1024, FX_OFF = 1280;

#define SB() __builtin_amdgcn_sched_barrier(0)
#define STAMP(kid, i) do {} while (0)

__device__ __forceinline__ unsigned cvtpk(float lo, float hi) {
  f32x2 v = {lo, hi};
  bf16x2v b = __builtin_convertvector(v, bf16x2v);
  return __builtin_bit_cast(unsigned, b);
}
__device__ __forceinline__ float bf_lo(unsigned u) { return __uint_as_float(u << 16); }
__device__ __forceinline__ float bf_hi(unsigned u) { return __uint_as_float(u & 0xffff0000u); }
__device__ __forceinline__ float rbf(float x) { return bf_lo(cvtpk(x, x)); }

__device__ __forceinline__ void glds16(const void* g, unsigned lds_dst) {
  unsigned keep;
  asm volatile("s_mov_b32 %0, m0\n\ts_mov_b32 m0, %2\n\ts_nop 0\n\tglobal_load_lds_dwordx4 %1, off\n\ts_mov_b32 m0, %0"
               : "=&s"(keep) : "v"(g), "s"(lds_dst) : "memory");
}
__device__ __forceinline__ void glds16s(const void* sbase, unsigned voff, unsigned lds_dst) {
  unsigned keep;
  asm volatile("s_mov_b32 %0, m0\n\ts_mov_b32 m0, %3\n\ts_nop 0\n\tglobal_load_lds_dwordx4 %1, %2\n\ts_mov_b32 m0, %0"
               : "=&s"(keep) : "v"(voff), "s"(sbase), "s"(lds_dst) : "memory");
}
__device__ __forceinline__ void glds16c(const void* sbase, unsigned voff, unsigned lds_dst) {
  unsigned keep;
  asm volatile("s_mov_b32 %0, m0\n\ts_mov_b32 m0, %3\n\ts_nop 0\n\tglobal_load_lds_dwordx4 %1, %2 sc1\n\ts_mov_b32 m0, %0"
               : "=&s"(keep) : "v"(voff), "s"(sbase), "s"(lds_dst) : "memory");
}
__device__ __forceinline__ void glds4c(const void* g, unsigned lds_dst) {
  unsigned keep;
  asm volatile("s_mov_b32 %0, m0\n\ts_mov_b32 m0, %2\n\ts_nop 0\n\tglobal_load_lds_dword %1, off sc1\n\ts_mov_b32 m0, %0"
               : "=&s"(keep) : "v"(g), "s"(lds_dst) : "memory");
}
__device__ __forceinline__ bf16x8 lds_rd128(lds_cptr p) { return *(const LAS bf16x8*)p; }
__device__ __forceinline__ s16x4 lds_tr(lds_cptr p) { return __builtin_amdgcn_ds_read_tr16_b64_v4i16((LAS s16x4*)p); }
__device__ __forceinline__ bf16x8 cat8(s16x4 a, s16x4 b) { return __builtin_shufflevector(a, b, 0, 1, 2, 3, 4, 5, 6, 7); }
__device__ __forceinline__ void st16wt(void* p, u32x4 v) {
  asm volatile("global_store_dwordx4 %0, %1, off sc1\n\ts_nop 1" :: "v"(p), "v"(v) : "memory");
}
__device__ __forceinline__ void wait_flags(const u64* f, u64 tok) {
  unsigned spins = 0;
  while (!__all(__hip_atomic_load(f, __ATOMIC_RELAXED, __HIP_MEMORY_SCOPE_AGENT) == tok)) {
    __builtin_amdgcn_s_sleep(3);
    if (++spins > (1u << 20)) break;
  }
  asm volatile("" ::: "memory");
}
__device__ __forceinline__ void set_flag(u64* f, u64 tok) { __hip_atomic_store(f, tok, __ATOMIC_RELAXED, __HIP_MEMORY_SCOPE_AGENT); }
__device__ __forceinline__ void publish(LAS unsigned* cnt, u64* flag, u64 tok, int lane) {
  asm volatile("s_waitcnt vmcnt(0)" ::: "memory");
  if (lane == 0) {
    const unsigned old = __hip_atomic_fetch_add(cnt, 1u, __ATOMIC_RELAXED, __HIP_MEMORY_SCOPE_WORKGROUP);
    if (old == 7u) set_flag(flag, tok);
  }
}
__device__ __forceinline__ void wg_wait(int w, int lane, const u64* f, u64 tok, volatile LAS unsigned* go) {
  if (w == 0) { wait_flags(f, tok); if (lane == 0) *go = 1u; }
  else { while (*go == 0u) __builtin_amdgcn_s_sleep(1); asm volatile("" ::: "memory"); }
}

#define MFMA16(a, b, c) __builtin_amdgcn_mfma_f32_16x16x32_bf16(a, b, c, 0, 0, 0)
#define MFMA32(a, b, c) __builtin_amdgcn_mfma_f32_32x32x16_bf16(a, b, c, 0, 0, 0)
#define WAITBAR(N) do { SB(); asm volatile("s_waitcnt vmcnt(" #N ") lgkmcnt(0)\n\ts_barrier" ::: "memory"); SB(); } while (0)
#define VMBAR(N) do { SB(); asm volatile("s_waitcnt vmcnt(" #N ")\n\ts_barrier" ::: "memory"); SB(); } while (0)
#define LGBAR() do { SB(); asm volatile("s_waitcnt lgkmcnt(0)\n\ts_barrier" ::: "memory"); SB(); } while (0)
#define BARRIER() do { SB(); asm volatile("s_barrier" ::: "memory"); SB(); } while (0)
#define MFMA16A(acc, a, b) asm volatile("v_mfma_f32_16x16x32_bf16 %0, %1, %2, %0" : "+v"(acc) : "v"(a), "v"(b))
#define MFMA16G(acc, a, b) MFMA16A(acc, a, b)
#define PINACC(x) asm volatile("" : "+v"(x))
#define ADV64(p, inc) asm volatile("v_mad_u64_u32 %0, vcc, %1, 1, %0" : "+v"(p) : "v"(inc) : "vcc")
#define PIN(x) asm volatile("" : "+v"(x))
#define EXP2(x) __builtin_amdgcn_exp2f(x)

constexpr int G1_AOFF = 0, G1_BOFF = 65536, G1_BREG = 10304, G1_BSLOT = 2 * G1_BREG;
constexpr int AT_STAGE = 32768, AT_Q = 98304, AT_EX = 131072, AT_EE = AT_EX + 16384, AT_HF = 150528;
constexpr int G2_SLOT = 49152, G2_NCH = 18;
constexpr int LDS_CTRL = 150016, LDS_SINK = 152576, LDS_TOTAL = LDS_SINK + 1024;

__device__ __forceinline__ float wave_sum(float v) {
#define ROR(n) v += __uint_as_float(__builtin_amdgcn_update_dpp(0u, __float_as_uint(v), 0x120 + (n), 0xf, 0xf, false))
  ROR(8); ROR(4); ROR(2); ROR(1);
#undef ROR
  auto a = __builtin_amdgcn_permlane16_swap(__float_as_uint(v), __float_as_uint(v), false, false);
  v = __uint_as_float(a[0]) + __uint_as_float(a[1]);
  auto b = __builtin_amdgcn_permlane32_swap(__float_as_uint(v), __float_as_uint(v), false, false);
  return __uint_as_float(b[0]) + __uint_as_float(b[1]);
}
__device__ __forceinline__ float max16(const f32x16& p) {
  float a = fmaxf(fmaxf(p[0], p[1]), p[2]), b = fmaxf(fmaxf(p[3], p[4]), p[5]);
  a = fmaxf(fmaxf(a, p[6]), p[7]); b = fmaxf(fmaxf(b, p[8]), p[9]);
  a = fmaxf(fmaxf(a, p[10]), p[11]); b = fmaxf(fmaxf(b, p[12]), p[13]);
  a = fmaxf(fmaxf(a, p[14]), p[15]);
  float m = fmaxf(a, b);
  auto rr = __builtin_amdgcn_permlane32_swap(__float_as_uint(m), __float_as_uint(m), false, false);
  return fmaxf(__uint_as_float(rr[0]), __uint_as_float(rr[1]));
}
__device__ __forceinline__ f32x16 splat16(float v) { f32x16 r;
#pragma unroll
  for (int i = 0; i < 16; ++i) r[i] = v;
  return r; }

struct Params {
  const bf16_t *x, *wqkv, *wg, *wu, *wo, *wd;
  bf16_t *xn, *Qb, *Kb, *Vb, *AH, *out;
  float* part; u64* flags;
};

__global__ __launch_bounds__(512, 2) void fused_kernel(Params P) {
  extern __shared__ __attribute__((aligned(16))) char smem[];
  const int w = __builtin_amdgcn_readfirstlane(threadIdx.x >> 6);
  const int g = w >> 2, w4 = w & 3;
  const int bid = blockIdx.x, xcd = bid & 7, jj = bid >> 3;
  const int cg = xcd * 4 + (jj & 3), rt = jj >> 2;
  const unsigned lds0 = (unsigned)(uintptr_t)smem;
  const lds_cptr L = (lds_cptr)smem;
  LAS unsigned* ctrl = (LAS unsigned*)((LAS char*)smem + LDS_CTRL);
  u64 tok;

  u32x4 hv[8];
  bf16_t* hp0; bf16_t* hp1;
#define HSTORE(i) st16wt((i) < 4 ? hp0 + (size_t)(i) * 16 * KA : hp1 + ((w4 & 1) ? (size_t)((i) & 1) * 32 * KA : (size_t)(((i) - 2) & 3) * 16 * KA), hv[i])
  const int h = xcd * 4 + (jj >> 3), qb = jj & 7;
  const char* kg = (const char*)(P.Kb + (size_t)h * HSTR) + w4 * 2048;
  const char* vg = (const char*)(P.Vb + (size_t)h * HSTR) + w4 * 2048;
#define DMA_ST(st, KH) do { const unsigned d_ = lds0 + ((st) & 3) * AT_STAGE + (KH) * 8192 + w4 * 2048; const int o_ = (KH) * 65536 + (st) * 8192; \
    glds16c(kg + o_, voff, d_); glds16c(kg + o_ + 1024, voff, d_ + 1024); \
    glds16c(vg + o_, voff, d_ + 16384); glds16c(vg + o_ + 1024, voff, d_ + 16384 + 1024); } while (0)

  {
    const int lane = threadIdx.x & 63;
    const int wm = 2 * g + (w4 >> 1), wn = w4 & 1;
    STAMP(1, 0);
    const int row0 = rt * 256;
    u32x4 xa, xb; u64 ep;
    asm volatile("global_load_dwordx4 %0, %3, off\n\tglobal_load_dwordx4 %1, %3, off offset:1024\n\tglobal_load_dwordx2 %2, %4, off sc1"
                 : "=&v"(xa), "=&v"(xb), "=&v"(ep) : "v"(P.x + (size_t)(bid * 8 + w) * DM + lane * 8), "v"(P.flags) : "memory");
    if (threadIdx.x < 8) ctrl[threadIdx.x] = 0u;
    asm volatile("s_waitcnt vmcnt(0) lgkmcnt(0)\n\ts_barrier" : "+v"(xa), "+v"(xb), "+v"(ep) :: "memory");
    STAMP(0, 0);
    tok = (((u64)(unsigned)__builtin_amdgcn_readfirstlane((unsigned)(ep >> 32)) << 32) | (unsigned)__builtin_amdgcn_readfirstlane((unsigned)ep)) + 1ull;
    {
      float v[16];
#pragma unroll
      for (int i = 0; i < 4; ++i) { v[2 * i] = bf_lo(xa[i]); v[2 * i + 1] = bf_hi(xa[i]); v[8 + 2 * i] = bf_lo(xb[i]); v[9 + 2 * i] = bf_hi(xb[i]); }
      float sm = 0.f;
#pragma unroll
      for (int i = 0; i < 16; ++i) sm += v[i];
      const float mean = wave_sum(sm) * (1.f / DM);
      float q = 0.f;
#pragma unroll
      for (int i = 0; i < 16; ++i) { const float d = v[i] - mean; q += d * d; }
      const float mb = rbf(mean), vb = rbf(wave_sum(q) * (1.f / DM));
      const float inv = 1.0f / rbf(sqrtf(rbf(vb + 1e-5f)));
      u32x4 oa, ob;
#pragma unroll
      for (int i = 0; i < 4; ++i) {
        const unsigned da = cvtpk(v[2 * i] - mb, v[2 * i + 1] - mb), db = cvtpk(v[8 + 2 * i] - mb, v[9 + 2 * i] - mb);
        oa[i] = cvtpk(bf_lo(da) * inv, bf_hi(da) * inv);
        ob[i] = cvtpk(bf_lo(db) * inv, bf_hi(db) * inv);
      }
      bf16_t* op = P.xn + (size_t)(bid * 8 + w) * DM + lane * 8;
      st16wt(op, oa); st16wt(op + 512, ob);
      STAMP(0, 4);
      publish(ctrl + 0, P.flags + FL_OFF + bid, tok, lane);
      STAMP(0, 1);
    }
    if (w >= 5) {
      const char* ta;
      if (w == 5) ta = (const char*)(P.AH + (size_t)(lane * 32 + 31) * KA + 4608);
      else if (w == 6) ta = lane < 48 ? (const char*)(P.Qb + (size_t)(2 * lane + 1) * HSTR - 64)
                                      : (const char*)P.part + (size_t)((lane - 48) * 16 + xcd) * 65536 + (xcd & 3) * 16384;
      else ta = lane < 8 ? (const char*)P.wo + lane * 262144 : (lane < 36 ? (const char*)P.wd + (lane - 8) * 262144
                         : (lane < 52 ? (const char*)P.out + (lane - 36) * 262144 : (const char*)P.x + (lane - 52) * 262144));
      glds4c(ta, lds0 + LDS_SINK);
    }
    unsigned avoff0 = (unsigned)((lane >> 3) * 2048 + (((lane & 7) ^ ((lane >> 4) + 2)) * 16)), avoff1 = avoff0 ^ 64u;
    const char* abase = (const char*)P.xn + (size_t)(row0 + 32 * w4) * 2048;
#define DMA_A(hh, t) do { _Pragma("unroll") for (int _i = 0; _i < 4; ++_i) \
    glds16c(abase + (size_t)(hh) * 262144 + _i * 16384 + (t) * 128, (_i & 1) ? avoff1 : avoff0, \
            lds0 + G1_AOFF + ((t) & 1) * 32768 + (hh) * 16384 + (4 * w4 + _i) * 1024); } while (0)
    const char* bptr[4]; unsigned bld[4]; unsigned bdst[4];
#pragma unroll
    for (int i = 0; i < 4; ++i) {
      const int b = (i == 0) ? (g ? 16 + w4 : 4 * w4) : 4 * w4 + i;
      const int region = b >= 10 ? 1 : 0, bi = b - 10 * region;
      const int Pc = bi * 64 + lane;
      const int k = Pc / 20, pc = Pc - 20 * k, f = pc >> 1, e8 = (pc & 1) * 8;
      const bool isq = f >= 8 || (region && (f & 3) == 3);
      const int qf = f >= 8 ? (region ? f - 4 : f - 8) : 2 + (f >> 2);
      const bf16_t* mat = isq ? P.wqkv : (f >= 4 ? P.wu : P.wg);
      const int ld = isq ? NQKV : FF;
      const int col = e8 + (isq ? 96 * cg + 16 * qf : 112 * cg + 64 * region + 16 * (f & 3));
      bptr[i] = (const char*)(mat + (size_t)k * ld + col);
      bld[i] = (unsigned)ld * 64u;
      bdst[i] = lds0 + G1_BOFF + region * G1_BREG + bi * 1024 + (bi >= 5 ? 32 : 0);
    }
#define DMA_B(i, s) glds16(bptr[i], bdst[i] + ((s) & 3) * G1_BSLOT)
#define ADV_B() do { ADV64(bptr[0], bld[0]); ADV64(bptr[1], bld[1]); ADV64(bptr[2], bld[2]); ADV64(bptr[3], bld[3]); } while (0)
    if (g == 0) {
      DMA_B(0, 0); DMA_B(1, 0); DMA_B(2, 0); DMA_B(3, 0); ADV_B();
      DMA_B(0, 1); ADV_B();
      DMA_B(0, 2); DMA_B(1, 2); DMA_B(2, 2); DMA_B(3, 2); ADV_B();
    } else {
      DMA_B(0, 0); ADV_B();
      DMA_B(0, 1); DMA_B(1, 1); DMA_B(2, 1); DMA_B(3, 1); ADV_B();
      DMA_B(0, 2); ADV_B();
    }
    const int fr = lane & 15, fq = lane >> 4, kq = ((fq & 1) << 1) | (fq >> 1);
    const int aoff0 = G1_AOFF + (64 * wm + fr) * 128 + ((kq ^ (((fr >> 1) + 2) & 7)) * 16);
    lds_cptr pa0 = L + aoff0, pa1 = L + (aoff0 ^ 64);
    lds_cptr pb0 = L + (G1_BOFF + (8 * kq + (fr >> 2)) * 320 + (fq & 1) * 32 + 8 * (lane & 3) + G1_BREG * wn);
    lds_cptr pb1 = pb0 + 2 * G1_BSLOT;
    asm volatile("" : "+v"(pa0), "+v"(pa1), "+v"(pb0), "+v"(pb1), "+v"(avoff0), "+v"(avoff1));
    f32x4 acc[4][10];
#pragma unroll
    for (int m = 0; m < 4; ++m)
#pragma unroll
      for (int j = 0; j < 10; ++j) { acc[m][j] = (f32x4){0.f, 0.f, 0.f, 0.f}; PINACC(acc[m][j]); }
    bf16x8 af[4], bfr[10];
    wg_wait(w, lane, P.flags + FL_OFF + rt * 32 + (lane & 31), tok, ctrl + 3);
    STAMP(0, 2);
    DMA_A(g, 0);
    STAMP(0, 3);
    if (g == 0) { WAITBAR(0); } else { DMA_A(0, 1); WAITBAR(4); BARRIER(); }
    STAMP(1, 1);
#define STEP(U, ISSUE, WN, LASTBAR) do { \
    { const lds_cptr pa = ((U) & 1) ? pa1 : pa0; const lds_cptr pb = (((U) & 2) ? pb1 : pb0) + ((U) & 1) * G1_BSLOT; \
      _Pragma("unroll") for (int m = 0; m < 4; ++m) af[m] = lds_rd128(pa + ((U) >> 1) * 32768 + m * 2048); \
      _Pragma("unroll") for (int j = 0; j < 10; ++j) bfr[j] = cat8(lds_tr(pb + j * 32), lds_tr(pb + j * 32 + 1280)); } \
    if (ISSUE) { DMA_B(0, (U) + 3); \
      if (g == ((U) & 1)) { DMA_A(1 - ((U) & 1), 2 * it + ((U) >> 1) + 1 + ((U) & 1)); } \
      else { DMA_B(1, (U) + 3); DMA_B(2, (U) + 3); DMA_B(3, (U) + 3); } } \
    WAITBAR(WN); \
    _Pragma("unroll") for (int j = 0; j < 10; ++j) { _Pragma("unroll") for (int m = 0; m < 4; ++m) MFMA16G(acc[m][j], bfr[j], af[m]); \
      if ((ISSUE) && (j & 1) && j < 8) ADV64(bptr[j >> 1], bld[j >> 1]); } \
    if (LASTBAR) BARRIER(); } while (0)
    int it = 0;
#pragma unroll 1
    for (; it < 7; ++it) { STEP(0, true, 9, true); STEP(1, true, 9, true); STEP(2, true, 9, true); STEP(3, true, 9, true); }
    STEP(0, true, 9, true); STEP(1, false, 4, true); STEP(2, false, 0, true); STEP(3, false, 0, g == 0);
#undef STEP
    asm volatile("s_nop 7\n\ts_nop 7" ::: "memory");
    SB();
    STAMP(1, 2);
#undef DMA_A
#undef DMA_B
#undef ADV_B
    int tz = threadIdx.x; asm volatile("" : "+v"(tz));
    const int fr2 = tz & 15, fq2 = (tz >> 4) & 3, sel1 = fq2 & 1, c8 = (fq2 >> 1) * 8;
    const float qscale = 0.17677669529663687f * 1.4426950408889634f;
#define SWAP16(A, B, OUT) do { auto r0_ = __builtin_amdgcn_permlane16_swap(A.x, B.x, false, false); \
    auto r1_ = __builtin_amdgcn_permlane16_swap(A.y, B.y, false, false); OUT = (u32x4){r0_[0], r1_[0], r0_[1], r1_[1]}; } while (0)
#define SILU4(m, j, OUT) do { float hv_[4]; _Pragma("unroll") for (int r = 0; r < 4; ++r) { \
      const float tg_ = acc[m][j][r], tu_ = acc[m][(j) + 4][r]; \
      hv_[r] = (tg_ * tu_) * __builtin_amdgcn_rcpf(1.f + __builtin_amdgcn_exp2f(-1.4426950408889634f * tg_)); } \
    OUT = (u32x2){cvtpk(hv_[0], hv_[1]), cvtpk(hv_[2], hv_[3])}; } while (0)
#define PK4(V, SC, OUT) do { const f32x4 v_ = (V) * (SC); OUT = (u32x2){cvtpk(v_[0], v_[1]), cvtpk(v_[2], v_[3])}; } while (0)
    const int rowb = row0 + 64 * wm + fr2;
#define QKV_HEAD(hd, ja, jb) do { const int typ_ = (hd) >> 5, h_ = (hd) & 31; const float sa = typ_ == 0 ? qscale : 1.f; \
      bf16_t* dst; int mstep; \
      if (typ_ == 1) { dst = P.Kb + (size_t)h_ * HSTR + (((rowb >> 5) * 2 + sel1) * 64 + (rowb & 31) + 4 * c8) * 8; mstep = 128; } \
      else { dst = (typ_ == 0 ? P.Qb : P.Vb) + (size_t)h_ * HSTR + rowb * 32 + 16 * sel1 + c8; mstep = 512; } \
      _Pragma("unroll") for (int m = 0; m < 4; ++m) { \
        u32x2 a_, b_; u32x4 o; PK4(acc[m][ja], sa, a_); PK4(acc[m][jb], sa, b_); SWAP16(a_, b_, o); \
        st16wt(dst + (m & 1) * mstep + (m >> 1) * 1024, o); } } while (0)
    __builtin_amdgcn_s_setprio(3);
    QKV_HEAD(3 * cg + 2 * wn, 8, 9);
    if (wn) QKV_HEAD(3 * cg + 1, 3, 7);
    __builtin_amdgcn_s_setprio(0);
    SB();
    bf16_t* hd = P.AH + (size_t)rowb * KA + DM + 112 * cg + 64 * wn + c8;
    hp0 = hd + 16 * sel1;
    hp1 = wn ? hd + 32 + (size_t)sel1 * 16 * KA : hd + 32 + 16 * sel1;
#pragma unroll
    for (int m = 0; m < 3; ++m) { u32x2 a_, b_; SILU4(m, 0, a_); SILU4(m, 1, b_); SWAP16(a_, b_, hv[m]); PIN(hv[m]); }
    STAMP(1, 4);
    publish(ctrl + 1, P.flags + FA_OFF + cg * 8 + rt, tok, tz & 63);
    { u32x2 a_, b_; SILU4(3, 0, a_); SILU4(3, 1, b_); SWAP16(a_, b_, hv[3]); PIN(hv[3]); }
    if (wn == 0) {
#pragma unroll
      for (int m = 0; m < 2; ++m) { u32x2 a_, b_; SILU4(m, 2, a_); SILU4(m, 3, b_); SWAP16(a_, b_, hv[6 + m]); PIN(hv[6 + m]); }
    }
    STAMP(1, 3);
    STAMP(2, 0);
    if (g == 0) {
      const int l6 = tz & 63;
      const int cgQ = h / 3, cgK = (32 + h) / 3, cgV = (64 + h) / 3;
      wg_wait(w, l6, P.flags + FA_OFF + (l6 < 8 ? cgK * 8 + l6 : (l6 < 16 ? cgV * 8 + l6 - 8 : cgQ * 8 + qb)), tok, ctrl + 4);
      const unsigned voff = l6 * 16;
      DMA_ST(0, 0); DMA_ST(0, 1);
      const char* qsrc = (const char*)(P.Qb + (size_t)h * HSTR + (qb * 256 + w4 * 64) * 32);
#pragma unroll
      for (int i = 0; i < 4; ++i) glds16c(qsrc + i * 1024, voff, lds0 + AT_Q + w4 * 4096 + i * 1024);
    }
    STAMP(2, 4);
    if (wn == 0) {
#pragma unroll
      for (int m = 2; m < 4; ++m) { u32x2 a_, b_; SILU4(m, 2, a_); SILU4(m, 3, b_); SWAP16(a_, b_, hv[2 + m]); }
    } else {
#pragma unroll
      for (int mp = 0; mp < 2; ++mp) { u32x2 a_, b_; SILU4(2 * mp, 2, a_); SILU4(2 * mp + 1, 2, b_); SWAP16(a_, b_, hv[4 + mp]); }
      hv[6] = hv[4]; hv[7] = hv[5];
    }
#undef SWAP16
#undef SILU4
#undef PK4
#undef QKV_HEAD
  }

  const int k4 = xcd & 3, rt2 = (xcd >> 2) * 4 + (jj >> 3), ct2 = jj & 7;
  int c_issued = 0;
#define G2_K(c) ((c) < 14 ? DM + (14 * k4 + (c)) * 64 : (4 * k4 + (c) - 14) * 64)
#define G2_DMA_A(c, sl) do { \
    const char* ab_ = (const char*)P.AH + (size_t)(rt2 * 256 + 64 * w4) * (KA * 2) + G2_K(c) * 2; \
    const unsigned d_ = lds0 + (sl) * G2_SLOT + w4 * 8192; unsigned keep_; \
    asm volatile("s_mov_b32 %0, m0\n\ts_mov_b32 m0, %7\n\ts_nop 0\n\t" \
                 "global_load_lds_dwordx4 %1, %5 sc1\n\tglobal_load_lds_dwordx4 %2, %5 offset:1024 sc1\n\t" \
                 "global_load_lds_dwordx4 %3, %5 offset:2048 sc1\n\tglobal_load_lds_dwordx4 %4, %5 offset:3072 sc1\n\t" \
                 "s_mov_b32 m0, %8\n\ts_nop 0\n\t" \
                 "global_load_lds_dwordx4 %1, %6 sc1\n\tglobal_load_lds_dwordx4 %2, %6 offset:1024 sc1\n\t" \
                 "global_load_lds_dwordx4 %3, %6 offset:2048 sc1\n\tglobal_load_lds_dwordx4 %4, %6 offset:3072 sc1\n\ts_mov_b32 m0, %0" \
                 : "=&s"(keep_) : "v"(avo[0]), "v"(avo[1]), "v"(avo[2]), "v"(avo[3]), "s"(ab_), "s"(ab_ + 32 * KA * 2), "s"(d_), "s"(d_ + 4096u) : "memory"); } while (0)
#define G2_DMA_B(c, sl) do { const int kg_ = G2_K(c); \
    const char* bb_ = (kg_ < DM ? (const char*)P.wo + (size_t)kg_ * 2048 : (const char*)P.wd + (size_t)(kg_ - DM) * 2048) + ct2 * 256 + w4 * 32768; \
    const unsigned d_ = lds0 + (sl) * G2_SLOT + 32768 + w4 * 4096; unsigned keep_; \
    asm volatile("s_mov_b32 %0, m0\n\ts_mov_b32 m0, %6\n\ts_nop 0\n\t" \
                 "global_load_lds_dwordx4 %1, %5\n\tglobal_load_lds_dwordx4 %2, %5 offset:1024\n\t" \
                 "global_load_lds_dwordx4 %3, %5 offset:2048\n\tglobal_load_lds_dwordx4 %4, %5 offset:3072\n\ts_mov_b32 m0, %0" \
                 : "=&s"(keep_) : "v"(bvo[0]), "v"(bvo[1]), "v"(bvo[2]), "v"(bvo[3]), "s"(bb_), "s"(d_) : "memory"); } while (0)
#define G2_DMA(c, sl) do { G2_DMA_A(c, sl); G2_DMA_B(c, sl); } while (0)
#define G2_OFFS(lane) unsigned avo[4], bvo[4]; _Pragma("unroll") for (int _i = 0; _i < 4; ++_i) { \
    avo[_i] = ((unsigned)(((lane) >> 3) * (KA * 2) + ((((lane) & 7) ^ (((lane) >> 4) + 2)) * 16)) ^ ((_i & 1) * 64u)) + _i * (8 * KA * 2 - 1024); \
    bvo[_i] = (unsigned)(((lane) >> 4) * 2048 + ((((lane) & 15) ^ (((lane) >> 4) << 1) ^ ((w4 & 1) << 3)) * 16)) + _i * (8192 - 1024); }

  {
    int tb = threadIdx.x; asm volatile("" : "+v"(tb));
    const int lane = tb & 63;
    const int qg = w4, kh = g;
    const int hi = lane >> 5;
    const unsigned voff = lane * 16;
    const short one = (((lane >> 4) & 1) == ((lane >> 2) & 1)) ? (short)0x3f80 : (short)0;
    const bf16x8 sel = {one, one, one, one, one, one, one, one};
    volatile LAS unsigned* flag = (volatile LAS unsigned*)(ctrl + 2);
    const int gq = lane >> 4;
    const int koff = kh * 8192 + lane * 16;
    const int voffl = 16384 + kh * 8192 + (4 * (gq >> 1) + ((lane & 15) >> 2)) * 64 + (gq & 1) * 32 + (lane & 3) * 8;
    const f32x16 zero16 = splat16(0.f);
    f32x16 oA = zero16, oB = zero16;
    f32x4 lA = {0.f, 0.f, 0.f, 0.f}, lB = {0.f, 0.f, 0.f, 0.f};
    float mA, mB;
    const unsigned* hflag = (const unsigned*)(P.flags + FH_OFF + (8 * k4 + ((lane >> 1) & 7)) * 8 + rt2) + (lane & 1);
    bf16x8 qa0, qa1, qb0, qb1;

#define LDK(K0, K1, p) do { K0 = lds_rd128(p); K1 = lds_rd128((p) + 1024); } while (0)
#define LDV(V0, V1, p) do { V0 = cat8(lds_tr(p), lds_tr((p) + 512)); V1 = cat8(lds_tr((p) + 1024), lds_tr((p) + 1536)); } while (0)
#define BF(x) __builtin_bit_cast(bf16x8, x)
    {
      WAITBAR(0);
      { const lds_cptr qp = L + AT_Q + qg * 4096 + (lane & 31) * 64 + hi * 16;
        qa0 = lds_rd128(qp); qa1 = lds_rd128(qp + 32); qb0 = lds_rd128(qp + 2048); qb1 = lds_rd128(qp + 2080); }
      bf16x8 kx0, kx1, ky0, ky1, vx0, vx1, vy0, vy1;
      LDK(kx0, kx1, L + koff);
      LDK(ky0, ky1, L + koff + 2048);
      LDV(vy0, vy1, L + voffl);
      DMA_ST(1, kh); DMA_ST(2, kh);
      f32x16 sA = MFMA32(kx0, qa0, zero16); sA = MFMA32(kx1, qa1, sA);
      f32x16 sB = MFMA32(kx0, qb0, zero16); sB = MFMA32(kx1, qb1, sB);
      mA = max16(sA); mB = max16(sB);
      f32x16 nA = splat16(-mA), nB = splat16(-mB);
      PIN(nA); PIN(nB);
      sA = MFMA32(kx0, qa0, nA); sA = MFMA32(kx1, qa1, sA);
      u32x4 pA0, pA1, pB0 = {0u, 0u, 0u, 0u}, pB1 = {0u, 0u, 0u, 0u};
      STAMP(2, 1);
#define E4(S, i) do { S[i] = EXP2(S[i]); S[i + 1] = EXP2(S[i + 1]); S[i + 2] = EXP2(S[i + 2]); S[i + 3] = EXP2(S[i + 3]); } while (0)
#define SLOT(SC, PC0, PC1, SN, K0, K1, Q0, Q1, NM, PP0, PP1, V0, V1, O, LS, LOADS) do { \
    SN = MFMA32(K0, Q0, NM); SB(); E4(SC, 0); PIN(SC); SB(); \
    SN = MFMA32(K1, Q1, SN); SB(); LOADS; SB(); E4(SC, 4); PIN(SC); SB(); \
    O = MFMA32(V0, BF(PP0), O); SB(); PC0[0] = cvtpk(SC[0], SC[1]); PC0[1] = cvtpk(SC[2], SC[3]); \
      SC[8] = EXP2(SC[8]); SC[9] = EXP2(SC[9]); SC[10] = EXP2(SC[10]); PIN(SC); PIN(PC0); SB(); \
    LS = MFMA16(sel, BF(PP0), LS); SB(); PC0[2] = cvtpk(SC[4], SC[5]); PC0[3] = cvtpk(SC[6], SC[7]); SC[11] = EXP2(SC[11]); PIN(SC); PIN(PC0); SB(); \
    O = MFMA32(V1, BF(PP1), O); SB(); E4(SC, 12); PIN(SC); SB(); \
    LS = MFMA16(sel, BF(PP1), LS); SB(); PC1[0] = cvtpk(SC[8], SC[9]); PC1[1] = cvtpk(SC[10], SC[11]); \
      PC1[2] = cvtpk(SC[12], SC[13]); PC1[3] = cvtpk(SC[14], SC[15]); PIN(PC1); SB(); } while (0)
#define SUB(KC0, KC1, KN0, KN1, VC0, VC1, VP0, VP1, vpu, kp2) do { \
    SLOT(sA, pA0, pA1, sB, KC0, KC1, qb0, qb1, nB, pB0, pB1, VP0, VP1, oB, lB, LDV(VC0, VC1, vpu)); \
    SLOT(sB, pB0, pB1, sA, KN0, KN1, qa0, qa1, nA, pA0, pA1, VC0, VC1, oA, lA, LDK(KC0, KC1, kp2)); } while (0)
      lds_cptr kb0 = L + koff, kb1 = L + koff + 2 * AT_STAGE, vb0 = L + voffl, vb1 = L + voffl + 2 * AT_STAGE;
      PIN(kb0); PIN(kb1); PIN(vb0); PIN(vb1);
#define KP(st) ((((st) & 2) ? kb1 : kb0) + ((st) & 1) * AT_STAGE)
#define VP(st) ((((st) & 2) ? vb1 : vb0) + ((st) & 1) * AT_STAGE)
#define HS(st, i) do { if ((st) < 2) HSTORE((4 * (st) + (i)) & 7); } while (0)
#define STAGE(st, WN, EXTRA) do { \
        SUB(kx0, kx1, ky0, ky1, vx0, vx1, vy0, vy1, VP(st), KP(st) + 4096); \
        HS(st, 0); \
        SUB(ky0, ky1, kx0, kx1, vy0, vy1, vx0, vx1, VP(st) + 2048, KP(st) + 6144); \
        VMBAR(WN); \
        HS(st, 1); \
        EXTRA; \
        if ((st) < 5) {   \
          const int pc = (st) * 8 + w, kb = bid * 36 + (pc < 36 ? pc : 35); \
          glds16s(kb < 2048 ? (const char*)P.wo + (size_t)kb * 1024 : (const char*)P.wd + (size_t)(kb - 2048) * 1024, voff, lds0 + LDS_SINK); \
          DMA_ST((st) + 3, kh); } \
        SUB(kx0, kx1, ky0, ky1, vx0, vx1, vy0, vy1, VP(st) + 4096, KP((st) + 1)); \
        HS(st, 2); \
        SUB(ky0, ky1, kx0, kx1, vy0, vy1, vx0, vx1, VP(st) + 6144, KP((st) + 1) + 2048); \
        HS(st, 3); } while (0)
      STAGE(0, 5, ); STAGE(1, 10, ); STAGE(2, 11, );
      STAGE(3, 5, if (w == 0 && lane == 0) set_flag(P.flags + FH_OFF + cg * 8 + rt, tok));
      STAGE(4, 5, );
      STAGE(5, 5, if (g == 1) glds4c(hflag, lds0 + AT_HF + w * 256));
      STAGE(6, 0, if (g == 1) {
                    const unsigned hf = *(volatile LAS unsigned*)((LAS char*)smem + AT_HF + w * 256 + lane * 4);
                    if (__all(hf == ((lane & 1) ? (unsigned)(tok >> 32) : (unsigned)tok))) {
                      G2_OFFS(lane); G2_DMA(0, 0); c_issued = 2; } });
      STAGE(7, 0, if (c_issued == 2) { G2_OFFS(lane); G2_DMA(1, 1); c_issued = 3; });
#undef STAGE
#undef HS
#undef KP
#undef VP
      oB = MFMA32(vy0, BF(pB0), oB); lB = MFMA16(sel, BF(pB0), lB);
      oB = MFMA32(vy1, BF(pB1), oB); lB = MFMA16(sel, BF(pB1), lB);
#undef SUB
#undef SLOT
#undef E4
    }
    STAMP(2, 2);
    LAS u32x4* xw = (LAS u32x4*)((LAS char*)smem + AT_EX) + w4 * 256;
    LAS float* ew = (LAS float*)((LAS char*)smem + AT_EE) + w4 * 128;
    float eA, eB;
#define FIN1(FIRST) do { \
        \
      float chk = 0.f; \
      _Pragma("unroll") for (int r = 0; r < 16; ++r) { chk = __builtin_fmaf(oA[r], 0.f, chk); chk = __builtin_fmaf(oB[r], 0.f, chk); } \
      const bool bad = !(lA[0] < 1.0e36f) || !(lB[0] < 1.0e36f) || !(chk == 0.f); \
      if (FIRST) { if (__any(bad) && lane == 0) *flag = 1u; \
                   if (c_issued == 3 && lane == 0) __hip_atomic_fetch_add(ctrl + 5, 1u, __ATOMIC_RELAXED, __HIP_MEMORY_SCOPE_WORKGROUP); } \
      oA *= __builtin_amdgcn_rcpf(lA[0]); oB *= __builtin_amdgcn_rcpf(lB[0]); \
      eA = mA + __builtin_amdgcn_logf(lA[0]); eB = mB + __builtin_amdgcn_logf(lB[0]);     \
      if (g == 0) { \
        _Pragma("unroll") for (int r = 0; r < 2; ++r) { \
          xw[r * 64 + lane] = (u32x4){cvtpk(oA[8 * r], oA[8 * r + 1]), cvtpk(oA[8 * r + 2], oA[8 * r + 3]), cvtpk(oA[8 * r + 4], oA[8 * r + 5]), cvtpk(oA[8 * r + 6], oA[8 * r + 7])}; \
          xw[128 + r * 64 + lane] = (u32x4){cvtpk(oB[8 * r], oB[8 * r + 1]), cvtpk(oB[8 * r + 2], oB[8 * r + 3]), cvtpk(oB[8 * r + 4], oB[8 * r + 5]), cvtpk(oB[8 * r + 6], oB[8 * r + 7])}; } \
        ew[lane] = eA; ew[64 + lane] = eB; } } while (0)
#define SAFE_UNIT(Q0, Q1, M, O, LS) do { \
            f32x16 s_ = MFMA32(k0, Q0, zero16); s_ = MFMA32(k1, Q1, s_); \
            const float mn_ = fmaxf(M, max16(s_)); const float al_ = EXP2(M - mn_); M = mn_; \
            O *= al_; LS *= al_; u32x4 p0_, p1_; \
            _Pragma("unroll") for (int r = 0; r < 4; ++r) { p0_[r] = cvtpk(EXP2(s_[2 * r] - mn_), EXP2(s_[2 * r + 1] - mn_)); \
              p1_[r] = cvtpk(EXP2(s_[8 + 2 * r] - mn_), EXP2(s_[9 + 2 * r] - mn_)); } \
            O = MFMA32(v0, BF(p0_), O); LS = MFMA16(sel, BF(p0_), LS); O = MFMA32(v1, BF(p1_), O); LS = MFMA16(sel, BF(p1_), LS); } while (0)
#define SAFE_PASS() do { \
        WAITBAR(0); \
        DMA_ST(0, kh); DMA_ST(1, kh); DMA_ST(2, kh); \
        oA = zero16; oB = zero16; lA = (f32x4){0.f, 0.f, 0.f, 0.f}; lB = lA; \
        mA = -3.0e38f; mB = -3.0e38f; \
        _Pragma("unroll 1") for (int st = 0; st < 8; ++st) { \
          if (st < 6) WAITBAR(8); else if (st == 6) WAITBAR(4); else WAITBAR(0); \
          if (st < 5) DMA_ST(st + 3, kh); \
          _Pragma("unroll 1") for (int u = 0; u < 4; ++u) { \
            bf16x8 k0, k1, v0, v1; \
            LDK(k0, k1, L + (st & 3) * AT_STAGE + koff + u * 2048); \
            LDV(v0, v1, L + (st & 3) * AT_STAGE + voffl + u * 2048); \
            SAFE_UNIT(qa0, qa1, mA, oA, lA); \
            SAFE_UNIT(qb0, qb1, mB, oB, lB); } } \
        FIN1(false); LGBAR(); } while (0)
    FIN1(true);
    STAMP(2, 3);

    STAMP(3, 0);
    {
      const int wm = w4 >> 1, wn = w4 & 1;
      const int fr = lane & 15, fq = lane >> 4, kq = ((fq & 1) << 1) | (fq >> 1), q4 = fr >> 2;
      const int aoff0 = (128 * wm + fr) * 128 + ((kq ^ (((fr >> 1) + 2) & 7)) * 16);
      const int boff0 = 32768 + (8 * kq + q4) * 256 + 8 * (lane & 3);
      const int bsw = q4 | ((fq & 1) << 2);
      lds_cptr pa[2], pb[4];
      pa[0] = L + aoff0; pa[1] = L + (aoff0 ^ 64); PIN(pa[0]); PIN(pa[1]);
#pragma unroll
      for (int n = 0; n < 4; ++n) { pb[n] = L + (boff0 + (((4 * wn + n) ^ bsw) * 32)); PIN(pb[n]); }
      bf16x8 fa0[8], fb0[4], fa1[8], fb1[4];
#define RD(i, KS, FA, FB) do { if ((i) < 4) FB[(i) & 3] = cat8(lds_tr(pb[(i) & 3] + (KS) * 8192), lds_tr(pb[(i) & 3] + (KS) * 8192 + 1024)); \
      else FA[((i) - 4) & 7] = lds_rd128(pa[KS] + (((i) - 4) & 7) * 2048); } while (0)
#define RD12(KS, FA, FB) do { RD(0, KS, FA, FB); RD(1, KS, FA, FB); RD(2, KS, FA, FB); RD(3, KS, FA, FB); RD(4, KS, FA, FB); RD(5, KS, FA, FB); \
      RD(6, KS, FA, FB); RD(7, KS, FA, FB); RD(8, KS, FA, FB); RD(9, KS, FA, FB); RD(10, KS, FA, FB); RD(11, KS, FA, FB); } while (0)
      if (g == 0) RD12(0, fa0, fb0);
      LGBAR();
      const bool ovf = *flag != 0u, fast = !ovf && *(volatile LAS unsigned*)(ctrl + 5) == 4u;
      if (!fast) {
        if (ovf) SAFE_PASS();
        if (g == 1) {
          G2_OFFS(lane);
          wait_flags(P.flags + FH_OFF + (8 * k4 + (lane & 7)) * 8 + rt2, tok);
          G2_DMA(0, 0); G2_DMA(1, 1);
        }
        VMBAR(12);
        if (g == 0) RD12(0, fa0, fb0);
        LGBAR();
      }
#undef RD12
      if (g == 1) {
#define FIN2(O, E, BL) do { \
      const float e2 = ew[(BL) * 64 + lane]; const u32x4 t0 = xw[(BL) * 128 + lane], t1 = xw[(BL) * 128 + 64 + lane]; \
      const float em = fmaxf(E, e2); float w1 = EXP2(E - em), w2 = EXP2(e2 - em); \
      const float rs = __builtin_amdgcn_rcpf(w1 + w2); w1 *= rs; w2 *= rs; u32x2 pk[4]; \
      _Pragma("unroll") for (int r = 0; r < 4; ++r) { \
        const unsigned ta = r < 2 ? t0[2 * r] : t1[2 * r - 4], tb2 = r < 2 ? t0[2 * r + 1] : t1[2 * r - 3]; \
        pk[r].x = cvtpk(O[4 * r] * w1 + bf_lo(ta) * w2, O[4 * r + 1] * w1 + bf_hi(ta) * w2); \
        pk[r].y = cvtpk(O[4 * r + 2] * w1 + bf_lo(tb2) * w2, O[4 * r + 3] * w1 + bf_hi(tb2) * w2); } \
      bf16_t* op = P.AH + (size_t)(qb * 256 + qg * 64 + (BL) * 32 + (lane & 31)) * KA + h * 32 + 8 * hi; \
      _Pragma("unroll") for (int k = 0; k < 4; k += 2) { \
        auto rx = __builtin_amdgcn_permlane32_swap(pk[k].x, pk[k + 1].x, false, false); \
        auto ry = __builtin_amdgcn_permlane32_swap(pk[k].y, pk[k + 1].y, false, false); \
        st16wt(op + 8 * k, (u32x4){rx[0], ry[0], rx[1], ry[1]}); } } while (0)
      __builtin_amdgcn_s_setprio(3);
      { G2_OFFS(lane); (void)bvo; G2_DMA_A(2, 2); }
      FIN2(oA, eA, 0); FIN2(oB, eB, 1);
#undef FIN2
      }
      SB();
      if (g == 1) {
      int tl = threadIdx.x; asm volatile("" : "+v"(tl));
      const int ll = tl & 63;
      G2_OFFS(ll);
      WAITBAR(12);
      G2_DMA_B(2, 2); G2_DMA(3, 0); VMBAR(12);
      if (w == 4 && ll == 0) set_flag(P.flags + FB_OFF + qb * 32 + h, tok);
      int sl = 1;
#pragma unroll 1
      for (int i = 2; i < G2_NCH - 2; ++i) {
        const u64* of = P.flags + FB_OFF + rt2 * 32 + 8 * k4;
        if (i == 10) glds4c((const unsigned*)(of + ((ll >> 1) & 7)) + (ll & 1), lds0 + AT_HF + w * 256);
        if (i == 12) {
          const unsigned hf = *(volatile LAS unsigned*)((LAS char*)smem + AT_HF + w * 256 + ll * 4);
          if (!__all(hf == ((ll & 1) ? (unsigned)(tok >> 32) : (unsigned)tok))) wait_flags(of + (ll & 7), tok);
        }
        G2_DMA(i + 2, sl); sl = sl == 2 ? 0 : sl + 1; VMBAR(12);
      }
      VMBAR(0);
      if (bid == 0 && w == 4) {
#pragma unroll
        for (int i = 0; i < 4; ++i) wait_flags(P.flags + FA_OFF + i * 64 + ll, tok);
        if (ll == 0) set_flag(P.flags, tok);
      }
      STAMP(3, 3);
      __builtin_amdgcn_endpgm();
      }
      f32x4 acc[8][4];
#pragma unroll
      for (int m = 0; m < 8; ++m)
#pragma unroll
        for (int n = 0; n < 4; ++n) { acc[m][n] = (f32x4){0.f, 0.f, 0.f, 0.f}; PIN(acc[m][n]); }
      asm volatile("s_nop 1" ::: "memory");
      STAMP(3, 1);
#define MM(i, FA, FB) MFMA16A(acc[(i) >> 2][(i) & 3], FB[(i) & 3], FA[(i) >> 2])
#define MR(i, KS, CA, CB, NA, NB) do { MM(i, CA, CB); RD(i, KS, NA, NB); SB(); } while (0)
#define MR4(i, KS, CA, CB, NA, NB) do { MR(i, KS, CA, CB, NA, NB); MR(i + 1, KS, CA, CB, NA, NB); MR(i + 2, KS, CA, CB, NA, NB); MR(i + 3, KS, CA, CB, NA, NB); } while (0)
#define MM4(i, CA, CB) do { MM(i, CA, CB); MM(i + 1, CA, CB); MM(i + 2, CA, CB); MM(i + 3, CA, CB); } while (0)
#define HALF(KS, CA, CB, NA, NB, END) do { \
      MR4(0, KS, CA, CB, NA, NB); MR4(4, KS, CA, CB, NA, NB); MR4(8, KS, CA, CB, NA, NB); \
      MM4(12, CA, CB); MM4(16, CA, CB); MM4(20, CA, CB); MM4(24, CA, CB); MM(28, CA, CB); MM(29, CA, CB); \
      END; MM(30, CA, CB); MM(31, CA, CB); SB(); } while (0)
#define LGW() do { SB(); asm volatile("s_waitcnt lgkmcnt(0)" ::: "memory"); SB(); } while (0)
      HALF(1, fa0, fb0, fa1, fb1, LGBAR());
      int sl = 0;
#pragma unroll 1
      for (int i = 1; i < G2_NCH; ++i) {
        const int dl = sl == 2 ? -2 * G2_SLOT : G2_SLOT; sl = sl == 2 ? 0 : sl + 1;
        pa[0] += dl; pa[1] += dl; pb[0] += dl; pb[1] += dl; pb[2] += dl; pb[3] += dl;
        PIN(pa[0]); PIN(pa[1]); PIN(pb[0]); PIN(pb[1]); PIN(pb[2]); PIN(pb[3]); SB();
        HALF(0, fa1, fb1, fa0, fb0, LGW());
        HALF(1, fa0, fb0, fa1, fb1, LGW(); if (i < G2_NCH - 1) BARRIER());
      }
      MM4(0, fa1, fb1); MM4(4, fa1, fb1); MM4(8, fa1, fb1); MM4(12, fa1, fb1); MM4(16, fa1, fb1); MM4(20, fa1, fb1); MM4(24, fa1, fb1); MM4(28, fa1, fb1);
#undef HALF
#undef LGW
#undef MR4
#undef MM4
#undef MR
#undef MM
#undef RD
      asm volatile("s_nop 7\n\ts_nop 7" ::: "memory");
      SB();
      STAMP(3, 2);
      int te = threadIdx.x; asm volatile("" : "+v"(te));
      const int le = te & 63;
      unsigned* mine = (unsigned*)P.part + (size_t)bid * 16384 + w4 * 1024 + le * 4;
      f32x4 keep[2][4];
#define PKP(A, B) (u32x4){cvtpk(A[0], A[1]), cvtpk(A[2], A[3]), cvtpk(B[0], B[1]), cvtpk(B[2], B[3])}
#pragma unroll
      for (int q = 0; q < 4; ++q) {
        if (q != k4) {
#pragma unroll
          for (int i = 0; i < 4; ++i) st16wt(mine + q * 4096 + i * 256, PKP(acc[2 * q + (i >> 1)][2 * (i & 1)], acc[2 * q + (i >> 1)][2 * (i & 1) + 1]));
        } else {
#pragma unroll
          for (int i = 0; i < 8; ++i) keep[i >> 2][i & 3] = acc[2 * q + (i >> 2)][i & 3];
        }
      }
#undef PKP
      asm volatile("s_waitcnt vmcnt(0)" ::: "memory");
      if (le == 0) set_flag(P.flags + FX_OFF + bid * 4 + w4, tok);
      const int orow = rt2 * 256 + 128 * wm + 32 * k4 + (le & 15), ocol = ct2 * 128 + 64 * wn + 4 * (le >> 4);
      u32x2 xv[2][4];
#pragma unroll
      for (int mm = 0; mm < 2; ++mm)
#pragma unroll
        for (int n = 0; n < 4; ++n) xv[mm][n] = *(const u32x2*)(P.x + (size_t)(orow + 16 * mm) * DM + ocol + 16 * n);
      wait_flags(P.flags + FX_OFF + (bid ^ (1 + le % 3)) * 4 + w4, tok);
      STAMP(3, 4);
      u32x4 pv[3][4];
      const unsigned* t1 = (const unsigned*)P.part + (size_t)(bid ^ 1) * 16384 + k4 * 4096 + w4 * 1024 + le * 4;
      const unsigned* t2 = (const unsigned*)P.part + (size_t)(bid ^ 2) * 16384 + k4 * 4096 + w4 * 1024 + le * 4;
      const unsigned* t3 = (const unsigned*)P.part + (size_t)(bid ^ 3) * 16384 + k4 * 4096 + w4 * 1024 + le * 4;
      asm volatile("global_load_dwordx4 %0, %12, off sc1\n\tglobal_load_dwordx4 %1, %12, off offset:1024 sc1\n\t"
                   "global_load_dwordx4 %2, %12, off offset:2048 sc1\n\tglobal_load_dwordx4 %3, %12, off offset:3072 sc1\n\t"
                   "global_load_dwordx4 %4, %13, off sc1\n\tglobal_load_dwordx4 %5, %13, off offset:1024 sc1\n\t"
                   "global_load_dwordx4 %6, %13, off offset:2048 sc1\n\tglobal_load_dwordx4 %7, %13, off offset:3072 sc1\n\t"
                   "global_load_dwordx4 %8, %14, off sc1\n\tglobal_load_dwordx4 %9, %14, off offset:1024 sc1\n\t"
                   "global_load_dwordx4 %10, %14, off offset:2048 sc1\n\tglobal_load_dwordx4 %11, %14, off offset:3072 sc1\n\t"
                   "s_waitcnt vmcnt(0)"
                   : "=&v"(pv[0][0]), "=&v"(pv[0][1]), "=&v"(pv[0][2]), "=&v"(pv[0][3]), "=&v"(pv[1][0]), "=&v"(pv[1][1]), "=&v"(pv[1][2]), "=&v"(pv[1][3]),
                     "=&v"(pv[2][0]), "=&v"(pv[2][1]), "=&v"(pv[2][2]), "=&v"(pv[2][3]) : "v"(t1), "v"(t2), "v"(t3) : "memory");
#pragma unroll
      for (int i = 0; i < 8; ++i) {
        f32x4 v = keep[i >> 2][i & 3];
#pragma unroll
        for (int q = 0; q < 3; ++q) {
          const unsigned p0 = pv[q][i >> 1][2 * (i & 1)], p1 = pv[q][i >> 1][2 * (i & 1) + 1];
          v += (f32x4){bf_lo(p0), bf_hi(p0), bf_lo(p1), bf_hi(p1)};
        }
        const u32x2 xx = xv[i >> 2][i & 3];
        u32x2 o = {cvtpk(bf_lo(xx.x) + v[0], bf_hi(xx.x) + v[1]), cvtpk(bf_lo(xx.y) + v[2], bf_hi(xx.y) + v[3])};
        *(u32x2*)(P.out + (size_t)(orow + 16 * (i >> 2)) * DM + ocol + 16 * (i & 3)) = o;
      }
      STAMP(3, 3);
    }
#undef LDK
#undef LDV
#undef BF
#undef FIN1
#undef SAFE_UNIT
#undef SAFE_PASS
  }
#undef G2_DMA
#undef G2_DMA_A
#undef G2_DMA_B
#undef G2_K
#undef G2_OFFS
#undef HSTORE
#undef DMA_ST
}

extern "C" void kernel_launch(void* const* d_in, const int* in_sizes, int n_in, void* d_out, int out_size, void* d_ws,
                              size_t ws_size, hipStream_t stream) {
  char* ws = (char*)d_ws;
  Params P;
  P.x = (const bf16_t*)d_in[0];
  P.wqkv = (const bf16_t*)d_in[1]; P.wo = (const bf16_t*)d_in[2];
  P.wg = (const bf16_t*)d_in[4]; P.wu = (const bf16_t*)d_in[5]; P.wd = (const bf16_t*)d_in[6];
  P.xn = (bf16_t*)(ws + WS_XN);
  P.Qb = (bf16_t*)(ws + WS_Q); P.Kb = (bf16_t*)(ws + WS_K); P.Vb = (bf16_t*)(ws + WS_V);
  P.AH = (bf16_t*)(ws + WS_AH); P.out = (bf16_t*)d_out;
  P.part = (float*)(ws + WS_PART); P.flags = (u64*)(ws + WS_FLAGS);
  (void)hipFuncSetAttribute((const void*)fused_kernel, hipFuncAttributeMaxDynamicSharedMemorySize, LDS_TOTAL);
  fused_kernel<<<256, 512, LDS_TOTAL, stream>>>(P);
}
```

```cpp
#include <hip/hip_runtime.h>
#include <hip/hip_bf16.h>
#include <stdint.h>

typedef unsigned short bf16_t;
typedef short bf16x8 __attribute__((ext_vector_type(8)));
typedef short s16x4 __attribute__((ext_vector_type(4)));
typedef float f32x4 __attribute__((ext_vector_type(4)));
typedef float f32x16 __attribute__((ext_vector_type(16)));
typedef float f32x2 __attribute__((ext_vector_type(2)));
typedef __bf16 bf16x2v __attribute__((ext_vector_type(2)));
typedef unsigned u32x4 __attribute__((ext_vector_type(4)));
typedef unsigned u32x2 __attribute__((ext_vector_type(2)));
#define LAS __attribute__((address_space(3)))
typedef LAS const char* lds_cptr;

constexpr int SEQ = 2048, DM = 1024, FF = 3584, NQKV = 3072;
constexpr int KA = 4608 + 64;
constexpr int HSTR = SEQ * 32 + 128;

constexpr size_t WS_FLAGS = 0;
constexpr size_t WS_XN = 1u << 20;
constexpr size_t WS_Q = WS_XN + (size_t)SEQ * DM * 2;
constexpr size_t WS_K = WS_Q + (size_t)32 * HSTR * 2;
constexpr size_t WS_V = WS_K + (size_t)32 * HSTR * 2;
constexpr size_t WS_AH = WS_V + (size_t)32 * HSTR * 2;
constexpr size_t WS_PART = WS_AH + (size_t)SEQ * KA * 2;
typedef unsigned long long u64;
constexpr int FL_OFF = 256, FA_OFF = 512, FH_OFF = 768, FB_OFF = 1024, FX_OFF = 1280;

#define SB() __builtin_amdgcn_sched_barrier(0)
#define STAMP(kid, i) do {} while (0)

__device__ __forceinline__ unsigned cvtpk(float lo, float hi) {
  f32x2 v = {lo, hi};
  bf16x2v b = __builtin_convertvector(v, bf16x2v);
  return __builtin_bit_cast(unsigned, b);
}
__device__ __forceinline__ float bf_lo(unsigned u) { return __uint_as_float(u << 16); }
__device__ __forceinline__ float bf_hi(unsigned u) { return __uint_as_float(u & 0xffff0000u); }
__device__ __forceinline__ float rbf(float x) { return bf_lo(cvtpk(x, x)); }

__device__ __forceinline__ void glds16(const void* g, unsigned lds_dst) {
  unsigned keep;
  asm volatile("s_mov_b32 %0, m0\n\ts_mov_b32 m0, %2\n\ts_nop 0\n\tglobal_load_lds_dwordx4 %1, off\n\ts_mov_b32 m0, %0"
               : "=&s"(keep) : "v"(g), "s"(lds_dst) : "memory");
}
__device__ __forceinline__ void glds16s(const void* sbase, unsigned voff, unsigned lds_dst) {
  unsigned keep;
  asm volatile("s_mov_b32 %0, m0\n\ts_mov_b32 m0, %3\n\ts_nop 0\n\tglobal_load_lds_dwordx4 %1, %2\n\ts_mov_b32 m0, %0"
               : "=&s"(keep) : "v"(voff), "s"(sbase), "s"(lds_dst) : "memory");
}
__device__ __forceinline__ void glds16c(const void* sbase, unsigned voff, unsigned lds_dst) {
  unsigned keep;
  asm volatile("s_mov_b32 %0, m0\n\ts_mov_b32 m0, %3\n\ts_nop 0\n\tglobal_load_lds_dwordx4 %1, %2 sc1\n\ts_mov_b32 m0, %0"
               : "=&s"(keep) : "v"(voff), "s"(sbase), "s"(lds_dst) : "memory");
}
__device__ __forceinline__ void glds4c(const void* g, unsigned lds_dst) {
  unsigned keep;
  asm volatile("s_mov_b32 %0, m0\n\ts_mov_b32 m0, %2\n\ts_nop 0\n\tglobal_load_lds_dword %1, off sc1\n\ts_mov_b32 m0, %0"
               : "=&s"(keep) : "v"(g), "s"(lds_dst) : "memory");
}
__device__ __forceinline__ bf16x8 lds_rd128(lds_cptr p) { return *(const LAS bf16x8*)p; }
__device__ __forceinline__ s16x4 lds_tr(lds_cptr p) { return __builtin_amdgcn_ds_read_tr16_b64_v4i16((LAS s16x4*)p); }
__device__ __forceinline__ bf16x8 cat8(s16x4 a, s16x4 b) { return __builtin_shufflevector(a, b, 0, 1, 2, 3, 4, 5, 6, 7); }
__device__ __forceinline__ void st16wt(void* p, u32x4 v) {
  asm volatile("global_store_dwordx4 %0, %1, off sc1\n\ts_nop 1" :: "v"(p), "v"(v) : "memory");
}
__device__ __forceinline__ void wait_flags(const u64* f, u64 tok) {
  unsigned spins = 0;
  while (!__all(__hip_atomic_load(f, __ATOMIC_RELAXED, __HIP_MEMORY_SCOPE_AGENT) == tok)) {
    __builtin_amdgcn_s_sleep(3);
    if (++spins > (1u << 20)) break;
  }
  asm volatile("" ::: "memory");
}
__device__ __forceinline__ void set_flag(u64* f, u64 tok) { __hip_atomic_store(f, tok, __ATOMIC_RELAXED, __HIP_MEMORY_SCOPE_AGENT); }
__device__ __forceinline__ void publish(LAS unsigned* cnt, u64* flag, u64 tok, int lane) {
  asm volatile("s_waitcnt vmcnt(0)" ::: "memory");
  if (lane == 0) {
    const unsigned old = __hip_atomic_fetch_add(cnt, 1u, __ATOMIC_RELAXED, __HIP_MEMORY_SCOPE_WORKGROUP);
    if (old == 7u) set_flag(flag, tok);
  }
}
__device__ __forceinline__ void wg_wait(int w, int lane, const u64* f, u64 tok, volatile LAS unsigned* go) {
  if (w == 0) { wait_flags(f, tok); if (lane == 0) *go = 1u; }
  else { while (*go == 0u) __builtin_amdgcn_s_sleep(1); asm volatile("" ::: "memory"); }
}

#define MFMA16(a, b, c) __builtin_amdgcn_mfma_f32_16x16x32_bf16(a, b, c, 0, 0, 0)
#define MFMA32(a, b, c) __builtin_amdgcn_mfma_f32_32x32x16_bf16(a, b, c, 0, 0, 0)
#define WAITBAR(N) do { SB(); asm volatile("s_waitcnt vmcnt(" #N ") lgkmcnt(0)\n\ts_barrier" ::: "memory"); SB(); } while (0)
#define VMBAR(N) do { SB(); asm volatile("s_waitcnt vmcnt(" #N ")\n\ts_barrier" ::: "memory"); SB(); } while (0)
#define LGBAR() do { SB(); asm volatile("s_waitcnt lgkmcnt(0)\n\ts_barrier" ::: "memory"); SB(); } while (0)
#define BARRIER() do { SB(); asm volatile("s_barrier" ::: "memory"); SB(); } while (0)
#define MFMA16A(acc, a, b) asm volatile("v_mfma_f32_16x16x32_bf16 %0, %1, %2, %0" : "+v"(acc) : "v"(a), "v"(b))
#define MFMA16G(acc, a, b) MFMA16A(acc, a, b)
#define PINACC(x) asm volatile("" : "+v"(x))
#define ADV64(p, inc) asm volatile("v_mad_u64_u32 %0, vcc, %1, 1, %0" : "+v"(p) : "v"(inc) : "vcc")
#define PIN(x) asm volatile("" : "+v"(x))
#define EXP2(x) __builtin_amdgcn_exp2f(x)

constexpr int G1_AOFF = 0, G1_BOFF = 65536, G1_BREG = 10304, G1_BSLOT = 2 * G1_BREG;
constexpr int AT_STAGE = 32768, AT_Q = 98304, AT_EX = 131072, AT_EE = AT_EX + 16384, AT_HF = 150528;
constexpr int G2_SLOT = 49152, G2_NCH = 18;
constexpr int LDS_CTRL = 150016, LDS_SINK = 152576, LDS_TOTAL = LDS_SINK + 1024;

__device__ __forceinline__ float wave_sum(float v) {
#define ROR(n) v += __uint_as_float(__builtin_amdgcn_update_dpp(0u, __float_as_uint(v), 0x120 + (n), 0xf, 0xf, false))
  ROR(8); ROR(4); ROR(2); ROR(1);
#undef ROR
  auto a = __builtin_amdgcn_permlane16_swap(__float_as_uint(v), __float_as_uint(v), false, false);
  v = __uint_as_float(a[0]) + __uint_as_float(a[1]);
  auto b = __builtin_amdgcn_permlane32_swap(__float_as_uint(v), __float_as_uint(v), false, false);
  return __uint_as_float(b[0]) + __uint_as_float(b[1]);
}
__device__ __forceinline__ float max16(const f32x16& p) {
  float a = fmaxf(fmaxf(p[0], p[1]), p[2]), b = fmaxf(fmaxf(p[3], p[4]), p[5]);
  a = fmaxf(fmaxf(a, p[6]), p[7]); b = fmaxf(fmaxf(b, p[8]), p[9]);
  a = fmaxf(fmaxf(a, p[10]), p[11]); b = fmaxf(fmaxf(b, p[12]), p[13]);
  a = fmaxf(fmaxf(a, p[14]), p[15]);
  float m = fmaxf(a, b);
  auto rr = __builtin_amdgcn_permlane32_swap(__float_as_uint(m), __float_as_uint(m), false, false);
  return fmaxf(__uint_as_float(rr[0]), __uint_as_float(rr[1]));
}
__device__ __forceinline__ f32x16 splat16(float v) { f32x16 r;
#pragma unroll
  for (int i = 0; i < 16; ++i) r[i] = v;
  return r; }

struct Params {
  const bf16_t *x, *wqkv, *wg, *wu, *wo, *wd;
  bf16_t *xn, *Qb, *Kb, *Vb, *AH, *out;
  float* part; u64* flags;
};

__global__ __launch_bounds__(512, 2) void fused_kernel(Params P) {
  extern __shared__ __attribute__((aligned(16))) char smem[];
  const int w = __builtin_amdgcn_readfirstlane(threadIdx.x >> 6);
  const int g = w >> 2, w4 = w & 3;
  const int bid = blockIdx.x, xcd = bid & 7, jj = bid >> 3;
  const int cg = xcd * 4 + (jj & 3), rt = jj >> 2;
  const unsigned lds0 = (unsigned)(uintptr_t)smem;
  const lds_cptr L = (lds_cptr)smem;
  LAS unsigned* ctrl = (LAS unsigned*)((LAS char*)smem + LDS_CTRL);
  u64 tok;

  u32x4 hv[8];
  bf16_t* hp0; bf16_t* hp1;
#define HSTORE(i) st16wt((i) < 4 ? hp0 + (size_t)(i) * 16 * KA : hp1 + ((w4 & 1) ? (size_t)((i) & 1) * 32 * KA : (size_t)(((i) - 2) & 3) * 16 * KA), hv[i])
  const int h = xcd * 4 + (jj >> 3), qb = jj & 7;
  const char* kg = (const char*)(P.Kb + (size_t)h * HSTR) + w4 * 2048;
  const char* vg = (const char*)(P.Vb + (size_t)h * HSTR) + w4 * 2048;
#define DMA_ST(st, KH) do { const unsigned d_ = lds0 + ((st) & 3) * AT_STAGE + (KH) * 8192 + w4 * 2048; const int o_ = (KH) * 65536 + (st) * 8192; \
    glds16c(kg + o_, voff, d_); glds16c(kg + o_ + 1024, voff, d_ + 1024); \
    glds16c(vg + o_, voff, d_ + 16384); glds16c(vg + o_ + 1024, voff, d_ + 16384 + 1024); } while (0)

  {
    const int lane = threadIdx.x & 63;
    const int wm = 2 * g + (w4 >> 1), wn = w4 & 1;
    STAMP(1, 0);
    const int row0 = rt * 256;
    u32x4 xa, xb; u64 ep;
    asm volatile("global_load_dwordx4 %0, %3, off\n\tglobal_load_dwordx4 %1, %3, off offset:1024\n\tglobal_load_dwordx2 %2, %4, off sc1"
                 : "=&v"(xa), "=&v"(xb), "=&v"(ep) : "v"(P.x + (size_t)(bid * 8 + w) * DM + lane * 8), "v"(P.flags) : "memory");
    if (threadIdx.x < 8) ctrl[threadIdx.x] = 0u;
    asm volatile("s_waitcnt vmcnt(0) lgkmcnt(0)\n\ts_barrier" : "+v"(xa), "+v"(xb), "+v"(ep) :: "memory");
    STAMP(0, 0);
    tok = (((u64)(unsigned)__builtin_amdgcn_readfirstlane((unsigned)(ep >> 32)) << 32) | (unsigned)__builtin_amdgcn_readfirstlane((unsigned)ep)) + 1ull;
    {
      float v[16];
#pragma unroll
      for (int i = 0; i < 4; ++i) { v[2 * i] = bf_lo(xa[i]); v[2 * i + 1] = bf_hi(xa[i]); v[8 + 2 * i] = bf_lo(xb[i]); v[9 + 2 * i] = bf_hi(xb[i]); }
      float sm = 0.f;
#pragma unroll
      for (int i = 0; i < 16; ++i) sm += v[i];
      const float mean = wave_sum(sm) * (1.f / DM);
      float q = 0.f;
#pragma unroll
      for (int i = 0; i < 16; ++i) { const float d = v[i] - mean; q += d * d; }
      const float mb = rbf(mean), vb = rbf(wave_sum(q) * (1.f / DM));
      const float inv = 1.0f / rbf(sqrtf(rbf(vb + 1e-5f)));
      u32x4 oa, ob;
#pragma unroll
      for (int i = 0; i < 4; ++i) {
        const unsigned da = cvtpk(v[2 * i] - mb, v[2 * i + 1] - mb), db = cvtpk(v[8 + 2 * i] - mb, v[9 + 2 * i] - mb);
        oa[i] = cvtpk(bf_lo(da) * inv, bf_hi(da) * inv);
        ob[i] = cvtpk(bf_lo(db) * inv, bf_hi(db) * inv);
      }
      bf16_t* op = P.xn + (size_t)(bid * 8 + w) * DM + lane * 8;
      st16wt(op, oa); st16wt(op + 512, ob);
      STAMP(0, 4);
      publish(ctrl + 0, P.flags + FL_OFF + bid, tok, lane);
      STAMP(0, 1);
    }
    if (w == 3) {
      u64 pc; asm volatile("s_getpc_b64 %0" : "=s"(pc));
      if (jj < 20) { glds16s((const char*)(pc & ~255ull) + jj * 2048, lane * 16, lds0 + LDS_SINK);
                     glds16s((const char*)(pc & ~255ull) + jj * 2048 + 1024, lane * 16, lds0 + LDS_SINK); }
    }
    if (w >= 4) {
      const char* ta;
      if (w == 4) ta = lane < 16 ? (const char*)P.wqkv + lane * 393216 : (const char*)((lane & 1) ? P.wg : P.wu) + ((lane - 16) >> 1) * 305152;
      else if (w == 5) ta = (const char*)(P.AH + (size_t)(lane * 32 + 31) * KA + 4608);
      else if (w == 6) ta = lane < 48 ? (const char*)(P.Qb + (size_t)(2 * lane + 1) * HSTR - 64)
                                      : (const char*)P.part + (size_t)((lane - 48) * 16 + xcd) * 65536 + (xcd & 3) * 16384;
      else ta = lane < 8 ? (const char*)P.wo + lane * 262144 : (lane < 36 ? (const char*)P.wd + (lane - 8) * 262144
                         : (lane < 52 ? (const char*)P.out + (lane - 36) * 262144 : (const char*)P.x + (lane - 52) * 262144));
      glds4c(ta, lds0 + LDS_SINK);
    }
    unsigned avoff0 = (unsigned)((lane >> 3) * 2048 + (((lane & 7) ^ ((lane >> 4) + 2)) * 16)), avoff1 = avoff0 ^ 64u;
    const char* abase = (const char*)P.xn + (size_t)(row0 + 32 * w4) * 2048;
#define DMA_A(hh, t) do { _Pragma("unroll") for (int _i = 0; _i < 4; ++_i) \
    glds16c(abase + (size_t)(hh) * 262144 + _i * 16384 + (t) * 128, (_i & 1) ? avoff1 : avoff0, \
            lds0 + G1_AOFF + ((t) & 1) * 32768 + (hh) * 16384 + (4 * w4 + _i) * 1024); } while (0)
    const char* bptr[4]; unsigned bld[4]; unsigned bdst[4];
#pragma unroll
    for (int i = 0; i < 4; ++i) {
      const int b = (i == 0) ? (g ? 16 + w4 : 4 * w4) : 4 * w4 + i;
      const int region = b >= 10 ? 1 : 0, bi = b - 10 * region;
      const int Pc = bi * 64 + lane;
      const int k = Pc / 20, pc = Pc - 20 * k, f = pc >> 1, e8 = (pc & 1) * 8;
      const bool isq = f >= 8 || (region && (f & 3) == 3);
      const int qf = f >= 8 ? (region ? f - 4 : f - 8) : 2 + (f >> 2);
      const bf16_t* mat = isq ? P.wqkv : (f >= 4 ? P.wu : P.wg);
      const int ld = isq ? NQKV : FF;
      const int col = e8 + (isq ? 96 * cg + 16 * qf : 112 * cg + 64 * region + 16 * (f & 3));
      bptr[i] = (const char*)(mat + (size_t)k * ld + col);
      bld[i] = (unsigned)ld * 64u;
      bdst[i] = lds0 + G1_BOFF + region * G1_BREG + bi * 1024 + (bi >= 5 ? 32 : 0);
    }
#define DMA_B(i, s) glds16(bptr[i], bdst[i] + ((s) & 3) * G1_BSLOT)
#define ADV_B() do { ADV64(bptr[0], bld[0]); ADV64(bptr[1], bld[1]); ADV64(bptr[2], bld[2]); ADV64(bptr[3], bld[3]); } while (0)
    if (g == 0) {
      DMA_B(0, 0); DMA_B(1, 0); DMA_B(2, 0); DMA_B(3, 0); ADV_B();
      DMA_B(0, 1); ADV_B();
      DMA_B(0, 2); DMA_B(1, 2); DMA_B(2, 2); DMA_B(3, 2); ADV_B();
    } else {
      DMA_B(0, 0); ADV_B();
      DMA_B(0, 1); DMA_B(1, 1); DMA_B(2, 1); DMA_B(3, 1); ADV_B();
      DMA_B(0, 2); ADV_B();
    }
    const int fr = lane & 15, fq = lane >> 4, kq = ((fq & 1) << 1) | (fq >> 1);
    const int aoff0 = G1_AOFF + (64 * wm + fr) * 128 + ((kq ^ (((fr >> 1) + 2) & 7)) * 16);
    lds_cptr pa0 = L + aoff0, pa1 = L + (aoff0 ^ 64);
    lds_cptr pb0 = L + (G1_BOFF + (8 * kq + (fr >> 2)) * 320 + (fq & 1) * 32 + 8 * (lane & 3) + G1_BREG * wn);
    lds_cptr pb1 = pb0 + 2 * G1_BSLOT;
    asm volatile("" : "+v"(pa0), "+v"(pa1), "+v"(pb0), "+v"(pb1), "+v"(avoff0), "+v"(avoff1));
    f32x4 acc[4][10];
#pragma unroll
    for (int m = 0; m < 4; ++m)
#pragma unroll
      for (int j = 0; j < 10; ++j) { acc[m][j] = (f32x4){0.f, 0.f, 0.f, 0.f}; PINACC(acc[m][j]); }
    bf16x8 af[4], bfr[10];
    wg_wait(w, lane, P.flags + FL_OFF + rt * 32 + (lane & 31), tok, ctrl + 3);
    STAMP(0, 2);
    DMA_A(g, 0);
    STAMP(0, 3);
    if (g == 0) { WAITBAR(0); } else { DMA_A(0, 1); WAITBAR(4); BARRIER(); }
    STAMP(1, 1);
#define STEP(U, ISSUE, WN, LASTBAR) do { \
    { const lds_cptr pa = ((U) & 1) ? pa1 : pa0; const lds_cptr pb = (((U) & 2) ? pb1 : pb0) + ((U) & 1) * G1_BSLOT; \
      _Pragma("unroll") for (int m = 0; m < 4; ++m) af[m] = lds_rd128(pa + ((U) >> 1) * 32768 + m * 2048); \
      _Pragma("unroll") for (int j = 0; j < 10; ++j) bfr[j] = cat8(lds_tr(pb + j * 32), lds_tr(pb + j * 32 + 1280)); } \
    if (ISSUE) { DMA_B(0, (U) + 3); \
      if (g == ((U) & 1)) { DMA_A(1 - ((U) & 1), 2 * it + ((U) >> 1) + 1 + ((U) & 1)); } \
      else { DMA_B(1, (U) + 3); DMA_B(2, (U) + 3); DMA_B(3, (U) + 3); } } \
    WAITBAR(WN); \
    _Pragma("unroll") for (int j = 0; j < 10; ++j) { _Pragma("unroll") for (int m = 0; m < 4; ++m) MFMA16G(acc[m][j], bfr[j], af[m]); \
      if ((ISSUE) && (j & 1) && j < 8) ADV64(bptr[j >> 1], bld[j >> 1]); } \
    if (LASTBAR) BARRIER(); } while (0)
    int it = 0;
#pragma unroll 1
    for (; it < 7; ++it) { STEP(0, true, 9, true); STEP(1, true, 9, true); STEP(2, true, 9, true); STEP(3, true, 9, true); }
    STEP(0, true, 9, true); STEP(1, false, 4, true); STEP(2, false, 0, true); STEP(3, false, 0, g == 0);
#undef STEP
    asm volatile("s_nop 7\n\ts_nop 7" ::: "memory");
    SB();
    STAMP(1, 2);
#undef DMA_A
#undef DMA_B
#undef ADV_B
    int tz = threadIdx.x; asm volatile("" : "+v"(tz));
    const int fr2 = tz & 15, fq2 = (tz >> 4) & 3, sel1 = fq2 & 1, c8 = (fq2 >> 1) * 8;
    const float qscale = 0.17677669529663687f * 1.4426950408889634f;
#define SWAP16(A, B, OUT) do { auto r0_ = __builtin_amdgcn_permlane16_swap(A.x, B.x, false, false); \
    auto r1_ = __builtin_amdgcn_permlane16_swap(A.y, B.y, false, false); OUT = (u32x4){r0_[0], r1_[0], r0_[1], r1_[1]}; } while (0)
#define SILU4(m, j, OUT) do { float hv_[4]; _Pragma("unroll") for (int r = 0; r < 4; ++r) { \
      const float tg_ = acc[m][j][r], tu_ = acc[m][(j) + 4][r]; \
      hv_[r] = (tg_ * tu_) * __builtin_amdgcn_rcpf(1.f + __builtin_amdgcn_exp2f(-1.4426950408889634f * tg_)); } \
    OUT = (u32x2){cvtpk(hv_[0], hv_[1]), cvtpk(hv_[2], hv_[3])}; } while (0)
#define PK4(V, SC, OUT) do { const f32x4 v_ = (V) * (SC); OUT = (u32x2){cvtpk(v_[0], v_[1]), cvtpk(v_[2], v_[3])}; } while (0)
    const int rowb = row0 + 64 * wm + fr2;
#define QKV_HEAD(hd, ja, jb) do { const int typ_ = (hd) >> 5, h_ = (hd) & 31; const float sa = typ_ == 0 ? qscale : 1.f; \
      bf16_t* dst; int mstep; \
      if (typ_ == 1) { dst = P.Kb + (size_t)h_ * HSTR + (((rowb >> 5) * 2 + sel1) * 64 + (rowb & 31) + 4 * c8) * 8; mstep = 128; } \
      else { dst = (typ_ == 0 ? P.Qb : P.Vb) + (size_t)h_ * HSTR + rowb * 32 + 16 * sel1 + c8; mstep = 512; } \
      _Pragma("unroll") for (int m = 0; m < 4; ++m) { \
        u32x2 a_, b_; u32x4 o; PK4(acc[m][ja], sa, a_); PK4(acc[m][jb], sa, b_); SWAP16(a_, b_, o); \
        st16wt(dst + (m & 1) * mstep + (m >> 1) * 1024, o); } } while (0)
    __builtin_amdgcn_s_setprio(3);
    QKV_HEAD(3 * cg + 2 * wn, 8, 9);
    if (wn) QKV_HEAD(3 * cg + 1, 3, 7);
    __builtin_amdgcn_s_setprio(0);
    SB();
    bf16_t* hd = P.AH + (size_t)rowb * KA + DM + 112 * cg + 64 * wn + c8;
    hp0 = hd + 16 * sel1;
    hp1 = wn ? hd + 32 + (size_t)sel1 * 16 * KA : hd + 32 + 16 * sel1;
#pragma unroll
    for (int m = 0; m < 3; ++m) { u32x2 a_, b_; SILU4(m, 0, a_); SILU4(m, 1, b_); SWAP16(a_, b_, hv[m]); PIN(hv[m]); }
    STAMP(1, 4);
    publish(ctrl + 1, P.flags + FA_OFF + cg * 8 + rt, tok, tz & 63);
    { u32x2 a_, b_; SILU4(3, 0, a_); SILU4(3, 1, b_); SWAP16(a_, b_, hv[3]); PIN(hv[3]); }
    if (wn == 0) {
#pragma unroll
      for (int m = 0; m < 2; ++m) { u32x2 a_, b_; SILU4(m, 2, a_); SILU4(m, 3, b_); SWAP16(a_, b_, hv[6 + m]); PIN(hv[6 + m]); }
    }
    STAMP(1, 3);
    STAMP(2, 0);
    if (g == 0) {
      const int l6 = tz & 63;
      const int cgQ = h / 3, cgK = (32 + h) / 3, cgV = (64 + h) / 3;
      wg_wait(w, l6, P.flags + FA_OFF + (l6 < 8 ? cgK * 8 + l6 : (l6 < 16 ? cgV * 8 + l6 - 8 : cgQ * 8 + qb)), tok, ctrl + 4);
      const unsigned voff = l6 * 16;
      DMA_ST(0, 0); DMA_ST(0, 1);
      const char* qsrc = (const char*)(P.Qb + (size_t)h * HSTR + (qb * 256 + w4 * 64) * 32);
#pragma unroll
      for (int i = 0; i < 4; ++i) glds16c(qsrc + i * 1024, voff, lds0 + AT_Q + w4 * 4096 + i * 1024);
    }
    STAMP(2, 4);
    if (wn == 0) {
#pragma unroll
      for (int m = 2; m < 4; ++m) { u32x2 a_, b_; SILU4(m, 2, a_); SILU4(m, 3, b_); SWAP16(a_, b_, hv[2 + m]); }
    } else {
#pragma unroll
      for (int mp = 0; mp < 2; ++mp) { u32x2 a_, b_; SILU4(2 * mp, 2, a_); SILU4(2 * mp + 1, 2, b_); SWAP16(a_, b_, hv[4 + mp]); }
      hv[6] = hv[4]; hv[7] = hv[5];
    }
#undef SWAP16
#undef SILU4
#undef PK4
#undef QKV_HEAD
  }

  const int k4 = xcd & 3, rt2 = (xcd >> 2) * 4 + (jj >> 3), ct2 = jj & 7;
  int c_issued = 0;
#define G2_K(c) ((c) < 14 ? DM + (14 * k4 + (c)) * 64 : (4 * k4 + (c) - 14) * 64)
#define G2_DMA_A(c, sl) do { \
    const char* ab_ = (const char*)P.AH + (size_t)(rt2 * 256 + 64 * w4) * (KA * 2) + G2_K(c) * 2; \
    const unsigned d_ = lds0 + (sl) * G2_SLOT + w4 * 8192; unsigned keep_; \
    asm volatile("s_mov_b32 %0, m0\n\ts_mov_b32 m0, %7\n\ts_nop 0\n\t" \
                 "global_load_lds_dwordx4 %1, %5 sc1\n\tglobal_load_lds_dwordx4 %2, %5 offset:1024 sc1\n\t" \
                 "global_load_lds_dwordx4 %3, %5 offset:2048 sc1\n\tglobal_load_lds_dwordx4 %4, %5 offset:3072 sc1\n\t" \
                 "s_mov_b32 m0, %8\n\ts_nop 0\n\t" \
                 "global_load_lds_dwordx4 %1, %6 sc1\n\tglobal_load_lds_dwordx4 %2, %6 offset:1024 sc1\n\t" \
                 "global_load_lds_dwordx4 %3, %6 offset:2048 sc1\n\tglobal_load_lds_dwordx4 %4, %6 offset:3072 sc1\n\ts_mov_b32 m0, %0" \
                 : "=&s"(keep_) : "v"(avo[0]), "v"(avo[1]), "v"(avo[2]), "v"(avo[3]), "s"(ab_), "s"(ab_ + 32 * KA * 2), "s"(d_), "s"(d_ + 4096u) : "memory"); } while (0)
#define G2_DMA_B(c, sl) do { const int kg_ = G2_K(c); \
    const char* bb_ = (kg_ < DM ? (const char*)P.wo + (size_t)kg_ * 2048 : (const char*)P.wd + (size_t)(kg_ - DM) * 2048) + ct2 * 256 + w4 * 32768; \
    const unsigned d_ = lds0 + (sl) * G2_SLOT + 32768 + w4 * 4096; unsigned keep_; \
    asm volatile("s_mov_b32 %0, m0\n\ts_mov_b32 m0, %6\n\ts_nop 0\n\t" \
                 "global_load_lds_dwordx4 %1, %5\n\tglobal_load_lds_dwordx4 %2, %5 offset:1024\n\t" \
                 "global_load_lds_dwordx4 %3, %5 offset:2048\n\tglobal_load_lds_dwordx4 %4, %5 offset:3072\n\ts_mov_b32 m0, %0" \
                 : "=&s"(keep_) : "v"(bvo[0]), "v"(bvo[1]), "v"(bvo[2]), "v"(bvo[3]), "s"(bb_), "s"(d_) : "memory"); } while (0)
#define G2_DMA(c, sl) do { G2_DMA_A(c, sl); G2_DMA_B(c, sl); } while (0)
#define G2_OFFS(lane) unsigned avo[4], bvo[4]; _Pragma("unroll") for (int _i = 0; _i < 4; ++_i) { \
    avo[_i] = ((unsigned)(((lane) >> 3) * (KA * 2) + ((((lane) & 7) ^ (((lane) >> 4) + 2)) * 16)) ^ ((_i & 1) * 64u)) + _i * (8 * KA * 2 - 1024); \
    bvo[_i] = (unsigned)(((lane) >> 4) * 2048 + ((((lane) & 15) ^ (((lane) >> 4) << 1) ^ ((w4 & 1) << 3)) * 16)) + _i * (8192 - 1024); }

  {
    int tb = threadIdx.x; asm volatile("" : "+v"(tb));
    const int lane = tb & 63;
    const int qg = w4, kh = g;
    const int hi = lane >> 5;
    const unsigned voff = lane * 16;
    const short one = (((lane >> 4) & 1) == ((lane >> 2) & 1)) ? (short)0x3f80 : (short)0;
    const bf16x8 sel = {one, one, one, one, one, one, one, one};
    volatile LAS unsigned* flag = (volatile LAS unsigned*)(ctrl + 2);
    const int gq = lane >> 4;
    const int koff = kh * 8192 + lane * 16;
    const int voffl = 16384 + kh * 8192 + (4 * (gq >> 1) + ((lane & 15) >> 2)) * 64 + (gq & 1) * 32 + (lane & 3) * 8;
    const f32x16 zero16 = splat16(0.f);
    f32x16 oA = zero16, oB = zero16;
    f32x4 lA = {0.f, 0.f, 0.f, 0.f}, lB = {0.f, 0.f, 0.f, 0.f};
    float mA, mB;
    const unsigned* hflag = (const unsigned*)(P.flags + FH_OFF + (8 * k4 + ((lane >> 1) & 7)) * 8 + rt2) + (lane & 1);
    bf16x8 qa0, qa1, qb0, qb1;

#define LDK(K0, K1, p) do { K0 = lds_rd128(p); K1 = lds_rd128((p) + 1024); } while (0)
#define LDV(V0, V1, p) do { V0 = cat8(lds_tr(p), lds_tr((p) + 512)); V1 = cat8(lds_tr((p) + 1024), lds_tr((p) + 1536)); } while (0)
#define BF(x) __builtin_bit_cast(bf16x8, x)
    {
      WAITBAR(0);
      { const lds_cptr qp = L + AT_Q + qg * 4096 + (lane & 31) * 64 + hi * 16;
        qa0 = lds_rd128(qp); qa1 = lds_rd128(qp + 32); qb0 = lds_rd128(qp + 2048); qb1 = lds_rd128(qp + 2080); }
      bf16x8 kx0, kx1, ky0, ky1, vx0, vx1, vy0, vy1;
      LDK(kx0, kx1, L + koff);
      LDK(ky0, ky1, L + koff + 2048);
      LDV(vy0, vy1, L + voffl);
      DMA_ST(1, kh); DMA_ST(2, kh);
      f32x16 sA = MFMA32(kx0, qa0, zero16); sA = MFMA32(kx1, qa1, sA);
      f32x16 sB = MFMA32(kx0, qb0, zero16); sB = MFMA32(kx1, qb1, sB);
      mA = max16(sA); mB = max16(sB);
      f32x16 nA = splat16(-mA), nB = splat16(-mB);
      PIN(nA); PIN(nB);
      sA = MFMA32(kx0, qa0, nA); sA = MFMA32(kx1, qa1, sA);
      u32x4 pA0, pA1, pB0 = {0u, 0u, 0u, 0u}, pB1 = {0u, 0u, 0u, 0u};
      STAMP(2, 1);
#define E4(S, i) do { S[i] = EXP2(S[i]); S[i + 1] = EXP2(S[i + 1]); S[i + 2] = EXP2(S[i + 2]); S[i + 3] = EXP2(S[i + 3]); } while (0)
#define SLOT(SC, PC0, PC1, SN, K0, K1, Q0, Q1, NM, PP0, PP1, V0, V1, O, LS, LOADS) do { \
    SN = MFMA32(K0, Q0, NM); SB(); E4(SC, 0); PIN(SC); SB(); \
    SN = MFMA32(K1, Q1, SN); SB(); LOADS; SB(); E4(SC, 4); PIN(SC); SB(); \
    O = MFMA32(V0, BF(PP0), O); SB(); PC0[0] = cvtpk(SC[0], SC[1]); PC0[1] = cvtpk(SC[2], SC[3]); \
      SC[8] = EXP2(SC[8]); SC[9] = EXP2(SC[9]); SC[10] = EXP2(SC[10]); PIN(SC); PIN(PC0); SB(); \
    LS = MFMA16(sel, BF(PP0), LS); SB(); PC0[2] = cvtpk(SC[4], SC[5]); PC0[3] = cvtpk(SC[6], SC[7]); SC[11] = EXP2(SC[11]); PIN(SC); PIN(PC0); SB(); \
    O = MFMA32(V1, BF(PP1), O); SB(); E4(SC, 12); PIN(SC); SB(); \
    LS = MFMA16(sel, BF(PP1), LS); SB(); PC1[0] = cvtpk(SC[8], SC[9]); PC1[1] = cvtpk(SC[10], SC[11]); \
      PC1[2] = cvtpk(SC[12], SC[13]); PC1[3] = cvtpk(SC[14], SC[15]); PIN(PC1); SB(); } while (0)
#define SUB(KC0, KC1, KN0, KN1, VC0, VC1, VP0, VP1, vpu, kp2) do { \
    SLOT(sA, pA0, pA1, sB, KC0, KC1, qb0, qb1, nB, pB0, pB1, VP0, VP1, oB, lB, LDV(VC0, VC1, vpu)); \
    SLOT(sB, pB0, pB1, sA, KN0, KN1, qa0, qa1, nA, pA0, pA1, VC0, VC1, oA, lA, LDK(KC0, KC1, kp2)); } while (0)
      lds_cptr kb0 = L + koff, kb1 = L + koff + 2 * AT_STAGE, vb0 = L + voffl, vb1 = L + voffl + 2 * AT_STAGE;
      PIN(kb0); PIN(kb1); PIN(vb0); PIN(vb1);
#define KP(st) ((((st) & 2) ? kb1 : kb0) + ((st) & 1) * AT_STAGE)
#define VP(st) ((((st) & 2) ? vb1 : vb0) + ((st) & 1) * AT_STAGE)
#define HS(st, i) do { if ((st) < 2) HSTORE((4 * (st) + (i)) & 7); } while (0)
#define STAGE(st, WN, EXTRA) do { \
        SUB(kx0, kx1, ky0, ky1, vx0, vx1, vy0, vy1, VP(st), KP(st) + 4096); \
        HS(st, 0); \
        SUB(ky0, ky1, kx0, kx1, vy0, vy1, vx0, vx1, VP(st) + 2048, KP(st) + 6144); \
        VMBAR(WN); \
        HS(st, 1); \
        EXTRA; \
        if ((st) < 5) {   \
          const int pc = (st) * 8 + w, kb = bid * 36 + (pc < 36 ? pc : 35); \
          glds16s(kb < 2048 ? (const char*)P.wo + (size_t)kb * 1024 : (const char*)P.wd + (size_t)(kb - 2048) * 1024, voff, lds0 + LDS_SINK); \
          DMA_ST((st) + 3, kh); } \
        SUB(kx0, kx1, ky0, ky1, vx0, vx1, vy0, vy1, VP(st) + 4096, KP((st) + 1)); \
        HS(st, 2); \
        SUB(ky0, ky1, kx0, kx1, vy0, vy1, vx0, vx1, VP(st) + 6144, KP((st) + 1) + 2048); \
        HS(st, 3); } while (0)
      STAGE(0, 5, ); STAGE(1, 10, ); STAGE(2, 11, );
      STAGE(3, 5, if (w == 0 && lane == 0) set_flag(P.flags + FH_OFF + cg * 8 + rt, tok));
      STAGE(4, 5, );
      STAGE(5, 5, if (g == 1) glds4c(hflag, lds0 + AT_HF + w * 256));
      STAGE(6, 0, if (g == 1) {
                    const unsigned hf = *(volatile LAS unsigned*)((LAS char*)smem + AT_HF + w * 256 + lane * 4);
                    if (__all(hf == ((lane & 1) ? (unsigned)(tok >> 32) : (unsigned)tok))) {
                      G2_OFFS(lane); G2_DMA(0, 0); c_issued = 2; } });
      STAGE(7, 0, if (c_issued == 2) { G2_OFFS(lane); G2_DMA(1, 1); c_issued = 3; });
#undef STAGE
#undef HS
#undef KP
#undef VP
      oB = MFMA32(vy0, BF(pB0), oB); lB = MFMA16(sel, BF(pB0), lB);
      oB = MFMA32(vy1, BF(pB1), oB); lB = MFMA16(sel, BF(pB1), lB);
#undef SUB
#undef SLOT
#undef E4
    }
    STAMP(2, 2);
    LAS u32x4* xw = (LAS u32x4*)((LAS char*)smem + AT_EX) + w4 * 256;
    LAS float* ew = (LAS float*)((LAS char*)smem + AT_EE) + w4 * 128;
    float eA, eB;
#define FIN1(FIRST) do { \
        \
      float chk = 0.f; \
      _Pragma("unroll") for (int r = 0; r < 16; ++r) { chk = __builtin_fmaf(oA[r], 0.f, chk); chk = __builtin_fmaf(oB[r], 0.f, chk); } \
      const bool bad = !(lA[0] < 1.0e36f) || !(lB[0] < 1.0e36f) || !(chk == 0.f); \
      if (FIRST) { if (__any(bad) && lane == 0) *flag = 1u; \
                   if (c_issued == 3 && lane == 0) __hip_atomic_fetch_add(ctrl + 5, 1u, __ATOMIC_RELAXED, __HIP_MEMORY_SCOPE_WORKGROUP); } \
      oA *= __builtin_amdgcn_rcpf(lA[0]); oB *= __builtin_amdgcn_rcpf(lB[0]); \
      eA = mA + __builtin_amdgcn_logf(lA[0]); eB = mB + __builtin_amdgcn_logf(lB[0]);     \
      if (g == 0) { \
        _Pragma("unroll") for (int r = 0; r < 2; ++r) { \
          xw[r * 64 + lane] = (u32x4){cvtpk(oA[8 * r], oA[8 * r + 1]), cvtpk(oA[8 * r + 2], oA[8 * r + 3]), cvtpk(oA[8 * r + 4], oA[8 * r + 5]), cvtpk(oA[8 * r + 6], oA[8 * r + 7])}; \
          xw[128 + r * 64 + lane] = (u32x4){cvtpk(oB[8 * r], oB[8 * r + 1]), cvtpk(oB[8 * r + 2], oB[8 * r + 3]), cvtpk(oB[8 * r + 4], oB[8 * r + 5]), cvtpk(oB[8 * r + 6], oB[8 * r + 7])}; } \
        ew[lane] = eA; ew[64 + lane] = eB; } } while (0)
#define SAFE_UNIT(Q0, Q1, M, O, LS) do { \
            f32x16 s_ = MFMA32(k0, Q0, zero16); s_ = MFMA32(k1, Q1, s_); \
            const float mn_ = fmaxf(M, max16(s_)); const float al_ = EXP2(M - mn_); M = mn_; \
            O *= al_; LS *= al_; u32x4 p0_, p1_; \
            _Pragma("unroll") for (int r = 0; r < 4; ++r) { p0_[r] = cvtpk(EXP2(s_[2 * r] - mn_), EXP2(s_[2 * r + 1] - mn_)); \
              p1_[r] = cvtpk(EXP2(s_[8 + 2 * r] - mn_), EXP2(s_[9 + 2 * r] - mn_)); } \
            O = MFMA32(v0, BF(p0_), O); LS = MFMA16(sel, BF(p0_), LS); O = MFMA32(v1, BF(p1_), O); LS = MFMA16(sel, BF(p1_), LS); } while (0)
#define SAFE_PASS() do { \
        WAITBAR(0); \
        DMA_ST(0, kh); DMA_ST(1, kh); DMA_ST(2, kh); \
        oA = zero16; oB = zero16; lA = (f32x4){0.f, 0.f, 0.f, 0.f}; lB = lA; \
        mA = -3.0e38f; mB = -3.0e38f; \
        _Pragma("unroll 1") for (int st = 0; st < 8; ++st) { \
          if (st < 6) WAITBAR(8); else if (st == 6) WAITBAR(4); else WAITBAR(0); \
          if (st < 5) DMA_ST(st + 3, kh); \
          _Pragma("unroll 1") for (int u = 0; u < 4; ++u) { \
            bf16x8 k0, k1, v0, v1; \
            LDK(k0, k1, L + (st & 3) * AT_STAGE + koff + u * 2048); \
            LDV(v0, v1, L + (st & 3) * AT_STAGE + voffl + u * 2048); \
            SAFE_UNIT(qa0, qa1, mA, oA, lA); \
            SAFE_UNIT(qb0, qb1, mB, oB, lB); } } \
        FIN1(false); LGBAR(); } while (0)
    FIN1(true);
    STAMP(2, 3);

    STAMP(3, 0);
    {
      const int wm = w4 >> 1, wn = w4 & 1;
      const int fr = lane & 15, fq = lane >> 4, kq = ((fq & 1) << 1) | (fq >> 1), q4 = fr >> 2;
      const int aoff0 = (128 * wm + fr) * 128 + ((kq ^ (((fr >> 1) + 2) & 7)) * 16);
      const int boff0 = 32768 + (8 * kq + q4) * 256 + 8 * (lane & 3);
      const int bsw = q4 | ((fq & 1) << 2);
      lds_cptr pa[2], pb[4];
      pa[0] = L + aoff0; pa[1] = L + (aoff0 ^ 64); PIN(pa[0]); PIN(pa[1]);
#pragma unroll
      for (int n = 0; n < 4; ++n) { pb[n] = L + (boff0 + (((4 * wn + n) ^ bsw) * 32)); PIN(pb[n]); }
      bf16x8 fa0[8], fb0[4], fa1[8], fb1[4];
#define RD(i, KS, FA, FB) do { if ((i) < 4) FB[(i) & 3] = cat8(lds_tr(pb[(i) & 3] + (KS) * 8192), lds_tr(pb[(i) & 3] + (KS) * 8192 + 1024)); \
      else FA[((i) - 4) & 7] = lds_rd128(pa[KS] + (((i) - 4) & 7) * 2048); } while (0)
#define RD12(KS, FA, FB) do { RD(0, KS, FA, FB); RD(1, KS, FA, FB); RD(2, KS, FA, FB); RD(3, KS, FA, FB); RD(4, KS, FA, FB); RD(5, KS, FA, FB); \
      RD(6, KS, FA, FB); RD(7, KS, FA, FB); RD(8, KS, FA, FB); RD(9, KS, FA, FB); RD(10, KS, FA, FB); RD(11, KS, FA, FB); } while (0)
      if (g == 0) RD12(0, fa0, fb0);
      LGBAR();
      const bool ovf = *flag != 0u, fast = !ovf && *(volatile LAS unsigned*)(ctrl + 5) == 4u;
      if (!fast) {
        if (ovf) SAFE_PASS();
        if (g == 1) {
          G2_OFFS(lane);
          wait_flags(P.flags + FH_OFF + (8 * k4 + (lane & 7)) * 8 + rt2, tok);
          G2_DMA(0, 0); G2_DMA(1, 1);
        }
        VMBAR(12);
        if (g == 0) RD12(0, fa0, fb0);
        LGBAR();
      }
#undef RD12
      if (g == 1) {
#define FIN2(O, E, BL) do { \
      const float e2 = ew[(BL) * 64 + lane]; const u32x4 t0 = xw[(BL) * 128 + lane], t1 = xw[(BL) * 128 + 64 + lane]; \
      const float em = fmaxf(E, e2); float w1 = EXP2(E - em), w2 = EXP2(e2 - em); \
      const float rs = __builtin_amdgcn_rcpf(w1 + w2); w1 *= rs; w2 *= rs; u32x2 pk[4]; \
      _Pragma("unroll") for (int r = 0; r < 4; ++r) { \
        const unsigned ta = r < 2 ? t0[2 * r] : t1[2 * r - 4], tb2 = r < 2 ? t0[2 * r + 1] : t1[2 * r - 3]; \
        pk[r].x = cvtpk(O[4 * r] * w1 + bf_lo(ta) * w2, O[4 * r + 1] * w1 + bf_hi(ta) * w2); \
        pk[r].y = cvtpk(O[4 * r + 2] * w1 + bf_lo(tb2) * w2, O[4 * r + 3] * w1 + bf_hi(tb2) * w2); } \
      bf16_t* op = P.AH + (size_t)(qb * 256 + qg * 64 + (BL) * 32 + (lane & 31)) * KA + h * 32 + 8 * hi; \
      _Pragma("unroll") for (int k = 0; k < 4; k += 2) { \
        auto rx = __builtin_amdgcn_permlane32_swap(pk[k].x, pk[k + 1].x, false, false); \
        auto ry = __builtin_amdgcn_permlane32_swap(pk[k].y, pk[k + 1].y, false, false); \
        st16wt(op + 8 * k, (u32x4){rx[0], ry[0], rx[1], ry[1]}); } } while (0)
      __builtin_amdgcn_s_setprio(3);
      { G2_OFFS(lane); (void)bvo; G2_DMA_A(2, 2); }
      FIN2(oA, eA, 0); FIN2(oB, eB, 1);
#undef FIN2
      }
      SB();
      if (g == 1) {
      int tl = threadIdx.x; asm volatile("" : "+v"(tl));
      const int ll = tl & 63;
      G2_OFFS(ll);
      WAITBAR(12);
      G2_DMA_B(2, 2); G2_DMA(3, 0); VMBAR(12);
      if (w == 4 && ll == 0) set_flag(P.flags + FB_OFF + qb * 32 + h, tok);
      int sl = 1;
#pragma unroll 1
      for (int i = 2; i < G2_NCH - 2; ++i) {
        const u64* of = P.flags + FB_OFF + rt2 * 32 + 8 * k4;
        if (i == 10) glds4c((const unsigned*)(of + ((ll >> 1) & 7)) + (ll & 1), lds0 + AT_HF + w * 256);
        if (i == 12) {
          const unsigned hf = *(volatile LAS unsigned*)((LAS char*)smem + AT_HF + w * 256 + ll * 4);
          if (!__all(hf == ((ll & 1) ? (unsigned)(tok >> 32) : (unsigned)tok))) wait_flags(of + (ll & 7), tok);
        }
        G2_DMA(i + 2, sl); sl = sl == 2 ? 0 : sl + 1; VMBAR(12);
      }
      VMBAR(0);
      if (bid == 0 && w == 4) {
#pragma unroll
        for (int i = 0; i < 4; ++i) wait_flags(P.flags + FA_OFF + i * 64 + ll, tok);
        if (ll == 0) set_flag(P.flags, tok);
      }
      STAMP(3, 3);
      __builtin_amdgcn_endpgm();
      }
      f32x4 acc[8][4];
#pragma unroll
      for (int m = 0; m < 8; ++m)
#pragma unroll
        for (int n = 0; n < 4; ++n) { acc[m][n] = (f32x4){0.f, 0.f, 0.f, 0.f}; PIN(acc[m][n]); }
      asm volatile("s_nop 1" ::: "memory");
      STAMP(3, 1);
#define MM(i, FA, FB) MFMA16A(acc[(i) >> 2][(i) & 3], FB[(i) & 3], FA[(i) >> 2])
#define MR(i, KS, CA, CB, NA, NB) do { MM(i, CA, CB); RD(i, KS, NA, NB); SB(); } while (0)
#define MR4(i, KS, CA, CB, NA, NB) do { MR(i, KS, CA, CB, NA, NB); MR(i + 1, KS, CA, CB, NA, NB); MR(i + 2, KS, CA, CB, NA, NB); MR(i + 3, KS, CA, CB, NA, NB); } while (0)
#define MM4(i, CA, CB) do { MM(i, CA, CB); MM(i + 1, CA, CB); MM(i + 2, CA, CB); MM(i + 3, CA, CB); } while (0)
#define HALF(KS, CA, CB, NA, NB, END) do { \
      MR4(0, KS, CA, CB, NA, NB); MR4(4, KS, CA, CB, NA, NB); MR4(8, KS, CA, CB, NA, NB); \
      MM4(12, CA, CB); MM4(16, CA, CB); MM4(20, CA, CB); MM4(24, CA, CB); MM(28, CA, CB); MM(29, CA, CB); \
      END; MM(30, CA, CB); MM(31, CA, CB); SB(); } while (0)
#define LGW() do { SB(); asm volatile("s_waitcnt lgkmcnt(0)" ::: "memory"); SB(); } while (0)
      HALF(1, fa0, fb0, fa1, fb1, LGBAR());
      int sl = 0;
#pragma unroll 1
      for (int i = 1; i < G2_NCH; ++i) {
        const int dl = sl == 2 ? -2 * G2_SLOT : G2_SLOT; sl = sl == 2 ? 0 : sl + 1;
        pa[0] += dl; pa[1] += dl; pb[0] += dl; pb[1] += dl; pb[2] += dl; pb[3] += dl;
        PIN(pa[0]); PIN(pa[1]); PIN(pb[0]); PIN(pb[1]); PIN(pb[2]); PIN(pb[3]); SB();
        HALF(0, fa1, fb1, fa0, fb0, LGW());
        HALF(1, fa0, fb0, fa1, fb1, LGW(); if (i < G2_NCH - 1) BARRIER());
      }
      MM4(0, fa1, fb1); MM4(4, fa1, fb1); MM4(8, fa1, fb1); MM4(12, fa1, fb1); MM4(16, fa1, fb1); MM4(20, fa1, fb1); MM4(24, fa1, fb1); MM4(28, fa1, fb1);
#undef HALF
#undef LGW
#undef MR4
#undef MM4
#undef MR
#undef MM
#undef RD
      asm volatile("s_nop 7\n\ts_nop 7" ::: "memory");
      SB();
      STAMP(3, 2);
      int te = threadIdx.x; asm volatile("" : "+v"(te));
      const int le = te & 63;
      unsigned* mine = (unsigned*)P.part + (size_t)bid * 16384 + w4 * 1024 + le * 4;
      f32x4 keep[2][4];
#define PKP(A, B) (u32x4){cvtpk(A[0], A[1]), cvtpk(A[2], A[3]), cvtpk(B[0], B[1]), cvtpk(B[2], B[3])}
#pragma unroll
      for (int q = 0; q < 4; ++q) {
        if (q != k4) {
#pragma unroll
          for (int i = 0; i < 4; ++i) st16wt(mine + q * 4096 + i * 256, PKP(acc[2 * q + (i >> 1)][2 * (i & 1)], acc[2 * q + (i >> 1)][2 * (i & 1) + 1]));
        } else {
#pragma unroll
          for (int i = 0; i < 8; ++i) keep[i >> 2][i & 3] = acc[2 * q + (i >> 2)][i & 3];
        }
      }
#undef PKP
      asm volatile("s_waitcnt vmcnt(0)" ::: "memory");
      if (le == 0) set_flag(P.flags + FX_OFF + bid * 4 + w4, tok);
      const int orow = rt2 * 256 + 128 * wm + 32 * k4 + (le & 15), ocol = ct2 * 128 + 64 * wn + 4 * (le >> 4);
      u32x2 xv[2][4];
#pragma unroll
      for (int mm = 0; mm < 2; ++mm)
#pragma unroll
        for (int n = 0; n < 4; ++n) xv[mm][n] = *(const u32x2*)(P.x + (size_t)(orow + 16 * mm) * DM + ocol + 16 * n);
      wait_flags(P.flags + FX_OFF + (bid ^ (1 + le % 3)) * 4 + w4, tok);
      STAMP(3, 4);
      u32x4 pv[3][4];
      const unsigned* t1 = (const unsigned*)P.part + (size_t)(bid ^ 1) * 16384 + k4 * 4096 + w4 * 1024 + le * 4;
      const unsigned* t2 = (const unsigned*)P.part + (size_t)(bid ^ 2) * 16384 + k4 * 4096 + w4 * 1024 + le * 4;
      const unsigned* t3 = (const unsigned*)P.part + (size_t)(bid ^ 3) * 16384 + k4 * 4096 + w4 * 1024 + le * 4;
      asm volatile("global_load_dwordx4 %0, %12, off sc1\n\tglobal_load_dwordx4 %1, %12, off offset:1024 sc1\n\t"
                   "global_load_dwordx4 %2, %12, off offset:2048 sc1\n\tglobal_load_dwordx4 %3, %12, off offset:3072 sc1\n\t"
                   "global_load_dwordx4 %4, %13, off sc1\n\tglobal_load_dwordx4 %5, %13, off offset:1024 sc1\n\t"
                   "global_load_dwordx4 %6, %13, off offset:2048 sc1\n\tglobal_load_dwordx4 %7, %13, off offset:3072 sc1\n\t"
                   "global_load_dwordx4 %8, %14, off sc1\n\tglobal_load_dwordx4 %9, %14, off offset:1024 sc1\n\t"
                   "global_load_dwordx4 %10, %14, off offset:2048 sc1\n\tglobal_load_dwordx4 %11, %14, off offset:3072 sc1\n\t"
                   "s_waitcnt vmcnt(0)"
                   : "=&v"(pv[0][0]), "=&v"(pv[0][1]), "=&v"(pv[0][2]), "=&v"(pv[0][3]), "=&v"(pv[1][0]), "=&v"(pv[1][1]), "=&v"(pv[1][2]), "=&v"(pv[1][3]),
                     "=&v"(pv[2][0]), "=&v"(pv[2][1]), "=&v"(pv[2][2]), "=&v"(pv[2][3]) : "v"(t1), "v"(t2), "v"(t3) : "memory");
#pragma unroll
      for (int i = 0; i < 8; ++i) {
        f32x4 v = keep[i >> 2][i & 3];
#pragma unroll
        for (int q = 0; q < 3; ++q) {
          const unsigned p0 = pv[q][i >> 1][2 * (i & 1)], p1 = pv[q][i >> 1][2 * (i & 1) + 1];
          v += (f32x4){bf_lo(p0), bf_hi(p0), bf_lo(p1), bf_hi(p1)};
        }
        const u32x2 xx = xv[i >> 2][i & 3];
        u32x2 o = {cvtpk(bf_lo(xx.x) + v[0], bf_hi(xx.x) + v[1]), cvtpk(bf_lo(xx.y) + v[2], bf_hi(xx.y) + v[3])};
        *(u32x2*)(P.out + (size_t)(orow + 16 * (i >> 2)) * DM + ocol + 16 * (i & 3)) = o;
      }
      STAMP(3, 3);
    }
#undef LDK
#undef LDV
#undef BF
#undef FIN1
#undef SAFE_UNIT
#undef SAFE_PASS
  }
#undef G2_DMA
#undef G2_DMA_A
#undef G2_DMA_B
#undef G2_K
#undef G2_OFFS
#undef HSTORE
#undef DMA_ST
}

extern "C" void kernel_launch(void* const* d_in, const int* in_sizes, int n_in, void* d_out, int out_size, void* d_ws,
                              size_t ws_size, hipStream_t stream) {
  char* ws = (char*)d_ws;
  Params P;
  P.x = (const bf16_t*)d_in[0];
  P.wqkv = (const bf16_t*)d_in[1]; P.wo = (const bf16_t*)d_in[2];
  P.wg = (const bf16_t*)d_in[4]; P.wu = (const bf16_t*)d_in[5]; P.wd = (const bf16_t*)d_in[6];
  P.xn = (bf16_t*)(ws + WS_XN);
  P.Qb = (bf16_t*)(ws + WS_Q); P.Kb = (bf16_t*)(ws + WS_K); P.Vb = (bf16_t*)(ws + WS_V);
  P.AH = (bf16_t*)(ws + WS_AH); P.out = (bf16_t*)d_out;
  P.part = (float*)(ws + WS_PART); P.flags = (u64*)(ws + WS_FLAGS);
  (void)hipFuncSetAttribute((const void*)fused_kernel, hipFuncAttributeMaxDynamicSharedMemorySize, LDS_TOTAL);
  fused_kernel<<<256, 512, LDS_TOTAL, stream>>>(P);
}
```

```cpp
#define NPF 12
#define NPF1 13
#include <hip/hip_runtime.h>
#include <hip/hip_bf16.h>
#include <stdint.h>

typedef unsigned short bf16_t;
typedef short bf16x8 __attribute__((ext_vector_type(8)));
typedef short s16x4 __attribute__((ext_vector_type(4)));
typedef float f32x4 __attribute__((ext_vector_type(4)));
typedef float f32x16 __attribute__((ext_vector_type(16)));
typedef float f32x2 __attribute__((ext_vector_type(2)));
typedef __bf16 bf16x2v __attribute__((ext_vector_type(2)));
typedef unsigned u32x4 __attribute__((ext_vector_type(4)));
typedef unsigned u32x2 __attribute__((ext_vector_type(2)));
#define LAS __attribute__((address_space(3)))
typedef LAS const char* lds_cptr;

constexpr int SEQ = 2048, DM = 1024, FF = 3584, NQKV = 3072;
constexpr int KA = 4608 + 64;
constexpr int HSTR = SEQ * 32 + 128;

constexpr size_t WS_FLAGS = 0;
constexpr size_t WS_XN = 1u << 20;
constexpr size_t WS_Q = WS_XN + (size_t)SEQ * DM * 2;
constexpr size_t WS_K = WS_Q + (size_t)32 * HSTR * 2;
constexpr size_t WS_V = WS_K + (size_t)32 * HSTR * 2;
constexpr size_t WS_AH = WS_V + (size_t)32 * HSTR * 2;
constexpr size_t WS_PART = WS_AH + (size_t)SEQ * KA * 2;
typedef unsigned long long u64;
constexpr int FL_OFF = 256, FA_OFF = 512, FH_OFF = 768, FB_OFF = 1024, FX_OFF = 1280;

#define SB() __builtin_amdgcn_sched_barrier(0)
#define STAMP(kid, i) do {} while (0)

__device__ __forceinline__ unsigned cvtpk(float lo, float hi) {
  f32x2 v = {lo, hi};
  bf16x2v b = __builtin_convertvector(v, bf16x2v);
  return __builtin_bit_cast(unsigned, b);
}
__device__ __forceinline__ float bf_lo(unsigned u) { return __uint_as_float(u << 16); }
__device__ __forceinline__ float bf_hi(unsigned u) { return __uint_as_float(u & 0xffff0000u); }
__device__ __forceinline__ float rbf(float x) { return bf_lo(cvtpk(x, x)); }

__device__ __forceinline__ void glds16(const void* g, unsigned lds_dst) {
  unsigned keep;
  asm volatile("s_mov_b32 %0, m0\n\ts_mov_b32 m0, %2\n\ts_nop 0\n\tglobal_load_lds_dwordx4 %1, off\n\ts_mov_b32 m0, %0"
               : "=&s"(keep) : "v"(g), "s"(lds_dst) : "memory");
}
__device__ __forceinline__ void glds16s(const void* sbase, unsigned voff, unsigned lds_dst) {
  unsigned keep;
  asm volatile("s_mov_b32 %0, m0\n\ts_mov_b32 m0, %3\n\ts_nop 0\n\tglobal_load_lds_dwordx4 %1, %2\n\ts_mov_b32 m0, %0"
               : "=&s"(keep) : "v"(voff), "s"(sbase), "s"(lds_dst) : "memory");
}
__device__ __forceinline__ void glds16c(const void* sbase, unsigned voff, unsigned lds_dst) {
  unsigned keep;
  asm volatile("s_mov_b32 %0, m0\n\ts_mov_b32 m0, %3\n\ts_nop 0\n\tglobal_load_lds_dwordx4 %1, %2 sc1\n\ts_mov_b32 m0, %0"
               : "=&s"(keep) : "v"(voff), "s"(sbase), "s"(lds_dst) : "memory");
}
__device__ __forceinline__ void glds4c(const void* g, unsigned lds_dst) {
  unsigned keep;
  asm volatile("s_mov_b32 %0, m0\n\ts_mov_b32 m0, %2\n\ts_nop 0\n\tglobal_load_lds_dword %1, off sc1\n\ts_mov_b32 m0, %0"
               : "=&s"(keep) : "v"(g), "s"(lds_dst) : "memory");
}
__device__ __forceinline__ bf16x8 lds_rd128(lds_cptr p) { return *(const LAS bf16x8*)p; }
__device__ __forceinline__ s16x4 lds_tr(lds_cptr p) { return __builtin_amdgcn_ds_read_tr16_b64_v4i16((LAS s16x4*)p); }
__device__ __forceinline__ bf16x8 cat8(s16x4 a, s16x4 b) { return __builtin_shufflevector(a, b, 0, 1, 2, 3, 4, 5, 6, 7); }
__device__ __forceinline__ void st16wt(void* p, u32x4 v) {
  asm volatile("global_store_dwordx4 %0, %1, off sc1\n\ts_nop 1" :: "v"(p), "v"(v) : "memory");
}
__device__ __forceinline__ void wait_flags(const u64* f, u64 tok) {
  unsigned spins = 0;
  while (!__all(__hip_atomic_load(f, __ATOMIC_RELAXED, __HIP_MEMORY_SCOPE_AGENT) == tok)) {
    __builtin_amdgcn_s_sleep(3);
    if (++spins > (1u << 20)) break;
  }
  asm volatile("" ::: "memory");
}
__device__ __forceinline__ void set_flag(u64* f, u64 tok) { __hip_atomic_store(f, tok, __ATOMIC_RELAXED, __HIP_MEMORY_SCOPE_AGENT); }
__device__ __forceinline__ void publish(LAS unsigned* cnt, u64* flag, u64 tok, int lane) {
  asm volatile("s_waitcnt vmcnt(0)" ::: "memory");
  if (lane == 0) {
    const unsigned old = __hip_atomic_fetch_add(cnt, 1u, __ATOMIC_RELAXED, __HIP_MEMORY_SCOPE_WORKGROUP);
    if (old == 7u) set_flag(flag, tok);
  }
}
__device__ __forceinline__ void wg_wait(int w, int lane, const u64* f, u64 tok, volatile LAS unsigned* go) {
  if (w == 0) { wait_flags(f, tok); if (lane == 0) *go = 1u; }
  else { while (*go == 0u) __builtin_amdgcn_s_sleep(1); asm volatile("" ::: "memory"); }
}

#define MFMA16(a, b, c) __builtin_amdgcn_mfma_f32_16x16x32_bf16(a, b, c, 0, 0, 0)
#define MFMA32(a, b, c) __builtin_amdgcn_mfma_f32_32x32x16_bf16(a, b, c, 0, 0, 0)
#define STR_(x) #x
#define WAITBAR(N) do { SB(); asm volatile("s_waitcnt vmcnt(" STR_(N) ") lgkmcnt(0)\n\ts_barrier" ::: "memory"); SB(); } while (0)
#define VMBAR(N) do { SB(); asm volatile("s_waitcnt vmcnt(" #N ")\n\ts_barrier" ::: "memory"); SB(); } while (0)
#define LGBAR() do { SB(); asm volatile("s_waitcnt lgkmcnt(0)\n\ts_barrier" ::: "memory"); SB(); } while (0)
#define BARRIER() do { SB(); asm volatile("s_barrier" ::: "memory"); SB(); } while (0)
#define MFMA16A(acc, a, b) asm volatile("v_mfma_f32_16x16x32_bf16 %0, %1, %2, %0" : "+v"(acc) : "v"(a), "v"(b))
#define MFMA16G(acc, a, b) MFMA16A(acc, a, b)
#define PINACC(x) asm volatile("" : "+v"(x))
#define ADV64(p, inc) asm volatile("v_mad_u64_u32 %0, vcc, %1, 1, %0" : "+v"(p) : "v"(inc) : "vcc")
#define PIN(x) asm volatile("" : "+v"(x))
#define EXP2(x) __builtin_amdgcn_exp2f(x)

constexpr int G1_AOFF = 0, G1_BOFF = 65536, G1_BREG = 10304, G1_BSLOT = 2 * G1_BREG;
constexpr int AT_STAGE = 32768, AT_Q = 98304, AT_EX = 131072, AT_EE = AT_EX + 16384, AT_HF = 150528;
constexpr int G2_SLOT = 49152, G2_NCH = 18;
constexpr int LDS_CTRL = 150016, LDS_SINK = 152576, LDS_TOTAL = LDS_SINK + 1024;

__device__ __forceinline__ float wave_sum(float v) {
#define ROR(n) v += __uint_as_float(__builtin_amdgcn_update_dpp(0u, __float_as_uint(v), 0x120 + (n), 0xf, 0xf, false))
  ROR(8); ROR(4); ROR(2); ROR(1);
#undef ROR
  auto a = __builtin_amdgcn_permlane16_swap(__float_as_uint(v), __float_as_uint(v), false, false);
  v = __uint_as_float(a[0]) + __uint_as_float(a[1]);
  auto b = __builtin_amdgcn_permlane32_swap(__float_as_uint(v), __float_as_uint(v), false, false);
  return __uint_as_float(b[0]) + __uint_as_float(b[1]);
}
__device__ __forceinline__ float max16(const f32x16& p) {
  float a = fmaxf(fmaxf(p[0], p[1]), p[2]), b = fmaxf(fmaxf(p[3], p[4]), p[5]);
  a = fmaxf(fmaxf(a, p[6]), p[7]); b = fmaxf(fmaxf(b, p[8]), p[9]);
  a = fmaxf(fmaxf(a, p[10]), p[11]); b = fmaxf(fmaxf(b, p[12]), p[13]);
  a = fmaxf(fmaxf(a, p[14]), p[15]);
  float m = fmaxf(a, b);
  auto rr = __builtin_amdgcn_permlane32_swap(__float_as_uint(m), __float_as_uint(m), false, false);
  return fmaxf(__uint_as_float(rr[0]), __uint_as_float(rr[1]));
}
__device__ __forceinline__ f32x16 splat16(float v) { f32x16 r;
#pragma unroll
  for (int i = 0; i < 16; ++i) r[i] = v;
  return r; }

struct Params {
  const bf16_t *x, *wqkv, *wg, *wu, *wo, *wd;
  bf16_t *xn, *Qb, *Kb, *Vb, *AH, *out;
  float* part; u64* flags;
};

__global__ __launch_bounds__(512, 2) void fused_kernel(Params P) {
  extern __shared__ __attribute__((aligned(16))) char smem[];
  const int w = __builtin_amdgcn_readfirstlane(threadIdx.x >> 6);
  const int g = w >> 2, w4 = w & 3;
  const int bid = blockIdx.x, xcd = bid & 7, jj = bid >> 3;
  const int cg = xcd * 4 + (jj & 3), rt = jj >> 2;
  const unsigned lds0 = (unsigned)(uintptr_t)smem;
  const lds_cptr L = (lds_cptr)smem;
  LAS unsigned* ctrl = (LAS unsigned*)((LAS char*)smem + LDS_CTRL);
  u64 tok;

  u32x4 hv[8];
  bf16_t* hp0; bf16_t* hp1;
#define HSTORE(i) st16wt((i) < 4 ? hp0 + (size_t)(i) * 16 * KA : hp1 + ((w4 & 1) ? (size_t)((i) & 1) * 32 * KA : (size_t)(((i) - 2) & 3) * 16 * KA), hv[i])
  const int h = xcd * 4 + (jj >> 3), qb = jj & 7;
  const char* kg = (const char*)(P.Kb + (size_t)h * HSTR) + w4 * 2048;
  const char* vg = (const char*)(P.Vb + (size_t)h * HSTR) + w4 * 2048;
#define DMA_ST(st, KH) do { const unsigned d_ = lds0 + ((st) & 3) * AT_STAGE + (KH) * 8192 + w4 * 2048; const int o_ = (KH) * 65536 + (st) * 8192; \
    glds16c(kg + o_, voff, d_); glds16c(kg + o_ + 1024, voff, d_ + 1024); \
    glds16c(vg + o_, voff, d_ + 16384); glds16c(vg + o_ + 1024, voff, d_ + 16384 + 1024); } while (0)

  {
    const int lane = threadIdx.x & 63;
    const int wm = 2 * g + (w4 >> 1), wn = w4 & 1;
    STAMP(1, 0);
    const int row0 = rt * 256;
    u32x4 xa, xb; u64 ep;
    asm volatile("global_load_dwordx4 %0, %3, off\n\tglobal_load_dwordx4 %1, %3, off offset:1024\n\tglobal_load_dwordx2 %2, %4, off sc1"
                 : "=&v"(xa), "=&v"(xb), "=&v"(ep) : "v"(P.x + (size_t)(bid * 8 + w) * DM + lane * 8), "v"(P.flags) : "memory");
    if (threadIdx.x < 8) ctrl[threadIdx.x] = 0u;
    asm volatile("s_waitcnt vmcnt(0) lgkmcnt(0)\n\ts_barrier" : "+v"(xa), "+v"(xb), "+v"(ep) :: "memory");
    STAMP(0, 0);
    tok = (((u64)(unsigned)__builtin_amdgcn_readfirstlane((unsigned)(ep >> 32)) << 32) | (unsigned)__builtin_amdgcn_readfirstlane((unsigned)ep)) + 1ull;
    {
      float v[16];
#pragma unroll
      for (int i = 0; i < 4; ++i) { v[2 * i] = bf_lo(xa[i]); v[2 * i + 1] = bf_hi(xa[i]); v[8 + 2 * i] = bf_lo(xb[i]); v[9 + 2 * i] = bf_hi(xb[i]); }
      float sm = 0.f;
#pragma unroll
      for (int i = 0; i < 16; ++i) sm += v[i];
      const float mean = wave_sum(sm) * (1.f / DM);
      float q = 0.f;
#pragma unroll
      for (int i = 0; i < 16; ++i) { const float d = v[i] - mean; q += d * d; }
      const float mb = rbf(mean), vb = rbf(wave_sum(q) * (1.f / DM));
      const float inv = 1.0f / rbf(sqrtf(rbf(vb + 1e-5f)));
      u32x4 oa, ob;
#pragma unroll
      for (int i = 0; i < 4; ++i) {
        const unsigned da = cvtpk(v[2 * i] - mb, v[2 * i + 1] - mb), db = cvtpk(v[8 + 2 * i] - mb, v[9 + 2 * i] - mb);
        oa[i] = cvtpk(bf_lo(da) * inv, bf_hi(da) * inv);
        ob[i] = cvtpk(bf_lo(db) * inv, bf_hi(db) * inv);
      }
      bf16_t* op = P.xn + (size_t)(bid * 8 + w) * DM + lane * 8;
      st16wt(op, oa); st16wt(op + 512, ob);
      STAMP(0, 4);
      publish(ctrl + 0, P.flags + FL_OFF + bid, tok, lane);
      STAMP(0, 1);
    }
    unsigned avoff0 = (unsigned)((lane >> 3) * 2048 + (((lane & 7) ^ ((lane >> 4) + 2)) * 16)), avoff1 = avoff0 ^ 64u;
    const char* abase = (const char*)P.xn + (size_t)(row0 + 32 * w4) * 2048;
#define DMA_A(hh, t) do { _Pragma("unroll") for (int _i = 0; _i < 4; ++_i) \
    glds16c(abase + (size_t)(hh) * 262144 + _i * 16384 + (t) * 128, (_i & 1) ? avoff1 : avoff0, \
            lds0 + G1_AOFF + ((t) & 1) * 32768 + (hh) * 16384 + (4 * w4 + _i) * 1024); } while (0)
    const char* bptr[4]; unsigned bld[4]; unsigned bdst[4];
#pragma unroll
    for (int i = 0; i < 4; ++i) {
      const int b = (i == 0) ? (g ? 16 + w4 : 4 * w4) : 4 * w4 + i;
      const int region = b >= 10 ? 1 : 0, bi = b - 10 * region;
      const int Pc = bi * 64 + lane;
      const int k = Pc / 20, pc = Pc - 20 * k, f = pc >> 1, e8 = (pc & 1) * 8;
      const bool isq = f >= 8 || (region && (f & 3) == 3);
      const int qf = f >= 8 ? (region ? f - 4 : f - 8) : 2 + (f >> 2);
      const bf16_t* mat = isq ? P.wqkv : (f >= 4 ? P.wu : P.wg);
      const int ld = isq ? NQKV : FF;
      const int col = e8 + (isq ? 96 * cg + 16 * qf : 112 * cg + 64 * region + 16 * (f & 3));
      bptr[i] = (const char*)(mat + (size_t)k * ld + col);
      bld[i] = (unsigned)ld * 64u;
      bdst[i] = lds0 + G1_BOFF + region * G1_BREG + bi * 1024 + (bi >= 5 ? 32 : 0);
    }
#define DMA_B(i, s) glds16(bptr[i], bdst[i] + ((s) & 3) * G1_BSLOT)
#define ADV_B() do { ADV64(bptr[0], bld[0]); ADV64(bptr[1], bld[1]); ADV64(bptr[2], bld[2]); ADV64(bptr[3], bld[3]); } while (0)
    if (g == 0) {
      DMA_B(0, 0); DMA_B(1, 0); DMA_B(2, 0); DMA_B(3, 0); ADV_B();
      DMA_B(0, 1); ADV_B();
      DMA_B(0, 2); DMA_B(1, 2); DMA_B(2, 2); DMA_B(3, 2); ADV_B();
    } else {
      DMA_B(0, 0); ADV_B();
      DMA_B(0, 1); DMA_B(1, 1); DMA_B(2, 1); DMA_B(3, 1); ADV_B();
      DMA_B(0, 2); ADV_B();
      const char* ta;
      if (w4 <= 1) ta = (const char*)(P.AH + (size_t)(lane * 32 + 15 + 16 * w4) * KA + 4608);
      else if (w4 == 2) ta = lane < 48 ? (const char*)(P.Qb + (size_t)(2 * lane + 1) * HSTR - 64)
                                       : (const char*)P.part + (size_t)((lane - 48) * 16 + xcd) * 65536 + (xcd & 3) * 16384;
      else ta = lane < 8 ? (const char*)P.wo + lane * 262144 : (lane < 36 ? (const char*)P.wd + (lane - 8) * 262144
                         : (lane < 52 ? (const char*)P.out + (lane - 36) * 262144 : (const char*)P.x + (lane - 52) * 262144));
      glds4c(ta, lds0 + LDS_SINK);
#pragma unroll 1
      for (int i = 0; i < NPF; ++i) {
        int id = 20480 - NPF * 1024 + i * 1024 + bid * 4 + w4; const int r_ = id / 20, c_ = id - 20 * r_;
        glds16s(c_ < 6 ? (const char*)P.wqkv + (size_t)r_ * 6144 + c_ * 1024
                       : (const char*)(c_ < 13 ? P.wg : P.wu) + (size_t)r_ * 7168 + (c_ < 13 ? c_ - 6 : c_ - 13) * 1024, lane * 16, lds0 + LDS_SINK);
      }
    }
    const int fr = lane & 15, fq = lane >> 4, kq = ((fq & 1) << 1) | (fq >> 1);
    const int aoff0 = G1_AOFF + (64 * wm + fr) * 128 + ((kq ^ (((fr >> 1) + 2) & 7)) * 16);
    lds_cptr pa0 = L + aoff0, pa1 = L + (aoff0 ^ 64);
    lds_cptr pb0 = L + (G1_BOFF + (8 * kq + (fr >> 2)) * 320 + (fq & 1) * 32 + 8 * (lane & 3) + G1_BREG * wn);
    lds_cptr pb1 = pb0 + 2 * G1_BSLOT;
    asm volatile("" : "+v"(pa0), "+v"(pa1), "+v"(pb0), "+v"(pb1), "+v"(avoff0), "+v"(avoff1));
    f32x4 acc[4][10];
#pragma unroll
    for (int m = 0; m < 4; ++m)
#pragma unroll
      for (int j = 0; j < 10; ++j) { acc[m][j] = (f32x4){0.f, 0.f, 0.f, 0.f}; PINACC(acc[m][j]); }
    bf16x8 af[4], bfr[10];
    if (g == 0) {
      wg_wait(w, lane, P.flags + FL_OFF + rt * 32 + (lane & 31), tok, ctrl + 3);
      STAMP(0, 2);
      DMA_A(0, 0); DMA_A(1, 0); DMA_A(0, 1);
      STAMP(0, 3);
      WAITBAR(4);
    } else { WAITBAR(NPF1); BARRIER(); }
    STAMP(1, 1);
#define STEP(U, ISSUE, WN, LASTBAR, RELAX) do { \
    { const lds_cptr pa = ((U) & 1) ? pa1 : pa0; const lds_cptr pb = (((U) & 2) ? pb1 : pb0) + ((U) & 1) * G1_BSLOT; \
      _Pragma("unroll") for (int m = 0; m < 4; ++m) af[m] = lds_rd128(pa + ((U) >> 1) * 32768 + m * 2048); \
      _Pragma("unroll") for (int j = 0; j < 10; ++j) bfr[j] = cat8(lds_tr(pb + j * 32), lds_tr(pb + j * 32 + 1280)); } \
    if (ISSUE) { DMA_B(0, (U) + 3); \
      if (g == ((U) & 1)) { DMA_A(1 - ((U) & 1), 2 * it + ((U) >> 1) + 1 + ((U) & 1)); } \
      else { DMA_B(1, (U) + 3); DMA_B(2, (U) + 3); DMA_B(3, (U) + 3); } } \
    if ((RELAX) && g) { WAITBAR(63); } else { WAITBAR(WN); } \
    _Pragma("unroll") for (int j = 0; j < 10; ++j) { _Pragma("unroll") for (int m = 0; m < 4; ++m) MFMA16G(acc[m][j], bfr[j], af[m]); \
      if ((ISSUE) && (j & 1) && j < 8) ADV64(bptr[j >> 1], bld[j >> 1]); } \
    if (LASTBAR) BARRIER(); } while (0)
    int it = 0;
#pragma unroll 1
    for (; it < 7; ++it) { STEP(0, true, 9, true, it == 0); STEP(1, true, 9, true, it == 0); STEP(2, true, 9, true, false); STEP(3, true, 9, true, false); }
    STEP(0, true, 9, true, false); STEP(1, false, 4, true, false); STEP(2, false, 0, true, false); STEP(3, false, 0, g == 0, false);
#undef STEP
    asm volatile("s_nop 7\n\ts_nop 7" ::: "memory");
    SB();
    STAMP(1, 2);
#undef DMA_A
#undef DMA_B
#undef ADV_B
    int tz = threadIdx.x; asm volatile("" : "+v"(tz));
    const int fr2 = tz & 15, fq2 = (tz >> 4) & 3, sel1 = fq2 & 1, c8 = (fq2 >> 1) * 8;
    const float qscale = 0.17677669529663687f * 1.4426950408889634f;
#define SWAP16(A, B, OUT) do { auto r0_ = __builtin_amdgcn_permlane16_swap(A.x, B.x, false, false); \
    auto r1_ = __builtin_amdgcn_permlane16_swap(A.y, B.y, false, false); OUT = (u32x4){r0_[0], r1_[0], r0_[1], r1_[1]}; } while (0)
#define SILU4(m, j, OUT) do { float hv_[4]; _Pragma("unroll") for (int r = 0; r < 4; ++r) { \
      const float tg_ = acc[m][j][r], tu_ = acc[m][(j) + 4][r]; \
      hv_[r] = (tg_ * tu_) * __builtin_amdgcn_rcpf(1.f + __builtin_amdgcn_exp2f(-1.4426950408889634f * tg_)); } \
    OUT = (u32x2){cvtpk(hv_[0], hv_[1]), cvtpk(hv_[2], hv_[3])}; } while (0)
#define PK4(V, SC, OUT) do { const f32x4 v_ = (V) * (SC); OUT = (u32x2){cvtpk(v_[0], v_[1]), cvtpk(v_[2], v_[3])}; } while (0)
    const int rowb = row0 + 64 * wm + fr2;
#define QKV_HEAD(hd, ja, jb) do { const int typ_ = (hd) >> 5, h_ = (hd) & 31; const float sa = typ_ == 0 ? qscale : 1.f; \
      bf16_t* dst; int mstep; \
      if (typ_ == 1) { dst = P.Kb + (size_t)h_ * HSTR + (((rowb >> 5) * 2 + sel1) * 64 + (rowb & 31) + 4 * c8) * 8; mstep = 128; } \
      else { dst = (typ_ == 0 ? P.Qb : P.Vb) + (size_t)h_ * HSTR + rowb * 32 + 16 * sel1 + c8; mstep = 512; } \
      _Pragma("unroll") for (int m = 0; m < 4; ++m) { \
        u32x2 a_, b_; u32x4 o; PK4(acc[m][ja], sa, a_); PK4(acc[m][jb], sa, b_); SWAP16(a_, b_, o); \
        st16wt(dst + (m & 1) * mstep + (m >> 1) * 1024, o); } } while (0)
    __builtin_amdgcn_s_setprio(3);
    QKV_HEAD(3 * cg + 2 * wn, 8, 9);
    if (wn) QKV_HEAD(3 * cg + 1, 3, 7);
    __builtin_amdgcn_s_setprio(0);
    SB();
    bf16_t* hd = P.AH + (size_t)rowb * KA + DM + 112 * cg + 64 * wn + c8;
    hp0 = hd + 16 * sel1;
    hp1 = wn ? hd + 32 + (size_t)sel1 * 16 * KA : hd + 32 + 16 * sel1;
#pragma unroll
    for (int m = 0; m < 3; ++m) { u32x2 a_, b_; SILU4(m, 0, a_); SILU4(m, 1, b_); SWAP16(a_, b_, hv[m]); PIN(hv[m]); }
    STAMP(1, 4);
    publish(ctrl + 1, P.flags + FA_OFF + cg * 8 + rt, tok, tz & 63);
    { u32x2 a_, b_; SILU4(3, 0, a_); SILU4(3, 1, b_); SWAP16(a_, b_, hv[3]); PIN(hv[3]); }
    if (wn == 0) {
#pragma unroll
      for (int m = 0; m < 2; ++m) { u32x2 a_, b_; SILU4(m, 2, a_); SILU4(m, 3, b_); SWAP16(a_, b_, hv[6 + m]); PIN(hv[6 + m]); }
    }
    STAMP(1, 3);
    STAMP(2, 0);
    if (g == 0) {
      const int l6 = tz & 63;
      const int cgQ = h / 3, cgK = (32 + h) / 3, cgV = (64 + h) / 3;
      wg_wait(w, l6, P.flags + FA_OFF + (l6 < 8 ? cgK * 8 + l6 : (l6 < 16 ? cgV * 8 + l6 - 8 : cgQ * 8 + qb)), tok, ctrl + 4);
      const unsigned voff = l6 * 16;
      DMA_ST(0, 0); DMA_ST(0, 1);
      const char* qsrc = (const char*)(P.Qb + (size_t)h * HSTR + (qb * 256 + w4 * 64) * 32);
#pragma unroll
      for (int i = 0; i < 4; ++i) glds16c(qsrc + i * 1024, voff, lds0 + AT_Q + w4 * 4096 + i * 1024);
    }
    STAMP(2, 4);
    if (wn == 0) {
#pragma unroll
      for (int m = 2; m < 4; ++m) { u32x2 a_, b_; SILU4(m, 2, a_); SILU4(m, 3, b_); SWAP16(a_, b_, hv[2 + m]); }
    } else {
#pragma unroll
      for (int mp = 0; mp < 2; ++mp) { u32x2 a_, b_; SILU4(2 * mp, 2, a_); SILU4(2 * mp + 1, 2, b_); SWAP16(a_, b_, hv[4 + mp]); }
      hv[6] = hv[4]; hv[7] = hv[5];
    }
#undef SWAP16
#undef SILU4
#undef PK4
#undef QKV_HEAD
  }

  const int k4 = xcd & 3, rt2 = (xcd >> 2) * 4 + (jj >> 3), ct2 = jj & 7;
  int c_issued = 0;
#define G2_K(c) ((c) < 14 ? DM + (14 * k4 + (c)) * 64 : (4 * k4 + (c) - 14) * 64)
#define G2_DMA_A(c, sl) do { \
    const char* ab_ = (const char*)P.AH + (size_t)(rt2 * 256 + 64 * w4) * (KA * 2) + G2_K(c) * 2; \
    const unsigned d_ = lds0 + (sl) * G2_SLOT + w4 * 8192; unsigned keep_; \
    asm volatile("s_mov_b32 %0, m0\n\ts_mov_b32 m0, %7\n\ts_nop 0\n\t" \
                 "global_load_lds_dwordx4 %1, %5 sc1\n\tglobal_load_lds_dwordx4 %2, %5 offset:1024 sc1\n\t" \
                 "global_load_lds_dwordx4 %3, %5 offset:2048 sc1\n\tglobal_load_lds_dwordx4 %4, %5 offset:3072 sc1\n\t" \
                 "s_mov_b32 m0, %8\n\ts_nop 0\n\t" \
                 "global_load_lds_dwordx4 %1, %6 sc1\n\tglobal_load_lds_dwordx4 %2, %6 offset:1024 sc1\n\t" \
                 "global_load_lds_dwordx4 %3, %6 offset:2048 sc1\n\tglobal_load_lds_dwordx4 %4, %6 offset:3072 sc1\n\ts_mov_b32 m0, %0" \
                 : "=&s"(keep_) : "v"(avo[0]), "v"(avo[1]), "v"(avo[2]), "v"(avo[3]), "s"(ab_), "s"(ab_ + 32 * KA * 2), "s"(d_), "s"(d_ + 4096u) : "memory"); } while (0)
#define G2_DMA_B(c, sl) do { const int kg_ = G2_K(c); \
    const char* bb_ = (kg_ < DM ? (const char*)P.wo + (size_t)kg_ * 2048 : (const char*)P.wd + (size_t)(kg_ - DM) * 2048) + ct2 * 256 + w4 * 32768; \
    const unsigned d_ = lds0 + (sl) * G2_SLOT + 32768 + w4 * 4096; unsigned keep_; \
    asm volatile("s_mov_b32 %0, m0\n\ts_mov_b32 m0, %6\n\ts_nop 0\n\t" \
                 "global_load_lds_dwordx4 %1, %5\n\tglobal_load_lds_dwordx4 %2, %5 offset:1024\n\t" \
                 "global_load_lds_dwordx4 %3, %5 offset:2048\n\tglobal_load_lds_dwordx4 %4, %5 offset:3072\n\ts_mov_b32 m0, %0" \
                 : "=&s"(keep_) : "v"(bvo[0]), "v"(bvo[1]), "v"(bvo[2]), "v"(bvo[3]), "s"(bb_), "s"(d_) : "memory"); } while (0)
#define G2_DMA(c, sl) do { G2_DMA_A(c, sl); G2_DMA_B(c, sl); } while (0)
#define G2_OFFS(lane) unsigned avo[4], bvo[4]; _Pragma("unroll") for (int _i = 0; _i < 4; ++_i) { \
    avo[_i] = ((unsigned)(((lane) >> 3) * (KA * 2) + ((((lane) & 7) ^ (((lane) >> 4) + 2)) * 16)) ^ ((_i & 1) * 64u)) + _i * (8 * KA * 2 - 1024); \
    bvo[_i] = (unsigned)(((lane) >> 4) * 2048 + ((((lane) & 15) ^ (((lane) >> 4) << 1) ^ ((w4 & 1) << 3)) * 16)) + _i * (8192 - 1024); }

  {
    int tb = threadIdx.x; asm volatile("" : "+v"(tb));
    const int lane = tb & 63;
    const int qg = w4, kh = g;
    const int hi = lane >> 5;
    const unsigned voff = lane * 16;
    const short one = (((lane >> 4) & 1) == ((lane >> 2) & 1)) ? (short)0x3f80 : (short)0;
    const bf16x8 sel = {one, one, one, one, one, one, one, one};
    volatile LAS unsigned* flag = (volatile LAS unsigned*)(ctrl + 2);
    const int gq = lane >> 4;
    const int koff = kh * 8192 + lane * 16;
    const int voffl = 16384 + kh * 8192 + (4 * (gq >> 1) + ((lane & 15) >> 2)) * 64 + (gq & 1) * 32 + (lane & 3) * 8;
    const f32x16 zero16 = splat16(0.f);
    f32x16 oA = zero16, oB = zero16;
    f32x4 lA = {0.f, 0.f, 0.f, 0.f}, lB = {0.f, 0.f, 0.f, 0.f};
    float mA, mB;
    const unsigned* hflag = (const unsigned*)(P.flags + FH_OFF + (8 * k4 + ((lane >> 1) & 7)) * 8 + rt2) + (lane & 1);
    bf16x8 qa0, qa1, qb0, qb1;

#define LDK(K0, K1, p) do { K0 = lds_rd128(p); K1 = lds_rd128((p) + 1024); } while (0)
#define LDV(V0, V1, p) do { V0 = cat8(lds_tr(p), lds_tr((p) + 512)); V1 = cat8(lds_tr((p) + 1024), lds_tr((p) + 1536)); } while (0)
#define BF(x) __builtin_bit_cast(bf16x8, x)
    {
      WAITBAR(0);
      { const lds_cptr qp = L + AT_Q + qg * 4096 + (lane & 31) * 64 + hi * 16;
        qa0 = lds_rd128(qp); qa1 = lds_rd128(qp + 32); qb0 = lds_rd128(qp + 2048); qb1 = lds_rd128(qp + 2080); }
      bf16x8 kx0, kx1, ky0, ky1, vx0, vx1, vy0, vy1;
      LDK(kx0, kx1, L + koff);
      LDK(ky0, ky1, L + koff + 2048);
      LDV(vy0, vy1, L + voffl);
      DMA_ST(1, kh); DMA_ST(2, kh);
      f32x16 sA = MFMA32(kx0, qa0, zero16); sA = MFMA32(kx1, qa1, sA);
      f32x16 sB = MFMA32(kx0, qb0, zero16); sB = MFMA32(kx1, qb1, sB);
      mA = max16(sA); mB = max16(sB);
      f32x16 nA = splat16(-mA), nB = splat16(-mB);
      PIN(nA); PIN(nB);
      sA = MFMA32(kx0, qa0, nA); sA = MFMA32(kx1, qa1, sA);
      u32x4 pA0, pA1, pB0 = {0u, 0u, 0u, 0u}, pB1 = {0u, 0u, 0u, 0u};
      STAMP(2, 1);
#define E4(S, i) do { S[i] = EXP2(S[i]); S[i + 1] = EXP2(S[i + 1]); S[i + 2] = EXP2(S[i + 2]); S[i + 3] = EXP2(S[i + 3]); } while (0)
#define SLOT(SC, PC0, PC1, SN, K0, K1, Q0, Q1, NM, PP0, PP1, V0, V1, O, LS, LOADS) do { \
    SN = MFMA32(K0, Q0, NM); SB(); E4(SC, 0); PIN(SC); SB(); \
    SN = MFMA32(K1, Q1, SN); SB(); LOADS; SB(); E4(SC, 4); PIN(SC); SB(); \
    O = MFMA32(V0, BF(PP0), O); SB(); PC0[0] = cvtpk(SC[0], SC[1]); PC0[1] = cvtpk(SC[2], SC[3]); \
      SC[8] = EXP2(SC[8]); SC[9] = EXP2(SC[9]); SC[10] = EXP2(SC[10]); PIN(SC); PIN(PC0); SB(); \
    LS = MFMA16(sel, BF(PP0), LS); SB(); PC0[2] = cvtpk(SC[4], SC[5]); PC0[3] = cvtpk(SC[6], SC[7]); SC[11] = EXP2(SC[11]); PIN(SC); PIN(PC0); SB(); \
    O = MFMA32(V1, BF(PP1), O); SB(); E4(SC, 12); PIN(SC); SB(); \
    LS = MFMA16(sel, BF(PP1), LS); SB(); PC1[0] = cvtpk(SC[8], SC[9]); PC1[1] = cvtpk(SC[10], SC[11]); \
      PC1[2] = cvtpk(SC[12], SC[13]); PC1[3] = cvtpk(SC[14], SC[15]); PIN(PC1); SB(); } while (0)
#define SUB(KC0, KC1, KN0, KN1, VC0, VC1, VP0, VP1, vpu, kp2) do { \
    SLOT(sA, pA0, pA1, sB, KC0, KC1, qb0, qb1, nB, pB0, pB1, VP0, VP1, oB, lB, LDV(VC0, VC1, vpu)); \
    SLOT(sB, pB0, pB1, sA, KN0, KN1, qa0, qa1, nA, pA0, pA1, VC0, VC1, oA, lA, LDK(KC0, KC1, kp2)); } while (0)
      lds_cptr kb0 = L + koff, kb1 = L + koff + 2 * AT_STAGE, vb0 = L + voffl, vb1 = L + voffl + 2 * AT_STAGE;
      PIN(kb0); PIN(kb1); PIN(vb0); PIN(vb1);
#define KP(st) ((((st) & 2) ? kb1 : kb0) + ((st) & 1) * AT_STAGE)
#define VP(st) ((((st) & 2) ? vb1 : vb0) + ((st) & 1) * AT_STAGE)
#define HS(st, i) do { if ((st) < 2) HSTORE((4 * (st) + (i)) & 7); } while (0)
#define STAGE(st, WN, EXTRA) do { \
        SUB(kx0, kx1, ky0, ky1, vx0, vx1, vy0, vy1, VP(st), KP(st) + 4096); \
        HS(st, 0); \
        SUB(ky0, ky1, kx0, kx1, vy0, vy1, vx0, vx1, VP(st) + 2048, KP(st) + 6144); \
        VMBAR(WN); \
        HS(st, 1); \
        EXTRA; \
        if ((st) < 5) {   \
          const int pc = (st) * 8 + w, kb = bid * 36 + (pc < 36 ? pc : 35); \
          glds16s(kb < 2048 ? (const char*)P.wo + (size_t)kb * 1024 : (const char*)P.wd + (size_t)(kb - 2048) * 1024, voff, lds0 + LDS_SINK); \
          DMA_ST((st) + 3, kh); } \
        SUB(kx0, kx1, ky0, ky1, vx0, vx1, vy0, vy1, VP(st) + 4096, KP((st) + 1)); \
        HS(st, 2); \
        SUB(ky0, ky1, kx0, kx1, vy0, vy1, vx0, vx1, VP(st) + 6144, KP((st) + 1) + 2048); \
        HS(st, 3); } while (0)
      STAGE(0, 5, ); STAGE(1, 10, ); STAGE(2, 11, );
      STAGE(3, 5, if (w == 0 && lane == 0) set_flag(P.flags + FH_OFF + cg * 8 + rt, tok));
      STAGE(4, 5, );
      STAGE(5, 5, if (g == 1) glds4c(hflag, lds0 + AT_HF + w * 256));
      STAGE(6, 0, if (g == 1) {
                    const unsigned hf = *(volatile LAS unsigned*)((LAS char*)smem + AT_HF + w * 256 + lane * 4);
                    if (__all(hf == ((lane & 1) ? (unsigned)(tok >> 32) : (unsigned)tok))) {
                      G2_OFFS(lane); G2_DMA(0, 0); c_issued = 2; } });
      STAGE(7, 0, if (c_issued == 2) { G2_OFFS(lane); G2_DMA(1, 1); c_issued = 3; });
#undef STAGE
#undef HS
#undef KP
#undef VP
      oB = MFMA32(vy0, BF(pB0), oB); lB = MFMA16(sel, BF(pB0), lB);
      oB = MFMA32(vy1, BF(pB1), oB); lB = MFMA16(sel, BF(pB1), lB);
#undef SUB
#undef SLOT
#undef E4
    }
    STAMP(2, 2);
    LAS u32x4* xw = (LAS u32x4*)((LAS char*)smem + AT_EX) + w4 * 256;
    LAS float* ew = (LAS float*)((LAS char*)smem + AT_EE) + w4 * 128;
    float eA, eB;
#define FIN1(FIRST) do { \
        \
      float chk = 0.f; \
      _Pragma("unroll") for (int r = 0; r < 16; ++r) { chk = __builtin_fmaf(oA[r], 0.f, chk); chk = __builtin_fmaf(oB[r], 0.f, chk); } \
      const bool bad = !(lA[0] < 1.0e36f) || !(lB[0] < 1.0e36f) || !(chk == 0.f); \
      if (FIRST) { if (__any(bad) && lane == 0) *flag = 1u; \
                   if (c_issued == 3 && lane == 0) __hip_atomic_fetch_add(ctrl + 5, 1u, __ATOMIC_RELAXED, __HIP_MEMORY_SCOPE_WORKGROUP); } \
      oA *= __builtin_amdgcn_rcpf(lA[0]); oB *= __builtin_amdgcn_rcpf(lB[0]); \
      eA = mA + __builtin_amdgcn_logf(lA[0]); eB = mB + __builtin_amdgcn_logf(lB[0]);     \
      if (g == 0) { \
        _Pragma("unroll") for (int r = 0; r < 2; ++r) { \
          xw[r * 64 + lane] = (u32x4){cvtpk(oA[8 * r], oA[8 * r + 1]), cvtpk(oA[8 * r + 2], oA[8 * r + 3]), cvtpk(oA[8 * r + 4], oA[8 * r + 5]), cvtpk(oA[8 * r + 6], oA[8 * r + 7])}; \
          xw[128 + r * 64 + lane] = (u32x4){cvtpk(oB[8 * r], oB[8 * r + 1]), cvtpk(oB[8 * r + 2], oB[8 * r + 3]), cvtpk(oB[8 * r + 4], oB[8 * r + 5]), cvtpk(oB[8 * r + 6], oB[8 * r + 7])}; } \
        ew[lane] = eA; ew[64 + lane] = eB; } } while (0)
#define SAFE_UNIT(Q0, Q1, M, O, LS) do { \
            f32x16 s_ = MFMA32(k0, Q0, zero16); s_ = MFMA32(k1, Q1, s_); \
            const float mn_ = fmaxf(M, max16(s_)); const float al_ = EXP2(M - mn_); M = mn_; \
            O *= al_; LS *= al_; u32x4 p0_, p1_; \
            _Pragma("unroll") for (int r = 0; r < 4; ++r) { p0_[r] = cvtpk(EXP2(s_[2 * r] - mn_), EXP2(s_[2 * r + 1] - mn_)); \
              p1_[r] = cvtpk(EXP2(s_[8 + 2 * r] - mn_), EXP2(s_[9 + 2 * r] - mn_)); } \
            O = MFMA32(v0, BF(p0_), O); LS = MFMA16(sel, BF(p0_), LS); O = MFMA32(v1, BF(p1_), O); LS = MFMA16(sel, BF(p1_), LS); } while (0)
#define SAFE_PASS() do { \
        WAITBAR(0); \
        DMA_ST(0, kh); DMA_ST(1, kh); DMA_ST(2, kh); \
        oA = zero16; oB = zero16; lA = (f32x4){0.f, 0.f, 0.f, 0.f}; lB = lA; \
        mA = -3.0e38f; mB = -3.0e38f; \
        _Pragma("unroll 1") for (int st = 0; st < 8; ++st) { \
          if (st < 6) WAITBAR(8); else if (st == 6) WAITBAR(4); else WAITBAR(0); \
          if (st < 5) DMA_ST(st + 3, kh); \
          _Pragma("unroll 1") for (int u = 0; u < 4; ++u) { \
            bf16x8 k0, k1, v0, v1; \
            LDK(k0, k1, L + (st & 3) * AT_STAGE + koff + u * 2048); \
            LDV(v0, v1, L + (st & 3) * AT_STAGE + voffl + u * 2048); \
            SAFE_UNIT(qa0, qa1, mA, oA, lA); \
            SAFE_UNIT(qb0, qb1, mB, oB, lB); } } \
        FIN1(false); LGBAR(); } while (0)
    FIN1(true);
    STAMP(2, 3);

    STAMP(3, 0);
    {
      const int wm = w4 >> 1, wn = w4 & 1;
      const int fr = lane & 15, fq = lane >> 4, kq = ((fq & 1) << 1) | (fq >> 1), q4 = fr >> 2;
      const int aoff0 = (128 * wm + fr) * 128 + ((kq ^ (((fr >> 1) + 2) & 7)) * 16);
      const int boff0 = 32768 + (8 * kq + q4) * 256 + 8 * (lane & 3);
      const int bsw = q4 | ((fq & 1) << 2);
      lds_cptr pa[2], pb[4];
      pa[0] = L + aoff0; pa[1] = L + (aoff0 ^ 64); PIN(pa[0]); PIN(pa[1]);
#pragma unroll
      for (int n = 0; n < 4; ++n) { pb[n] = L + (boff0 + (((4 * wn + n) ^ bsw) * 32)); PIN(pb[n]); }
      bf16x8 fa0[8], fb0[4], fa1[8], fb1[4];
#define RD(i, KS, FA, FB) do { if ((i) < 4) FB[(i) & 3] = cat8(lds_tr(pb[(i) & 3] + (KS) * 8192), lds_tr(pb[(i) & 3] + (KS) * 8192 + 1024)); \
      else FA[((i) - 4) & 7] = lds_rd128(pa[KS] + (((i) - 4) & 7) * 2048); } while (0)
#define RD12(KS, FA, FB) do { RD(0, KS, FA, FB); RD(1, KS, FA, FB); RD(2, KS, FA, FB); RD(3, KS, FA, FB); RD(4, KS, FA, FB); RD(5, KS, FA, FB); \
      RD(6, KS, FA, FB); RD(7, KS, FA, FB); RD(8, KS, FA, FB); RD(9, KS, FA, FB); RD(10, KS, FA, FB); RD(11, KS, FA, FB); } while (0)
      if (g == 0) RD12(0, fa0, fb0);
      LGBAR();
      const bool ovf = *flag != 0u, fast = !ovf && *(volatile LAS unsigned*)(ctrl + 5) == 4u;
      if (!fast) {
        if (ovf) SAFE_PASS();
        if (g == 1) {
          G2_OFFS(lane);
          wait_flags(P.flags + FH_OFF + (8 * k4 + (lane & 7)) * 8 + rt2, tok);
          G2_DMA(0, 0); G2_DMA(1, 1);
        }
        VMBAR(12);
        if (g == 0) RD12(0, fa0, fb0);
        LGBAR();
      }
#undef RD12
      if (g == 1) {
#define FIN2(O, E, BL) do { \
      const float e2 = ew[(BL) * 64 + lane]; const u32x4 t0 = xw[(BL) * 128 + lane], t1 = xw[(BL) * 128 + 64 + lane]; \
      const float em = fmaxf(E, e2); float w1 = EXP2(E - em), w2 = EXP2(e2 - em); \
      const float rs = __builtin_amdgcn_rcpf(w1 + w2); w1 *= rs; w2 *= rs; u32x2 pk[4]; \
      _Pragma("unroll") for (int r = 0; r < 4; ++r) { \
        const unsigned ta = r < 2 ? t0[2 * r] : t1[2 * r - 4], tb2 = r < 2 ? t0[2 * r + 1] : t1[2 * r - 3]; \
        pk[r].x = cvtpk(O[4 * r] * w1 + bf_lo(ta) * w2, O[4 * r + 1] * w1 + bf_hi(ta) * w2); \
        pk[r].y = cvtpk(O[4 * r + 2] * w1 + bf_lo(tb2) * w2, O[4 * r + 3] * w1 + bf_hi(tb2) * w2); } \
      bf16_t* op = P.AH + (size_t)(qb * 256 + qg * 64 + (BL) * 32 + (lane & 31)) * KA + h * 32 + 8 * hi; \
      _Pragma("unroll") for (int k = 0; k < 4; k += 2) { \
        auto rx = __builtin_amdgcn_permlane32_swap(pk[k].x, pk[k + 1].x, false, false); \
        auto ry = __builtin_amdgcn_permlane32_swap(pk[k].y, pk[k + 1].y, false, false); \
        st16wt(op + 8 * k, (u32x4){rx[0], ry[0], rx[1], ry[1]}); } } while (0)
      __builtin_amdgcn_s_setprio(3);
      { G2_OFFS(lane); (void)bvo; G2_DMA_A(2, 2); }
      FIN2(oA, eA, 0); FIN2(oB, eB, 1);
#undef FIN2
      }
      SB();
      if (g == 1) {
      int tl = threadIdx.x; asm volatile("" : "+v"(tl));
      const int ll = tl & 63;
      G2_OFFS(ll);
      WAITBAR(12);
      G2_DMA_B(2, 2); G2_DMA(3, 0); VMBAR(12);
      if (w == 4 && ll == 0) set_flag(P.flags + FB_OFF + qb * 32 + h, tok);
      int sl = 1;
#pragma unroll 1
      for (int i = 2; i < G2_NCH - 2; ++i) {
        const u64* of = P.flags + FB_OFF + rt2 * 32 + 8 * k4;
        if (i == 10) glds4c((const unsigned*)(of + ((ll >> 1) & 7)) + (ll & 1), lds0 + AT_HF + w * 256);
        if (i == 12) {
          const unsigned hf = *(volatile LAS unsigned*)((LAS char*)smem + AT_HF + w * 256 + ll * 4);
          if (!__all(hf == ((ll & 1) ? (unsigned)(tok >> 32) : (unsigned)tok))) wait_flags(of + (ll & 7), tok);
        }
        G2_DMA(i + 2, sl); sl = sl == 2 ? 0 : sl + 1; VMBAR(12);
      }
      VMBAR(0);
      if (bid == 0 && w == 4) {
#pragma unroll
        for (int i = 0; i < 4; ++i) wait_flags(P.flags + FA_OFF + i * 64 + ll, tok);
        if (ll == 0) set_flag(P.flags, tok);
      }
      STAMP(3, 3);
      __builtin_amdgcn_endpgm();
      }
      f32x4 acc[8][4];
#pragma unroll
      for (int m = 0; m < 8; ++m)
#pragma unroll
        for (int n = 0; n < 4; ++n) { acc[m][n] = (f32x4){0.f, 0.f, 0.f, 0.f}; PIN(acc[m][n]); }
      asm volatile("s_nop 1" ::: "memory");
      STAMP(3, 1);
#define MM(i, FA, FB) MFMA16A(acc[(i) >> 2][(i) & 3], FB[(i) & 3], FA[(i) >> 2])
#define MR(i, KS, CA, CB, NA, NB) do { MM(i, CA, CB); RD(i, KS, NA, NB); SB(); } while (0)
#define MR4(i, KS, CA, CB, NA, NB) do { MR(i, KS, CA, CB, NA, NB); MR(i + 1, KS, CA, CB, NA, NB); MR(i + 2, KS, CA, CB, NA, NB); MR(i + 3, KS, CA, CB, NA, NB); } while (0)
#define MM4(i, CA, CB) do { MM(i, CA, CB); MM(i + 1, CA, CB); MM(i + 2, CA, CB); MM(i + 3, CA, CB); } while (0)
#define HALF(KS, CA, CB, NA, NB, END) do { \
      MR4(0, KS, CA, CB, NA, NB); MR4(4, KS, CA, CB, NA, NB); MR4(8, KS, CA, CB, NA, NB); \
      MM4(12, CA, CB); MM4(16, CA, CB); MM4(20, CA, CB); MM4(24, CA, CB); MM(28, CA, CB); MM(29, CA, CB); \
      END; MM(30, CA, CB); MM(31, CA, CB); SB(); } while (0)
#define LGW() do { SB(); asm volatile("s_waitcnt lgkmcnt(0)" ::: "memory"); SB(); } while (0)
      HALF(1, fa0, fb0, fa1, fb1, LGBAR());
      int sl = 0;
#pragma unroll 1
      for (int i = 1; i < G2_NCH; ++i) {
        const int dl = sl == 2 ? -2 * G2_SLOT : G2_SLOT; sl = sl == 2 ? 0 : sl + 1;
        pa[0] += dl; pa[1] += dl; pb[0] += dl; pb[1] += dl; pb[2] += dl; pb[3] += dl;
        PIN(pa[0]); PIN(pa[1]); PIN(pb[0]); PIN(pb[1]); PIN(pb[2]); PIN(pb[3]); SB();
        HALF(0, fa1, fb1, fa0, fb0, LGW());
        HALF(1, fa0, fb0, fa1, fb1, LGW(); if (i < G2_NCH - 1) BARRIER());
      }
      MM4(0, fa1, fb1); MM4(4, fa1, fb1); MM4(8, fa1, fb1); MM4(12, fa1, fb1); MM4(16, fa1, fb1); MM4(20, fa1, fb1); MM4(24, fa1, fb1); MM4(28, fa1, fb1);
#undef HALF
#undef LGW
#undef MR4
#undef MM4
#undef MR
#undef MM
#undef RD
      asm volatile("s_nop 7\n\ts_nop 7" ::: "memory");
      SB();
      STAMP(3, 2);
      int te = threadIdx.x; asm volatile("" : "+v"(te));
      const int le = te & 63;
      unsigned* mine = (unsigned*)P.part + (size_t)bid * 16384 + w4 * 1024 + le * 4;
      f32x4 keep[2][4];
#define PKP(A, B) (u32x4){cvtpk(A[0], A[1]), cvtpk(A[2], A[3]), cvtpk(B[0], B[1]), cvtpk(B[2], B[3])}
#pragma unroll
      for (int q = 0; q < 4; ++q) {
        if (q != k4) {
#pragma unroll
          for (int i = 0; i < 4; ++i) st16wt(mine + q * 4096 + i * 256, PKP(acc[2 * q + (i >> 1)][2 * (i & 1)], acc[2 * q + (i >> 1)][2 * (i & 1) + 1]));
        } else {
#pragma unroll
          for (int i = 0; i < 8; ++i) keep[i >> 2][i & 3] = acc[2 * q + (i >> 2)][i & 3];
        }
      }
#undef PKP
      asm volatile("s_waitcnt vmcnt(0)" ::: "memory");
      if (le == 0) set_flag(P.flags + FX_OFF + bid * 4 + w4, tok);
      const int orow = rt2 * 256 + 128 * wm + 32 * k4 + (le & 15), ocol = ct2 * 128 + 64 * wn + 4 * (le >> 4);
      u32x2 xv[2][4];
#pragma unroll
      for (int mm = 0; mm < 2; ++mm)
#pragma unroll
        for (int n = 0; n < 4; ++n) xv[mm][n] = *(const u32x2*)(P.x + (size_t)(orow + 16 * mm) * DM + ocol + 16 * n);
      wait_flags(P.flags + FX_OFF + (bid ^ (1 + le % 3)) * 4 + w4, tok);
      STAMP(3, 4);
      u32x4 pv[3][4];
      const unsigned* t1 = (const unsigned*)P.part + (size_t)(bid ^ 1) * 16384 + k4 * 4096 + w4 * 1024 + le * 4;
      const unsigned* t2 = (const unsigned*)P.part + (size_t)(bid ^ 2) * 16384 + k4 * 4096 + w4 * 1024 + le * 4;
      const unsigned* t3 = (const unsigned*)P.part + (size_t)(bid ^ 3) * 16384 + k4 * 4096 + w4 * 1024 + le * 4;
      asm volatile("global_load_dwordx4 %0, %12, off sc1\n\tglobal_load_dwordx4 %1, %12, off offset:1024 sc1\n\t"
                   "global_load_dwordx4 %2, %12, off offset:2048 sc1\n\tglobal_load_dwordx4 %3, %12, off offset:3072 sc1\n\t"
                   "global_load_dwordx4 %4, %13, off sc1\n\tglobal_load_dwordx4 %5, %13, off offset:1024 sc1\n\t"
                   "global_load_dwordx4 %6, %13, off offset:2048 sc1\n\tglobal_load_dwordx4 %7, %13, off offset:3072 sc1\n\t"
                   "global_load_dwordx4 %8, %14, off sc1\n\tglobal_load_dwordx4 %9, %14, off offset:1024 sc1\n\t"
                   "global_load_dwordx4 %10, %14, off offset:2048 sc1\n\tglobal_load_dwordx4 %11, %14, off offset:3072 sc1\n\t"
                   "s_waitcnt vmcnt(0)"
                   : "=&v"(pv[0][0]), "=&v"(pv[0][1]), "=&v"(pv[0][2]), "=&v"(pv[0][3]), "=&v"(pv[1][0]), "=&v"(pv[1][1]), "=&v"(pv[1][2]), "=&v"(pv[1][3]),
                     "=&v"(pv[2][0]), "=&v"(pv[2][1]), "=&v"(pv[2][2]), "=&v"(pv[2][3]) : "v"(t1), "v"(t2), "v"(t3) : "memory");
#pragma unroll
      for (int i = 0; i < 8; ++i) {
        f32x4 v = keep[i >> 2][i & 3];
#pragma unroll
        for (int q = 0; q < 3; ++q) {
          const unsigned p0 = pv[q][i >> 1][2 * (i & 1)], p1 = pv[q][i >> 1][2 * (i & 1) + 1];
          v += (f32x4){bf_lo(p0), bf_hi(p0), bf_lo(p1), bf_hi(p1)};
        }
        const u32x2 xx = xv[i >> 2][i & 3];
        u32x2 o = {cvtpk(bf_lo(xx.x) + v[0], bf_hi(xx.x) + v[1]), cvtpk(bf_lo(xx.y) + v[2], bf_hi(xx.y) + v[3])};
        *(u32x2*)(P.out + (size_t)(orow + 16 * (i >> 2)) * DM + ocol + 16 * (i & 3)) = o;
      }
      STAMP(3, 3);
    }
#undef LDK
#undef LDV
#undef BF
#undef FIN1
#undef SAFE_UNIT
#undef SAFE_PASS
  }
#undef G2_DMA
#undef G2_DMA_A
#undef G2_DMA_B
#undef G2_K
#undef G2_OFFS
#undef HSTORE
#undef DMA_ST
}

extern "C" void kernel_launch(void* const* d_in, const int* in_sizes, int n_in, void* d_out, int out_size, void* d_ws,
                              size_t ws_size, hipStream_t stream) {
  char* ws = (char*)d_ws;
  Params P;
  P.x = (const bf16_t*)d_in[0];
  P.wqkv = (const bf16_t*)d_in[1]; P.wo = (const bf16_t*)d_in[2];
  P.wg = (const bf16_t*)d_in[4]; P.wu = (const bf16_t*)d_in[5]; P.wd = (const bf16_t*)d_in[6];
  P.xn = (bf16_t*)(ws + WS_XN);
  P.Qb = (bf16_t*)(ws + WS_Q); P.Kb = (bf16_t*)(ws + WS_K); P.Vb = (bf16_t*)(ws + WS_V);
  P.AH = (bf16_t*)(ws + WS_AH); P.out = (bf16_t*)d_out;
  P.part = (float*)(ws + WS_PART); P.flags = (u64*)(ws + WS_FLAGS);
  (void)hipFuncSetAttribute((const void*)fused_kernel, hipFuncAttributeMaxDynamicSharedMemorySize, LDS_TOTAL);
  fused_kernel<<<256, 512, LDS_TOTAL, stream>>>(P);
}
```

```cpp
#include <hip/hip_runtime.h>
#include <hip/hip_bf16.h>
#include <stdint.h>

typedef unsigned short bf16_t;
typedef short bf16x8 __attribute__((ext_vector_type(8)));
typedef short s16x4 __attribute__((ext_vector_type(4)));
typedef float f32x4 __attribute__((ext_vector_type(4)));
typedef float f32x16 __attribute__((ext_vector_type(16)));
typedef float f32x2 __attribute__((ext_vector_type(2)));
typedef __bf16 bf16x2v __attribute__((ext_vector_type(2)));
typedef unsigned u32x4 __attribute__((ext_vector_type(4)));
typedef unsigned u32x2 __attribute__((ext_vector_type(2)));
#define LAS __attribute__((address_space(3)))
typedef LAS const char* lds_cptr;

constexpr int SEQ = 2048, DM = 1024, FF = 3584, NQKV = 3072;
constexpr int KA = 4608 + 64;
constexpr int HSTR = SEQ * 32 + 128;

constexpr size_t WS_FLAGS = 0;
constexpr size_t WS_XN = 1u << 20;
constexpr size_t WS_Q = WS_XN + (size_t)SEQ * DM * 2;
constexpr size_t WS_K = WS_Q + (size_t)32 * HSTR * 2;
constexpr size_t WS_V = WS_K + (size_t)32 * HSTR * 2;
constexpr size_t WS_AH = WS_V + (size_t)32 * HSTR * 2;
constexpr size_t WS_PART = WS_AH + (size_t)SEQ * KA * 2;
typedef unsigned long long u64;
constexpr int FL_OFF = 256, FA_OFF = 512, FH_OFF = 768, FB_OFF = 1024, FX_OFF = 1280;

#define SB() __builtin_amdgcn_sched_barrier(0)
#define STAMP(kid, i) do {} while (0)

__device__ __forceinline__ unsigned cvtpk(float lo, float hi) {
  f32x2 v = {lo, hi};
  bf16x2v b = __builtin_convertvector(v, bf16x2v);
  return __builtin_bit_cast(unsigned, b);
}
__device__ __forceinline__ float bf_lo(unsigned u) { return __uint_as_float(u << 16); }
__device__ __forceinline__ float bf_hi(unsigned u) { return __uint_as_float(u & 0xffff0000u); }
__device__ __forceinline__ float rbf(float x) { return bf_lo(cvtpk(x, x)); }

__device__ __forceinline__ void glds16(const void* g, unsigned lds_dst) {
  unsigned keep;
  asm volatile("s_mov_b32 %0, m0\n\ts_mov_b32 m0, %2\n\ts_nop 0\n\tglobal_load_lds_dwordx4 %1, off\n\ts_mov_b32 m0, %0"
               : "=&s"(keep) : "v"(g), "s"(lds_dst) : "memory");
}
__device__ __forceinline__ void glds16s(const void* sbase, unsigned voff, unsigned lds_dst) {
  unsigned keep;
  asm volatile("s_mov_b32 %0, m0\n\ts_mov_b32 m0, %3\n\ts_nop 0\n\tglobal_load_lds_dwordx4 %1, %2\n\ts_mov_b32 m0, %0"
               : "=&s"(keep) : "v"(voff), "s"(sbase), "s"(lds_dst) : "memory");
}
__device__ __forceinline__ void glds16c(const void* sbase, unsigned voff, unsigned lds_dst) {
  unsigned keep;
  asm volatile("s_mov_b32 %0, m0\n\ts_mov_b32 m0, %3\n\ts_nop 0\n\tglobal_load_lds_dwordx4 %1, %2 sc1\n\ts_mov_b32 m0, %0"
               : "=&s"(keep) : "v"(voff), "s"(sbase), "s"(lds_dst) : "memory");
}
__device__ __forceinline__ void glds4c(const void* g, unsigned lds_dst) {
  unsigned keep;
  asm volatile("s_mov_b32 %0, m0\n\ts_mov_b32 m0, %2\n\ts_nop 0\n\tglobal_load_lds_dword %1, off sc1\n\ts_mov_b32 m0, %0"
               : "=&s"(keep) : "v"(g), "s"(lds_dst) : "memory");
}
__device__ __forceinline__ bf16x8 lds_rd128(lds_cptr p) { return *(const LAS bf16x8*)p; }
__device__ __forceinline__ s16x4 lds_tr(lds_cptr p) { return __builtin_amdgcn_ds_read_tr16_b64_v4i16((LAS s16x4*)p); }
__device__ __forceinline__ bf16x8 cat8(s16x4 a, s16x4 b) { return __builtin_shufflevector(a, b, 0, 1, 2, 3, 4, 5, 6, 7); }
__device__ __forceinline__ void st16wt(void* p, u32x4 v) {
  asm volatile("global_store_dwordx4 %0, %1, off sc1\n\ts_nop 1" :: "v"(p), "v"(v) : "memory");
}
__device__ __forceinline__ void wait_flags(const u64* f, u64 tok) {
  unsigned spins = 0;
  while (!__all(__hip_atomic_load(f, __ATOMIC_RELAXED, __HIP_MEMORY_SCOPE_AGENT) == tok)) {
    __builtin_amdgcn_s_sleep(3);
    if (++spins > (1u << 20)) break;
  }
  asm volatile("" ::: "memory");
}
__device__ __forceinline__ void set_flag(u64* f, u64 tok) { __hip_atomic_store(f, tok, __ATOMIC_RELAXED, __HIP_MEMORY_SCOPE_AGENT); }
__device__ __forceinline__ void publish(LAS unsigned* cnt, u64* flag, u64 tok, int lane) {
  asm volatile("s_waitcnt vmcnt(0)" ::: "memory");
  if (lane == 0) {
    const unsigned old = __hip_atomic_fetch_add(cnt, 1u, __ATOMIC_RELAXED, __HIP_MEMORY_SCOPE_WORKGROUP);
    if (old == 7u) set_flag(flag, tok);
  }
}
__device__ __forceinline__ void wg_wait(int w, int lane, const u64* f, u64 tok, volatile LAS unsigned* go) {
  if (w == 0) { wait_flags(f, tok); if (lane == 0) *go = 1u; }
  else { while (*go == 0u) __builtin_amdgcn_s_sleep(1); asm volatile("" ::: "memory"); }
}

#define MFMA16(a, b, c) __builtin_amdgcn_mfma_f32_16x16x32_bf16(a, b, c, 0, 0, 0)
#define MFMA32(a, b, c) __builtin_amdgcn_mfma_f32_32x32x16_bf16(a, b, c, 0, 0, 0)
#define STR_(x) #x
#define WAITBAR(N) do { SB(); asm volatile("s_waitcnt vmcnt(" STR_(N) ") lgkmcnt(0)\n\ts_barrier" ::: "memory"); SB(); } while (0)
#define VMBAR(N) do { SB(); asm volatile("s_waitcnt vmcnt(" #N ")\n\ts_barrier" ::: "memory"); SB(); } while (0)
#define LGBAR() do { SB(); asm volatile("s_waitcnt lgkmcnt(0)\n\ts_barrier" ::: "memory"); SB(); } while (0)
#define BARRIER() do { SB(); asm volatile("s_barrier" ::: "memory"); SB(); } while (0)
#define MFMA16A(acc, a, b) asm volatile("v_mfma_f32_16x16x32_bf16 %0, %1, %2, %0" : "+v"(acc) : "v"(a), "v"(b))
#define ADV64(p, inc) asm volatile("v_mad_u64_u32 %0, vcc, %1, 1, %0" : "+v"(p) : "v"(inc) : "vcc")
#define PIN(x) asm volatile("" : "+v"(x))
#define EXP2(x) __builtin_amdgcn_exp2f(x)

constexpr int G1_AOFF = 0, G1_BOFF = 65536, G1_BREG = 10304, G1_BSLOT = 2 * G1_BREG;
constexpr int AT_STAGE = 32768, AT_Q = 98304, AT_EX = 131072, AT_EE = AT_EX + 16384, AT_HF = 150528;
constexpr int G2_SLOT = 49152, G2_NCH = 18;
constexpr int LDS_CTRL = 150016, LDS_SINK = 152576, LDS_TOTAL = LDS_SINK + 1024;

__device__ __forceinline__ float wave_sum(float v) {
#define ROR(n) v += __uint_as_float(__builtin_amdgcn_update_dpp(0u, __float_as_uint(v), 0x120 + (n), 0xf, 0xf, false))
  ROR(8); ROR(4); ROR(2); ROR(1);
#undef ROR
  auto a = __builtin_amdgcn_permlane16_swap(__float_as_uint(v), __float_as_uint(v), false, false);
  v = __uint_as_float(a[0]) + __uint_as_float(a[1]);
  auto b = __builtin_amdgcn_permlane32_swap(__float_as_uint(v), __float_as_uint(v), false, false);
  return __uint_as_float(b[0]) + __uint_as_float(b[1]);
}
__device__ __forceinline__ float max16(const f32x16& p) {
  float a = fmaxf(fmaxf(p[0], p[1]), p[2]), b = fmaxf(fmaxf(p[3], p[4]), p[5]);
  a = fmaxf(fmaxf(a, p[6]), p[7]); b = fmaxf(fmaxf(b, p[8]), p[9]);
  a = fmaxf(fmaxf(a, p[10]), p[11]); b = fmaxf(fmaxf(b, p[12]), p[13]);
  a = fmaxf(fmaxf(a, p[14]), p[15]);
  float m = fmaxf(a, b);
  auto rr = __builtin_amdgcn_permlane32_swap(__float_as_uint(m), __float_as_uint(m), false, false);
  return fmaxf(__uint_as_float(rr[0]), __uint_as_float(rr[1]));
}
__device__ __forceinline__ f32x16 splat16(float v) { f32x16 r;
#pragma unroll
  for (int i = 0; i < 16; ++i) r[i] = v;
  return r; }

struct Params {
  const bf16_t *x, *wqkv, *wg, *wu, *wo, *wd;
  bf16_t *xn, *Qb, *Kb, *Vb, *AH, *out;
  float* part; u64* flags;
};

__global__ __launch_bounds__(512, 2) void fused_kernel(Params P) {
  extern __shared__ __attribute__((aligned(16))) char smem[];
  const int w = __builtin_amdgcn_readfirstlane(threadIdx.x >> 6);
  const int g = w >> 2, w4 = w & 3;
  const int bid = blockIdx.x, xcd = bid & 7, jj = bid >> 3;
  const int cg = xcd * 4 + (jj & 3), rt = jj >> 2;
  const unsigned lds0 = (unsigned)(uintptr_t)smem;
  const lds_cptr L = (lds_cptr)smem;
  LAS unsigned* ctrl = (LAS unsigned*)((LAS char*)smem + LDS_CTRL);
  u64 tok;

  u32x4 hv[8];
  bf16_t* hp0; bf16_t* hp1;
#define HSTORE(i) st16wt((i) < 4 ? hp0 + (size_t)(i) * 16 * KA : hp1 + ((w4 & 1) ? (size_t)((i) & 1) * 32 * KA : (size_t)(((i) - 2) & 3) * 16 * KA), hv[i])
  const int h = cg, qb = rt;
  const char* kg = (const char*)(P.Kb + (size_t)h * HSTR) + w4 * 2048;
  const char* vg = (const char*)(P.Vb + (size_t)h * HSTR) + w4 * 2048;
#define DMA_ST(st, KH) do { const unsigned d_ = lds0 + ((st) & 3) * AT_STAGE + (KH) * 8192 + w4 * 2048; \
    const int o_ = ((rt + (st)) & 7) * 16384 + (KH) * 8192; unsigned keep_; \
    asm volatile("s_mov_b32 %0, m0\n\ts_mov_b32 m0, %4\n\ts_nop 0\n\t" \
                 "global_load_lds_dwordx4 %1, %2 sc1\n\tglobal_load_lds_dwordx4 %1, %2 offset:1024 sc1\n\t" \
                 "s_mov_b32 m0, %5\n\ts_nop 0\n\t" \
                 "global_load_lds_dwordx4 %1, %3 sc1\n\tglobal_load_lds_dwordx4 %1, %3 offset:1024 sc1\n\ts_mov_b32 m0, %0" \
                 : "=&s"(keep_) : "v"(voff), "s"(kg + o_), "s"(vg + o_), "s"(d_), "s"(d_ + 16384u) : "memory"); } while (0)

  {
    const int lane = threadIdx.x & 63;
    const int wm = 2 * g + (w4 >> 1), wn = w4 & 1;
    STAMP(1, 0);
    const int row0 = rt * 256;
    u32x4 xa, xb; u64 ep;
    asm volatile("global_load_dwordx4 %0, %3, off\n\tglobal_load_dwordx4 %1, %3, off offset:1024\n\tglobal_load_dwordx2 %2, %4, off sc1"
                 : "=&v"(xa), "=&v"(xb), "=&v"(ep) : "v"(P.x + (size_t)(bid * 8 + w) * DM + lane * 8), "v"(P.flags) : "memory");
    if (threadIdx.x < 8) ctrl[threadIdx.x] = 0u;
    asm volatile("s_waitcnt vmcnt(0) lgkmcnt(0)\n\ts_barrier" : "+v"(xa), "+v"(xb), "+v"(ep) :: "memory");
    STAMP(0, 0);
    tok = (((u64)(unsigned)__builtin_amdgcn_readfirstlane((unsigned)(ep >> 32)) << 32) | (unsigned)__builtin_amdgcn_readfirstlane((unsigned)ep)) + 1ull;
    {
      float v[16];
#pragma unroll
      for (int i = 0; i < 4; ++i) { v[2 * i] = bf_lo(xa[i]); v[2 * i + 1] = bf_hi(xa[i]); v[8 + 2 * i] = bf_lo(xb[i]); v[9 + 2 * i] = bf_hi(xb[i]); }
      float sm = 0.f;
#pragma unroll
      for (int i = 0; i < 16; ++i) sm += v[i];
      const float mean = wave_sum(sm) * (1.f / DM);
      float q = 0.f;
#pragma unroll
      for (int i = 0; i < 16; ++i) { const float d = v[i] - mean; q += d * d; }
      const float mb = rbf(mean), vb = rbf(wave_sum(q) * (1.f / DM));
      const float inv = 1.0f / rbf(sqrtf(rbf(vb + 1e-5f)));
      u32x4 oa, ob;
#pragma unroll
      for (int i = 0; i < 4; ++i) {
        const unsigned da = cvtpk(v[2 * i] - mb, v[2 * i + 1] - mb), db = cvtpk(v[8 + 2 * i] - mb, v[9 + 2 * i] - mb);
        oa[i] = cvtpk(bf_lo(da) * inv, bf_hi(da) * inv);
        ob[i] = cvtpk(bf_lo(db) * inv, bf_hi(db) * inv);
      }
      bf16_t* op = P.xn + (size_t)(bid * 8 + w) * DM + lane * 8;
      st16wt(op, oa); st16wt(op + 512, ob);
      STAMP(0, 4);
      publish(ctrl + 0, P.flags + FL_OFF + bid, tok, lane);
      STAMP(0, 1);
    }
    unsigned avoff0 = (unsigned)((lane >> 3) * 2048 + (((lane & 7) ^ ((lane >> 4) + 2)) * 16)), avoff1 = avoff0 ^ 64u;
    const char* abase = (const char*)P.xn + (size_t)(row0 + 32 * w4) * 2048;
#define DMA_A(hh, t) do { _Pragma("unroll") for (int _i = 0; _i < 4; ++_i) \
    glds16c(abase + (size_t)(hh) * 262144 + _i * 16384 + (t) * 128, (_i & 1) ? avoff1 : avoff0, \
            lds0 + G1_AOFF + ((t) & 1) * 32768 + (hh) * 16384 + (4 * w4 + _i) * 1024); } while (0)
    const char* bptr[4]; unsigned bld[4]; unsigned bdst[4];
#pragma unroll
    for (int i = 0; i < 4; ++i) {
      const int b = (i == 0) ? (g ? 16 + w4 : 4 * w4) : 4 * w4 + i;
      const int region = b >= 10 ? 1 : 0, bi = b - 10 * region;
      const int Pc = bi * 64 + lane;
      const int k = Pc / 20, pc = Pc - 20 * k, f = pc >> 1, e8 = (pc & 1) * 8;
      const bool isq = f >= 8 || (region && (f & 3) == 3);
      const int qf = f >= 8 ? (region ? f - 4 : f - 8) : 2 + (f >> 2);
      const bf16_t* mat = isq ? P.wqkv : (f >= 4 ? P.wu : P.wg);
      const int ld = isq ? NQKV : FF;
      const int col = e8 + (isq ? (qf >> 1) * DM + 32 * cg + 16 * (qf & 1) : 112 * cg + 64 * region + 16 * (f & 3));
      bptr[i] = (const char*)(mat + (size_t)k * ld + col);
      bld[i] = (unsigned)ld * 64u;
      bdst[i] = lds0 + G1_BOFF + region * G1_BREG + bi * 1024 + (bi >= 5 ? 32 : 0);
    }
#define DMA_B(i, s) glds16(bptr[i], bdst[i] + ((s) & 3) * G1_BSLOT)
#define ADV_B() do { ADV64(bptr[0], bld[0]); ADV64(bptr[1], bld[1]); ADV64(bptr[2], bld[2]); ADV64(bptr[3], bld[3]); } while (0)
    if (g == 0) {
      DMA_B(0, 0); DMA_B(1, 0); DMA_B(2, 0); DMA_B(3, 0); ADV_B();
      DMA_B(0, 1); ADV_B();
      DMA_B(0, 2); DMA_B(1, 2); DMA_B(2, 2); DMA_B(3, 2); ADV_B();
    } else {
      DMA_B(0, 0); ADV_B();
      DMA_B(0, 1); DMA_B(1, 1); DMA_B(2, 1); DMA_B(3, 1); ADV_B();
      DMA_B(0, 2); ADV_B();
      const char* ta;
      if (w4 <= 1) ta = (const char*)(P.AH + (size_t)(lane * 32 + 15 + 16 * w4) * KA + 4608);
      else if (w4 == 2) ta = lane < 48 ? (const char*)(P.Qb + (size_t)(2 * lane + 1) * HSTR - 64)
                                       : (const char*)P.part + (size_t)((lane - 48) * 16 + xcd) * 65536 + (xcd & 3) * 16384;
      else ta = lane < 8 ? (const char*)P.wo + lane * 262144 : (lane < 36 ? (const char*)P.wd + (lane - 8) * 262144
                         : (lane < 52 ? (const char*)P.out + (lane - 36) * 262144 : (const char*)P.x + (lane - 52) * 262144));
      glds4c(ta, lds0 + LDS_SINK);
    }
    const int fr = lane & 15, fq = lane >> 4, kq = ((fq & 1) << 1) | (fq >> 1);
    const int aoff0 = G1_AOFF + (64 * wm + fr) * 128 + ((kq ^ (((fr >> 1) + 2) & 7)) * 16);
    lds_cptr pa0 = L + aoff0, pa1 = L + (aoff0 ^ 64);
    lds_cptr pb0 = L + (G1_BOFF + (8 * kq + (fr >> 2)) * 320 + (fq & 1) * 32 + 8 * (lane & 3) + G1_BREG * wn);
    lds_cptr pb1 = pb0 + 2 * G1_BSLOT;
    asm volatile("" : "+v"(pa0), "+v"(pa1), "+v"(pb0), "+v"(pb1), "+v"(avoff0), "+v"(avoff1));
    f32x4 acc[4][10];
#pragma unroll
    for (int m = 0; m < 4; ++m)
#pragma unroll
      for (int j = 0; j < 10; ++j) { acc[m][j] = (f32x4){0.f, 0.f, 0.f, 0.f}; PIN(acc[m][j]); }
    bf16x8 af[4], bfr[10];
    if (g == 0) {
      wg_wait(w, lane, P.flags + FL_OFF + rt * 32 + (lane & 31), tok, ctrl + 3);
      STAMP(0, 2);
      DMA_A(0, 0); DMA_A(1, 0); DMA_A(0, 1);
      STAMP(0, 3);
      WAITBAR(4);
    } else { WAITBAR(1); BARRIER(); }
    STAMP(1, 1);
#define STEP(U, ISSUE, WN, LASTBAR, RELAX) do { \
    { const lds_cptr pa = ((U) & 1) ? pa1 : pa0; const lds_cptr pb = (((U) & 2) ? pb1 : pb0) + ((U) & 1) * G1_BSLOT; \
      _Pragma("unroll") for (int m = 0; m < 4; ++m) af[m] = lds_rd128(pa + ((U) >> 1) * 32768 + m * 2048); \
      _Pragma("unroll") for (int j = 0; j < 10; ++j) bfr[j] = cat8(lds_tr(pb + j * 32), lds_tr(pb + j * 32 + 1280)); } \
    if (ISSUE) { DMA_B(0, (U) + 3); \
      if (g == ((U) & 1)) { DMA_A(1 - ((U) & 1), 2 * it + ((U) >> 1) + 1 + ((U) & 1)); } \
      else { DMA_B(1, (U) + 3); DMA_B(2, (U) + 3); DMA_B(3, (U) + 3); } } \
    if ((RELAX) && g) { WAITBAR(63); } else { WAITBAR(WN); } \
    _Pragma("unroll") for (int j = 0; j < 10; ++j) { _Pragma("unroll") for (int m = 0; m < 4; ++m) MFMA16A(acc[m][j], bfr[j], af[m]); \
      if ((ISSUE) && (j & 1) && j < 8) ADV64(bptr[j >> 1], bld[j >> 1]); } \
    if (LASTBAR) BARRIER(); } while (0)
    int it = 0;
#pragma unroll 1
    for (; it < 7; ++it) { STEP(0, true, 9, true, it == 0); STEP(1, true, 9, true, it == 0); STEP(2, true, 9, true, false); STEP(3, true, 9, true, false); }
    STEP(0, true, 9, true, false); STEP(1, false, 4, true, false); STEP(2, false, 0, true, false); STEP(3, false, 0, g == 0, false);
#undef STEP
    asm volatile("s_nop 7\n\ts_nop 7" ::: "memory");
    SB();
    STAMP(1, 2);
#undef DMA_A
#undef DMA_B
#undef ADV_B
    int tz = threadIdx.x; asm volatile("" : "+v"(tz));
    const int fr2 = tz & 15, fq2 = (tz >> 4) & 3, sel1 = fq2 & 1, c8 = (fq2 >> 1) * 8;
    const float qscale = 0.17677669529663687f * 1.4426950408889634f;
#define SWAP16(A, B, OUT) do { auto r0_ = __builtin_amdgcn_permlane16_swap(A.x, B.x, false, false); \
    auto r1_ = __builtin_amdgcn_permlane16_swap(A.y, B.y, false, false); OUT = (u32x4){r0_[0], r1_[0], r0_[1], r1_[1]}; } while (0)
#define SILU4(m, j, OUT) do { float hv_[4]; _Pragma("unroll") for (int r = 0; r < 4; ++r) { \
      const float tg_ = acc[m][j][r], tu_ = acc[m][(j) + 4][r]; \
      hv_[r] = (tg_ * tu_) * __builtin_amdgcn_rcpf(1.f + __builtin_amdgcn_exp2f(-1.4426950408889634f * tg_)); } \
    OUT = (u32x2){cvtpk(hv_[0], hv_[1]), cvtpk(hv_[2], hv_[3])}; } while (0)
#define PK4(V, SC, OUT) do { const f32x4 v_ = (V) * (SC); OUT = (u32x2){cvtpk(v_[0], v_[1]), cvtpk(v_[2], v_[3])}; } while (0)
    const int rowb = row0 + 64 * wm + fr2;
    const int rl = 64 * wm + fr2;
    __builtin_amdgcn_s_setprio(3);
    if (wn) {
      bf16_t* kd = P.Kb + (size_t)h * HSTR + (((rowb >> 5) * 2 + sel1) * 64 + (rowb & 31) + 4 * c8) * 8;
      bf16_t* vd = P.Vb + (size_t)h * HSTR + rowb * 32 + 16 * sel1 + c8;
      LAS char* kl = (LAS char*)smem + (((rl >> 5) * 2 + sel1) * 64 + (rl & 31) + 4 * c8) * 16;
      LAS char* vl = (LAS char*)smem + 16384 + rl * 64 + (16 * sel1 + c8) * 2;
#pragma unroll
      for (int m = 0; m < 4; ++m) {
        u32x2 a_, b_; u32x4 o;
        PK4(acc[m][3], 1.f, a_); PK4(acc[m][7], 1.f, b_); SWAP16(a_, b_, o);
        st16wt(kd + (m & 1) * 128 + (m >> 1) * 1024, o); *(LAS u32x4*)(kl + (m & 1) * 256 + (m >> 1) * 2048) = o;
        PK4(acc[m][8], 1.f, a_); PK4(acc[m][9], 1.f, b_); SWAP16(a_, b_, o);
        st16wt(vd + m * 512, o); *(LAS u32x4*)(vl + m * 1024) = o;
      }
    } else {
      LAS char* ql = (LAS char*)smem + AT_Q + rl * 64 + (16 * sel1 + c8) * 2;
#pragma unroll
      for (int m = 0; m < 4; ++m) {
        u32x2 a_, b_; u32x4 o; PK4(acc[m][8], qscale, a_); PK4(acc[m][9], qscale, b_); SWAP16(a_, b_, o);
        *(LAS u32x4*)(ql + m * 1024) = o;
      }
    }
    __builtin_amdgcn_s_setprio(0);
    SB();
    bf16_t* hd = P.AH + (size_t)rowb * KA + DM + 112 * cg + 64 * wn + c8;
    hp0 = hd + 16 * sel1;
    hp1 = wn ? hd + 32 + (size_t)sel1 * 16 * KA : hd + 32 + 16 * sel1;
#pragma unroll
    for (int m = 0; m < 2; ++m) { u32x2 a_, b_; SILU4(m, 0, a_); SILU4(m, 1, b_); SWAP16(a_, b_, hv[m]); PIN(hv[m]); }
    STAMP(1, 4);
    publish(ctrl + 1, P.flags + FA_OFF + cg * 8 + rt, tok, tz & 63);
#pragma unroll
    for (int m = 2; m < 4; ++m) { u32x2 a_, b_; SILU4(m, 0, a_); SILU4(m, 1, b_); SWAP16(a_, b_, hv[m]); }
    if (wn == 0) {
#pragma unroll
      for (int m = 0; m < 4; ++m) { u32x2 a_, b_; SILU4(m, 2, a_); SILU4(m, 3, b_); SWAP16(a_, b_, hv[4 + ((m + 2) & 3)]); }
    } else {
#pragma unroll
      for (int mp = 0; mp < 2; ++mp) { u32x2 a_, b_; SILU4(2 * mp, 2, a_); SILU4(2 * mp + 1, 2, b_); SWAP16(a_, b_, hv[4 + mp]); }
      hv[6] = hv[4]; hv[7] = hv[5];
    }
    STAMP(1, 3);
    STAMP(2, 0);
#undef SWAP16
#undef SILU4
#undef PK4
  }

  const int k4 = xcd & 3, rt2 = (xcd >> 2) * 4 + (jj >> 3), ct2 = jj & 7;
  int c_issued = 0;
#define G2_K(c) ((c) < 14 ? DM + (14 * k4 + (c)) * 64 : (4 * k4 + (c) - 14) * 64)
#define G2_DMA_A(c, sl) do { \
    const char* ab_ = (const char*)P.AH + (size_t)(rt2 * 256 + 64 * w4) * (KA * 2) + G2_K(c) * 2; \
    const unsigned d_ = lds0 + (sl) * G2_SLOT + w4 * 8192; unsigned keep_; \
    asm volatile("s_mov_b32 %0, m0\n\ts_mov_b32 m0, %7\n\ts_nop 0\n\t" \
                 "global_load_lds_dwordx4 %1, %5 sc1\n\tglobal_load_lds_dwordx4 %2, %5 offset:1024 sc1\n\t" \
                 "global_load_lds_dwordx4 %3, %5 offset:2048 sc1\n\tglobal_load_lds_dwordx4 %4, %5 offset:3072 sc1\n\t" \
                 "s_mov_b32 m0, %8\n\ts_nop 0\n\t" \
                 "global_load_lds_dwordx4 %1, %6 sc1\n\tglobal_load_lds_dwordx4 %2, %6 offset:1024 sc1\n\t" \
                 "global_load_lds_dwordx4 %3, %6 offset:2048 sc1\n\tglobal_load_lds_dwordx4 %4, %6 offset:3072 sc1\n\ts_mov_b32 m0, %0" \
                 : "=&s"(keep_) : "v"(avo[0]), "v"(avo[1]), "v"(avo[2]), "v"(avo[3]), "s"(ab_), "s"(ab_ + 32 * KA * 2), "s"(d_), "s"(d_ + 4096u) : "memory"); } while (0)
#define G2_DMA_B(c, sl) do { const int kg_ = G2_K(c); \
    const char* bb_ = (kg_ < DM ? (const char*)P.wo + (size_t)kg_ * 2048 : (const char*)P.wd + (size_t)(kg_ - DM) * 2048) + ct2 * 256 + w4 * 32768; \
    const unsigned d_ = lds0 + (sl) * G2_SLOT + 32768 + w4 * 4096; unsigned keep_; \
    asm volatile("s_mov_b32 %0, m0\n\ts_mov_b32 m0, %6\n\ts_nop 0\n\t" \
                 "global_load_lds_dwordx4 %1, %5\n\tglobal_load_lds_dwordx4 %2, %5 offset:1024\n\t" \
                 "global_load_lds_dwordx4 %3, %5 offset:2048\n\tglobal_load_lds_dwordx4 %4, %5 offset:3072\n\ts_mov_b32 m0, %0" \
                 : "=&s"(keep_) : "v"(bvo[0]), "v"(bvo[1]), "v"(bvo[2]), "v"(bvo[3]), "s"(bb_), "s"(d_) : "memory"); } while (0)
#define G2_DMA(c, sl) do { G2_DMA_A(c, sl); G2_DMA_B(c, sl); } while (0)
#define G2_OFFS(lane) unsigned avo[4], bvo[4]; _Pragma("unroll") for (int _i = 0; _i < 4; ++_i) { \
    avo[_i] = ((unsigned)(((lane) >> 3) * (KA * 2) + ((((lane) & 7) ^ (((lane) >> 4) + 2)) * 16)) ^ ((_i & 1) * 64u)) + _i * (8 * KA * 2 - 1024); \
    bvo[_i] = (unsigned)(((lane) >> 4) * 2048 + ((((lane) & 15) ^ (((lane) >> 4) << 1) ^ ((w4 & 1) << 3)) * 16)) + _i * (8192 - 1024); }

  {
    int tb = threadIdx.x; asm volatile("" : "+v"(tb));
    const int lane = tb & 63;
    const int qg = w4, kh = g;
    const int hi = lane >> 5;
    const unsigned voff = lane * 16;
    const short one = (((lane >> 4) & 1) == ((lane >> 2) & 1)) ? (short)0x3f80 : (short)0;
    const bf16x8 sel = {one, one, one, one, one, one, one, one};
    volatile LAS unsigned* flag = (volatile LAS unsigned*)(ctrl + 2);
    const int gq = lane >> 4;
    const int koff = kh * 8192 + lane * 16;
    const int voffl = 16384 + kh * 8192 + (4 * (gq >> 1) + ((lane & 15) >> 2)) * 64 + (gq & 1) * 32 + (lane & 3) * 8;
    const f32x16 zero16 = splat16(0.f);
    f32x16 oA = zero16, oB = zero16;
    f32x4 lA = {0.f, 0.f, 0.f, 0.f}, lB = {0.f, 0.f, 0.f, 0.f};
    float mA, mB;
    const unsigned* hflag = (const unsigned*)(P.flags + FH_OFF + (8 * k4 + ((lane >> 1) & 7)) * 8 + rt2) + (lane & 1);
    bf16x8 qa0, qa1, qb0, qb1;

#define LDK(K0, K1, p) do { K0 = lds_rd128(p); K1 = lds_rd128((p) + 1024); } while (0)
#define LDV(V0, V1, p) do { V0 = cat8(lds_tr(p), lds_tr((p) + 512)); V1 = cat8(lds_tr((p) + 1024), lds_tr((p) + 1536)); } while (0)
#define BF(x) __builtin_bit_cast(bf16x8, x)
    {
      const unsigned* kvflag = (const unsigned*)(P.flags + FA_OFF + cg * 8 + ((lane >> 1) & 7)) + (lane & 1);
      const unsigned tokh = (lane & 1) ? (unsigned)(tok >> 32) : (unsigned)tok;
#define FLAGS_OK() __all(*(volatile LAS unsigned*)((LAS char*)smem + AT_HF + w * 256 + lane * 4) == tokh)
      LGBAR();
      STAMP(2, 4);
      glds4c(kvflag, lds0 + AT_HF + w * 256);
      { const lds_cptr qp = L + AT_Q + qg * 4096 + (lane & 31) * 64 + hi * 16;
        qa0 = lds_rd128(qp); qa1 = lds_rd128(qp + 32); qb0 = lds_rd128(qp + 2048); qb1 = lds_rd128(qp + 2080); }
      bf16x8 kx0, kx1, ky0, ky1, vx0, vx1, vy0, vy1;
      LDK(kx0, kx1, L + koff);
      LDK(ky0, ky1, L + koff + 2048);
      LDV(vy0, vy1, L + voffl);
      f32x16 sA = MFMA32(kx0, qa0, zero16); sA = MFMA32(kx1, qa1, sA);
      f32x16 sB = MFMA32(kx0, qb0, zero16); sB = MFMA32(kx1, qb1, sB);
      mA = max16(sA); mB = max16(sB);
      f32x16 nA = splat16(-mA), nB = splat16(-mB);
      PIN(nA); PIN(nB);
      sA = MFMA32(kx0, qa0, nA); sA = MFMA32(kx1, qa1, sA);
      u32x4 pA0, pA1, pB0 = {0u, 0u, 0u, 0u}, pB1 = {0u, 0u, 0u, 0u};
      STAMP(2, 1);
#define E4(S, i) do { S[i] = EXP2(S[i]); S[i + 1] = EXP2(S[i + 1]); S[i + 2] = EXP2(S[i + 2]); S[i + 3] = EXP2(S[i + 3]); } while (0)
#define SLOT(SC, PC0, PC1, SN, K0, K1, Q0, Q1, NM, PP0, PP1, V0, V1, O, LS, LOADS) do { \
    SN = MFMA32(K0, Q0, NM); SB(); E4(SC, 0); PIN(SC); SB(); \
    SN = MFMA32(K1, Q1, SN); SB(); LOADS; SB(); E4(SC, 4); PIN(SC); SB(); \
    O = MFMA32(V0, BF(PP0), O); SB(); PC0[0] = cvtpk(SC[0], SC[1]); PC0[1] = cvtpk(SC[2], SC[3]); \
      SC[8] = EXP2(SC[8]); SC[9] = EXP2(SC[9]); SC[10] = EXP2(SC[10]); PIN(SC); PIN(PC0); SB(); \
    LS = MFMA16(sel, BF(PP0), LS); SB(); PC0[2] = cvtpk(SC[4], SC[5]); PC0[3] = cvtpk(SC[6], SC[7]); SC[11] = EXP2(SC[11]); PIN(SC); PIN(PC0); SB(); \
    O = MFMA32(V1, BF(PP1), O); SB(); E4(SC, 12); PIN(SC); SB(); \
    LS = MFMA16(sel, BF(PP1), LS); SB(); PC1[0] = cvtpk(SC[8], SC[9]); PC1[1] = cvtpk(SC[10], SC[11]); \
      PC1[2] = cvtpk(SC[12], SC[13]); PC1[3] = cvtpk(SC[14], SC[15]); PIN(PC1); SB(); } while (0)
#define SUB(KC0, KC1, KN0, KN1, VC0, VC1, VP0, VP1, vpu, kp2) do { \
    SLOT(sA, pA0, pA1, sB, KC0, KC1, qb0, qb1, nB, pB0, pB1, VP0, VP1, oB, lB, LDV(VC0, VC1, vpu)); \
    SLOT(sB, pB0, pB1, sA, KN0, KN1, qa0, qa1, nA, pA0, pA1, VC0, VC1, oA, lA, LDK(KC0, KC1, kp2)); } while (0)
      lds_cptr kb0 = L + koff, kb1 = L + koff + 2 * AT_STAGE, vb0 = L + voffl, vb1 = L + voffl + 2 * AT_STAGE;
      PIN(kb0); PIN(kb1); PIN(vb0); PIN(vb1);
      bool kvok = false;
#define KP(st) ((((st) & 2) ? kb1 : kb0) + ((st) & 1) * AT_STAGE)
#define VP(st) ((((st) & 2) ? vb1 : vb0) + ((st) & 1) * AT_STAGE)
#define HS(st, i) do { if ((st) == 1 || (st) == 2) HSTORE((4 * ((st) - 1) + (i)) & 7); } while (0)
#define VMW0() asm volatile("s_waitcnt vmcnt(0)" ::: "memory")
#define STAGE(st, WN, EXTRA) do { \
        SUB(kx0, kx1, ky0, ky1, vx0, vx1, vy0, vy1, VP(st), KP(st) + 4096); \
        HS(st, 0); \
        if ((st) == 0) { VMW0(); kvok = FLAGS_OK(); if (kvok) { DMA_ST(1, kh); DMA_ST(2, kh); } else glds4c(kvflag, lds0 + AT_HF + w * 256); } \
        SUB(ky0, ky1, kx0, kx1, vy0, vy1, vx0, vx1, VP(st) + 2048, KP(st) + 6144); \
        if ((st) == 0 && !kvok) { VMW0(); if (!FLAGS_OK()) wait_flags(P.flags + FA_OFF + cg * 8 + (lane & 7), tok); DMA_ST(1, kh); DMA_ST(2, kh); } \
        VMBAR(WN); \
        HS(st, 1); \
        EXTRA; \
        if ((st) < 5) {   \
          const int pc = (st) * 8 + w, kb = bid * 36 + (pc < 36 ? pc : 35); \
          glds16s(kb < 2048 ? (const char*)P.wo + (size_t)kb * 1024 : (const char*)P.wd + (size_t)(kb - 2048) * 1024, voff, lds0 + LDS_SINK); \
          DMA_ST((st) + 3, kh); } \
        SUB(kx0, kx1, ky0, ky1, vx0, vx1, vy0, vy1, VP(st) + 4096, KP((st) + 1)); \
        HS(st, 2); \
        SUB(ky0, ky1, kx0, kx1, vy0, vy1, vx0, vx1, VP(st) + 6144, KP((st) + 1) + 2048); \
        HS(st, 3); } while (0)
      STAGE(0, 4, ); STAGE(1, 6, ); STAGE(2, 10, ); STAGE(3, 11, );
      STAGE(4, 5, if (w == 0 && lane == 0) set_flag(P.flags + FH_OFF + cg * 8 + rt, tok));
      STAGE(5, 5, if (g == 1) glds4c(hflag, lds0 + AT_HF + w * 256));
      STAGE(6, 0, if (g == 1 && FLAGS_OK()) { G2_OFFS(lane); G2_DMA(0, 0); c_issued = 2; });
      STAGE(7, 0, if (c_issued == 2) { G2_OFFS(lane); G2_DMA(1, 1); c_issued = 3; });
#undef VMW0
#undef FLAGS_OK
#undef STAGE
#undef HS
#undef KP
#undef VP
      oB = MFMA32(vy0, BF(pB0), oB); lB = MFMA16(sel, BF(pB0), lB);
      oB = MFMA32(vy1, BF(pB1), oB); lB = MFMA16(sel, BF(pB1), lB);
#undef SUB
#undef SLOT
#undef E4
    }
    STAMP(2, 2);
    LAS u32x4* xw = (LAS u32x4*)((LAS char*)smem + AT_EX) + w4 * 256;
    LAS float* ew = (LAS float*)((LAS char*)smem + AT_EE) + w4 * 128;
    float eA, eB;
#define FIN1(FIRST) do { \
        \
      float chk = 0.f; \
      _Pragma("unroll") for (int r = 0; r < 16; ++r) { chk = __builtin_fmaf(oA[r], 0.f, chk); chk = __builtin_fmaf(oB[r], 0.f, chk); } \
      const bool bad = !(lA[0] < 1.0e36f) || !(lB[0] < 1.0e36f) || !(chk == 0.f); \
      if (FIRST) { if (__any(bad) && lane == 0) *flag = 1u; \
                   if (c_issued == 3 && lane == 0) __hip_atomic_fetch_add(ctrl + 5, 1u, __ATOMIC_RELAXED, __HIP_MEMORY_SCOPE_WORKGROUP); } \
      oA *= __builtin_amdgcn_rcpf(lA[0]); oB *= __builtin_amdgcn_rcpf(lB[0]); \
      eA = mA + __builtin_amdgcn_logf(lA[0]); eB = mB + __builtin_amdgcn_logf(lB[0]);     \
      if (g == 0) { \
        _Pragma("unroll") for (int r = 0; r < 2; ++r) { \
          xw[r * 64 + lane] = (u32x4){cvtpk(oA[8 * r], oA[8 * r + 1]), cvtpk(oA[8 * r + 2], oA[8 * r + 3]), cvtpk(oA[8 * r + 4], oA[8 * r + 5]), cvtpk(oA[8 * r + 6], oA[8 * r + 7])}; \
          xw[128 + r * 64 + lane] = (u32x4){cvtpk(oB[8 * r], oB[8 * r + 1]), cvtpk(oB[8 * r + 2], oB[8 * r + 3]), cvtpk(oB[8 * r + 4], oB[8 * r + 5]), cvtpk(oB[8 * r + 6], oB[8 * r + 7])}; } \
        ew[lane] = eA; ew[64 + lane] = eB; } } while (0)
#define SAFE_UNIT(Q0, Q1, M, O, LS) do { \
            f32x16 s_ = MFMA32(k0, Q0, zero16); s_ = MFMA32(k1, Q1, s_); \
            const float mn_ = fmaxf(M, max16(s_)); const float al_ = EXP2(M - mn_); M = mn_; \
            O *= al_; LS *= al_; u32x4 p0_, p1_; \
            _Pragma("unroll") for (int r = 0; r < 4; ++r) { p0_[r] = cvtpk(EXP2(s_[2 * r] - mn_), EXP2(s_[2 * r + 1] - mn_)); \
              p1_[r] = cvtpk(EXP2(s_[8 + 2 * r] - mn_), EXP2(s_[9 + 2 * r] - mn_)); } \
            O = MFMA32(v0, BF(p0_), O); LS = MFMA16(sel, BF(p0_), LS); O = MFMA32(v1, BF(p1_), O); LS = MFMA16(sel, BF(p1_), LS); } while (0)
#define SAFE_PASS() do { \
        WAITBAR(0); \
        DMA_ST(0, kh); DMA_ST(1, kh); DMA_ST(2, kh); \
        oA = zero16; oB = zero16; lA = (f32x4){0.f, 0.f, 0.f, 0.f}; lB = lA; \
        mA = -3.0e38f; mB = -3.0e38f; \
        _Pragma("unroll 1") for (int st = 0; st < 8; ++st) { \
          if (st < 6) WAITBAR(8); else if (st == 6) WAITBAR(4); else WAITBAR(0); \
          if (st < 5) DMA_ST(st + 3, kh); \
          _Pragma("unroll 1") for (int u = 0; u < 4; ++u) { \
            bf16x8 k0, k1, v0, v1; \
            LDK(k0, k1, L + (st & 3) * AT_STAGE + koff + u * 2048); \
            LDV(v0, v1, L + (st & 3) * AT_STAGE + voffl + u * 2048); \
            SAFE_UNIT(qa0, qa1, mA, oA, lA); \
            SAFE_UNIT(qb0, qb1, mB, oB, lB); } } \
        FIN1(false); LGBAR(); } while (0)
    FIN1(true);
    STAMP(2, 3);

    STAMP(3, 0);
    {
      const int wm = w4 >> 1, wn = w4 & 1;
      const int fr = lane & 15, fq = lane >> 4, kq = ((fq & 1) << 1) | (fq >> 1), q4 = fr >> 2;
      const int aoff0 = (128 * wm + fr) * 128 + ((kq ^ (((fr >> 1) + 2) & 7)) * 16);
      const int boff0 = 32768 + (8 * kq + q4) * 256 + 8 * (lane & 3);
      const int bsw = q4 | ((fq & 1) << 2);
      lds_cptr pa[2], pb[4];
      pa[0] = L + aoff0; pa[1] = L + (aoff0 ^ 64); PIN(pa[0]); PIN(pa[1]);
#pragma unroll
      for (int n = 0; n < 4; ++n) { pb[n] = L + (boff0 + (((4 * wn + n) ^ bsw) * 32)); PIN(pb[n]); }
      bf16x8 fa0[8], fb0[4], fa1[8], fb1[4];
#define RD(i, KS, FA, FB) do { if ((i) < 4) FB[(i) & 3] = cat8(lds_tr(pb[(i) & 3] + (KS) * 8192), lds_tr(pb[(i) & 3] + (KS) * 8192 + 1024)); \
      else FA[((i) - 4) & 7] = lds_rd128(pa[KS] + (((i) - 4) & 7) * 2048); } while (0)
#define RD12(KS, FA, FB) do { RD(0, KS, FA, FB); RD(1, KS, FA, FB); RD(2, KS, FA, FB); RD(3, KS, FA, FB); RD(4, KS, FA, FB); RD(5, KS, FA, FB); \
      RD(6, KS, FA, FB); RD(7, KS, FA, FB); RD(8, KS, FA, FB); RD(9, KS, FA, FB); RD(10, KS, FA, FB); RD(11, KS, FA, FB); } while (0)
      if (g == 0) RD12(0, fa0, fb0);
      LGBAR();
      const bool ovf = *flag != 0u, fast = !ovf && *(volatile LAS unsigned*)(ctrl + 5) == 4u;
      if (!fast) {
        if (ovf) SAFE_PASS();
        if (g == 1) {
          G2_OFFS(lane);
          wait_flags(P.flags + FH_OFF + (8 * k4 + (lane & 7)) * 8 + rt2, tok);
          G2_DMA(0, 0); G2_DMA(1, 1);
        }
        VMBAR(12);
        if (g == 0) RD12(0, fa0, fb0);
        LGBAR();
      }
#undef RD12
      if (g == 1) {
#define FIN2(O, E, BL) do { \
      const float e2 = ew[(BL) * 64 + lane]; const u32x4 t0 = xw[(BL) * 128 + lane], t1 = xw[(BL) * 128 + 64 + lane]; \
      const float em = fmaxf(E, e2); float w1 = EXP2(E - em), w2 = EXP2(e2 - em); \
      const float rs = __builtin_amdgcn_rcpf(w1 + w2); w1 *= rs; w2 *= rs; u32x2 pk[4]; \
      _Pragma("unroll") for (int r = 0; r < 4; ++r) { \
        const unsigned ta = r < 2 ? t0[2 * r] : t1[2 * r - 4], tb2 = r < 2 ? t0[2 * r + 1] : t1[2 * r - 3]; \
        pk[r].x = cvtpk(O[4 * r] * w1 + bf_lo(ta) * w2, O[4 * r + 1] * w1 + bf_hi(ta) * w2); \
        pk[r].y = cvtpk(O[4 * r + 2] * w1 + bf_lo(tb2) * w2, O[4 * r + 3] * w1 + bf_hi(tb2) * w2); } \
      bf16_t* op = P.AH + (size_t)(qb * 256 + qg * 64 + (BL) * 32 + (lane & 31)) * KA + h * 32 + 8 * hi; \
      _Pragma("unroll") for (int k = 0; k < 4; k += 2) { \
        auto rx = __builtin_amdgcn_permlane32_swap(pk[k].x, pk[k + 1].x, false, false); \
        auto ry = __builtin_amdgcn_permlane32_swap(pk[k].y, pk[k + 1].y, false, false); \
        st16wt(op + 8 * k, (u32x4){rx[0], ry[0], rx[1], ry[1]}); } } while (0)
      __builtin_amdgcn_s_setprio(3);
      { G2_OFFS(lane); (void)bvo; G2_DMA_A(2, 2); }
      FIN2(oA, eA, 0); FIN2(oB, eB, 1);
#undef FIN2
      }
      SB();
      if (g == 1) {
      int tl = threadIdx.x; asm volatile("" : "+v"(tl));
      const int ll = tl & 63;
      G2_OFFS(ll);
      WAITBAR(12);
      G2_DMA_B(2, 2); G2_DMA(3, 0); VMBAR(12);
      if (w == 4 && ll == 0) set_flag(P.flags + FB_OFF + qb * 32 + h, tok);
      int sl = 1;
#pragma unroll 1
      for (int i = 2; i < G2_NCH - 2; ++i) {
        const u64* of = P.flags + FB_OFF + rt2 * 32 + 8 * k4;
        if (i == 10) glds4c((const unsigned*)(of + ((ll >> 1) & 7)) + (ll & 1), lds0 + AT_HF + w * 256);
        if (i == 12) {
          const unsigned hf = *(volatile LAS unsigned*)((LAS char*)smem + AT_HF + w * 256 + ll * 4);
          if (!__all(hf == ((ll & 1) ? (unsigned)(tok >> 32) : (unsigned)tok))) wait_flags(of + (ll & 7), tok);
        }
        G2_DMA(i + 2, sl); sl = sl == 2 ? 0 : sl + 1; VMBAR(12);
      }
      VMBAR(0);
      if (bid == 0 && w == 4) {
#pragma unroll
        for (int i = 0; i < 4; ++i) wait_flags(P.flags + FA_OFF + i * 64 + ll, tok);
        if (ll == 0) set_flag(P.flags, tok);
      }
      STAMP(3, 3);
      __builtin_amdgcn_endpgm();
      }
      f32x4 acc[8][4];
#pragma unroll
      for (int m = 0; m < 8; ++m)
#pragma unroll
        for (int n = 0; n < 4; ++n) { acc[m][n] = (f32x4){0.f, 0.f, 0.f, 0.f}; PIN(acc[m][n]); }
      asm volatile("s_nop 1" ::: "memory");
      STAMP(3, 1);
#define MM(i, FA, FB) MFMA16A(acc[(i) >> 2][(i) & 3], FB[(i) & 3], FA[(i) >> 2])
#define MR(i, KS, CA, CB, NA, NB) do { MM(i, CA, CB); RD(i, KS, NA, NB); SB(); } while (0)
#define MR4(i, KS, CA, CB, NA, NB) do { MR(i, KS, CA, CB, NA, NB); MR(i + 1, KS, CA, CB, NA, NB); MR(i + 2, KS, CA, CB, NA, NB); MR(i + 3, KS, CA, CB, NA, NB); } while (0)
#define MM4(i, CA, CB) do { MM(i, CA, CB); MM(i + 1, CA, CB); MM(i + 2, CA, CB); MM(i + 3, CA, CB); } while (0)
#define HALF(KS, CA, CB, NA, NB, END) do { \
      MR4(0, KS, CA, CB, NA, NB); MR4(4, KS, CA, CB, NA, NB); MR4(8, KS, CA, CB, NA, NB); \
      MM4(12, CA, CB); MM4(16, CA, CB); MM4(20, CA, CB); MM4(24, CA, CB); MM(28, CA, CB); MM(29, CA, CB); \
      END; MM(30, CA, CB); MM(31, CA, CB); SB(); } while (0)
#define LGW() do { SB(); asm volatile("s_waitcnt lgkmcnt(0)" ::: "memory"); SB(); } while (0)
      HALF(1, fa0, fb0, fa1, fb1, LGBAR());
      int sl = 0;
#pragma unroll 1
      for (int i = 1; i < G2_NCH; ++i) {
        const int dl = sl == 2 ? -2 * G2_SLOT : G2_SLOT; sl = sl == 2 ? 0 : sl + 1;
        pa[0] += dl; pa[1] += dl; pb[0] += dl; pb[1] += dl; pb[2] += dl; pb[3] += dl;
        PIN(pa[0]); PIN(pa[1]); PIN(pb[0]); PIN(pb[1]); PIN(pb[2]); PIN(pb[3]); SB();
        HALF(0, fa1, fb1, fa0, fb0, LGW());
        HALF(1, fa0, fb0, fa1, fb1, LGW(); if (i < G2_NCH - 1) BARRIER());
      }
      MM4(0, fa1, fb1); MM4(4, fa1, fb1); MM4(8, fa1, fb1); MM4(12, fa1, fb1); MM4(16, fa1, fb1); MM4(20, fa1, fb1); MM4(24, fa1, fb1); MM4(28, fa1, fb1);
#undef HALF
#undef LGW
#undef MR4
#undef MM4
#undef MR
#undef MM
#undef RD
      asm volatile("s_nop 7\n\ts_nop 7" ::: "memory");
      SB();
      STAMP(3, 2);
      int te = threadIdx.x; asm volatile("" : "+v"(te));
      const int le = te & 63;
      unsigned* mine = (unsigned*)P.part + (size_t)bid * 16384 + w4 * 1024 + le * 4;
      f32x4 keep[2][4];
#define PKP(A, B) (u32x4){cvtpk(A[0], A[1]), cvtpk(A[2], A[3]), cvtpk(B[0], B[1]), cvtpk(B[2], B[3])}
#pragma unroll
      for (int q = 0; q < 4; ++q) {
        if (q != k4) {
#pragma unroll
          for (int i = 0; i < 4; ++i) st16wt(mine + q * 4096 + i * 256, PKP(acc[2 * q + (i >> 1)][2 * (i & 1)], acc[2 * q + (i >> 1)][2 * (i & 1) + 1]));
        } else {
#pragma unroll
          for (int i = 0; i < 8; ++i) keep[i >> 2][i & 3] = acc[2 * q + (i >> 2)][i & 3];
        }
      }
#undef PKP
      asm volatile("s_waitcnt vmcnt(0)" ::: "memory");
      if (le == 0) set_flag(P.flags + FX_OFF + bid * 4 + w4, tok);
      const int orow = rt2 * 256 + 128 * wm + 32 * k4 + (le & 15), ocol = ct2 * 128 + 64 * wn + 4 * (le >> 4);
      u32x2 xv[2][4];
#pragma unroll
      for (int mm = 0; mm < 2; ++mm)
#pragma unroll
        for (int n = 0; n < 4; ++n) xv[mm][n] = *(const u32x2*)(P.x + (size_t)(orow + 16 * mm) * DM + ocol + 16 * n);
      wait_flags(P.flags + FX_OFF + (bid ^ (1 + le % 3)) * 4 + w4, tok);
      STAMP(3, 4);
      u32x4 pv[3][4];
      const unsigned* t1 = (const unsigned*)P.part + (size_t)(bid ^ 1) * 16384 + k4 * 4096 + w4 * 1024 + le * 4;
      const unsigned* t2 = (const unsigned*)P.part + (size_t)(bid ^ 2) * 16384 + k4 * 4096 + w4 * 1024 + le * 4;
      const unsigned* t3 = (const unsigned*)P.part + (size_t)(bid ^ 3) * 16384 + k4 * 4096 + w4 * 1024 + le * 4;
      asm volatile("global_load_dwordx4 %0, %12, off sc1\n\tglobal_load_dwordx4 %1, %12, off offset:1024 sc1\n\t"
                   "global_load_dwordx4 %2, %12, off offset:2048 sc1\n\tglobal_load_dwordx4 %3, %12, off offset:3072 sc1\n\t"
                   "global_load_dwordx4 %4, %13, off sc1\n\tglobal_load_dwordx4 %5, %13, off offset:1024 sc1\n\t"
                   "global_load_dwordx4 %6, %13, off offset:2048 sc1\n\tglobal_load_dwordx4 %7, %13, off offset:3072 sc1\n\t"
                   "global_load_dwordx4 %8, %14, off sc1\n\tglobal_load_dwordx4 %9, %14, off offset:1024 sc1\n\t"
                   "global_load_dwordx4 %10, %14, off offset:2048 sc1\n\tglobal_load_dwordx4 %11, %14, off offset:3072 sc1\n\t"
                   "s_waitcnt vmcnt(0)"
                   : "=&v"(pv[0][0]), "=&v"(pv[0][1]), "=&v"(pv[0][2]), "=&v"(pv[0][3]), "=&v"(pv[1][0]), "=&v"(pv[1][1]), "=&v"(pv[1][2]), "=&v"(pv[1][3]),
                     "=&v"(pv[2][0]), "=&v"(pv[2][1]), "=&v"(pv[2][2]), "=&v"(pv[2][3]) : "v"(t1), "v"(t2), "v"(t3) : "memory");
#pragma unroll
      for (int i = 0; i < 8; ++i) {
        f32x4 v = keep[i >> 2][i & 3];
#pragma unroll
        for (int q = 0; q < 3; ++q) {
          const unsigned p0 = pv[q][i >> 1][2 * (i & 1)], p1 = pv[q][i >> 1][2 * (i & 1) + 1];
          v += (f32x4){bf_lo(p0), bf_hi(p0), bf_lo(p1), bf_hi(p1)};
        }
        const u32x2 xx = xv[i >> 2][i & 3];
        u32x2 o = {cvtpk(bf_lo(xx.x) + v[0], bf_hi(xx.x) + v[1]), cvtpk(bf_lo(xx.y) + v[2], bf_hi(xx.y) + v[3])};
        *(u32x2*)(P.out + (size_t)(orow + 16 * (i >> 2)) * DM + ocol + 16 * (i & 3)) = o;
      }
      STAMP(3, 3);
    }
#undef LDK
#undef LDV
#undef BF
#undef FIN1
#undef SAFE_UNIT
#undef SAFE_PASS
  }
#undef G2_DMA
#undef G2_DMA_A
#undef G2_DMA_B
#undef G2_K
#undef G2_OFFS
#undef HSTORE
#undef DMA_ST
}

extern "C" void kernel_launch(void* const* d_in, const int* in_sizes, int n_in, void* d_out, int out_size, void* d_ws,
                              size_t ws_size, hipStream_t stream) {
  char* ws = (char*)d_ws;
  Params P;
  P.x = (const bf16_t*)d_in[0];
  P.wqkv = (const bf16_t*)d_in[1]; P.wo = (const bf16_t*)d_in[2];
  P.wg = (const bf16_t*)d_in[4]; P.wu = (const bf16_t*)d_in[5]; P.wd = (const bf16_t*)d_in[6];
  P.xn = (bf16_t*)(ws + WS_XN);
  P.Qb = (bf16_t*)(ws + WS_Q); P.Kb = (bf16_t*)(ws + WS_K); P.Vb = (bf16_t*)(ws + WS_V);
  P.AH = (bf16_t*)(ws + WS_AH); P.out = (bf16_t*)d_out;
  P.part = (float*)(ws + WS_PART); P.flags = (u64*)(ws + WS_FLAGS);
  (void)hipFuncSetAttribute((const void*)fused_kernel, hipFuncAttributeMaxDynamicSharedMemorySize, LDS_TOTAL);
  fused_kernel<<<256, 512, LDS_TOTAL, stream>>>(P);
}
```

```cpp
#include <hip/hip_runtime.h>
#include <hip/hip_bf16.h>
#include <stdint.h>

typedef unsigned short bf16_t;
typedef short bf16x8 __attribute__((ext_vector_type(8)));
typedef short s16x4 __attribute__((ext_vector_type(4)));
typedef float f32x4 __attribute__((ext_vector_type(4)));
typedef float f32x16 __attribute__((ext_vector_type(16)));
typedef float f32x2 __attribute__((ext_vector_type(2)));
typedef __bf16 bf16x2v __attribute__((ext_vector_type(2)));
typedef unsigned u32x4 __attribute__((ext_vector_type(4)));
typedef unsigned u32x2 __attribute__((ext_vector_type(2)));
#define LAS __attribute__((address_space(3)))
typedef LAS const char* lds_cptr;

constexpr int SEQ = 2048, DM = 1024, FF = 3584, NQKV = 3072;
constexpr int KA = 4608 + 64;
constexpr int HSTR = SEQ * 32 + 128;

constexpr size_t WS_FLAGS = 0;
constexpr size_t WS_XN = 1u << 20;
constexpr size_t WS_Q = WS_XN + (size_t)SEQ * DM * 2;
constexpr size_t WS_K = WS_Q + (size_t)32 * HSTR * 2;
constexpr size_t WS_V = WS_K + (size_t)32 * HSTR * 2;
constexpr size_t WS_AH = WS_V + (size_t)32 * HSTR * 2;
constexpr size_t WS_PART = WS_AH + (size_t)SEQ * KA * 2;
typedef unsigned long long u64;
constexpr int FL_OFF = 256, FA_OFF = 512, FH_OFF = 768, FB_OFF = 1024, FX_OFF = 1280;

#define SB() __builtin_amdgcn_sched_barrier(0)
#define STAMP(kid, i) do {} while (0)

__device__ __forceinline__ unsigned cvtpk(float lo, float hi) {
  f32x2 v = {lo, hi};
  bf16x2v b = __builtin_convertvector(v, bf16x2v);
  return __builtin_bit_cast(unsigned, b);
}
__device__ __forceinline__ float bf_lo(unsigned u) { return __uint_as_float(u << 16); }
__device__ __forceinline__ float bf_hi(unsigned u) { return __uint_as_float(u & 0xffff0000u); }
__device__ __forceinline__ float rbf(float x) { return bf_lo(cvtpk(x, x)); }

__device__ __forceinline__ void glds16(const void* g, unsigned lds_dst) {
  unsigned keep;
  asm volatile("s_mov_b32 %0, m0\n\ts_mov_b32 m0, %2\n\ts_nop 0\n\tglobal_load_lds_dwordx4 %1, off\n\ts_mov_b32 m0, %0"
               : "=&s"(keep) : "v"(g), "s"(lds_dst) : "memory");
}
__device__ __forceinline__ void glds16s(const void* sbase, unsigned voff, unsigned lds_dst) {
  unsigned keep;
  asm volatile("s_mov_b32 %0, m0\n\ts_mov_b32 m0, %3\n\ts_nop 0\n\tglobal_load_lds_dwordx4 %1, %2\n\ts_mov_b32 m0, %0"
               : "=&s"(keep) : "v"(voff), "s"(sbase), "s"(lds_dst) : "memory");
}
__device__ __forceinline__ void glds16c(const void* sbase, unsigned voff, unsigned lds_dst) {
  unsigned keep;
  asm volatile("s_mov_b32 %0, m0\n\ts_mov_b32 m0, %3\n\ts_nop 0\n\tglobal_load_lds_dwordx4 %1, %2 sc1\n\ts_mov_b32 m0, %0"
               : "=&s"(keep) : "v"(voff), "s"(sbase), "s"(lds_dst) : "memory");
}
__device__ __forceinline__ void glds4c(const void* g, unsigned lds_dst) {
  unsigned keep;
  asm volatile("s_mov_b32 %0, m0\n\ts_mov_b32 m0, %2\n\ts_nop 0\n\tglobal_load_lds_dword %1, off sc1\n\ts_mov_b32 m0, %0"
               : "=&s"(keep) : "v"(g), "s"(lds_dst) : "memory");
}
__device__ __forceinline__ bf16x8 lds_rd128(lds_cptr p) { return *(const LAS bf16x8*)p; }
__device__ __forceinline__ s16x4 lds_tr(lds_cptr p) { return __builtin_amdgcn_ds_read_tr16_b64_v4i16((LAS s16x4*)p); }
__device__ __forceinline__ bf16x8 cat8(s16x4 a, s16x4 b) { return __builtin_shufflevector(a, b, 0, 1, 2, 3, 4, 5, 6, 7); }
__device__ __forceinline__ void st16wt(void* p, u32x4 v) {
  asm volatile("global_store_dwordx4 %0, %1, off sc1\n\ts_nop 1" :: "v"(p), "v"(v) : "memory");
}
__device__ __forceinline__ void wait_flags(const u64* f, u64 tok) {
  unsigned spins = 0;
  while (!__all(__hip_atomic_load(f, __ATOMIC_RELAXED, __HIP_MEMORY_SCOPE_AGENT) == tok)) {
    __builtin_amdgcn_s_sleep(3);
    if (++spins > (1u << 20)) break;
  }
  asm volatile("" ::: "memory");
}
__device__ __forceinline__ void set_flag(u64* f, u64 tok) { __hip_atomic_store(f, tok, __ATOMIC_RELAXED, __HIP_MEMORY_SCOPE_AGENT); }
__device__ __forceinline__ void publish(LAS unsigned* cnt, u64* flag, u64 tok, int lane) {
  asm volatile("s_waitcnt vmcnt(0)" ::: "memory");
  if (lane == 0) {
    const unsigned old = __hip_atomic_fetch_add(cnt, 1u, __ATOMIC_RELAXED, __HIP_MEMORY_SCOPE_WORKGROUP);
    if (old == 7u) set_flag(flag, tok);
  }
}
__device__ __forceinline__ void wg_wait(int w, int lane, const u64* f, u64 tok, volatile LAS unsigned* go) {
  if (w == 0) { wait_flags(f, tok); if (lane == 0) *go = 1u; }
  else { while (*go == 0u) __builtin_amdgcn_s_sleep(1); asm volatile("" ::: "memory"); }
}

#define MFMA16(a, b, c) __builtin_amdgcn_mfma_f32_16x16x32_bf16(a, b, c, 0, 0, 0)
#define MFMA32(a, b, c) __builtin_amdgcn_mfma_f32_32x32x16_bf16(a, b, c, 0, 0, 0)
#define STR_(x) #x
#define WAITBAR(N) do { SB(); asm volatile("s_waitcnt vmcnt(" STR_(N) ") lgkmcnt(0)\n\ts_barrier" ::: "memory"); SB(); } while (0)
#define VMBAR(N) do { SB(); asm volatile("s_waitcnt vmcnt(" #N ")\n\ts_barrier" ::: "memory"); SB(); } while (0)
#define LGBAR() do { SB(); asm volatile("s_waitcnt lgkmcnt(0)\n\ts_barrier" ::: "memory"); SB(); } while (0)
#define BARRIER() do { SB(); asm volatile("s_barrier" ::: "memory"); SB(); } while (0)
#define MFMA16A(acc, a, b) asm volatile("v_mfma_f32_16x16x32_bf16 %0, %1, %2, %0" : "+v"(acc) : "v"(a), "v"(b))
#define ADV64(p, inc) asm volatile("v_mad_u64_u32 %0, vcc, %1, 1, %0" : "+v"(p) : "v"(inc) : "vcc")
#define PIN(x) asm volatile("" : "+v"(x))
#define EXP2(x) __builtin_amdgcn_exp2f(x)

constexpr int G1_AOFF = 0, G1_BOFF = 65536, G1_BREG = 10304, G1_BSLOT = 2 * G1_BREG;
constexpr int AT_STAGE = 32768, AT_Q = 98304, AT_EX = 131072, AT_EE = AT_EX + 16384, AT_HF = 150528;
constexpr int G2_SLOT = 49152, G2_NCH = 18;
constexpr int LDS_CTRL = 150016, LDS_SINK = 152576, LDS_TOTAL = LDS_SINK + 1024;

__device__ __forceinline__ float wave_sum(float v) {
#define ROR(n) v += __uint_as_float(__builtin_amdgcn_update_dpp(0u, __float_as_uint(v), 0x120 + (n), 0xf, 0xf, false))
  ROR(8); ROR(4); ROR(2); ROR(1);
#undef ROR
  auto a = __builtin_amdgcn_permlane16_swap(__float_as_uint(v), __float_as_uint(v), false, false);
  v = __uint_as_float(a[0]) + __uint_as_float(a[1]);
  auto b = __builtin_amdgcn_permlane32_swap(__float_as_uint(v), __float_as_uint(v), false, false);
  return __uint_as_float(b[0]) + __uint_as_float(b[1]);
}
__device__ __forceinline__ float max16(const f32x16& p) {
  float a = fmaxf(fmaxf(p[0], p[1]), p[2]), b = fmaxf(fmaxf(p[3], p[4]), p[5]);
  a = fmaxf(fmaxf(a, p[6]), p[7]); b = fmaxf(fmaxf(b, p[8]), p[9]);
  a = fmaxf(fmaxf(a, p[10]), p[11]); b = fmaxf(fmaxf(b, p[12]), p[13]);
  a = fmaxf(fmaxf(a, p[14]), p[15]);
  float m = fmaxf(a, b);
  auto rr = __builtin_amdgcn_permlane32_swap(__float_as_uint(m), __float_as_uint(m), false, false);
  return fmaxf(__uint_as_float(rr[0]), __uint_as_float(rr[1]));
}
__device__ __forceinline__ f32x16 splat16(float v) { f32x16 r;
#pragma unroll
  for (int i = 0; i < 16; ++i) r[i] = v;
  return r; }

struct Params {
  const bf16_t *x, *wqkv, *wg, *wu, *wo, *wd;
  bf16_t *xn, *Qb, *Kb, *Vb, *AH, *out;
  float* part; u64* flags;
};

__global__ __launch_bounds__(512, 2) void fused_kernel(Params P) {
  extern __shared__ __attribute__((aligned(16))) char smem[];
  const int w = __builtin_amdgcn_readfirstlane(threadIdx.x >> 6);
  const int g = w >> 2, w4 = w & 3;
  const int bid = blockIdx.x, xcd = bid & 7, jj = bid >> 3;
  const int cg = xcd * 4 + (jj & 3), rt = jj >> 2;
  const unsigned lds0 = (unsigned)(uintptr_t)smem;
  const lds_cptr L = (lds_cptr)smem;
  LAS unsigned* ctrl = (LAS unsigned*)((LAS char*)smem + LDS_CTRL);
  u64 tok;

  u32x4 hv[8];
  bf16_t* hp0; bf16_t* hp1;
#define HSTORE(i) st16wt((i) < 4 ? hp0 + (size_t)(i) * 16 * KA : hp1 + ((w4 & 1) ? (size_t)((i) & 1) * 32 * KA : (size_t)(((i) - 2) & 3) * 16 * KA), hv[i])
  const int h = cg, qb = rt;
  const char* kg = (const char*)(P.Kb + (size_t)h * HSTR) + w4 * 2048;
  const char* vg = (const char*)(P.Vb + (size_t)h * HSTR) + w4 * 2048;
#define DMA_ST(st, KH) do { const unsigned d_ = lds0 + ((st) & 3) * AT_STAGE + (KH) * 8192 + w4 * 2048; \
    const int o_ = ((rt + (st)) & 7) * 16384 + (KH) * 8192; unsigned keep_; \
    asm volatile("s_mov_b32 %0, m0\n\ts_mov_b32 m0, %4\n\ts_nop 0\n\t" \
                 "global_load_lds_dwordx4 %1, %2 sc1\n\tglobal_load_lds_dwordx4 %1, %2 offset:1024 sc1\n\t" \
                 "s_mov_b32 m0, %5\n\ts_nop 0\n\t" \
                 "global_load_lds_dwordx4 %1, %3 sc1\n\tglobal_load_lds_dwordx4 %1, %3 offset:1024 sc1\n\ts_mov_b32 m0, %0" \
                 : "=&s"(keep_) : "v"(voff), "s"(kg + o_), "s"(vg + o_), "s"(d_), "s"(d_ + 16384u) : "memory"); } while (0)

  {
    const int lane = threadIdx.x & 63;
    const int wm = 2 * g + (w4 >> 1), wn = w4 & 1;
    STAMP(1, 0);
    const int row0 = rt * 256;
    u32x4 xa, xb; u64 ep;
    asm volatile("global_load_dwordx4 %0, %3, off\n\tglobal_load_dwordx4 %1, %3, off offset:1024\n\tglobal_load_dwordx2 %2, %4, off sc1"
                 : "=&v"(xa), "=&v"(xb), "=&v"(ep) : "v"(P.x + (size_t)(bid * 8 + w) * DM + lane * 8), "v"(P.flags) : "memory");
    if (threadIdx.x < 8) ctrl[threadIdx.x] = 0u;
    asm volatile("s_waitcnt vmcnt(0) lgkmcnt(0)\n\ts_barrier" : "+v"(xa), "+v"(xb), "+v"(ep) :: "memory");
    STAMP(0, 0);
    tok = (((u64)(unsigned)__builtin_amdgcn_readfirstlane((unsigned)(ep >> 32)) << 32) | (unsigned)__builtin_amdgcn_readfirstlane((unsigned)ep)) + 1ull;
    {
      float v[16];
#pragma unroll
      for (int i = 0; i < 4; ++i) { v[2 * i] = bf_lo(xa[i]); v[2 * i + 1] = bf_hi(xa[i]); v[8 + 2 * i] = bf_lo(xb[i]); v[9 + 2 * i] = bf_hi(xb[i]); }
      float sm = 0.f;
#pragma unroll
      for (int i = 0; i < 16; ++i) sm += v[i];
      const float mean = wave_sum(sm) * (1.f / DM);
      float q = 0.f;
#pragma unroll
      for (int i = 0; i < 16; ++i) { const float d = v[i] - mean; q += d * d; }
      const float mb = rbf(mean), vb = rbf(wave_sum(q) * (1.f / DM));
      const float inv = 1.0f / rbf(sqrtf(rbf(vb + 1e-5f)));
      u32x4 oa, ob;
#pragma unroll
      for (int i = 0; i < 4; ++i) {
        const unsigned da = cvtpk(v[2 * i] - mb, v[2 * i + 1] - mb), db = cvtpk(v[8 + 2 * i] - mb, v[9 + 2 * i] - mb);
        oa[i] = cvtpk(bf_lo(da) * inv, bf_hi(da) * inv);
        ob[i] = cvtpk(bf_lo(db) * inv, bf_hi(db) * inv);
      }
      bf16_t* op = P.xn + (size_t)(bid * 8 + w) * DM + lane * 8;
      st16wt(op, oa); st16wt(op + 512, ob);
      STAMP(0, 4);
      publish(ctrl + 0, P.flags + FL_OFF + bid, tok, lane);
      STAMP(0, 1);
    }
    unsigned avoff0 = (unsigned)((lane >> 3) * 2048 + (((lane & 7) ^ ((lane >> 4) + 2)) * 16)), avoff1 = avoff0 ^ 64u;
    const char* abase = (const char*)P.xn + (size_t)(row0 + 32 * w4) * 2048;
#define DMA_A(hh, t) do { _Pragma("unroll") for (int _i = 0; _i < 4; ++_i) \
    glds16c(abase + (size_t)(hh) * 262144 + _i * 16384 + (t) * 128, (_i & 1) ? avoff1 : avoff0, \
            lds0 + G1_AOFF + ((t) & 1) * 32768 + (hh) * 16384 + (4 * w4 + _i) * 1024); } while (0)
    const char* bptr[4]; unsigned bld[4]; unsigned bdst[4];
#pragma unroll
    for (int i = 0; i < 4; ++i) {
      const int b = (i == 0) ? (g ? 16 + w4 : 4 * w4) : 4 * w4 + i;
      const int region = b >= 10 ? 1 : 0, bi = b - 10 * region;
      const int Pc = bi * 64 + lane;
      const int k = Pc / 20, pc = Pc - 20 * k, f = pc >> 1, e8 = (pc & 1) * 8;
      const bool isq = f >= 8 || (region && (f & 3) == 3);
      const int qf = f >= 8 ? (region ? f - 4 : f - 8) : 2 + (f >> 2);
      const bf16_t* mat = isq ? P.wqkv : (f >= 4 ? P.wu : P.wg);
      const int ld = isq ? NQKV : FF;
      const int col = e8 + (isq ? (qf >> 1) * DM + 32 * cg + 16 * (qf & 1) : 112 * cg + 64 * region + 16 * (f & 3));
      bptr[i] = (const char*)(mat + (size_t)k * ld + col);
      bld[i] = (unsigned)ld * 64u;
      bdst[i] = lds0 + G1_BOFF + region * G1_BREG + bi * 1024 + (bi >= 5 ? 32 : 0);
    }
#define DMA_B(i, s) glds16(bptr[i], bdst[i] + ((s) & 3) * G1_BSLOT)
#define ADV_B() do { ADV64(bptr[0], bld[0]); ADV64(bptr[1], bld[1]); ADV64(bptr[2], bld[2]); ADV64(bptr[3], bld[3]); } while (0)
    if (g == 0) {
      DMA_B(0, 0); DMA_B(1, 0); DMA_B(2, 0); DMA_B(3, 0); ADV_B();
      DMA_B(0, 1); ADV_B();
      DMA_B(0, 2); DMA_B(1, 2); DMA_B(2, 2); DMA_B(3, 2); ADV_B();
    } else {
      DMA_B(0, 0); ADV_B();
      DMA_B(0, 1); DMA_B(1, 1); DMA_B(2, 1); DMA_B(3, 1); ADV_B();
      DMA_B(0, 2); ADV_B();
      const char* ta;
      if (w4 <= 1) ta = (const char*)(P.AH + (size_t)(lane * 32 + 15 + 16 * w4) * KA + 4608);
      else if (w4 == 2) ta = lane < 48 ? (const char*)(P.Qb + (size_t)(2 * lane + 1) * HSTR - 64)
                                       : (const char*)P.part + (size_t)((lane - 48) * 16 + xcd) * 65536 + (xcd & 3) * 16384;
      else ta = lane < 8 ? (const char*)P.wo + lane * 262144 : (lane < 36 ? (const char*)P.wd + (lane - 8) * 262144
                         : (lane < 52 ? (const char*)P.out + (lane - 36) * 262144 : (const char*)P.x + (lane - 52) * 262144));
      glds4c(ta, lds0 + LDS_SINK);
    }
    const int fr = lane & 15, fq = lane >> 4, kq = ((fq & 1) << 1) | (fq >> 1);
    const int aoff0 = G1_AOFF + (64 * wm + fr) * 128 + ((kq ^ (((fr >> 1) + 2) & 7)) * 16);
    lds_cptr pa0 = L + aoff0, pa1 = L + (aoff0 ^ 64);
    lds_cptr pb0 = L + (G1_BOFF + (8 * kq + (fr >> 2)) * 320 + (fq & 1) * 32 + 8 * (lane & 3) + G1_BREG * wn);
    lds_cptr pb1 = pb0 + 2 * G1_BSLOT;
    asm volatile("" : "+v"(pa0), "+v"(pa1), "+v"(pb0), "+v"(pb1), "+v"(avoff0), "+v"(avoff1));
    f32x4 acc[4][10];
#pragma unroll
    for (int m = 0; m < 4; ++m)
#pragma unroll
      for (int j = 0; j < 10; ++j) { acc[m][j] = (f32x4){0.f, 0.f, 0.f, 0.f}; PIN(acc[m][j]); }
    bf16x8 af[4], bfr[10];
    if (g == 0) {
      wg_wait(w, lane, P.flags + FL_OFF + rt * 32 + (lane & 31), tok, ctrl + 3);
      STAMP(0, 2);
      DMA_A(0, 0); DMA_A(1, 0); DMA_A(0, 1);
      STAMP(0, 3);
      WAITBAR(4);
    } else { WAITBAR(1); BARRIER(); }
    STAMP(1, 1);
#define STEP(U, ISSUE, WN, LASTBAR, RELAX, PF) do { \
    { const lds_cptr pa = ((U) & 1) ? pa1 : pa0; const lds_cptr pb = (((U) & 2) ? pb1 : pb0) + ((U) & 1) * G1_BSLOT; \
      _Pragma("unroll") for (int m = 0; m < 4; ++m) af[m] = lds_rd128(pa + ((U) >> 1) * 32768 + m * 2048); \
      _Pragma("unroll") for (int j = 0; j < 10; ++j) bfr[j] = cat8(lds_tr(pb + j * 32), lds_tr(pb + j * 32 + 1280)); } \
    if (ISSUE) { DMA_B(0, (U) + 3); \
      if (g == ((U) & 1)) { DMA_A(1 - ((U) & 1), 2 * it + ((U) >> 1) + 1 + ((U) & 1)); } \
      else { DMA_B(1, (U) + 3); DMA_B(2, (U) + 3); DMA_B(3, (U) + 3); \
        if (PF) { glds16s(pfp, pfoff, lds0 + LDS_SINK); pfp += pfinc; } } } \
    if ((RELAX) && g) { WAITBAR(63); } else { WAITBAR(WN); } \
    _Pragma("unroll") for (int j = 0; j < 10; ++j) { _Pragma("unroll") for (int m = 0; m < 4; ++m) MFMA16A(acc[m][j], bfr[j], af[m]); \
      if ((ISSUE) && (j & 1) && j < 8) ADV64(bptr[j >> 1], bld[j >> 1]); } \
    if (LASTBAR) BARRIER(); } while (0)
    unsigned pfoff = lane * 16; PIN(pfoff);
    const int pj = bid * 4 + w4, pn = pj < 308 ? 308 : 358;
    const char* pfp = (pj < 308 ? (const char*)P.wqkv + (size_t)(1216 + pj) * 1024
                                : (pj < 666 ? (const char*)P.wg + (size_t)(1440 + pj - 308) * 1024 : (const char*)P.wu + (size_t)(1440 + pj - 666) * 1024))
                      + (size_t)(1 - g) * pn * 1024;
    const unsigned pfinc = 2048u * pn;
    int it = 0;
    STEP(0, true, 9, true, true, true); STEP(1, true, 10, true, true, true); STEP(2, true, 10, true, false, true); STEP(3, true, 10, true, false, true);
#pragma unroll 1
    for (it = 1; it < 4; ++it) { STEP(0, true, 10, true, false, true); STEP(1, true, 10, true, false, true); STEP(2, true, 10, true, false, true); STEP(3, true, 10, true, false, true); }
#pragma unroll 1
    for (; it < 7; ++it) { STEP(0, true, 9, true, false, false); STEP(1, true, 9, true, false, false); STEP(2, true, 9, true, false, false); STEP(3, true, 9, true, false, false); }
    STEP(0, true, 9, true, false, false); STEP(1, false, 4, true, false, false); STEP(2, false, 0, true, false, false); STEP(3, false, 0, g == 0, false, false);
#undef STEP
    asm volatile("s_nop 7\n\ts_nop 7" ::: "memory");
    SB();
    STAMP(1, 2);
#undef DMA_A
#undef DMA_B
#undef ADV_B
    int tz = threadIdx.x; asm volatile("" : "+v"(tz));
    const int fr2 = tz & 15, fq2 = (tz >> 4) & 3, sel1 = fq2 & 1, c8 = (fq2 >> 1) * 8;
    const float qscale = 0.17677669529663687f * 1.4426950408889634f;
#define SWAP16(A, B, OUT) do { auto r0_ = __builtin_amdgcn_permlane16_swap(A.x, B.x, false, false); \
    auto r1_ = __builtin_amdgcn_permlane16_swap(A.y, B.y, false, false); OUT = (u32x4){r0_[0], r1_[0], r0_[1], r1_[1]}; } while (0)
#define SILU4(m, j, OUT) do { float hv_[4]; _Pragma("unroll") for (int r = 0; r < 4; ++r) { \
      const float tg_ = acc[m][j][r], tu_ = acc[m][(j) + 4][r]; \
      hv_[r] = (tg_ * tu_) * __builtin_amdgcn_rcpf(1.f + __builtin_amdgcn_exp2f(-1.4426950408889634f * tg_)); } \
    OUT = (u32x2){cvtpk(hv_[0], hv_[1]), cvtpk(hv_[2], hv_[3])}; } while (0)
#define PK4(V, SC, OUT) do { const f32x4 v_ = (V) * (SC); OUT = (u32x2){cvtpk(v_[0], v_[1]), cvtpk(v_[2], v_[3])}; } while (0)
    const int rowb = row0 + 64 * wm + fr2;
    const int rl = 64 * wm + fr2;
    __builtin_amdgcn_s_setprio(3);
    if (wn) {
      bf16_t* kd = P.Kb + (size_t)h * HSTR + (((rowb >> 5) * 2 + sel1) * 64 + (rowb & 31) + 4 * c8) * 8;
      bf16_t* vd = P.Vb + (size_t)h * HSTR + rowb * 32 + 16 * sel1 + c8;
      LAS char* kl = (LAS char*)smem + (((rl >> 5) * 2 + sel1) * 64 + (rl & 31) + 4 * c8) * 16;
      LAS char* vl = (LAS char*)smem + 16384 + rl * 64 + (16 * sel1 + c8) * 2;
#pragma unroll
      for (int m = 0; m < 4; ++m) {
        u32x2 a_, b_; u32x4 o;
        PK4(acc[m][3], 1.f, a_); PK4(acc[m][7], 1.f, b_); SWAP16(a_, b_, o);
        st16wt(kd + (m & 1) * 128 + (m >> 1) * 1024, o); *(LAS u32x4*)(kl + (m & 1) * 256 + (m >> 1) * 2048) = o;
        PK4(acc[m][8], 1.f, a_); PK4(acc[m][9], 1.f, b_); SWAP16(a_, b_, o);
        st16wt(vd + m * 512, o); *(LAS u32x4*)(vl + m * 1024) = o;
      }
    } else {
      LAS char* ql = (LAS char*)smem + AT_Q + rl * 64 + (16 * sel1 + c8) * 2;
#pragma unroll
      for (int m = 0; m < 4; ++m) {
        u32x2 a_, b_; u32x4 o; PK4(acc[m][8], qscale, a_); PK4(acc[m][9], qscale, b_); SWAP16(a_, b_, o);
        *(LAS u32x4*)(ql + m * 1024) = o;
      }
    }
    __builtin_amdgcn_s_setprio(0);
    SB();
    bf16_t* hd = P.AH + (size_t)rowb * KA + DM + 112 * cg + 64 * wn + c8;
    hp0 = hd + 16 * sel1;
    hp1 = wn ? hd + 32 + (size_t)sel1 * 16 * KA : hd + 32 + 16 * sel1;
#pragma unroll
    for (int m = 0; m < 2; ++m) { u32x2 a_, b_; SILU4(m, 0, a_); SILU4(m, 1, b_); SWAP16(a_, b_, hv[m]); PIN(hv[m]); }
    STAMP(1, 4);
    publish(ctrl + 1, P.flags + FA_OFF + cg * 8 + rt, tok, tz & 63);
#pragma unroll
    for (int m = 2; m < 4; ++m) { u32x2 a_, b_; SILU4(m, 0, a_); SILU4(m, 1, b_); SWAP16(a_, b_, hv[m]); }
    if (wn == 0) {
#pragma unroll
      for (int m = 0; m < 4; ++m) { u32x2 a_, b_; SILU4(m, 2, a_); SILU4(m, 3, b_); SWAP16(a_, b_, hv[4 + ((m + 2) & 3)]); }
    } else {
#pragma unroll
      for (int mp = 0; mp < 2; ++mp) { u32x2 a_, b_; SILU4(2 * mp, 2, a_); SILU4(2 * mp + 1, 2, b_); SWAP16(a_, b_, hv[4 + mp]); }
      hv[6] = hv[4]; hv[7] = hv[5];
    }
    STAMP(1, 3);
    STAMP(2, 0);
#undef SWAP16
#undef SILU4
#undef PK4
  }

  const int k4 = xcd & 3, rt2 = (xcd >> 2) * 4 + (jj >> 3), ct2 = jj & 7;
  int c_issued = 0;
#define G2_K(c) ((c) < 14 ? DM + (14 * k4 + (c)) * 64 : (4 * k4 + (c) - 14) * 64)
#define G2_DMA_A(c, sl) do { \
    const char* ab_ = (const char*)P.AH + (size_t)(rt2 * 256 + 64 * w4) * (KA * 2) + G2_K(c) * 2; \
    const unsigned d_ = lds0 + (sl) * G2_SLOT + w4 * 8192; unsigned keep_; \
    asm volatile("s_mov_b32 %0, m0\n\ts_mov_b32 m0, %7\n\ts_nop 0\n\t" \
                 "global_load_lds_dwordx4 %1, %5 sc1\n\tglobal_load_lds_dwordx4 %2, %5 offset:1024 sc1\n\t" \
                 "global_load_lds_dwordx4 %3, %5 offset:2048 sc1\n\tglobal_load_lds_dwordx4 %4, %5 offset:3072 sc1\n\t" \
                 "s_mov_b32 m0, %8\n\ts_nop 0\n\t" \
                 "global_load_lds_dwordx4 %1, %6 sc1\n\tglobal_load_lds_dwordx4 %2, %6 offset:1024 sc1\n\t" \
                 "global_load_lds_dwordx4 %3, %6 offset:2048 sc1\n\tglobal_load_lds_dwordx4 %4, %6 offset:3072 sc1\n\ts_mov_b32 m0, %0" \
                 : "=&s"(keep_) : "v"(avo[0]), "v"(avo[1]), "v"(avo[2]), "v"(avo[3]), "s"(ab_), "s"(ab_ + 32 * KA * 2), "s"(d_), "s"(d_ + 4096u) : "memory"); } while (0)
#define G2_DMA_B(c, sl) do { const int kg_ = G2_K(c); \
    const char* bb_ = (kg_ < DM ? (const char*)P.wo + (size_t)kg_ * 2048 : (const char*)P.wd + (size_t)(kg_ - DM) * 2048) + ct2 * 256 + w4 * 32768; \
    const unsigned d_ = lds0 + (sl) * G2_SLOT + 32768 + w4 * 4096; unsigned keep_; \
    asm volatile("s_mov_b32 %0, m0\n\ts_mov_b32 m0, %6\n\ts_nop 0\n\t" \
                 "global_load_lds_dwordx4 %1, %5\n\tglobal_load_lds_dwordx4 %2, %5 offset:1024\n\t" \
                 "global_load_lds_dwordx4 %3, %5 offset:2048\n\tglobal_load_lds_dwordx4 %4, %5 offset:3072\n\ts_mov_b32 m0, %0" \
                 : "=&s"(keep_) : "v"(bvo[0]), "v"(bvo[1]), "v"(bvo[2]), "v"(bvo[3]), "s"(bb_), "s"(d_) : "memory"); } while (0)
#define G2_DMA(c, sl) do { G2_DMA_A(c, sl); G2_DMA_B(c, sl); } while (0)
#define G2_OFFS(lane) unsigned avo[4], bvo[4]; _Pragma("unroll") for (int _i = 0; _i < 4; ++_i) { \
    avo[_i] = ((unsigned)(((lane) >> 3) * (KA * 2) + ((((lane) & 7) ^ (((lane) >> 4) + 2)) * 16)) ^ ((_i & 1) * 64u)) + _i * (8 * KA * 2 - 1024); \
    bvo[_i] = (unsigned)(((lane) >> 4) * 2048 + ((((lane) & 15) ^ (((lane) >> 4) << 1) ^ ((w4 & 1) << 3)) * 16)) + _i * (8192 - 1024); }

  {
    int tb = threadIdx.x; asm volatile("" : "+v"(tb));
    const int lane = tb & 63;
    const int qg = w4, kh = g;
    const int hi = lane >> 5;
    const unsigned voff = lane * 16;
    const short one = (((lane >> 4) & 1) == ((lane >> 2) & 1)) ? (short)0x3f80 : (short)0;
    const bf16x8 sel = {one, one, one, one, one, one, one, one};
    volatile LAS unsigned* flag = (volatile LAS unsigned*)(ctrl + 2);
    const int gq = lane >> 4;
    const int koff = kh * 8192 + lane * 16;
    const int voffl = 16384 + kh * 8192 + (4 * (gq >> 1) + ((lane & 15) >> 2)) * 64 + (gq & 1) * 32 + (lane & 3) * 8;
    const f32x16 zero16 = splat16(0.f);
    f32x16 oA = zero16, oB = zero16;
    f32x4 lA = {0.f, 0.f, 0.f, 0.f}, lB = {0.f, 0.f, 0.f, 0.f};
    float mA, mB;
    const unsigned* hflag = (const unsigned*)(P.flags + FH_OFF + (8 * k4 + ((lane >> 1) & 7)) * 8 + rt2) + (lane & 1);
    bf16x8 qa0, qa1, qb0, qb1;

#define LDK(K0, K1, p) do { K0 = lds_rd128(p); K1 = lds_rd128((p) + 1024); } while (0)
#define LDV(V0, V1, p) do { V0 = cat8(lds_tr(p), lds_tr((p) + 512)); V1 = cat8(lds_tr((p) + 1024), lds_tr((p) + 1536)); } while (0)
#define BF(x) __builtin_bit_cast(bf16x8, x)
    {
      const unsigned* kvflag = (const unsigned*)(P.flags + FA_OFF + cg * 8 + ((lane >> 1) & 7)) + (lane & 1);
      const unsigned tokh = (lane & 1) ? (unsigned)(tok >> 32) : (unsigned)tok;
#define FLAGS_OK() __all(*(volatile LAS unsigned*)((LAS char*)smem + AT_HF + w * 256 + lane * 4) == tokh)
      LGBAR();
      STAMP(2, 4);
      glds4c(kvflag, lds0 + AT_HF + w * 256);
      { const lds_cptr qp = L + AT_Q + qg * 4096 + (lane & 31) * 64 + hi * 16;
        qa0 = lds_rd128(qp); qa1 = lds_rd128(qp + 32); qb0 = lds_rd128(qp + 2048); qb1 = lds_rd128(qp + 2080); }
      bf16x8 kx0, kx1, ky0, ky1, vx0, vx1, vy0, vy1;
      LDK(kx0, kx1, L + koff);
      LDK(ky0, ky1, L + koff + 2048);
      LDV(vy0, vy1, L + voffl);
      f32x16 sA = MFMA32(kx0, qa0, zero16); sA = MFMA32(kx1, qa1, sA);
      f32x16 sB = MFMA32(kx0, qb0, zero16); sB = MFMA32(kx1, qb1, sB);
      mA = max16(sA); mB = max16(sB);
      f32x16 nA = splat16(-mA), nB = splat16(-mB);
      PIN(nA); PIN(nB);
      sA = MFMA32(kx0, qa0, nA); sA = MFMA32(kx1, qa1, sA);
      u32x4 pA0, pA1, pB0 = {0u, 0u, 0u, 0u}, pB1 = {0u, 0u, 0u, 0u};
      STAMP(2, 1);
#define E4(S, i) do { S[i] = EXP2(S[i]); S[i + 1] = EXP2(S[i + 1]); S[i + 2] = EXP2(S[i + 2]); S[i + 3] = EXP2(S[i + 3]); } while (0)
#define SLOT(SC, PC0, PC1, SN, K0, K1, Q0, Q1, NM, PP0, PP1, V0, V1, O, LS, LOADS) do { \
    SN = MFMA32(K0, Q0, NM); SB(); E4(SC, 0); PIN(SC); SB(); \
    SN = MFMA32(K1, Q1, SN); SB(); LOADS; SB(); E4(SC, 4); PIN(SC); SB(); \
    O = MFMA32(V0, BF(PP0), O); SB(); PC0[0] = cvtpk(SC[0], SC[1]); PC0[1] = cvtpk(SC[2], SC[3]); \
      SC[8] = EXP2(SC[8]); SC[9] = EXP2(SC[9]); SC[10] = EXP2(SC[10]); PIN(SC); PIN(PC0); SB(); \
    LS = MFMA16(sel, BF(PP0), LS); SB(); PC0[2] = cvtpk(SC[4], SC[5]); PC0[3] = cvtpk(SC[6], SC[7]); SC[11] = EXP2(SC[11]); PIN(SC); PIN(PC0); SB(); \
    O = MFMA32(V1, BF(PP1), O); SB(); E4(SC, 12); PIN(SC); SB(); \
    LS = MFMA16(sel, BF(PP1), LS); SB(); PC1[0] = cvtpk(SC[8], SC[9]); PC1[1] = cvtpk(SC[10], SC[11]); \
      PC1[2] = cvtpk(SC[12], SC[13]); PC1[3] = cvtpk(SC[14], SC[15]); PIN(PC1); SB(); } while (0)
#define SUB(KC0, KC1, KN0, KN1, VC0, VC1, VP0, VP1, vpu, kp2) do { \
    SLOT(sA, pA0, pA1, sB, KC0, KC1, qb0, qb1, nB, pB0, pB1, VP0, VP1, oB, lB, LDV(VC0, VC1, vpu)); \
    SLOT(sB, pB0, pB1, sA, KN0, KN1, qa0, qa1, nA, pA0, pA1, VC0, VC1, oA, lA, LDK(KC0, KC1, kp2)); } while (0)
      lds_cptr kb0 = L + koff, kb1 = L + koff + 2 * AT_STAGE, vb0 = L + voffl, vb1 = L + voffl + 2 * AT_STAGE;
      PIN(kb0); PIN(kb1); PIN(vb0); PIN(vb1);
      bool kvok = false;
#define KP(st) ((((st) & 2) ? kb1 : kb0) + ((st) & 1) * AT_STAGE)
#define VP(st) ((((st) & 2) ? vb1 : vb0) + ((st) & 1) * AT_STAGE)
#define HS(st, i) do { if ((st) == 1 || (st) == 2) HSTORE((4 * ((st) - 1) + (i)) & 7); } while (0)
#define VMW0() asm volatile("s_waitcnt vmcnt(0)" ::: "memory")
#define STAGE(st, WN, EXTRA) do { \
        SUB(kx0, kx1, ky0, ky1, vx0, vx1, vy0, vy1, VP(st), KP(st) + 4096); \
        HS(st, 0); \
        if ((st) == 0) { VMW0(); kvok = FLAGS_OK(); if (kvok) { DMA_ST(1, kh); DMA_ST(2, kh); } else glds4c(kvflag, lds0 + AT_HF + w * 256); } \
        SUB(ky0, ky1, kx0, kx1, vy0, vy1, vx0, vx1, VP(st) + 2048, KP(st) + 6144); \
        if ((st) == 0 && !kvok) { VMW0(); if (!FLAGS_OK()) wait_flags(P.flags + FA_OFF + cg * 8 + (lane & 7), tok); DMA_ST(1, kh); DMA_ST(2, kh); } \
        VMBAR(WN); \
        HS(st, 1); \
        EXTRA; \
        if ((st) < 5) {   \
          const int pc = (st) * 8 + w, kb = bid * 36 + (pc < 36 ? pc : 35); \
          glds16s(kb < 2048 ? (const char*)P.wo + (size_t)kb * 1024 : (const char*)P.wd + (size_t)(kb - 2048) * 1024, voff, lds0 + LDS_SINK); \
          DMA_ST((st) + 3, kh); } \
        SUB(kx0, kx1, ky0, ky1, vx0, vx1, vy0, vy1, VP(st) + 4096, KP((st) + 1)); \
        HS(st, 2); \
        SUB(ky0, ky1, kx0, kx1, vy0, vy1, vx0, vx1, VP(st) + 6144, KP((st) + 1) + 2048); \
        HS(st, 3); } while (0)
      STAGE(0, 4, ); STAGE(1, 6, ); STAGE(2, 10, ); STAGE(3, 11, );
      STAGE(4, 5, if (w == 0 && lane == 0) set_flag(P.flags + FH_OFF + cg * 8 + rt, tok));
      STAGE(5, 5, if (g == 1) glds4c(hflag, lds0 + AT_HF + w * 256));
      STAGE(6, 0, if (g == 1 && FLAGS_OK()) { G2_OFFS(lane); G2_DMA(0, 0); c_issued = 2; });
      STAGE(7, 0, if (c_issued == 2) { G2_OFFS(lane); G2_DMA(1, 1); c_issued = 3; });
#undef VMW0
#undef FLAGS_OK
#undef STAGE
#undef HS
#undef KP
#undef VP
      oB = MFMA32(vy0, BF(pB0), oB); lB = MFMA16(sel, BF(pB0), lB);
      oB = MFMA32(vy1, BF(pB1), oB); lB = MFMA16(sel, BF(pB1), lB);
#undef SUB
#undef SLOT
#undef E4
    }
    STAMP(2, 2);
    LAS u32x4* xw = (LAS u32x4*)((LAS char*)smem + AT_EX) + w4 * 256;
    LAS float* ew = (LAS float*)((LAS char*)smem + AT_EE) + w4 * 128;
    float eA, eB;
#define FIN1(FIRST) do { \
        \
      float chk = 0.f; \
      _Pragma("unroll") for (int r = 0; r < 16; ++r) { chk = __builtin_fmaf(oA[r], 0.f, chk); chk = __builtin_fmaf(oB[r], 0.f, chk); } \
      const bool bad = !(lA[0] < 1.0e36f) || !(lB[0] < 1.0e36f) || !(chk == 0.f); \
      if (FIRST) { if (__any(bad) && lane == 0) *flag = 1u; \
                   if (c_issued == 3 && lane == 0) __hip_atomic_fetch_add(ctrl + 5, 1u, __ATOMIC_RELAXED, __HIP_MEMORY_SCOPE_WORKGROUP); } \
      oA *= __builtin_amdgcn_rcpf(lA[0]); oB *= __builtin_amdgcn_rcpf(lB[0]); \
      eA = mA + __builtin_amdgcn_logf(lA[0]); eB = mB + __builtin_amdgcn_logf(lB[0]);     \
      if (g == 0) { \
        _Pragma("unroll") for (int r = 0; r < 2; ++r) { \
          xw[r * 64 + lane] = (u32x4){cvtpk(oA[8 * r], oA[8 * r + 1]), cvtpk(oA[8 * r + 2], oA[8 * r + 3]), cvtpk(oA[8 * r + 4], oA[8 * r + 5]), cvtpk(oA[8 * r + 6], oA[8 * r + 7])}; \
          xw[128 + r * 64 + lane] = (u32x4){cvtpk(oB[8 * r], oB[8 * r + 1]), cvtpk(oB[8 * r + 2], oB[8 * r + 3]), cvtpk(oB[8 * r + 4], oB[8 * r + 5]), cvtpk(oB[8 * r + 6], oB[8 * r + 7])}; } \
        ew[lane] = eA; ew[64 + lane] = eB; } } while (0)
#define SAFE_UNIT(Q0, Q1, M, O, LS) do { \
            f32x16 s_ = MFMA32(k0, Q0, zero16); s_ = MFMA32(k1, Q1, s_); \
            const float mn_ = fmaxf(M, max16(s_)); const float al_ = EXP2(M - mn_); M = mn_; \
            O *= al_; LS *= al_; u32x4 p0_, p1_; \
            _Pragma("unroll") for (int r = 0; r < 4; ++r) { p0_[r] = cvtpk(EXP2(s_[2 * r] - mn_), EXP2(s_[2 * r + 1] - mn_)); \
              p1_[r] = cvtpk(EXP2(s_[8 + 2 * r] - mn_), EXP2(s_[9 + 2 * r] - mn_)); } \
            O = MFMA32(v0, BF(p0_), O); LS = MFMA16(sel, BF(p0_), LS); O = MFMA32(v1, BF(p1_), O); LS = MFMA16(sel, BF(p1_), LS); } while (0)
#define SAFE_PASS() do { \
        WAITBAR(0); \
        DMA_ST(0, kh); DMA_ST(1, kh); DMA_ST(2, kh); \
        oA = zero16; oB = zero16; lA = (f32x4){0.f, 0.f, 0.f, 0.f}; lB = lA; \
        mA = -3.0e38f; mB = -3.0e38f; \
        _Pragma("unroll 1") for (int st = 0; st < 8; ++st) { \
          if (st < 6) WAITBAR(8); else if (st == 6) WAITBAR(4); else WAITBAR(0); \
          if (st < 5) DMA_ST(st + 3, kh); \
          _Pragma("unroll 1") for (int u = 0; u < 4; ++u) { \
            bf16x8 k0, k1, v0, v1; \
            LDK(k0, k1, L + (st & 3) * AT_STAGE + koff + u * 2048); \
            LDV(v0, v1, L + (st & 3) * AT_STAGE + voffl + u * 2048); \
            SAFE_UNIT(qa0, qa1, mA, oA, lA); \
            SAFE_UNIT(qb0, qb1, mB, oB, lB); } } \
        FIN1(false); LGBAR(); } while (0)
    FIN1(true);
    STAMP(2, 3);

    STAMP(3, 0);
    {
      const int wm = w4 >> 1, wn = w4 & 1;
      const int fr = lane & 15, fq = lane >> 4, kq = ((fq & 1) << 1) | (fq >> 1), q4 = fr >> 2;
      const int aoff0 = (128 * wm + fr) * 128 + ((kq ^ (((fr >> 1) + 2) & 7)) * 16);
      const int boff0 = 32768 + (8 * kq + q4) * 256 + 8 * (lane & 3);
      const int bsw = q4 | ((fq & 1) << 2);
      lds_cptr pa[2], pb[4];
      pa[0] = L + aoff0; pa[1] = L + (aoff0 ^ 64); PIN(pa[0]); PIN(pa[1]);
#pragma unroll
      for (int n = 0; n < 4; ++n) { pb[n] = L + (boff0 + (((4 * wn + n) ^ bsw) * 32)); PIN(pb[n]); }
      bf16x8 fa0[8], fb0[4], fa1[8], fb1[4];
#define RD(i, KS, FA, FB) do { if ((i) < 4) FB[(i) & 3] = cat8(lds_tr(pb[(i) & 3] + (KS) * 8192), lds_tr(pb[(i) & 3] + (KS) * 8192 + 1024)); \
      else FA[((i) - 4) & 7] = lds_rd128(pa[KS] + (((i) - 4) & 7) * 2048); } while (0)
#define RD12(KS, FA, FB) do { RD(0, KS, FA, FB); RD(1, KS, FA, FB); RD(2, KS, FA, FB); RD(3, KS, FA, FB); RD(4, KS, FA, FB); RD(5, KS, FA, FB); \
      RD(6, KS, FA, FB); RD(7, KS, FA, FB); RD(8, KS, FA, FB); RD(9, KS, FA, FB); RD(10, KS, FA, FB); RD(11, KS, FA, FB); } while (0)
      if (g == 0) RD12(0, fa0, fb0);
      LGBAR();
      const bool ovf = *flag != 0u, fast = !ovf && *(volatile LAS unsigned*)(ctrl + 5) == 4u;
      if (!fast) {
        if (ovf) SAFE_PASS();
        if (g == 1) {
          G2_OFFS(lane);
          wait_flags(P.flags + FH_OFF + (8 * k4 + (lane & 7)) * 8 + rt2, tok);
          G2_DMA(0, 0); G2_DMA(1, 1);
        }
        VMBAR(12);
        if (g == 0) RD12(0, fa0, fb0);
        LGBAR();
      }
#undef RD12
      if (g == 1) {
#define FIN2(O, E, BL) do { \
      const float e2 = ew[(BL) * 64 + lane]; const u32x4 t0 = xw[(BL) * 128 + lane], t1 = xw[(BL) * 128 + 64 + lane]; \
      const float em = fmaxf(E, e2); float w1 = EXP2(E - em), w2 = EXP2(e2 - em); \
      const float rs = __builtin_amdgcn_rcpf(w1 + w2); w1 *= rs; w2 *= rs; u32x2 pk[4]; \
      _Pragma("unroll") for (int r = 0; r < 4; ++r) { \
        const unsigned ta = r < 2 ? t0[2 * r] : t1[2 * r - 4], tb2 = r < 2 ? t0[2 * r + 1] : t1[2 * r - 3]; \
        pk[r].x = cvtpk(O[4 * r] * w1 + bf_lo(ta) * w2, O[4 * r + 1] * w1 + bf_hi(ta) * w2); \
        pk[r].y = cvtpk(O[4 * r + 2] * w1 + bf_lo(tb2) * w2, O[4 * r + 3] * w1 + bf_hi(tb2) * w2); } \
      bf16_t* op = P.AH + (size_t)(qb * 256 + qg * 64 + (BL) * 32 + (lane & 31)) * KA + h * 32 + 8 * hi; \
      _Pragma("unroll") for (int k = 0; k < 4; k += 2) { \
        auto rx = __builtin_amdgcn_permlane32_swap(pk[k].x, pk[k + 1].x, false, false); \
        auto ry = __builtin_amdgcn_permlane32_swap(pk[k].y, pk[k + 1].y, false, false); \
        st16wt(op + 8 * k, (u32x4){rx[0], ry[0], rx[1], ry[1]}); } } while (0)
      __builtin_amdgcn_s_setprio(3);
      { G2_OFFS(lane); (void)bvo; G2_DMA_A(2, 2); }
      FIN2(oA, eA, 0); FIN2(oB, eB, 1);
#undef FIN2
      }
      SB();
      if (g == 1) {
      int tl = threadIdx.x; asm volatile("" : "+v"(tl));
      const int ll = tl & 63;
      G2_OFFS(ll);
      WAITBAR(12);
      G2_DMA_B(2, 2); G2_DMA(3, 0); VMBAR(12);
      if (w == 4 && ll == 0) set_flag(P.flags + FB_OFF + qb * 32 + h, tok);
      int sl = 1;
#pragma unroll 1
      for (int i = 2; i < G2_NCH - 2; ++i) {
        const u64* of = P.flags + FB_OFF + rt2 * 32 + 8 * k4;
        if (i == 10) glds4c((const unsigned*)(of + ((ll >> 1) & 7)) + (ll & 1), lds0 + AT_HF + w * 256);
        if (i == 12) {
          const unsigned hf = *(volatile LAS unsigned*)((LAS char*)smem + AT_HF + w * 256 + ll * 4);
          if (!__all(hf == ((ll & 1) ? (unsigned)(tok >> 32) : (unsigned)tok))) wait_flags(of + (ll & 7), tok);
        }
        G2_DMA(i + 2, sl); sl = sl == 2 ? 0 : sl + 1; VMBAR(12);
      }
      VMBAR(0);
      if (bid == 0 && w == 4) {
#pragma unroll
        for (int i = 0; i < 4; ++i) wait_flags(P.flags + FA_OFF + i * 64 + ll, tok);
        if (ll == 0) set_flag(P.flags, tok);
      }
      STAMP(3, 3);
      __builtin_amdgcn_endpgm();
      }
      f32x4 acc[8][4];
#pragma unroll
      for (int m = 0; m < 8; ++m)
#pragma unroll
        for (int n = 0; n < 4; ++n) { acc[m][n] = (f32x4){0.f, 0.f, 0.f, 0.f}; PIN(acc[m][n]); }
      asm volatile("s_nop 1" ::: "memory");
      STAMP(3, 1);
#define MM(i, FA, FB) MFMA16A(acc[(i) >> 2][(i) & 3], FB[(i) & 3], FA[(i) >> 2])
#define MR(i, KS, CA, CB, NA, NB) do { MM(i, CA, CB); RD(i, KS, NA, NB); SB(); } while (0)
#define MR4(i, KS, CA, CB, NA, NB) do { MR(i, KS, CA, CB, NA, NB); MR(i + 1, KS, CA, CB, NA, NB); MR(i + 2, KS, CA, CB, NA, NB); MR(i + 3, KS, CA, CB, NA, NB); } while (0)
#define MM4(i, CA, CB) do { MM(i, CA, CB); MM(i + 1, CA, CB); MM(i + 2, CA, CB); MM(i + 3, CA, CB); } while (0)
#define HALF(KS, CA, CB, NA, NB, END) do { \
      MR4(0, KS, CA, CB, NA, NB); MR4(4, KS, CA, CB, NA, NB); MR4(8, KS, CA, CB, NA, NB); \
      MM4(12, CA, CB); MM4(16, CA, CB); MM4(20, CA, CB); MM4(24, CA, CB); MM(28, CA, CB); MM(29, CA, CB); \
      END; MM(30, CA, CB); MM(31, CA, CB); SB(); } while (0)
#define LGW() do { SB(); asm volatile("s_waitcnt lgkmcnt(0)" ::: "memory"); SB(); } while (0)
      HALF(1, fa0, fb0, fa1, fb1, LGBAR());
      int sl = 0;
#pragma unroll 1
      for (int i = 1; i < G2_NCH; ++i) {
        const int dl = sl == 2 ? -2 * G2_SLOT : G2_SLOT; sl = sl == 2 ? 0 : sl + 1;
        pa[0] += dl; pa[1] += dl; pb[0] += dl; pb[1] += dl; pb[2] += dl; pb[3] += dl;
        PIN(pa[0]); PIN(pa[1]); PIN(pb[0]); PIN(pb[1]); PIN(pb[2]); PIN(pb[3]); SB();
        HALF(0, fa1, fb1, fa0, fb0, LGW());
        HALF(1, fa0, fb0, fa1, fb1, LGW(); if (i < G2_NCH - 1) BARRIER());
      }
      MM4(0, fa1, fb1); MM4(4, fa1, fb1); MM4(8, fa1, fb1); MM4(12, fa1, fb1); MM4(16, fa1, fb1); MM4(20, fa1, fb1); MM4(24, fa1, fb1); MM4(28, fa1, fb1);
#undef HALF
#undef LGW
#undef MR4
#undef MM4
#undef MR
#undef MM
#undef RD
      asm volatile("s_nop 7\n\ts_nop 7" ::: "memory");
      SB();
      STAMP(3, 2);
      int te = threadIdx.x; asm volatile("" : "+v"(te));
      const int le = te & 63;
      unsigned* mine = (unsigned*)P.part + (size_t)bid * 16384 + w4 * 1024 + le * 4;
      f32x4 keep[2][4];
#define PKP(A, B) (u32x4){cvtpk(A[0], A[1]), cvtpk(A[2], A[3]), cvtpk(B[0], B[1]), cvtpk(B[2], B[3])}
#pragma unroll
      for (int q = 0; q < 4; ++q) {
        if (q != k4) {
#pragma unroll
          for (int i = 0; i < 4; ++i) st16wt(mine + q * 4096 + i * 256, PKP(acc[2 * q + (i >> 1)][2 * (i & 1)], acc[2 * q + (i >> 1)][2 * (i & 1) + 1]));
        } else {
#pragma unroll
          for (int i = 0; i < 8; ++i) keep[i >> 2][i & 3] = acc[2 * q + (i >> 2)][i & 3];
        }
      }
#undef PKP
      asm volatile("s_waitcnt vmcnt(0)" ::: "memory");
      if (le == 0) set_flag(P.flags + FX_OFF + bid * 4 + w4, tok);
      const int orow = rt2 * 256 + 128 * wm + 32 * k4 + (le & 15), ocol = ct2 * 128 + 64 * wn + 4 * (le >> 4);
      u32x2 xv[2][4];
#pragma unroll
      for (int mm = 0; mm < 2; ++mm)
#pragma unroll
        for (int n = 0; n < 4; ++n) xv[mm][n] = *(const u32x2*)(P.x + (size_t)(orow + 16 * mm) * DM + ocol + 16 * n);
      wait_flags(P.flags + FX_OFF + (bid ^ (1 + le % 3)) * 4 + w4, tok);
      STAMP(3, 4);
      u32x4 pv[3][4];
      const unsigned* t1 = (const unsigned*)P.part + (size_t)(bid ^ 1) * 16384 + k4 * 4096 + w4 * 1024 + le * 4;
      const unsigned* t2 = (const unsigned*)P.part + (size_t)(bid ^ 2) * 16384 + k4 * 4096 + w4 * 1024 + le * 4;
      const unsigned* t3 = (const unsigned*)P.part + (size_t)(bid ^ 3) * 16384 + k4 * 4096 + w4 * 1024 + le * 4;
      asm volatile("global_load_dwordx4 %0, %12, off sc1\n\tglobal_load_dwordx4 %1, %12, off offset:1024 sc1\n\t"
                   "global_load_dwordx4 %2, %12, off offset:2048 sc1\n\tglobal_load_dwordx4 %3, %12, off offset:3072 sc1\n\t"
                   "global_load_dwordx4 %4, %13, off sc1\n\tglobal_load_dwordx4 %5, %13, off offset:1024 sc1\n\t"
                   "global_load_dwordx4 %6, %13, off offset:2048 sc1\n\tglobal_load_dwordx4 %7, %13, off offset:3072 sc1\n\t"
                   "global_load_dwordx4 %8, %14, off sc1\n\tglobal_load_dwordx4 %9, %14, off offset:1024 sc1\n\t"
                   "global_load_dwordx4 %10, %14, off offset:2048 sc1\n\tglobal_load_dwordx4 %11, %14, off offset:3072 sc1\n\t"
                   "s_waitcnt vmcnt(0)"
                   : "=&v"(pv[0][0]), "=&v"(pv[0][1]), "=&v"(pv[0][2]), "=&v"(pv[0][3]), "=&v"(pv[1][0]), "=&v"(pv[1][1]), "=&v"(pv[1][2]), "=&v"(pv[1][3]),
                     "=&v"(pv[2][0]), "=&v"(pv[2][1]), "=&v"(pv[2][2]), "=&v"(pv[2][3]) : "v"(t1), "v"(t2), "v"(t3) : "memory");
#pragma unroll
      for (int i = 0; i < 8; ++i) {
        f32x4 v = keep[i >> 2][i & 3];
#pragma unroll
        for (int q = 0; q < 3; ++q) {
          const unsigned p0 = pv[q][i >> 1][2 * (i & 1)], p1 = pv[q][i >> 1][2 * (i & 1) + 1];
          v += (f32x4){bf_lo(p0), bf_hi(p0), bf_lo(p1), bf_hi(p1)};
        }
        const u32x2 xx = xv[i >> 2][i & 3];
        u32x2 o = {cvtpk(bf_lo(xx.x) + v[0], bf_hi(xx.x) + v[1]), cvtpk(bf_lo(xx.y) + v[2], bf_hi(xx.y) + v[3])};
        *(u32x2*)(P.out + (size_t)(orow + 16 * (i >> 2)) * DM + ocol + 16 * (i & 3)) = o;
      }
      STAMP(3, 3);
    }
#undef LDK
#undef LDV
#undef BF
#undef FIN1
#undef SAFE_UNIT
#undef SAFE_PASS
  }
#undef G2_DMA
#undef G2_DMA_A
#undef G2_DMA_B
#undef G2_K
#undef G2_OFFS
#undef HSTORE
#undef DMA_ST
}

extern "C" void kernel_launch(void* const* d_in, const int* in_sizes, int n_in, void* d_out, int out_size, void* d_ws,
                              size_t ws_size, hipStream_t stream) {
  char* ws = (char*)d_ws;
  Params P;
  P.x = (const bf16_t*)d_in[0];
  P.wqkv = (const bf16_t*)d_in[1]; P.wo = (const bf16_t*)d_in[2];
  P.wg = (const bf16_t*)d_in[4]; P.wu = (const bf16_t*)d_in[5]; P.wd = (const bf16_t*)d_in[6];
  P.xn = (bf16_t*)(ws + WS_XN);
  P.Qb = (bf16_t*)(ws + WS_Q); P.Kb = (bf16_t*)(ws + WS_K); P.Vb = (bf16_t*)(ws + WS_V);
  P.AH = (bf16_t*)(ws + WS_AH); P.out = (bf16_t*)d_out;
  P.part = (float*)(ws + WS_PART); P.flags = (u64*)(ws + WS_FLAGS);
  (void)hipFuncSetAttribute((const void*)fused_kernel, hipFuncAttributeMaxDynamicSharedMemorySize, LDS_TOTAL);
  fused_kernel<<<256, 512, LDS_TOTAL, stream>>>(P);
}
```

```cpp
#define DA 11
#define DB 12
#define DSEL (xcd==0)
#define DMUL 2
#include <hip/hip_runtime.h>
#include <hip/hip_bf16.h>
#include <stdint.h>

typedef unsigned short bf16_t;
typedef short bf16x8 __attribute__((ext_vector_type(8)));
typedef short s16x4 __attribute__((ext_vector_type(4)));
typedef float f32x4 __attribute__((ext_vector_type(4)));
typedef float f32x16 __attribute__((ext_vector_type(16)));
typedef float f32x2 __attribute__((ext_vector_type(2)));
typedef __bf16 bf16x2v __attribute__((ext_vector_type(2)));
typedef unsigned u32x4 __attribute__((ext_vector_type(4)));
typedef unsigned u32x2 __attribute__((ext_vector_type(2)));
#define LAS __attribute__((address_space(3)))
typedef LAS const char* lds_cptr;

constexpr int SEQ = 2048, DM = 1024, FF = 3584, NQKV = 3072;
constexpr int KA = 4608 + 64;
constexpr int HSTR = SEQ * 32 + 128;

constexpr size_t WS_FLAGS = 0;
constexpr size_t WS_XN = 1u << 20;
constexpr size_t WS_Q = WS_XN + (size_t)SEQ * DM * 2;
constexpr size_t WS_K = WS_Q + (size_t)32 * HSTR * 2;
constexpr size_t WS_V = WS_K + (size_t)32 * HSTR * 2;
constexpr size_t WS_AH = WS_V + (size_t)32 * HSTR * 2;
constexpr size_t WS_PART = WS_AH + (size_t)SEQ * KA * 2;
typedef unsigned long long u64;
constexpr int FL_OFF = 256, FA_OFF = 512, FH_OFF = 768, FB_OFF = 1024, FX_OFF = 1280;

#define SB() __builtin_amdgcn_sched_barrier(0)
#define STAMP(kid, i) do { if ((kid) * 10 + (i) == DA) dA = __builtin_amdgcn_s_memrealtime(); if ((kid) * 10 + (i) == DB) dB = __builtin_amdgcn_s_memrealtime(); } while (0)

__device__ __forceinline__ unsigned cvtpk(float lo, float hi) {
  f32x2 v = {lo, hi};
  bf16x2v b = __builtin_convertvector(v, bf16x2v);
  return __builtin_bit_cast(unsigned, b);
}
__device__ __forceinline__ float bf_lo(unsigned u) { return __uint_as_float(u << 16); }
__device__ __forceinline__ float bf_hi(unsigned u) { return __uint_as_float(u & 0xffff0000u); }
__device__ __forceinline__ float rbf(float x) { return bf_lo(cvtpk(x, x)); }

__device__ __forceinline__ void glds16(const void* g, unsigned lds_dst) {
  unsigned keep;
  asm volatile("s_mov_b32 %0, m0\n\ts_mov_b32 m0, %2\n\ts_nop 0\n\tglobal_load_lds_dwordx4 %1, off\n\ts_mov_b32 m0, %0"
               : "=&s"(keep) : "v"(g), "s"(lds_dst) : "memory");
}
__device__ __forceinline__ void glds16s(const void* sbase, unsigned voff, unsigned lds_dst) {
  unsigned keep;
  asm volatile("s_mov_b32 %0, m0\n\ts_mov_b32 m0, %3\n\ts_nop 0\n\tglobal_load_lds_dwordx4 %1, %2\n\ts_mov_b32 m0, %0"
               : "=&s"(keep) : "v"(voff), "s"(sbase), "s"(lds_dst) : "memory");
}
__device__ __forceinline__ void glds16c(const void* sbase, unsigned voff, unsigned lds_dst) {
  unsigned keep;
  asm volatile("s_mov_b32 %0, m0\n\ts_mov_b32 m0, %3\n\ts_nop 0\n\tglobal_load_lds_dwordx4 %1, %2 sc1\n\ts_mov_b32 m0, %0"
               : "=&s"(keep) : "v"(voff), "s"(sbase), "s"(lds_dst) : "memory");
}
__device__ __forceinline__ void glds4c(const void* g, unsigned lds_dst) {
  unsigned keep;
  asm volatile("s_mov_b32 %0, m0\n\ts_mov_b32 m0, %2\n\ts_nop 0\n\tglobal_load_lds_dword %1, off sc1\n\ts_mov_b32 m0, %0"
               : "=&s"(keep) : "v"(g), "s"(lds_dst) : "memory");
}
__device__ __forceinline__ bf16x8 lds_rd128(lds_cptr p) { return *(const LAS bf16x8*)p; }
__device__ __forceinline__ s16x4 lds_tr(lds_cptr p) { return __builtin_amdgcn_ds_read_tr16_b64_v4i16((LAS s16x4*)p); }
__device__ __forceinline__ bf16x8 cat8(s16x4 a, s16x4 b) { return __builtin_shufflevector(a, b, 0, 1, 2, 3, 4, 5, 6, 7); }
__device__ __forceinline__ void st16wt(void* p, u32x4 v) {
  asm volatile("global_store_dwordx4 %0, %1, off sc1\n\ts_nop 1" :: "v"(p), "v"(v) : "memory");
}
__device__ __forceinline__ void wait_flags(const u64* f, u64 tok) {
  unsigned spins = 0;
  while (!__all(__hip_atomic_load(f, __ATOMIC_RELAXED, __HIP_MEMORY_SCOPE_AGENT) == tok)) {
    __builtin_amdgcn_s_sleep(3);
    if (++spins > (1u << 20)) break;
  }
  asm volatile("" ::: "memory");
}
__device__ __forceinline__ void set_flag(u64* f, u64 tok) { __hip_atomic_store(f, tok, __ATOMIC_RELAXED, __HIP_MEMORY_SCOPE_AGENT); }
__device__ __forceinline__ void publish(LAS unsigned* cnt, u64* flag, u64 tok, int lane) {
  asm volatile("s_waitcnt vmcnt(0)" ::: "memory");
  if (lane == 0) {
    const unsigned old = __hip_atomic_fetch_add(cnt, 1u, __ATOMIC_RELAXED, __HIP_MEMORY_SCOPE_WORKGROUP);
    if (old == 7u) set_flag(flag, tok);
  }
}
__device__ __forceinline__ void wg_wait(int w, int lane, const u64* f, u64 tok, volatile LAS unsigned* go) {
  if (w == 0) { wait_flags(f, tok); if (lane == 0) *go = 1u; }
  else { while (*go == 0u) __builtin_amdgcn_s_sleep(1); asm volatile("" ::: "memory"); }
}

#define MFMA16(a, b, c) __builtin_amdgcn_mfma_f32_16x16x32_bf16(a, b, c, 0, 0, 0)
#define MFMA32(a, b, c) __builtin_amdgcn_mfma_f32_32x32x16_bf16(a, b, c, 0, 0, 0)
#define STR_(x) #x
#define WAITBAR(N) do { SB(); asm volatile("s_waitcnt vmcnt(" STR_(N) ") lgkmcnt(0)\n\ts_barrier" ::: "memory"); SB(); } while (0)
#define VMBAR(N) do { SB(); asm volatile("s_waitcnt vmcnt(" #N ")\n\ts_barrier" ::: "memory"); SB(); } while (0)
#define LGBAR() do { SB(); asm volatile("s_waitcnt lgkmcnt(0)\n\ts_barrier" ::: "memory"); SB(); } while (0)
#define BARRIER() do { SB(); asm volatile("s_barrier" ::: "memory"); SB(); } while (0)
#define MFMA16A(acc, a, b) asm volatile("v_mfma_f32_16x16x32_bf16 %0, %1, %2, %0" : "+v"(acc) : "v"(a), "v"(b))
#define ADV64(p, inc) asm volatile("v_mad_u64_u32 %0, vcc, %1, 1, %0" : "+v"(p) : "v"(inc) : "vcc")
#define PIN(x) asm volatile("" : "+v"(x))
#define EXP2(x) __builtin_amdgcn_exp2f(x)

constexpr int G1_AOFF = 0, G1_BOFF = 65536, G1_BREG = 10304, G1_BSLOT = 2 * G1_BREG;
constexpr int AT_STAGE = 32768, AT_Q = 98304, AT_EX = 131072, AT_EE = AT_EX + 16384, AT_HF = 150528;
constexpr int G2_SLOT = 49152, G2_NCH = 18;
constexpr int LDS_CTRL = 150016, LDS_SINK = 152576, LDS_TOTAL = LDS_SINK + 1024;

__device__ __forceinline__ float wave_sum(float v) {
#define ROR(n) v += __uint_as_float(__builtin_amdgcn_update_dpp(0u, __float_as_uint(v), 0x120 + (n), 0xf, 0xf, false))
  ROR(8); ROR(4); ROR(2); ROR(1);
#undef ROR
  auto a = __builtin_amdgcn_permlane16_swap(__float_as_uint(v), __float_as_uint(v), false, false);
  v = __uint_as_float(a[0]) + __uint_as_float(a[1]);
  auto b = __builtin_amdgcn_permlane32_swap(__float_as_uint(v), __float_as_uint(v), false, false);
  return __uint_as_float(b[0]) + __uint_as_float(b[1]);
}
__device__ __forceinline__ float max16(const f32x16& p) {
  float a = fmaxf(fmaxf(p[0], p[1]), p[2]), b = fmaxf(fmaxf(p[3], p[4]), p[5]);
  a = fmaxf(fmaxf(a, p[6]), p[7]); b = fmaxf(fmaxf(b, p[8]), p[9]);
  a = fmaxf(fmaxf(a, p[10]), p[11]); b = fmaxf(fmaxf(b, p[12]), p[13]);
  a = fmaxf(fmaxf(a, p[14]), p[15]);
  float m = fmaxf(a, b);
  auto rr = __builtin_amdgcn_permlane32_swap(__float_as_uint(m), __float_as_uint(m), false, false);
  return fmaxf(__uint_as_float(rr[0]), __uint_as_float(rr[1]));
}
__device__ __forceinline__ f32x16 splat16(float v) { f32x16 r;
#pragma unroll
  for (int i = 0; i < 16; ++i) r[i] = v;
  return r; }

struct Params {
  const bf16_t *x, *wqkv, *wg, *wu, *wo, *wd;
  bf16_t *xn, *Qb, *Kb, *Vb, *AH, *out;
  float* part; u64* flags;
};

__global__ __launch_bounds__(512, 2) void fused_kernel(Params P) {
  extern __shared__ __attribute__((aligned(16))) char smem[];
  const int w = __builtin_amdgcn_readfirstlane(threadIdx.x >> 6);
  const int g = w >> 2, w4 = w & 3;
  const int bid = blockIdx.x, xcd = bid & 7, jj = bid >> 3;
  const int cg = xcd * 4 + (jj & 3), rt = jj >> 2;
  const unsigned lds0 = (unsigned)(uintptr_t)smem;
  const lds_cptr L = (lds_cptr)smem;
  LAS unsigned* ctrl = (LAS unsigned*)((LAS char*)smem + LDS_CTRL);
  u64 tok;
  u64 dA = 0, dB = 0;

  u32x4 hv[8];
  bf16_t* hp0; bf16_t* hp1;
#define HSTORE(i) st16wt((i) < 4 ? hp0 + (size_t)(i) * 16 * KA : hp1 + ((w4 & 1) ? (size_t)((i) & 1) * 32 * KA : (size_t)(((i) - 2) & 3) * 16 * KA), hv[i])
  const int h = cg, qb = rt;
  const char* kg = (const char*)(P.Kb + (size_t)h * HSTR) + w4 * 2048;
  const char* vg = (const char*)(P.Vb + (size_t)h * HSTR) + w4 * 2048;
#define DMA_ST(st, KH) do { const unsigned d_ = lds0 + ((st) & 3) * AT_STAGE + (KH) * 8192 + w4 * 2048; \
    const int o_ = ((rt + (st)) & 7) * 16384 + (KH) * 8192; unsigned keep_; \
    asm volatile("s_mov_b32 %0, m0\n\ts_mov_b32 m0, %4\n\ts_nop 0\n\t" \
                 "global_load_lds_dwordx4 %1, %2 sc1\n\tglobal_load_lds_dwordx4 %1, %2 offset:1024 sc1\n\t" \
                 "s_mov_b32 m0, %5\n\ts_nop 0\n\t" \
                 "global_load_lds_dwordx4 %1, %3 sc1\n\tglobal_load_lds_dwordx4 %1, %3 offset:1024 sc1\n\ts_mov_b32 m0, %0" \
                 : "=&s"(keep_) : "v"(voff), "s"(kg + o_), "s"(vg + o_), "s"(d_), "s"(d_ + 16384u) : "memory"); } while (0)

  {
    const int lane = threadIdx.x & 63;
    const int wm = 2 * g + (w4 >> 1), wn = w4 & 1;
    STAMP(1, 0);
    const int row0 = rt * 256;
    u32x4 xa, xb; u64 ep;
    asm volatile("global_load_dwordx4 %0, %3, off\n\tglobal_load_dwordx4 %1, %3, off offset:1024\n\tglobal_load_dwordx2 %2, %4, off sc1"
                 : "=&v"(xa), "=&v"(xb), "=&v"(ep) : "v"(P.x + (size_t)(bid * 8 + w) * DM + lane * 8), "v"(P.flags) : "memory");
    if (threadIdx.x < 8) ctrl[threadIdx.x] = 0u;
    asm volatile("s_waitcnt vmcnt(0) lgkmcnt(0)\n\ts_barrier" : "+v"(xa), "+v"(xb), "+v"(ep) :: "memory");
    STAMP(0, 0);
    tok = (((u64)(unsigned)__builtin_amdgcn_readfirstlane((unsigned)(ep >> 32)) << 32) | (unsigned)__builtin_amdgcn_readfirstlane((unsigned)ep)) + 1ull;
    {
      float v[16];
#pragma unroll
      for (int i = 0; i < 4; ++i) { v[2 * i] = bf_lo(xa[i]); v[2 * i + 1] = bf_hi(xa[i]); v[8 + 2 * i] = bf_lo(xb[i]); v[9 + 2 * i] = bf_hi(xb[i]); }
      float sm = 0.f;
#pragma unroll
      for (int i = 0; i < 16; ++i) sm += v[i];
      const float mean = wave_sum(sm) * (1.f / DM);
      float q = 0.f;
#pragma unroll
      for (int i = 0; i < 16; ++i) { const float d = v[i] - mean; q += d * d; }
      const float mb = rbf(mean), vb = rbf(wave_sum(q) * (1.f / DM));
      const float inv = 1.0f / rbf(sqrtf(rbf(vb + 1e-5f)));
      u32x4 oa, ob;
#pragma unroll
      for (int i = 0; i < 4; ++i) {
        const unsigned da = cvtpk(v[2 * i] - mb, v[2 * i + 1] - mb), db = cvtpk(v[8 + 2 * i] - mb, v[9 + 2 * i] - mb);
        oa[i] = cvtpk(bf_lo(da) * inv, bf_hi(da) * inv);
        ob[i] = cvtpk(bf_lo(db) * inv, bf_hi(db) * inv);
      }
      bf16_t* op = P.xn + (size_t)(bid * 8 + w) * DM + lane * 8;
      st16wt(op, oa); st16wt(op + 512, ob);
      STAMP(0, 4);
      publish(ctrl + 0, P.flags + FL_OFF + bid, tok, lane);
      STAMP(0, 1);
    }
    unsigned avoff0 = (unsigned)((lane >> 3) * 2048 + (((lane & 7) ^ ((lane >> 4) + 2)) * 16)), avoff1 = avoff0 ^ 64u;
    const char* abase = (const char*)P.xn + (size_t)(row0 + 32 * w4) * 2048;
#define DMA_A(hh, t) do { _Pragma("unroll") for (int _i = 0; _i < 4; ++_i) \
    glds16c(abase + (size_t)(hh) * 262144 + _i * 16384 + (t) * 128, (_i & 1) ? avoff1 : avoff0, \
            lds0 + G1_AOFF + ((t) & 1) * 32768 + (hh) * 16384 + (4 * w4 + _i) * 1024); } while (0)
    const char* bptr[4]; unsigned bld[4]; unsigned bdst[4];
#pragma unroll
    for (int i = 0; i < 4; ++i) {
      const int b = (i == 0) ? (g ? 16 + w4 : 4 * w4) : 4 * w4 + i;
      const int region = b >= 10 ? 1 : 0, bi = b - 10 * region;
      const int Pc = bi * 64 + lane;
      const int k = Pc / 20, pc = Pc - 20 * k, f = pc >> 1, e8 = (pc & 1) * 8;
      const bool isq = f >= 8 || (region && (f & 3) == 3);
      const int qf = f >= 8 ? (region ? f - 4 : f - 8) : 2 + (f >> 2);
      const bf16_t* mat = isq ? P.wqkv : (f >= 4 ? P.wu : P.wg);
      const int ld = isq ? NQKV : FF;
      const int col = e8 + (isq ? (qf >> 1) * DM + 32 * cg + 16 * (qf & 1) : 112 * cg + 64 * region + 16 * (f & 3));
      bptr[i] = (const char*)(mat + (size_t)k * ld + col);
      bld[i] = (unsigned)ld * 64u;
      bdst[i] = lds0 + G1_BOFF + region * G1_BREG + bi * 1024 + (bi >= 5 ? 32 : 0);
    }
#define DMA_B(i, s) glds16(bptr[i], bdst[i] + ((s) & 3) * G1_BSLOT)
#define ADV_B() do { ADV64(bptr[0], bld[0]); ADV64(bptr[1], bld[1]); ADV64(bptr[2], bld[2]); ADV64(bptr[3], bld[3]); } while (0)
    if (g == 0) {
      DMA_B(0, 0); DMA_B(1, 0); DMA_B(2, 0); DMA_B(3, 0); ADV_B();
      DMA_B(0, 1); ADV_B();
      DMA_B(0, 2); DMA_B(1, 2); DMA_B(2, 2); DMA_B(3, 2); ADV_B();
    } else {
      DMA_B(0, 0); ADV_B();
      DMA_B(0, 1); DMA_B(1, 1); DMA_B(2, 1); DMA_B(3, 1); ADV_B();
      DMA_B(0, 2); ADV_B();
      const char* ta;
      if (w4 <= 1) ta = (const char*)(P.AH + (size_t)(lane * 32 + 15 + 16 * w4) * KA + 4608);
      else if (w4 == 2) ta = lane < 48 ? (const char*)(P.Qb + (size_t)(2 * lane + 1) * HSTR - 64)
                                       : (const char*)P.part + (size_t)((lane - 48) * 16 + xcd) * 65536 + (xcd & 3) * 16384;
      else ta = lane < 8 ? (const char*)P.wo + lane * 262144 : (lane < 36 ? (const char*)P.wd + (lane - 8) * 262144
                         : (lane < 52 ? (const char*)P.out + (lane - 36) * 262144 : (const char*)P.x + (lane - 52) * 262144));
      glds4c(ta, lds0 + LDS_SINK);
    }
    const int fr = lane & 15, fq = lane >> 4, kq = ((fq & 1) << 1) | (fq >> 1);
    const int aoff0 = G1_AOFF + (64 * wm + fr) * 128 + ((kq ^ (((fr >> 1) + 2) & 7)) * 16);
    lds_cptr pa0 = L + aoff0, pa1 = L + (aoff0 ^ 64);
    lds_cptr pb0 = L + (G1_BOFF + (8 * kq + (fr >> 2)) * 320 + (fq & 1) * 32 + 8 * (lane & 3) + G1_BREG * wn);
    lds_cptr pb1 = pb0 + 2 * G1_BSLOT;
    asm volatile("" : "+v"(pa0), "+v"(pa1), "+v"(pb0), "+v"(pb1), "+v"(avoff0), "+v"(avoff1));
    f32x4 acc[4][10];
#pragma unroll
    for (int m = 0; m < 4; ++m)
#pragma unroll
      for (int j = 0; j < 10; ++j) { acc[m][j] = (f32x4){0.f, 0.f, 0.f, 0.f}; PIN(acc[m][j]); }
    bf16x8 af[4], bfr[10];
    if (g == 0) {
      wg_wait(w, lane, P.flags + FL_OFF + rt * 32 + (lane & 31), tok, ctrl + 3);
      STAMP(0, 2);
      DMA_A(0, 0); DMA_A(1, 0); DMA_A(0, 1);
      STAMP(0, 3);
      WAITBAR(4);
    } else { WAITBAR(1); BARRIER(); }
    STAMP(1, 1);
#define STEP(U, ISSUE, WN, LASTBAR, RELAX, PF) do { \
    { const lds_cptr pa = ((U) & 1) ? pa1 : pa0; const lds_cptr pb = (((U) & 2) ? pb1 : pb0) + ((U) & 1) * G1_BSLOT; \
      _Pragma("unroll") for (int m = 0; m < 4; ++m) af[m] = lds_rd128(pa + ((U) >> 1) * 32768 + m * 2048); \
      _Pragma("unroll") for (int j = 0; j < 10; ++j) bfr[j] = cat8(lds_tr(pb + j * 32), lds_tr(pb + j * 32 + 1280)); } \
    if (ISSUE) { DMA_B(0, (U) + 3); \
      if (g == ((U) & 1)) { DMA_A(1 - ((U) & 1), 2 * it + ((U) >> 1) + 1 + ((U) & 1)); } \
      else { DMA_B(1, (U) + 3); DMA_B(2, (U) + 3); DMA_B(3, (U) + 3); \
        if (PF) { glds16s(pfp, pfoff, lds0 + LDS_SINK); pfp += pfinc; } } } \
    if ((RELAX) && g) { WAITBAR(63); } else { WAITBAR(WN); } \
    _Pragma("unroll") for (int j = 0; j < 10; ++j) { _Pragma("unroll") for (int m = 0; m < 4; ++m) MFMA16A(acc[m][j], bfr[j], af[m]); \
      if ((ISSUE) && (j & 1) && j < 8) ADV64(bptr[j >> 1], bld[j >> 1]); } \
    if (LASTBAR) BARRIER(); } while (0)
    unsigned pfoff = lane * 16; PIN(pfoff);
    const int pj = bid * 4 + w4, pn = pj < 308 ? 308 : 358;
    const char* pfp = (pj < 308 ? (const char*)P.wqkv + (size_t)(1216 + pj) * 1024
                                : (pj < 666 ? (const char*)P.wg + (size_t)(1440 + pj - 308) * 1024 : (const char*)P.wu + (size_t)(1440 + pj - 666) * 1024))
                      + (size_t)(1 - g) * pn * 1024;
    const unsigned pfinc = 2048u * pn;
    int it = 0;
    STEP(0, true, 9, true, true, true); STEP(1, true, 10, true, true, true); STEP(2, true, 10, true, false, true); STEP(3, true, 10, true, false, true);
#pragma unroll 1
    for (it = 1; it < 4; ++it) { STEP(0, true, 10, true, false, true); STEP(1, true, 10, true, false, true); STEP(2, true, 10, true, false, true); STEP(3, true, 10, true, false, true); }
#pragma unroll 1
    for (; it < 7; ++it) { STEP(0, true, 9, true, false, false); STEP(1, true, 9, true, false, false); STEP(2, true, 9, true, false, false); STEP(3, true, 9, true, false, false); }
    STEP(0, true, 9, true, false, false); STEP(1, false, 4, true, false, false); STEP(2, false, 0, true, false, false); STEP(3, false, 0, g == 0, false, false);
#undef STEP
    asm volatile("s_nop 7\n\ts_nop 7" ::: "memory");
    SB();
    STAMP(1, 2);
#undef DMA_A
#undef DMA_B
#undef ADV_B
    int tz = threadIdx.x; asm volatile("" : "+v"(tz));
    const int fr2 = tz & 15, fq2 = (tz >> 4) & 3, sel1 = fq2 & 1, c8 = (fq2 >> 1) * 8;
    const float qscale = 0.17677669529663687f * 1.4426950408889634f;
#define SWAP16(A, B, OUT) do { auto r0_ = __builtin_amdgcn_permlane16_swap(A.x, B.x, false, false); \
    auto r1_ = __builtin_amdgcn_permlane16_swap(A.y, B.y, false, false); OUT = (u32x4){r0_[0], r1_[0], r0_[1], r1_[1]}; } while (0)
#define SILU4(m, j, OUT) do { float hv_[4]; _Pragma("unroll") for (int r = 0; r < 4; ++r) { \
      const float tg_ = acc[m][j][r], tu_ = acc[m][(j) + 4][r]; \
      hv_[r] = (tg_ * tu_) * __builtin_amdgcn_rcpf(1.f + __builtin_amdgcn_exp2f(-1.4426950408889634f * tg_)); } \
    OUT = (u32x2){cvtpk(hv_[0], hv_[1]), cvtpk(hv_[2], hv_[3])}; } while (0)
#define PK4(V, SC, OUT) do { const f32x4 v_ = (V) * (SC); OUT = (u32x2){cvtpk(v_[0], v_[1]), cvtpk(v_[2], v_[3])}; } while (0)
    const int rowb = row0 + 64 * wm + fr2;
    const int rl = 64 * wm + fr2;
    __builtin_amdgcn_s_setprio(3);
    if (wn) {
      bf16_t* kd = P.Kb + (size_t)h * HSTR + (((rowb >> 5) * 2 + sel1) * 64 + (rowb & 31) + 4 * c8) * 8;
      bf16_t* vd = P.Vb + (size_t)h * HSTR + rowb * 32 + 16 * sel1 + c8;
      LAS char* kl = (LAS char*)smem + (((rl >> 5) * 2 + sel1) * 64 + (rl & 31) + 4 * c8) * 16;
      LAS char* vl = (LAS char*)smem + 16384 + rl * 64 + (16 * sel1 + c8) * 2;
#pragma unroll
      for (int m = 0; m < 4; ++m) {
        u32x2 a_, b_; u32x4 o;
        PK4(acc[m][3], 1.f, a_); PK4(acc[m][7], 1.f, b_); SWAP16(a_, b_, o);
        st16wt(kd + (m & 1) * 128 + (m >> 1) * 1024, o); *(LAS u32x4*)(kl + (m & 1) * 256 + (m >> 1) * 2048) = o;
        PK4(acc[m][8], 1.f, a_); PK4(acc[m][9], 1.f, b_); SWAP16(a_, b_, o);
        st16wt(vd + m * 512, o); *(LAS u32x4*)(vl + m * 1024) = o;
      }
    } else {
      LAS char* ql = (LAS char*)smem + AT_Q + rl * 64 + (16 * sel1 + c8) * 2;
#pragma unroll
      for (int m = 0; m < 4; ++m) {
        u32x2 a_, b_; u32x4 o; PK4(acc[m][8], qscale, a_); PK4(acc[m][9], qscale, b_); SWAP16(a_, b_, o);
        *(LAS u32x4*)(ql + m * 1024) = o;
      }
    }
    __builtin_amdgcn_s_setprio(0);
    SB();
    bf16_t* hd = P.AH + (size_t)rowb * KA + DM + 112 * cg + 64 * wn + c8;
    hp0 = hd + 16 * sel1;
    hp1 = wn ? hd + 32 + (size_t)sel1 * 16 * KA : hd + 32 + 16 * sel1;
#pragma unroll
    for (int m = 0; m < 2; ++m) { u32x2 a_, b_; SILU4(m, 0, a_); SILU4(m, 1, b_); SWAP16(a_, b_, hv[m]); PIN(hv[m]); }
    STAMP(1, 4);
    publish(ctrl + 1, P.flags + FA_OFF + cg * 8 + rt, tok, tz & 63);
#pragma unroll
    for (int m = 2; m < 4; ++m) { u32x2 a_, b_; SILU4(m, 0, a_); SILU4(m, 1, b_); SWAP16(a_, b_, hv[m]); }
    if (wn == 0) {
#pragma unroll
      for (int m = 0; m < 4; ++m) { u32x2 a_, b_; SILU4(m, 2, a_); SILU4(m, 3, b_); SWAP16(a_, b_, hv[4 + ((m + 2) & 3)]); }
    } else {
#pragma unroll
      for (int mp = 0; mp < 2; ++mp) { u32x2 a_, b_; SILU4(2 * mp, 2, a_); SILU4(2 * mp + 1, 2, b_); SWAP16(a_, b_, hv[4 + mp]); }
      hv[6] = hv[4]; hv[7] = hv[5];
    }
    STAMP(1, 3);
    STAMP(2, 0);
#undef SWAP16
#undef SILU4
#undef PK4
  }

  const int k4 = xcd & 3, rt2 = (xcd >> 2) * 4 + (jj >> 3), ct2 = jj & 7;
  int c_issued = 0;
#define G2_K(c) ((c) < 14 ? DM + (14 * k4 + (c)) * 64 : (4 * k4 + (c) - 14) * 64)
#define G2_DMA_A(c, sl) do { \
    const char* ab_ = (const char*)P.AH + (size_t)(rt2 * 256 + 64 * w4) * (KA * 2) + G2_K(c) * 2; \
    const unsigned d_ = lds0 + (sl) * G2_SLOT + w4 * 8192; unsigned keep_; \
    asm volatile("s_mov_b32 %0, m0\n\ts_mov_b32 m0, %7\n\ts_nop 0\n\t" \
                 "global_load_lds_dwordx4 %1, %5 sc1\n\tglobal_load_lds_dwordx4 %2, %5 offset:1024 sc1\n\t" \
                 "global_load_lds_dwordx4 %3, %5 offset:2048 sc1\n\tglobal_load_lds_dwordx4 %4, %5 offset:3072 sc1\n\t" \
                 "s_mov_b32 m0, %8\n\ts_nop 0\n\t" \
                 "global_load_lds_dwordx4 %1, %6 sc1\n\tglobal_load_lds_dwordx4 %2, %6 offset:1024 sc1\n\t" \
                 "global_load_lds_dwordx4 %3, %6 offset:2048 sc1\n\tglobal_load_lds_dwordx4 %4, %6 offset:3072 sc1\n\ts_mov_b32 m0, %0" \
                 : "=&s"(keep_) : "v"(avo[0]), "v"(avo[1]), "v"(avo[2]), "v"(avo[3]), "s"(ab_), "s"(ab_ + 32 * KA * 2), "s"(d_), "s"(d_ + 4096u) : "memory"); } while (0)
#define G2_DMA_B(c, sl) do { const int kg_ = G2_K(c); \
    const char* bb_ = (kg_ < DM ? (const char*)P.wo + (size_t)kg_ * 2048 : (const char*)P.wd + (size_t)(kg_ - DM) * 2048) + ct2 * 256 + w4 * 32768; \
    const unsigned d_ = lds0 + (sl) * G2_SLOT + 32768 + w4 * 4096; unsigned keep_; \
    asm volatile("s_mov_b32 %0, m0\n\ts_mov_b32 m0, %6\n\ts_nop 0\n\t" \
                 "global_load_lds_dwordx4 %1, %5\n\tglobal_load_lds_dwordx4 %2, %5 offset:1024\n\t" \
                 "global_load_lds_dwordx4 %3, %5 offset:2048\n\tglobal_load_lds_dwordx4 %4, %5 offset:3072\n\ts_mov_b32 m0, %0" \
                 : "=&s"(keep_) : "v"(bvo[0]), "v"(bvo[1]), "v"(bvo[2]), "v"(bvo[3]), "s"(bb_), "s"(d_) : "memory"); } while (0)
#define G2_DMA(c, sl) do { G2_DMA_A(c, sl); G2_DMA_B(c, sl); } while (0)
#define G2_OFFS(lane) unsigned avo[4], bvo[4]; _Pragma("unroll") for (int _i = 0; _i < 4; ++_i) { \
    avo[_i] = ((unsigned)(((lane) >> 3) * (KA * 2) + ((((lane) & 7) ^ (((lane) >> 4) + 2)) * 16)) ^ ((_i & 1) * 64u)) + _i * (8 * KA * 2 - 1024); \
    bvo[_i] = (unsigned)(((lane) >> 4) * 2048 + ((((lane) & 15) ^ (((lane) >> 4) << 1) ^ ((w4 & 1) << 3)) * 16)) + _i * (8192 - 1024); }

  {
    int tb = threadIdx.x; asm volatile("" : "+v"(tb));
    const int lane = tb & 63;
    const int qg = w4, kh = g;
    const int hi = lane >> 5;
    const unsigned voff = lane * 16;
    const short one = (((lane >> 4) & 1) == ((lane >> 2) & 1)) ? (short)0x3f80 : (short)0;
    const bf16x8 sel = {one, one, one, one, one, one, one, one};
    volatile LAS unsigned* flag = (volatile LAS unsigned*)(ctrl + 2);
    const int gq = lane >> 4;
    const int koff = kh * 8192 + lane * 16;
    const int voffl = 16384 + kh * 8192 + (4 * (gq >> 1) + ((lane & 15) >> 2)) * 64 + (gq & 1) * 32 + (lane & 3) * 8;
    const f32x16 zero16 = splat16(0.f);
    f32x16 oA = zero16, oB = zero16;
    f32x4 lA = {0.f, 0.f, 0.f, 0.f}, lB = {0.f, 0.f, 0.f, 0.f};
    float mA, mB;
    const unsigned* hflag = (const unsigned*)(P.flags + FH_OFF + (8 * k4 + ((lane >> 1) & 7)) * 8 + rt2) + (lane & 1);
    bf16x8 qa0, qa1, qb0, qb1;

#define LDK(K0, K1, p) do { K0 = lds_rd128(p); K1 = lds_rd128((p) + 1024); } while (0)
#define LDV(V0, V1, p) do { V0 = cat8(lds_tr(p), lds_tr((p) + 512)); V1 = cat8(lds_tr((p) + 1024), lds_tr((p) + 1536)); } while (0)
#define BF(x) __builtin_bit_cast(bf16x8, x)
    {
      const unsigned* kvflag = (const unsigned*)(P.flags + FA_OFF + cg * 8 + ((lane >> 1) & 7)) + (lane & 1);
      const unsigned tokh = (lane & 1) ? (unsigned)(tok >> 32) : (unsigned)tok;
#define FLAGS_OK() __all(*(volatile LAS unsigned*)((LAS char*)smem + AT_HF + w * 256 + lane * 4) == tokh)
      LGBAR();
      STAMP(2, 4);
      glds4c(kvflag, lds0 + AT_HF + w * 256);
      { const lds_cptr qp = L + AT_Q + qg * 4096 + (lane & 31) * 64 + hi * 16;
        qa0 = lds_rd128(qp); qa1 = lds_rd128(qp + 32); qb0 = lds_rd128(qp + 2048); qb1 = lds_rd128(qp + 2080); }
      bf16x8 kx0, kx1, ky0, ky1, vx0, vx1, vy0, vy1;
      LDK(kx0, kx1, L + koff);
      LDK(ky0, ky1, L + koff + 2048);
      LDV(vy0, vy1, L + voffl);
      f32x16 sA = MFMA32(kx0, qa0, zero16); sA = MFMA32(kx1, qa1, sA);
      f32x16 sB = MFMA32(kx0, qb0, zero16); sB = MFMA32(kx1, qb1, sB);
      mA = max16(sA); mB = max16(sB);
      f32x16 nA = splat16(-mA), nB = splat16(-mB);
      PIN(nA); PIN(nB);
      sA = MFMA32(kx0, qa0, nA); sA = MFMA32(kx1, qa1, sA);
      u32x4 pA0, pA1, pB0 = {0u, 0u, 0u, 0u}, pB1 = {0u, 0u, 0u, 0u};
      STAMP(2, 1);
#define E4(S, i) do { S[i] = EXP2(S[i]); S[i + 1] = EXP2(S[i + 1]); S[i + 2] = EXP2(S[i + 2]); S[i + 3] = EXP2(S[i + 3]); } while (0)
#define SLOT(SC, PC0, PC1, SN, K0, K1, Q0, Q1, NM, PP0, PP1, V0, V1, O, LS, LOADS) do { \
    SN = MFMA32(K0, Q0, NM); SB(); E4(SC, 0); PIN(SC); SB(); \
    SN = MFMA32(K1, Q1, SN); SB(); LOADS; SB(); E4(SC, 4); PIN(SC); SB(); \
    O = MFMA32(V0, BF(PP0), O); SB(); PC0[0] = cvtpk(SC[0], SC[1]); PC0[1] = cvtpk(SC[2], SC[3]); \
      SC[8] = EXP2(SC[8]); SC[9] = EXP2(SC[9]); SC[10] = EXP2(SC[10]); PIN(SC); PIN(PC0); SB(); \
    LS = MFMA16(sel, BF(PP0), LS); SB(); PC0[2] = cvtpk(SC[4], SC[5]); PC0[3] = cvtpk(SC[6], SC[7]); SC[11] = EXP2(SC[11]); PIN(SC); PIN(PC0); SB(); \
    O = MFMA32(V1, BF(PP1), O); SB(); E4(SC, 12); PIN(SC); SB(); \
    LS = MFMA16(sel, BF(PP1), LS); SB(); PC1[0] = cvtpk(SC[8], SC[9]); PC1[1] = cvtpk(SC[10], SC[11]); \
      PC1[2] = cvtpk(SC[12], SC[13]); PC1[3] = cvtpk(SC[14], SC[15]); PIN(PC1); SB(); } while (0)
#define SUB(KC0, KC1, KN0, KN1, VC0, VC1, VP0, VP1, vpu, kp2) do { \
    SLOT(sA, pA0, pA1, sB, KC0, KC1, qb0, qb1, nB, pB0, pB1, VP0, VP1, oB, lB, LDV(VC0, VC1, vpu)); \
    SLOT(sB, pB0, pB1, sA, KN0, KN1, qa0, qa1, nA, pA0, pA1, VC0, VC1, oA, lA, LDK(KC0, KC1, kp2)); } while (0)
      lds_cptr kb0 = L + koff, kb1 = L + koff + 2 * AT_STAGE, vb0 = L + voffl, vb1 = L + voffl + 2 * AT_STAGE;
      PIN(kb0); PIN(kb1); PIN(vb0); PIN(vb1);
      bool kvok = false;
#define KP(st) ((((st) & 2) ? kb1 : kb0) + ((st) & 1) * AT_STAGE)
#define VP(st) ((((st) & 2) ? vb1 : vb0) + ((st) & 1) * AT_STAGE)
#define HS(st, i) do { if ((st) == 1 || (st) == 2) HSTORE((4 * ((st) - 1) + (i)) & 7); } while (0)
#define VMW0() asm volatile("s_waitcnt vmcnt(0)" ::: "memory")
#define STAGE(st, WN, EXTRA) do { \
        SUB(kx0, kx1, ky0, ky1, vx0, vx1, vy0, vy1, VP(st), KP(st) + 4096); \
        HS(st, 0); \
        if ((st) == 0) { VMW0(); kvok = FLAGS_OK(); if (kvok) { DMA_ST(1, kh); DMA_ST(2, kh); } else glds4c(kvflag, lds0 + AT_HF + w * 256); } \
        SUB(ky0, ky1, kx0, kx1, vy0, vy1, vx0, vx1, VP(st) + 2048, KP(st) + 6144); \
        if ((st) == 0 && !kvok) { VMW0(); if (!FLAGS_OK()) wait_flags(P.flags + FA_OFF + cg * 8 + (lane & 7), tok); DMA_ST(1, kh); DMA_ST(2, kh); } \
        VMBAR(WN); \
        HS(st, 1); \
        EXTRA; \
        if ((st) < 5) {   \
          const int pc = (st) * 8 + w, kb = bid * 36 + (pc < 36 ? pc : 35); \
          glds16s(kb < 2048 ? (const char*)P.wo + (size_t)kb * 1024 : (const char*)P.wd + (size_t)(kb - 2048) * 1024, voff, lds0 + LDS_SINK); \
          DMA_ST((st) + 3, kh); } \
        SUB(kx0, kx1, ky0, ky1, vx0, vx1, vy0, vy1, VP(st) + 4096, KP((st) + 1)); \
        HS(st, 2); \
        SUB(ky0, ky1, kx0, kx1, vy0, vy1, vx0, vx1, VP(st) + 6144, KP((st) + 1) + 2048); \
        HS(st, 3); } while (0)
      STAGE(0, 4, ); STAGE(1, 6, ); STAGE(2, 10, ); STAGE(3, 11, );
      STAGE(4, 5, if (w == 0 && lane == 0) set_flag(P.flags + FH_OFF + cg * 8 + rt, tok));
      STAGE(5, 5, if (g == 1) glds4c(hflag, lds0 + AT_HF + w * 256));
      STAGE(6, 0, if (g == 1 && FLAGS_OK()) { G2_OFFS(lane); G2_DMA(0, 0); c_issued = 2; });
      STAGE(7, 0, if (c_issued == 2) { G2_OFFS(lane); G2_DMA(1, 1); c_issued = 3; });
#undef VMW0
#undef FLAGS_OK
#undef STAGE
#undef HS
#undef KP
#undef VP
      oB = MFMA32(vy0, BF(pB0), oB); lB = MFMA16(sel, BF(pB0), lB);
      oB = MFMA32(vy1, BF(pB1), oB); lB = MFMA16(sel, BF(pB1), lB);
#undef SUB
#undef SLOT
#undef E4
    }
    STAMP(2, 2);
    LAS u32x4* xw = (LAS u32x4*)((LAS char*)smem + AT_EX) + w4 * 256;
    LAS float* ew = (LAS float*)((LAS char*)smem + AT_EE) + w4 * 128;
    float eA, eB;
#define FIN1(FIRST) do { \
        \
      float chk = 0.f; \
      _Pragma("unroll") for (int r = 0; r < 16; ++r) { chk = __builtin_fmaf(oA[r], 0.f, chk); chk = __builtin_fmaf(oB[r], 0.f, chk); } \
      const bool bad = !(lA[0] < 1.0e36f) || !(lB[0] < 1.0e36f) || !(chk == 0.f); \
      if (FIRST) { if (__any(bad) && lane == 0) *flag = 1u; \
                   if (c_issued == 3 && lane == 0) __hip_atomic_fetch_add(ctrl + 5, 1u, __ATOMIC_RELAXED, __HIP_MEMORY_SCOPE_WORKGROUP); } \
      oA *= __builtin_amdgcn_rcpf(lA[0]); oB *= __builtin_amdgcn_rcpf(lB[0]); \
      eA = mA + __builtin_amdgcn_logf(lA[0]); eB = mB + __builtin_amdgcn_logf(lB[0]);     \
      if (g == 0) { \
        _Pragma("unroll") for (int r = 0; r < 2; ++r) { \
          xw[r * 64 + lane] = (u32x4){cvtpk(oA[8 * r], oA[8 * r + 1]), cvtpk(oA[8 * r + 2], oA[8 * r + 3]), cvtpk(oA[8 * r + 4], oA[8 * r + 5]), cvtpk(oA[8 * r + 6], oA[8 * r + 7])}; \
          xw[128 + r * 64 + lane] = (u32x4){cvtpk(oB[8 * r], oB[8 * r + 1]), cvtpk(oB[8 * r + 2], oB[8 * r + 3]), cvtpk(oB[8 * r + 4], oB[8 * r + 5]), cvtpk(oB[8 * r + 6], oB[8 * r + 7])}; } \
        ew[lane] = eA; ew[64 + lane] = eB; } } while (0)
#define SAFE_UNIT(Q0, Q1, M, O, LS) do { \
            f32x16 s_ = MFMA32(k0, Q0, zero16); s_ = MFMA32(k1, Q1, s_); \
            const float mn_ = fmaxf(M, max16(s_)); const float al_ = EXP2(M - mn_); M = mn_; \
            O *= al_; LS *= al_; u32x4 p0_, p1_; \
            _Pragma("unroll") for (int r = 0; r < 4; ++r) { p0_[r] = cvtpk(EXP2(s_[2 * r] - mn_), EXP2(s_[2 * r + 1] - mn_)); \
              p1_[r] = cvtpk(EXP2(s_[8 + 2 * r] - mn_), EXP2(s_[9 + 2 * r] - mn_)); } \
            O = MFMA32(v0, BF(p0_), O); LS = MFMA16(sel, BF(p0_), LS); O = MFMA32(v1, BF(p1_), O); LS = MFMA16(sel, BF(p1_), LS); } while (0)
#define SAFE_PASS() do { \
        WAITBAR(0); \
        DMA_ST(0, kh); DMA_ST(1, kh); DMA_ST(2, kh); \
        oA = zero16; oB = zero16; lA = (f32x4){0.f, 0.f, 0.f, 0.f}; lB = lA; \
        mA = -3.0e38f; mB = -3.0e38f; \
        _Pragma("unroll 1") for (int st = 0; st < 8; ++st) { \
          if (st < 6) WAITBAR(8); else if (st == 6) WAITBAR(4); else WAITBAR(0); \
          if (st < 5) DMA_ST(st + 3, kh); \
          _Pragma("unroll 1") for (int u = 0; u < 4; ++u) { \
            bf16x8 k0, k1, v0, v1; \
            LDK(k0, k1, L + (st & 3) * AT_STAGE + koff + u * 2048); \
            LDV(v0, v1, L + (st & 3) * AT_STAGE + voffl + u * 2048); \
            SAFE_UNIT(qa0, qa1, mA, oA, lA); \
            SAFE_UNIT(qb0, qb1, mB, oB, lB); } } \
        FIN1(false); LGBAR(); } while (0)
    FIN1(true);
    STAMP(2, 3);

    STAMP(3, 0);
    {
      const int wm = w4 >> 1, wn = w4 & 1;
      const int fr = lane & 15, fq = lane >> 4, kq = ((fq & 1) << 1) | (fq >> 1), q4 = fr >> 2;
      const int aoff0 = (128 * wm + fr) * 128 + ((kq ^ (((fr >> 1) + 2) & 7)) * 16);
      const int boff0 = 32768 + (8 * kq + q4) * 256 + 8 * (lane & 3);
      const int bsw = q4 | ((fq & 1) << 2);
      lds_cptr pa[2], pb[4];
      pa[0] = L + aoff0; pa[1] = L + (aoff0 ^ 64); PIN(pa[0]); PIN(pa[1]);
#pragma unroll
      for (int n = 0; n < 4; ++n) { pb[n] = L + (boff0 + (((4 * wn + n) ^ bsw) * 32)); PIN(pb[n]); }
      bf16x8 fa0[8], fb0[4], fa1[8], fb1[4];
#define RD(i, KS, FA, FB) do { if ((i) < 4) FB[(i) & 3] = cat8(lds_tr(pb[(i) & 3] + (KS) * 8192), lds_tr(pb[(i) & 3] + (KS) * 8192 + 1024)); \
      else FA[((i) - 4) & 7] = lds_rd128(pa[KS] + (((i) - 4) & 7) * 2048); } while (0)
#define RD12(KS, FA, FB) do { RD(0, KS, FA, FB); RD(1, KS, FA, FB); RD(2, KS, FA, FB); RD(3, KS, FA, FB); RD(4, KS, FA, FB); RD(5, KS, FA, FB); \
      RD(6, KS, FA, FB); RD(7, KS, FA, FB); RD(8, KS, FA, FB); RD(9, KS, FA, FB); RD(10, KS, FA, FB); RD(11, KS, FA, FB); } while (0)
      if (g == 0) RD12(0, fa0, fb0);
      LGBAR();
      const bool ovf = *flag != 0u, fast = !ovf && *(volatile LAS unsigned*)(ctrl + 5) == 4u;
      if (!fast) {
        if (ovf) SAFE_PASS();
        if (g == 1) {
          G2_OFFS(lane);
          wait_flags(P.flags + FH_OFF + (8 * k4 + (lane & 7)) * 8 + rt2, tok);
          G2_DMA(0, 0); G2_DMA(1, 1);
        }
        VMBAR(12);
        if (g == 0) RD12(0, fa0, fb0);
        LGBAR();
      }
#undef RD12
      if (g == 1) {
#define FIN2(O, E, BL) do { \
      const float e2 = ew[(BL) * 64 + lane]; const u32x4 t0 = xw[(BL) * 128 + lane], t1 = xw[(BL) * 128 + 64 + lane]; \
      const float em = fmaxf(E, e2); float w1 = EXP2(E - em), w2 = EXP2(e2 - em); \
      const float rs = __builtin_amdgcn_rcpf(w1 + w2); w1 *= rs; w2 *= rs; u32x2 pk[4]; \
      _Pragma("unroll") for (int r = 0; r < 4; ++r) { \
        const unsigned ta = r < 2 ? t0[2 * r] : t1[2 * r - 4], tb2 = r < 2 ? t0[2 * r + 1] : t1[2 * r - 3]; \
        pk[r].x = cvtpk(O[4 * r] * w1 + bf_lo(ta) * w2, O[4 * r + 1] * w1 + bf_hi(ta) * w2); \
        pk[r].y = cvtpk(O[4 * r + 2] * w1 + bf_lo(tb2) * w2, O[4 * r + 3] * w1 + bf_hi(tb2) * w2); } \
      bf16_t* op = P.AH + (size_t)(qb * 256 + qg * 64 + (BL) * 32 + (lane & 31)) * KA + h * 32 + 8 * hi; \
      _Pragma("unroll") for (int k = 0; k < 4; k += 2) { \
        auto rx = __builtin_amdgcn_permlane32_swap(pk[k].x, pk[k + 1].x, false, false); \
        auto ry = __builtin_amdgcn_permlane32_swap(pk[k].y, pk[k + 1].y, false, false); \
        st16wt(op + 8 * k, (u32x4){rx[0], ry[0], rx[1], ry[1]}); } } while (0)
      __builtin_amdgcn_s_setprio(3);
      { G2_OFFS(lane); (void)bvo; G2_DMA_A(2, 2); }
      FIN2(oA, eA, 0); FIN2(oB, eB, 1);
#undef FIN2
      }
      SB();
      if (g == 1) {
      int tl = threadIdx.x; asm volatile("" : "+v"(tl));
      const int ll = tl & 63;
      G2_OFFS(ll);
      WAITBAR(12);
      G2_DMA_B(2, 2); G2_DMA(3, 0); VMBAR(12);
      if (w == 4 && ll == 0) set_flag(P.flags + FB_OFF + qb * 32 + h, tok);
      int sl = 1;
#pragma unroll 1
      for (int i = 2; i < G2_NCH - 2; ++i) {
        const u64* of = P.flags + FB_OFF + rt2 * 32 + 8 * k4;
        if (i == 10) glds4c((const unsigned*)(of + ((ll >> 1) & 7)) + (ll & 1), lds0 + AT_HF + w * 256);
        if (i == 12) {
          const unsigned hf = *(volatile LAS unsigned*)((LAS char*)smem + AT_HF + w * 256 + ll * 4);
          if (!__all(hf == ((ll & 1) ? (unsigned)(tok >> 32) : (unsigned)tok))) wait_flags(of + (ll & 7), tok);
        }
        G2_DMA(i + 2, sl); sl = sl == 2 ? 0 : sl + 1; VMBAR(12);
      }
      VMBAR(0);
      if (bid == 0 && w == 4) {
#pragma unroll
        for (int i = 0; i < 4; ++i) wait_flags(P.flags + FA_OFF + i * 64 + ll, tok);
        if (ll == 0) set_flag(P.flags, tok);
      }
      STAMP(3, 3);
      __builtin_amdgcn_endpgm();
      }
      f32x4 acc[8][4];
#pragma unroll
      for (int m = 0; m < 8; ++m)
#pragma unroll
        for (int n = 0; n < 4; ++n) { acc[m][n] = (f32x4){0.f, 0.f, 0.f, 0.f}; PIN(acc[m][n]); }
      asm volatile("s_nop 1" ::: "memory");
      STAMP(3, 1);
#define MM(i, FA, FB) MFMA16A(acc[(i) >> 2][(i) & 3], FB[(i) & 3], FA[(i) >> 2])
#define MR(i, KS, CA, CB, NA, NB) do { MM(i, CA, CB); RD(i, KS, NA, NB); SB(); } while (0)
#define MR4(i, KS, CA, CB, NA, NB) do { MR(i, KS, CA, CB, NA, NB); MR(i + 1, KS, CA, CB, NA, NB); MR(i + 2, KS, CA, CB, NA, NB); MR(i + 3, KS, CA, CB, NA, NB); } while (0)
#define MM4(i, CA, CB) do { MM(i, CA, CB); MM(i + 1, CA, CB); MM(i + 2, CA, CB); MM(i + 3, CA, CB); } while (0)
#define HALF(KS, CA, CB, NA, NB, END) do { \
      MR4(0, KS, CA, CB, NA, NB); MR4(4, KS, CA, CB, NA, NB); MR4(8, KS, CA, CB, NA, NB); \
      MM4(12, CA, CB); MM4(16, CA, CB); MM4(20, CA, CB); MM4(24, CA, CB); MM(28, CA, CB); MM(29, CA, CB); \
      END; MM(30, CA, CB); MM(31, CA, CB); SB(); } while (0)
#define LGW() do { SB(); asm volatile("s_waitcnt lgkmcnt(0)" ::: "memory"); SB(); } while (0)
      HALF(1, fa0, fb0, fa1, fb1, LGBAR());
      int sl = 0;
#pragma unroll 1
      for (int i = 1; i < G2_NCH; ++i) {
        const int dl = sl == 2 ? -2 * G2_SLOT : G2_SLOT; sl = sl == 2 ? 0 : sl + 1;
        pa[0] += dl; pa[1] += dl; pb[0] += dl; pb[1] += dl; pb[2] += dl; pb[3] += dl;
        PIN(pa[0]); PIN(pa[1]); PIN(pb[0]); PIN(pb[1]); PIN(pb[2]); PIN(pb[3]); SB();
        HALF(0, fa1, fb1, fa0, fb0, LGW());
        HALF(1, fa0, fb0, fa1, fb1, LGW(); if (i < G2_NCH - 1) BARRIER());
      }
      MM4(0, fa1, fb1); MM4(4, fa1, fb1); MM4(8, fa1, fb1); MM4(12, fa1, fb1); MM4(16, fa1, fb1); MM4(20, fa1, fb1); MM4(24, fa1, fb1); MM4(28, fa1, fb1);
#undef HALF
#undef LGW
#undef MR4
#undef MM4
#undef MR
#undef MM
#undef RD
      asm volatile("s_nop 7\n\ts_nop 7" ::: "memory");
      SB();
      STAMP(3, 2);
      int te = threadIdx.x; asm volatile("" : "+v"(te));
      const int le = te & 63;
      unsigned* mine = (unsigned*)P.part + (size_t)bid * 16384 + w4 * 1024 + le * 4;
      f32x4 keep[2][4];
#define PKP(A, B) (u32x4){cvtpk(A[0], A[1]), cvtpk(A[2], A[3]), cvtpk(B[0], B[1]), cvtpk(B[2], B[3])}
#pragma unroll
      for (int q = 0; q < 4; ++q) {
        if (q != k4) {
#pragma unroll
          for (int i = 0; i < 4; ++i) st16wt(mine + q * 4096 + i * 256, PKP(acc[2 * q + (i >> 1)][2 * (i & 1)], acc[2 * q + (i >> 1)][2 * (i & 1) + 1]));
        } else {
#pragma unroll
          for (int i = 0; i < 8; ++i) keep[i >> 2][i & 3] = acc[2 * q + (i >> 2)][i & 3];
        }
      }
#undef PKP
      asm volatile("s_waitcnt vmcnt(0)" ::: "memory");
      if (le == 0) set_flag(P.flags + FX_OFF + bid * 4 + w4, tok);
      const int orow = rt2 * 256 + 128 * wm + 32 * k4 + (le & 15), ocol = ct2 * 128 + 64 * wn + 4 * (le >> 4);
      u32x2 xv[2][4];
#pragma unroll
      for (int mm = 0; mm < 2; ++mm)
#pragma unroll
        for (int n = 0; n < 4; ++n) xv[mm][n] = *(const u32x2*)(P.x + (size_t)(orow + 16 * mm) * DM + ocol + 16 * n);
      wait_flags(P.flags + FX_OFF + (bid ^ (1 + le % 3)) * 4 + w4, tok);
      STAMP(3, 4);
      u32x4 pv[3][4];
      const unsigned* t1 = (const unsigned*)P.part + (size_t)(bid ^ 1) * 16384 + k4 * 4096 + w4 * 1024 + le * 4;
      const unsigned* t2 = (const unsigned*)P.part + (size_t)(bid ^ 2) * 16384 + k4 * 4096 + w4 * 1024 + le * 4;
      const unsigned* t3 = (const unsigned*)P.part + (size_t)(bid ^ 3) * 16384 + k4 * 4096 + w4 * 1024 + le * 4;
      asm volatile("global_load_dwordx4 %0, %12, off sc1\n\tglobal_load_dwordx4 %1, %12, off offset:1024 sc1\n\t"
                   "global_load_dwordx4 %2, %12, off offset:2048 sc1\n\tglobal_load_dwordx4 %3, %12, off offset:3072 sc1\n\t"
                   "global_load_dwordx4 %4, %13, off sc1\n\tglobal_load_dwordx4 %5, %13, off offset:1024 sc1\n\t"
                   "global_load_dwordx4 %6, %13, off offset:2048 sc1\n\tglobal_load_dwordx4 %7, %13, off offset:3072 sc1\n\t"
                   "global_load_dwordx4 %8, %14, off sc1\n\tglobal_load_dwordx4 %9, %14, off offset:1024 sc1\n\t"
                   "global_load_dwordx4 %10, %14, off offset:2048 sc1\n\tglobal_load_dwordx4 %11, %14, off offset:3072 sc1\n\t"
                   "s_waitcnt vmcnt(0)"
                   : "=&v"(pv[0][0]), "=&v"(pv[0][1]), "=&v"(pv[0][2]), "=&v"(pv[0][3]), "=&v"(pv[1][0]), "=&v"(pv[1][1]), "=&v"(pv[1][2]), "=&v"(pv[1][3]),
                     "=&v"(pv[2][0]), "=&v"(pv[2][1]), "=&v"(pv[2][2]), "=&v"(pv[2][3]) : "v"(t1), "v"(t2), "v"(t3) : "memory");
#pragma unroll
      for (int i = 0; i < 8; ++i) {
        f32x4 v = keep[i >> 2][i & 3];
#pragma unroll
        for (int q = 0; q < 3; ++q) {
          const unsigned p0 = pv[q][i >> 1][2 * (i & 1)], p1 = pv[q][i >> 1][2 * (i & 1) + 1];
          v += (f32x4){bf_lo(p0), bf_hi(p0), bf_lo(p1), bf_hi(p1)};
        }
        const u32x2 xx = xv[i >> 2][i & 3];
        u32x2 o = {cvtpk(bf_lo(xx.x) + v[0], bf_hi(xx.x) + v[1]), cvtpk(bf_lo(xx.y) + v[2], bf_hi(xx.y) + v[3])};
        *(u32x2*)(P.out + (size_t)(orow + 16 * (i >> 2)) * DM + ocol + 16 * (i & 3)) = o;
      }
      STAMP(3, 3);
      if (DSEL) { const u64 tend = __builtin_amdgcn_s_memrealtime() + DMUL * (dB - dA); while (__builtin_amdgcn_s_memrealtime() < tend) __builtin_amdgcn_s_sleep(1); }
    }
#undef LDK
#undef LDV
#undef BF
#undef FIN1
#undef SAFE_UNIT
#undef SAFE_PASS
  }
#undef G2_DMA
#undef G2_DMA_A
#undef G2_DMA_B
#undef G2_K
#undef G2_OFFS
#undef HSTORE
#undef DMA_ST
}

extern "C" void kernel_launch(void* const* d_in, const int* in_sizes, int n_in, void* d_out, int out_size, void* d_ws,
                              size_t ws_size, hipStream_t stream) {
  char* ws = (char*)d_ws;
  Params P;
  P.x = (const bf16_t*)d_in[0];
  P.wqkv = (const bf16_t*)d_in[1]; P.wo = (const bf16_t*)d_in[2];
  P.wg = (const bf16_t*)d_in[4]; P.wu = (const bf16_t*)d_in[5]; P.wd = (const bf16_t*)d_in[6];
  P.xn = (bf16_t*)(ws + WS_XN);
  P.Qb = (bf16_t*)(ws + WS_Q); P.Kb = (bf16_t*)(ws + WS_K); P.Vb = (bf16_t*)(ws + WS_V);
  P.AH = (bf16_t*)(ws + WS_AH); P.out = (bf16_t*)d_out;
  P.part = (float*)(ws + WS_PART); P.flags = (u64*)(ws + WS_FLAGS);
  (void)hipFuncSetAttribute((const void*)fused_kernel, hipFuncAttributeMaxDynamicSharedMemorySize, LDS_TOTAL);
  fused_kernel<<<256, 512, LDS_TOTAL, stream>>>(P);
}
```

```cpp
#include <hip/hip_runtime.h>
#include <hip/hip_bf16.h>
#include <stdint.h>

typedef unsigned short bf16_t;
typedef short bf16x8 __attribute__((ext_vector_type(8)));
typedef short s16x4 __attribute__((ext_vector_type(4)));
typedef float f32x4 __attribute__((ext_vector_type(4)));
typedef float f32x16 __attribute__((ext_vector_type(16)));
typedef float f32x2 __attribute__((ext_vector_type(2)));
typedef __bf16 bf16x2v __attribute__((ext_vector_type(2)));
typedef unsigned u32x4 __attribute__((ext_vector_type(4)));
typedef unsigned u32x2 __attribute__((ext_vector_type(2)));
#define LAS __attribute__((address_space(3)))
typedef LAS const char* lds_cptr;

constexpr int SEQ = 2048, DM = 1024, FF = 3584, NQKV = 3072;
constexpr int KA = 4608 + 64;
constexpr int HSTR = SEQ * 32 + 128;

constexpr size_t WS_FLAGS = 0;
constexpr size_t WS_XN = 1u << 20;
constexpr size_t WS_Q = WS_XN + (size_t)SEQ * DM * 2;
constexpr size_t WS_K = WS_Q + (size_t)32 * HSTR * 2;
constexpr size_t WS_V = WS_K + (size_t)32 * HSTR * 2;
constexpr size_t WS_AH = WS_V + (size_t)32 * HSTR * 2;
constexpr size_t WS_PART = WS_AH + (size_t)SEQ * KA * 2;
typedef unsigned long long u64;
constexpr int FL_OFF = 256, FA_OFF = 512, FH_OFF = 768, FB_OFF = 1024, FX_OFF = 1280;

#define SB() __builtin_amdgcn_sched_barrier(0)

__device__ __forceinline__ unsigned cvtpk(float lo, float hi) {
  f32x2 v = {lo, hi};
  bf16x2v b = __builtin_convertvector(v, bf16x2v);
  return __builtin_bit_cast(unsigned, b);
}
__device__ __forceinline__ float bf_lo(unsigned u) { return __uint_as_float(u << 16); }
__device__ __forceinline__ float bf_hi(unsigned u) { return __uint_as_float(u & 0xffff0000u); }
__device__ __forceinline__ float rbf(float x) { return bf_lo(cvtpk(x, x)); }

__device__ __forceinline__ void glds16(const void* g, unsigned lds_dst) {
  unsigned keep;
  asm volatile("s_mov_b32 %0, m0\n\ts_mov_b32 m0, %2\n\ts_nop 0\n\tglobal_load_lds_dwordx4 %1, off\n\ts_mov_b32 m0, %0"
               : "=&s"(keep) : "v"(g), "s"(lds_dst) : "memory");
}
__device__ __forceinline__ void glds16s(const void* sbase, unsigned voff, unsigned lds_dst) {
  unsigned keep;
  asm volatile("s_mov_b32 %0, m0\n\ts_mov_b32 m0, %3\n\ts_nop 0\n\tglobal_load_lds_dwordx4 %1, %2\n\ts_mov_b32 m0, %0"
               : "=&s"(keep) : "v"(voff), "s"(sbase), "s"(lds_dst) : "memory");
}
__device__ __forceinline__ void glds16c(const void* sbase, unsigned voff, unsigned lds_dst) {
  unsigned keep;
  asm volatile("s_mov_b32 %0, m0\n\ts_mov_b32 m0, %3\n\ts_nop 0\n\tglobal_load_lds_dwordx4 %1, %2 sc1\n\ts_mov_b32 m0, %0"
               : "=&s"(keep) : "v"(voff), "s"(sbase), "s"(lds_dst) : "memory");
}
__device__ __forceinline__ void glds4c(const void* g, unsigned lds_dst) {
  unsigned keep;
  asm volatile("s_mov_b32 %0, m0\n\ts_mov_b32 m0, %2\n\ts_nop 0\n\tglobal_load_lds_dword %1, off sc1\n\ts_mov_b32 m0, %0"
               : "=&s"(keep) : "v"(g), "s"(lds_dst) : "memory");
}
__device__ __forceinline__ bf16x8 lds_rd128(lds_cptr p) { return *(const LAS bf16x8*)p; }
__device__ __forceinline__ s16x4 lds_tr(lds_cptr p) { return __builtin_amdgcn_ds_read_tr16_b64_v4i16((LAS s16x4*)p); }
__device__ __forceinline__ bf16x8 cat8(s16x4 a, s16x4 b) { return __builtin_shufflevector(a, b, 0, 1, 2, 3, 4, 5, 6, 7); }
__device__ __forceinline__ void st16wt(void* p, u32x4 v) {
  asm volatile("global_store_dwordx4 %0, %1, off sc1\n\ts_nop 1" :: "v"(p), "v"(v) : "memory");
}
__device__ __forceinline__ void wait_flags(const u64* f, u64 tok) {
  unsigned spins = 0;
  while (!__all(__hip_atomic_load(f, __ATOMIC_RELAXED, __HIP_MEMORY_SCOPE_AGENT) == tok)) {
    __builtin_amdgcn_s_sleep(3);
    if (++spins > (1u << 20)) break;
  }
  asm volatile("" ::: "memory");
}
__device__ __forceinline__ void set_flag(u64* f, u64 tok) { __hip_atomic_store(f, tok, __ATOMIC_RELAXED, __HIP_MEMORY_SCOPE_AGENT); }
__device__ __forceinline__ void publish(LAS unsigned* cnt, u64* flag, u64 tok, int lane) {
  asm volatile("s_waitcnt vmcnt(0)" ::: "memory");
  if (lane == 0) {
    const unsigned old = __hip_atomic_fetch_add(cnt, 1u, __ATOMIC_RELAXED, __HIP_MEMORY_SCOPE_WORKGROUP);
    if (old == 7u) set_flag(flag, tok);
  }
}
__device__ __forceinline__ void wg_wait(int w, int lane, const u64* f, u64 tok, volatile LAS unsigned* go) {
  if (w == 0) { wait_flags(f, tok); if (lane == 0) *go = 1u; }
  else { while (*go == 0u) __builtin_amdgcn_s_sleep(1); asm volatile("" ::: "memory"); }
}

#define MFMA16(a, b, c) __builtin_amdgcn_mfma_f32_16x16x32_bf16(a, b, c, 0, 0, 0)
#define MFMA32(a, b, c) __builtin_amdgcn_mfma_f32_32x32x16_bf16(a, b, c, 0, 0, 0)
#define STR_(x) #x
#define WAITBAR(N) do { SB(); asm volatile("s_waitcnt vmcnt(" STR_(N) ") lgkmcnt(0)\n\ts_barrier" ::: "memory"); SB(); } while (0)
#define VMBAR(N) do { SB(); asm volatile("s_waitcnt vmcnt(" #N ")\n\ts_barrier" ::: "memory"); SB(); } while (0)
#define LGBAR() do { SB(); asm volatile("s_waitcnt lgkmcnt(0)\n\ts_barrier" ::: "memory"); SB(); } while (0)
#define BARRIER() do { SB(); asm volatile("s_barrier" ::: "memory"); SB(); } while (0)
#define MFMA16A(acc, a, b) asm volatile("v_mfma_f32_16x16x32_bf16 %0, %1, %2, %0" : "+v"(acc) : "v"(a), "v"(b))
#define ADV64(p, inc) asm volatile("v_mad_u64_u32 %0, vcc, %1, 1, %0" : "+v"(p) : "v"(inc) : "vcc")
#define PIN(x) asm volatile("" : "+v"(x))
#define EXP2(x) __builtin_amdgcn_exp2f(x)

constexpr int G1_AOFF = 0, G1_BOFF = 65536, G1_BREG = 10304, G1_BSLOT = 2 * G1_BREG;
constexpr int AT_STAGE = 32768, AT_Q = 98304, AT_EX = 131072, AT_EE = AT_EX + 16384, AT_HF = 150528;
constexpr int G2_SLOT = 49152, G2_NCH = 18;
constexpr int LDS_CTRL = 150016, LDS_SINK = 152576, LDS_TOTAL = LDS_SINK + 1024;

__device__ __forceinline__ float wave_sum(float v) {
#define ROR(n) v += __uint_as_float(__builtin_amdgcn_update_dpp(0u, __float_as_uint(v), 0x120 + (n), 0xf, 0xf, false))
  ROR(8); ROR(4); ROR(2); ROR(1);
#undef ROR
  auto a = __builtin_amdgcn_permlane16_swap(__float_as_uint(v), __float_as_uint(v), false, false);
  v = __uint_as_float(a[0]) + __uint_as_float(a[1]);
  auto b = __builtin_amdgcn_permlane32_swap(__float_as_uint(v), __float_as_uint(v), false, false);
  return __uint_as_float(b[0]) + __uint_as_float(b[1]);
}
__device__ __forceinline__ float max16(const f32x16& p) {
  float a = fmaxf(fmaxf(p[0], p[1]), p[2]), b = fmaxf(fmaxf(p[3], p[4]), p[5]);
  a = fmaxf(fmaxf(a, p[6]), p[7]); b = fmaxf(fmaxf(b, p[8]), p[9]);
  a = fmaxf(fmaxf(a, p[10]), p[11]); b = fmaxf(fmaxf(b, p[12]), p[13]);
  a = fmaxf(fmaxf(a, p[14]), p[15]);
  float m = fmaxf(a, b);
  auto rr = __builtin_amdgcn_permlane32_swap(__float_as_uint(m), __float_as_uint(m), false, false);
  return fmaxf(__uint_as_float(rr[0]), __uint_as_float(rr[1]));
}
__device__ __forceinline__ f32x16 splat16(float v) { f32x16 r;
#pragma unroll
  for (int i = 0; i < 16; ++i) r[i] = v;
  return r; }

struct Params {
  const bf16_t *x, *wqkv, *wg, *wu, *wo, *wd;
  bf16_t *xn, *Qb, *Kb, *Vb, *AH, *out;
  float* part; u64* flags;
};

__global__ __launch_bounds__(512, 2) void fused_kernel(Params P) {
  extern __shared__ __attribute__((aligned(16))) char smem[];
  const int w = __builtin_amdgcn_readfirstlane(threadIdx.x >> 6);
  const int g = w >> 2, w4 = w & 3;
  const int bid = blockIdx.x, xcd = bid & 7, jj = bid >> 3;
  const int cg = xcd * 4 + (jj & 3), rt = jj >> 2;
  const unsigned lds0 = (unsigned)(uintptr_t)smem;
  const lds_cptr L = (lds_cptr)smem;
  LAS unsigned* ctrl = (LAS unsigned*)((LAS char*)smem + LDS_CTRL);
  u64 tok;

  u32x4 hv[8];
  bf16_t* hp0; bf16_t* hp1;
#define HSTORE(i) st16wt((i) < 4 ? hp0 + (size_t)(i) * 16 * KA : hp1 + ((w4 & 1) ? (size_t)((i) & 1) * 32 * KA : (size_t)(((i) - 2) & 3) * 16 * KA), hv[i])
  const int h = cg, qb = rt;
  const char* kg = (const char*)(P.Kb + (size_t)h * HSTR) + w4 * 2048;
  const char* vg = (const char*)(P.Vb + (size_t)h * HSTR) + w4 * 2048;
#define DMA_ST(st, KH) do { const unsigned d_ = lds0 + ((st) & 3) * AT_STAGE + (KH) * 8192 + w4 * 2048; \
    const int o_ = ((rt + (st)) & 7) * 16384 + (KH) * 8192; unsigned keep_; \
    asm volatile("s_mov_b32 %0, m0\n\ts_mov_b32 m0, %4\n\ts_nop 0\n\t" \
                 "global_load_lds_dwordx4 %1, %2 sc1\n\tglobal_load_lds_dwordx4 %1, %2 offset:1024 sc1\n\t" \
                 "s_mov_b32 m0, %5\n\ts_nop 0\n\t" \
                 "global_load_lds_dwordx4 %1, %3 sc1\n\tglobal_load_lds_dwordx4 %1, %3 offset:1024 sc1\n\ts_mov_b32 m0, %0" \
                 : "=&s"(keep_) : "v"(voff), "s"(kg + o_), "s"(vg + o_), "s"(d_), "s"(d_ + 16384u) : "memory"); } while (0)

  {
    const int lane = threadIdx.x & 63;
    const int wm = 2 * g + (w4 >> 1), wn = w4 & 1;
    const int row0 = rt * 256;
    u32x4 xa, xb; u64 ep;
    asm volatile("global_load_dwordx4 %0, %3, off\n\tglobal_load_dwordx4 %1, %3, off offset:1024\n\tglobal_load_dwordx2 %2, %4, off sc1"
                 : "=&v"(xa), "=&v"(xb), "=&v"(ep) : "v"(P.x + (size_t)(bid * 8 + w) * DM + lane * 8), "v"(P.flags) : "memory");
    if (threadIdx.x < 8) ctrl[threadIdx.x] = 0u;
    asm volatile("s_waitcnt vmcnt(0) lgkmcnt(0)\n\ts_barrier" : "+v"(xa), "+v"(xb), "+v"(ep) :: "memory");
    tok = (((u64)(unsigned)__builtin_amdgcn_readfirstlane((unsigned)(ep >> 32)) << 32) | (unsigned)__builtin_amdgcn_readfirstlane((unsigned)ep)) + 1ull;
    {
      float v[16];
#pragma unroll
      for (int i = 0; i < 4; ++i) { v[2 * i] = bf_lo(xa[i]); v[2 * i + 1] = bf_hi(xa[i]); v[8 + 2 * i] = bf_lo(xb[i]); v[9 + 2 * i] = bf_hi(xb[i]); }
      float sm = 0.f;
#pragma unroll
      for (int i = 0; i < 16; ++i) sm += v[i];
      const float mean = wave_sum(sm) * (1.f / DM);
      float q = 0.f;
#pragma unroll
      for (int i = 0; i < 16; ++i) { const float d = v[i] - mean; q += d * d; }
      const float mb = rbf(mean), vb = rbf(wave_sum(q) * (1.f / DM));
      const float inv = 1.0f / rbf(sqrtf(rbf(vb + 1e-5f)));
      u32x4 oa, ob;
#pragma unroll
      for (int i = 0; i < 4; ++i) {
        const unsigned da = cvtpk(v[2 * i] - mb, v[2 * i + 1] - mb), db = cvtpk(v[8 + 2 * i] - mb, v[9 + 2 * i] - mb);
        oa[i] = cvtpk(bf_lo(da) * inv, bf_hi(da) * inv);
        ob[i] = cvtpk(bf_lo(db) * inv, bf_hi(db) * inv);
      }
      bf16_t* op = P.xn + (size_t)(bid * 8 + w) * DM + lane * 8;
      st16wt(op, oa); st16wt(op + 512, ob);
      publish(ctrl + 0, P.flags + FL_OFF + bid, tok, lane);
    }
    unsigned avoff0 = (unsigned)((lane >> 3) * 2048 + (((lane & 7) ^ ((lane >> 4) + 2)) * 16)), avoff1 = avoff0 ^ 64u;
    const char* abase = (const char*)P.xn + (size_t)(row0 + 32 * w4) * 2048;
#define DMA_A(hh, t) do { _Pragma("unroll") for (int _i = 0; _i < 4; ++_i) \
    glds16c(abase + (size_t)(hh) * 262144 + _i * 16384 + (t) * 128, (_i & 1) ? avoff1 : avoff0, \
            lds0 + G1_AOFF + ((t) & 1) * 32768 + (hh) * 16384 + (4 * w4 + _i) * 1024); } while (0)
    const char* bptr[4]; unsigned bld[4]; unsigned bdst[4];
#pragma unroll
    for (int i = 0; i < 4; ++i) {
      const int b = (i == 0) ? (g ? 16 + w4 : 4 * w4) : 4 * w4 + i;
      const int region = b >= 10 ? 1 : 0, bi = b - 10 * region;
      const int Pc = bi * 64 + lane;
      const int k = Pc / 20, pc = Pc - 20 * k, f = pc >> 1, e8 = (pc & 1) * 8;
      const bool isq = f >= 8 || (region && (f & 3) == 3);
      const int qf = f >= 8 ? (region ? f - 4 : f - 8) : 2 + (f >> 2);
      const bf16_t* mat = isq ? P.wqkv : (f >= 4 ? P.wu : P.wg);
      const int ld = isq ? NQKV : FF;
      const int col = e8 + (isq ? (qf >> 1) * DM + 32 * cg + 16 * (qf & 1) : 112 * cg + 64 * region + 16 * (f & 3));
      bptr[i] = (const char*)(mat + (size_t)k * ld + col);
      bld[i] = (unsigned)ld * 64u;
      bdst[i] = lds0 + G1_BOFF + region * G1_BREG + bi * 1024 + (bi >= 5 ? 32 : 0);
    }
#define DMA_B(i, s) glds16(bptr[i], bdst[i] + ((s) & 3) * G1_BSLOT)
#define ADV_B() do { ADV64(bptr[0], bld[0]); ADV64(bptr[1], bld[1]); ADV64(bptr[2], bld[2]); ADV64(bptr[3], bld[3]); } while (0)
    if (g == 0) {
      DMA_B(0, 0); DMA_B(1, 0); DMA_B(2, 0); DMA_B(3, 0); ADV_B();
      DMA_B(0, 1); ADV_B();
      DMA_B(0, 2); DMA_B(1, 2); DMA_B(2, 2); DMA_B(3, 2); ADV_B();
    } else {
      DMA_B(0, 0); ADV_B();
      DMA_B(0, 1); DMA_B(1, 1); DMA_B(2, 1); DMA_B(3, 1); ADV_B();
      DMA_B(0, 2); ADV_B();
      const char* ta;
      if (w4 <= 1) ta = (const char*)(P.AH + (size_t)(lane * 32 + 15 + 16 * w4) * KA + 4608);
      else if (w4 == 2) ta = lane < 48 ? (const char*)(P.Qb + (size_t)(2 * lane + 1) * HSTR - 64)
                                       : (const char*)P.part + (size_t)((lane - 48) * 16 + xcd) * 65536 + (xcd & 3) * 16384;
      else ta = lane < 8 ? (const char*)P.wo + lane * 262144 : (lane < 36 ? (const char*)P.wd + (lane - 8) * 262144
                         : (lane < 52 ? (const char*)P.out + (lane - 36) * 262144 : (const char*)P.x + (lane - 52) * 262144));
      glds4c(ta, lds0 + LDS_SINK);
    }
    const int fr = lane & 15, fq = lane >> 4, kq = ((fq & 1) << 1) | (fq >> 1);
    const int aoff0 = G1_AOFF + (64 * wm + fr) * 128 + ((kq ^ (((fr >> 1) + 2) & 7)) * 16);
    lds_cptr pa0 = L + aoff0, pa1 = L + (aoff0 ^ 64);
    lds_cptr pb0 = L + (G1_BOFF + (8 * kq + (fr >> 2)) * 320 + (fq & 1) * 32 + 8 * (lane & 3) + G1_BREG * wn);
    lds_cptr pb1 = pb0 + 2 * G1_BSLOT;
    asm volatile("" : "+v"(pa0), "+v"(pa1), "+v"(pb0), "+v"(pb1), "+v"(avoff0), "+v"(avoff1));
    f32x4 acc[4][10];
#pragma unroll
    for (int m = 0; m < 4; ++m)
#pragma unroll
      for (int j = 0; j < 10; ++j) { acc[m][j] = (f32x4){0.f, 0.f, 0.f, 0.f}; PIN(acc[m][j]); }
    bf16x8 af[4], bfr[10];
    if (g == 0) {
      wg_wait(w, lane, P.flags + FL_OFF + rt * 32 + (lane & 31), tok, ctrl + 3);
      DMA_A(0, 0); DMA_A(1, 0); DMA_A(0, 1);
      WAITBAR(4);
    } else { WAITBAR(1); BARRIER(); }
#define STEP(U, ISSUE, WN, LASTBAR, RELAX, PF) do { \
    { const lds_cptr pa = ((U) & 1) ? pa1 : pa0; const lds_cptr pb = (((U) & 2) ? pb1 : pb0) + ((U) & 1) * G1_BSLOT; \
      _Pragma("unroll") for (int m = 0; m < 4; ++m) af[m] = lds_rd128(pa + ((U) >> 1) * 32768 + m * 2048); \
      _Pragma("unroll") for (int j = 0; j < 10; ++j) bfr[j] = cat8(lds_tr(pb + j * 32), lds_tr(pb + j * 32 + 1280)); } \
    if (ISSUE) { DMA_B(0, (U) + 3); \
      if (g == ((U) & 1)) { DMA_A(1 - ((U) & 1), 2 * it + ((U) >> 1) + 1 + ((U) & 1)); } \
      else { DMA_B(1, (U) + 3); DMA_B(2, (U) + 3); DMA_B(3, (U) + 3); \
        if (PF) { glds16s(pfp, pfoff, lds0 + LDS_SINK); pfp += pfinc; } } } \
    if ((RELAX) && g) { WAITBAR(63); } else { WAITBAR(WN); } \
    _Pragma("unroll") for (int j = 0; j < 10; ++j) { _Pragma("unroll") for (int m = 0; m < 4; ++m) MFMA16A(acc[m][j], bfr[j], af[m]); \
      if ((ISSUE) && (j & 1) && j < 8) ADV64(bptr[j >> 1], bld[j >> 1]); } \
    if (LASTBAR) BARRIER(); } while (0)
    unsigned pfoff = lane * 16; PIN(pfoff);
    const int pj = bid * 4 + w4, pn = pj < 308 ? 308 : 358;
    const char* pfp = (pj < 308 ? (const char*)P.wqkv + (size_t)(1216 + pj) * 1024
                                : (pj < 666 ? (const char*)P.wg + (size_t)(1440 + pj - 308) * 1024 : (const char*)P.wu + (size_t)(1440 + pj - 666) * 1024))
                      + (size_t)(1 - g) * pn * 1024;
    const unsigned pfinc = 2048u * pn;
    int it = 0;
    STEP(0, true, 9, true, true, true); STEP(1, true, 10, true, true, true); STEP(2, true, 10, true, false, true); STEP(3, true, 10, true, false, true);
#pragma unroll 1
    for (it = 1; it < 4; ++it) { STEP(0, true, 10, true, false, true); STEP(1, true, 10, true, false, true); STEP(2, true, 10, true, false, true); STEP(3, true, 10, true, false, true); }
#pragma unroll 1
    for (; it < 7; ++it) { STEP(0, true, 9, true, false, false); STEP(1, true, 9, true, false, false); STEP(2, true, 9, true, false, false); STEP(3, true, 9, true, false, false); }
    STEP(0, true, 9, true, false, false); STEP(1, false, 4, true, false, false); STEP(2, false, 0, true, false, false); STEP(3, false, 0, g == 0, false, false);
#undef STEP
    asm volatile("s_nop 7\n\ts_nop 7" ::: "memory");
    SB();
#undef DMA_A
#undef DMA_B
#undef ADV_B
    int tz = threadIdx.x; asm volatile("" : "+v"(tz));
    const int fr2 = tz & 15, fq2 = (tz >> 4) & 3, sel1 = fq2 & 1, c8 = (fq2 >> 1) * 8;
    const float qscale = 0.17677669529663687f * 1.4426950408889634f;
#define SWAP16(A, B, OUT) do { auto r0_ = __builtin_amdgcn_permlane16_swap(A.x, B.x, false, false); \
    auto r1_ = __builtin_amdgcn_permlane16_swap(A.y, B.y, false, false); OUT = (u32x4){r0_[0], r1_[0], r0_[1], r1_[1]}; } while (0)
#define SILU4(m, j, OUT) do { float hv_[4]; _Pragma("unroll") for (int r = 0; r < 4; ++r) { \
      const float tg_ = acc[m][j][r], tu_ = acc[m][(j) + 4][r]; \
      hv_[r] = (tg_ * tu_) * __builtin_amdgcn_rcpf(1.f + __builtin_amdgcn_exp2f(-1.4426950408889634f * tg_)); } \
    OUT = (u32x2){cvtpk(hv_[0], hv_[1]), cvtpk(hv_[2], hv_[3])}; } while (0)
#define PK4(V, SC, OUT) do { const f32x4 v_ = (V) * (SC); OUT = (u32x2){cvtpk(v_[0], v_[1]), cvtpk(v_[2], v_[3])}; } while (0)
    const int rowb = row0 + 64 * wm + fr2;
    const int rl = 64 * wm + fr2;
    __builtin_amdgcn_s_setprio(3);
    if (wn) {
      bf16_t* kd = P.Kb + (size_t)h * HSTR + (((rowb >> 5) * 2 + sel1) * 64 + (rowb & 31) + 4 * c8) * 8;
      bf16_t* vd = P.Vb + (size_t)h * HSTR + rowb * 32 + 16 * sel1 + c8;
      LAS char* kl = (LAS char*)smem + (((rl >> 5) * 2 + sel1) * 64 + (rl & 31) + 4 * c8) * 16;
      LAS char* vl = (LAS char*)smem + 16384 + rl * 64 + (16 * sel1 + c8) * 2;
#pragma unroll
      for (int m = 0; m < 4; ++m) {
        u32x2 a_, b_; u32x4 o;
        PK4(acc[m][3], 1.f, a_); PK4(acc[m][7], 1.f, b_); SWAP16(a_, b_, o);
        st16wt(kd + (m & 1) * 128 + (m >> 1) * 1024, o); *(LAS u32x4*)(kl + (m & 1) * 256 + (m >> 1) * 2048) = o;
        PK4(acc[m][8], 1.f, a_); PK4(acc[m][9], 1.f, b_); SWAP16(a_, b_, o);
        st16wt(vd + m * 512, o); *(LAS u32x4*)(vl + m * 1024) = o;
      }
    } else {
      LAS char* ql = (LAS char*)smem + AT_Q + rl * 64 + (16 * sel1 + c8) * 2;
#pragma unroll
      for (int m = 0; m < 4; ++m) {
        u32x2 a_, b_; u32x4 o; PK4(acc[m][8], qscale, a_); PK4(acc[m][9], qscale, b_); SWAP16(a_, b_, o);
        *(LAS u32x4*)(ql + m * 1024) = o;
      }
    }
    __builtin_amdgcn_s_setprio(0);
    SB();
    bf16_t* hd = P.AH + (size_t)rowb * KA + DM + 112 * cg + 64 * wn + c8;
    hp0 = hd + 16 * sel1;
    hp1 = wn ? hd + 32 + (size_t)sel1 * 16 * KA : hd + 32 + 16 * sel1;
#pragma unroll
    for (int m = 0; m < 2; ++m) { u32x2 a_, b_; SILU4(m, 0, a_); SILU4(m, 1, b_); SWAP16(a_, b_, hv[m]); PIN(hv[m]); }
    publish(ctrl + 1, P.flags + FA_OFF + cg * 8 + rt, tok, tz & 63);
#pragma unroll
    for (int m = 2; m < 4; ++m) { u32x2 a_, b_; SILU4(m, 0, a_); SILU4(m, 1, b_); SWAP16(a_, b_, hv[m]); }
    if (wn == 0) {
#pragma unroll
      for (int m = 0; m < 4; ++m) { u32x2 a_, b_; SILU4(m, 2, a_); SILU4(m, 3, b_); SWAP16(a_, b_, hv[4 + ((m + 2) & 3)]); }
    } else {
#pragma unroll
      for (int mp = 0; mp < 2; ++mp) { u32x2 a_, b_; SILU4(2 * mp, 2, a_); SILU4(2 * mp + 1, 2, b_); SWAP16(a_, b_, hv[4 + mp]); }
      hv[6] = hv[4]; hv[7] = hv[5];
    }
#undef SWAP16
#undef SILU4
#undef PK4
  }

  const int k4 = xcd & 3, rt2 = (xcd >> 2) * 4 + (jj >> 3), ct2 = jj & 7;
  int c_issued = 0;
#define G2_K(c) ((c) < 14 ? DM + (14 * k4 + (c)) * 64 : (4 * k4 + (c) - 14) * 64)
#define G2_DMA_A(c, sl) do { \
    const char* ab_ = (const char*)P.AH + (size_t)(rt2 * 256 + 64 * w4) * (KA * 2) + G2_K(c) * 2; \
    const unsigned d_ = lds0 + (sl) * G2_SLOT + w4 * 8192; unsigned keep_; \
    asm volatile("s_mov_b32 %0, m0\n\ts_mov_b32 m0, %7\n\ts_nop 0\n\t" \
                 "global_load_lds_dwordx4 %1, %5 sc1\n\tglobal_load_lds_dwordx4 %2, %5 offset:1024 sc1\n\t" \
                 "global_load_lds_dwordx4 %3, %5 offset:2048 sc1\n\tglobal_load_lds_dwordx4 %4, %5 offset:3072 sc1\n\t" \
                 "s_mov_b32 m0, %8\n\ts_nop 0\n\t" \
                 "global_load_lds_dwordx4 %1, %6 sc1\n\tglobal_load_lds_dwordx4 %2, %6 offset:1024 sc1\n\t" \
                 "global_load_lds_dwordx4 %3, %6 offset:2048 sc1\n\tglobal_load_lds_dwordx4 %4, %6 offset:3072 sc1\n\ts_mov_b32 m0, %0" \
                 : "=&s"(keep_) : "v"(avo[0]), "v"(avo[1]), "v"(avo[2]), "v"(avo[3]), "s"(ab_), "s"(ab_ + 32 * KA * 2), "s"(d_), "s"(d_ + 4096u) : "memory"); } while (0)
#define G2_DMA_B(c, sl) do { const int kg_ = G2_K(c); \
    const char* bb_ = (kg_ < DM ? (const char*)P.wo + (size_t)kg_ * 2048 : (const char*)P.wd + (size_t)(kg_ - DM) * 2048) + ct2 * 256 + w4 * 32768; \
    const unsigned d_ = lds0 + (sl) * G2_SLOT + 32768 + w4 * 4096; unsigned keep_; \
    asm volatile("s_mov_b32 %0, m0\n\ts_mov_b32 m0, %6\n\ts_nop 0\n\t" \
                 "global_load_lds_dwordx4 %1, %5\n\tglobal_load_lds_dwordx4 %2, %5 offset:1024\n\t" \
                 "global_load_lds_dwordx4 %3, %5 offset:2048\n\tglobal_load_lds_dwordx4 %4, %5 offset:3072\n\ts_mov_b32 m0, %0" \
                 : "=&s"(keep_) : "v"(bvo[0]), "v"(bvo[1]), "v"(bvo[2]), "v"(bvo[3]), "s"(bb_), "s"(d_) : "memory"); } while (0)
#define G2_DMA(c, sl) do { G2_DMA_A(c, sl); G2_DMA_B(c, sl); } while (0)
#define G2_OFFS(lane) unsigned avo[4], bvo[4]; _Pragma("unroll") for (int _i = 0; _i < 4; ++_i) { \
    avo[_i] = ((unsigned)(((lane) >> 3) * (KA * 2) + ((((lane) & 7) ^ (((lane) >> 4) + 2)) * 16)) ^ ((_i & 1) * 64u)) + _i * (8 * KA * 2 - 1024); \
    bvo[_i] = (unsigned)(((lane) >> 4) * 2048 + ((((lane) & 15) ^ (((lane) >> 4) << 1) ^ ((w4 & 1) << 3)) * 16)) + _i * (8192 - 1024); }

  {
    int tb = threadIdx.x; asm volatile("" : "+v"(tb));
    const int lane = tb & 63;
    const int qg = w4, kh = g;
    const int hi = lane >> 5;
    const unsigned voff = lane * 16;
    const short one = (((lane >> 4) & 1) == ((lane >> 2) & 1)) ? (short)0x3f80 : (short)0;
    const bf16x8 sel = {one, one, one, one, one, one, one, one};
    volatile LAS unsigned* flag = (volatile LAS unsigned*)(ctrl + 2);
    const int gq = lane >> 4;
    const int koff = kh * 8192 + lane * 16;
    const int voffl = 16384 + kh * 8192 + (4 * (gq >> 1) + ((lane & 15) >> 2)) * 64 + (gq & 1) * 32 + (lane & 3) * 8;
    const f32x16 zero16 = splat16(0.f);
    f32x16 oA = zero16, oB = zero16;
    f32x4 lA = {0.f, 0.f, 0.f, 0.f}, lB = {0.f, 0.f, 0.f, 0.f};
    float mA, mB;
    const unsigned* hflag = (const unsigned*)(P.flags + FH_OFF + (8 * k4 + ((lane >> 1) & 7)) * 8 + rt2) + (lane & 1);
    bf16x8 qa0, qa1, qb0, qb1;

#define LDK(K0, K1, p) do { K0 = lds_rd128(p); K1 = lds_rd128((p) + 1024); } while (0)
#define LDV(V0, V1, p) do { V0 = cat8(lds_tr(p), lds_tr((p) + 512)); V1 = cat8(lds_tr((p) + 1024), lds_tr((p) + 1536)); } while (0)
#define BF(x) __builtin_bit_cast(bf16x8, x)
    {
      const unsigned* kvflag = (const unsigned*)(P.flags + FA_OFF + cg * 8 + ((lane >> 1) & 7)) + (lane & 1);
      const unsigned tokh = (lane & 1) ? (unsigned)(tok >> 32) : (unsigned)tok;
#define FLAGS_OK() __all(*(volatile LAS unsigned*)((LAS char*)smem + AT_HF + w * 256 + lane * 4) == tokh)
      LGBAR();
      glds4c(kvflag, lds0 + AT_HF + w * 256);
      { const lds_cptr qp = L + AT_Q + qg * 4096 + (lane & 31) * 64 + hi * 16;
        qa0 = lds_rd128(qp); qa1 = lds_rd128(qp + 32); qb0 = lds_rd128(qp + 2048); qb1 = lds_rd128(qp + 2080); }
      bf16x8 kx0, kx1, ky0, ky1, vx0, vx1, vy0, vy1;
      LDK(kx0, kx1, L + koff);
      LDK(ky0, ky1, L + koff + 2048);
      LDV(vy0, vy1, L + voffl);
      f32x16 sA = MFMA32(kx0, qa0, zero16); sA = MFMA32(kx1, qa1, sA);
      f32x16 sB = MFMA32(kx0, qb0, zero16); sB = MFMA32(kx1, qb1, sB);
      mA = max16(sA); mB = max16(sB);
      f32x16 nA = splat16(-mA), nB = splat16(-mB);
      PIN(nA); PIN(nB);
      sA = MFMA32(kx0, qa0, nA); sA = MFMA32(kx1, qa1, sA);
      u32x4 pA0, pA1, pB0 = {0u, 0u, 0u, 0u}, pB1 = {0u, 0u, 0u, 0u};
#define E4(S, i) do { S[i] = EXP2(S[i]); S[i + 1] = EXP2(S[i + 1]); S[i + 2] = EXP2(S[i + 2]); S[i + 3] = EXP2(S[i + 3]); } while (0)
#define SLOT(SC, PC0, PC1, SN, K0, K1, Q0, Q1, NM, PP0, PP1, V0, V1, O, LS, LOADS) do { \
    SN = MFMA32(K0, Q0, NM); SB(); E4(SC, 0); PIN(SC); SB(); \
    SN = MFMA32(K1, Q1, SN); SB(); LOADS; SB(); E4(SC, 4); PIN(SC); SB(); \
    O = MFMA32(V0, BF(PP0), O); SB(); PC0[0] = cvtpk(SC[0], SC[1]); PC0[1] = cvtpk(SC[2], SC[3]); \
      SC[8] = EXP2(SC[8]); SC[9] = EXP2(SC[9]); SC[10] = EXP2(SC[10]); PIN(SC); PIN(PC0); SB(); \
    LS = MFMA16(sel, BF(PP0), LS); SB(); PC0[2] = cvtpk(SC[4], SC[5]); PC0[3] = cvtpk(SC[6], SC[7]); SC[11] = EXP2(SC[11]); PIN(SC); PIN(PC0); SB(); \
    O = MFMA32(V1, BF(PP1), O); SB(); E4(SC, 12); PIN(SC); SB(); \
    LS = MFMA16(sel, BF(PP1), LS); SB(); PC1[0] = cvtpk(SC[8], SC[9]); PC1[1] = cvtpk(SC[10], SC[11]); \
      PC1[2] = cvtpk(SC[12], SC[13]); PC1[3] = cvtpk(SC[14], SC[15]); PIN(PC1); SB(); } while (0)
#define SUB(KC0, KC1, KN0, KN1, VC0, VC1, VP0, VP1, vpu, kp2) do { \
    SLOT(sA, pA0, pA1, sB, KC0, KC1, qb0, qb1, nB, pB0, pB1, VP0, VP1, oB, lB, LDV(VC0, VC1, vpu)); \
    SLOT(sB, pB0, pB1, sA, KN0, KN1, qa0, qa1, nA, pA0, pA1, VC0, VC1, oA, lA, LDK(KC0, KC1, kp2)); } while (0)
      lds_cptr kb0 = L + koff, kb1 = L + koff + 2 * AT_STAGE, vb0 = L + voffl, vb1 = L + voffl + 2 * AT_STAGE;
      PIN(kb0); PIN(kb1); PIN(vb0); PIN(vb1);
      bool kvok = false;
#define KP(st) ((((st) & 2) ? kb1 : kb0) + ((st) & 1) * AT_STAGE)
#define VP(st) ((((st) & 2) ? vb1 : vb0) + ((st) & 1) * AT_STAGE)
#define HS(st, i) do { if ((st) == 1 || (st) == 2) HSTORE((4 * ((st) - 1) + (i)) & 7); } while (0)
#define VMW0() asm volatile("s_waitcnt vmcnt(0)" ::: "memory")
#define STAGE(st, WN, EXTRA) do { \
        SUB(kx0, kx1, ky0, ky1, vx0, vx1, vy0, vy1, VP(st), KP(st) + 4096); \
        HS(st, 0); \
        if ((st) == 0) { VMW0(); kvok = FLAGS_OK(); if (kvok) { DMA_ST(1, kh); DMA_ST(2, kh); } else glds4c(kvflag, lds0 + AT_HF + w * 256); } \
        SUB(ky0, ky1, kx0, kx1, vy0, vy1, vx0, vx1, VP(st) + 2048, KP(st) + 6144); \
        if ((st) == 0 && !kvok) { VMW0(); if (!FLAGS_OK()) wait_flags(P.flags + FA_OFF + cg * 8 + (lane & 7), tok); DMA_ST(1, kh); DMA_ST(2, kh); } \
        VMBAR(WN); \
        HS(st, 1); \
        EXTRA; \
        if ((st) < 5) {   \
          const int pc = (st) * 8 + w, kb = bid * 36 + (pc < 36 ? pc : 35); \
          glds16s(kb < 2048 ? (const char*)P.wo + (size_t)kb * 1024 : (const char*)P.wd + (size_t)(kb - 2048) * 1024, voff, lds0 + LDS_SINK); \
          DMA_ST((st) + 3, kh); } \
        SUB(kx0, kx1, ky0, ky1, vx0, vx1, vy0, vy1, VP(st) + 4096, KP((st) + 1)); \
        HS(st, 2); \
        SUB(ky0, ky1, kx0, kx1, vy0, vy1, vx0, vx1, VP(st) + 6144, KP((st) + 1) + 2048); \
        HS(st, 3); } while (0)
      STAGE(0, 4, ); STAGE(1, 6, ); STAGE(2, 10, ); STAGE(3, 11, );
      STAGE(4, 5, if (w == 0 && lane == 0) set_flag(P.flags + FH_OFF + cg * 8 + rt, tok));
      STAGE(5, 5, if (g == 1) glds4c(hflag, lds0 + AT_HF + w * 256));
      STAGE(6, 0, if (g == 1 && FLAGS_OK()) { G2_OFFS(lane); G2_DMA(0, 0); c_issued = 2; });
      STAGE(7, 0, if (c_issued == 2) { G2_OFFS(lane); G2_DMA(1, 1); c_issued = 3; });
#undef VMW0
#undef FLAGS_OK
#undef STAGE
#undef HS
#undef KP
#undef VP
      oB = MFMA32(vy0, BF(pB0), oB); lB = MFMA16(sel, BF(pB0), lB);
      oB = MFMA32(vy1, BF(pB1), oB); lB = MFMA16(sel, BF(pB1), lB);
#undef SUB
#undef SLOT
#undef E4
    }
    LAS u32x4* xw = (LAS u32x4*)((LAS char*)smem + AT_EX) + w4 * 256;
    LAS float* ew = (LAS float*)((LAS char*)smem + AT_EE) + w4 * 128;
    float eA, eB;
#define FIN1(FIRST) do { \
        \
      float chk = 0.f; \
      _Pragma("unroll") for (int r = 0; r < 16; ++r) { chk = __builtin_fmaf(oA[r], 0.f, chk); chk = __builtin_fmaf(oB[r], 0.f, chk); } \
      const bool bad = !(lA[0] < 1.0e36f) || !(lB[0] < 1.0e36f) || !(chk == 0.f); \
      if (FIRST) { if (__any(bad) && lane == 0) *flag = 1u; \
                   if (c_issued == 3 && lane == 0) __hip_atomic_fetch_add(ctrl + 5, 1u, __ATOMIC_RELAXED, __HIP_MEMORY_SCOPE_WORKGROUP); } \
      oA *= __builtin_amdgcn_rcpf(lA[0]); oB *= __builtin_amdgcn_rcpf(lB[0]); \
      eA = mA + __builtin_amdgcn_logf(lA[0]); eB = mB + __builtin_amdgcn_logf(lB[0]);     \
      if (g == 0) { \
        _Pragma("unroll") for (int r = 0; r < 2; ++r) { \
          xw[r * 64 + lane] = (u32x4){cvtpk(oA[8 * r], oA[8 * r + 1]), cvtpk(oA[8 * r + 2], oA[8 * r + 3]), cvtpk(oA[8 * r + 4], oA[8 * r + 5]), cvtpk(oA[8 * r + 6], oA[8 * r + 7])}; \
          xw[128 + r * 64 + lane] = (u32x4){cvtpk(oB[8 * r], oB[8 * r + 1]), cvtpk(oB[8 * r + 2], oB[8 * r + 3]), cvtpk(oB[8 * r + 4], oB[8 * r + 5]), cvtpk(oB[8 * r + 6], oB[8 * r + 7])}; } \
        ew[lane] = eA; ew[64 + lane] = eB; } } while (0)
#define SAFE_UNIT(Q0, Q1, M, O, LS) do { \
            f32x16 s_ = MFMA32(k0, Q0, zero16); s_ = MFMA32(k1, Q1, s_); \
            const float mn_ = fmaxf(M, max16(s_)); const float al_ = EXP2(M - mn_); M = mn_; \
            O *= al_; LS *= al_; u32x4 p0_, p1_; \
            _Pragma("unroll") for (int r = 0; r < 4; ++r) { p0_[r] = cvtpk(EXP2(s_[2 * r] - mn_), EXP2(s_[2 * r + 1] - mn_)); \
              p1_[r] = cvtpk(EXP2(s_[8 + 2 * r] - mn_), EXP2(s_[9 + 2 * r] - mn_)); } \
            O = MFMA32(v0, BF(p0_), O); LS = MFMA16(sel, BF(p0_), LS); O = MFMA32(v1, BF(p1_), O); LS = MFMA16(sel, BF(p1_), LS); } while (0)
#define SAFE_PASS() do { \
        WAITBAR(0); \
        DMA_ST(0, kh); DMA_ST(1, kh); DMA_ST(2, kh); \
        oA = zero16; oB = zero16; lA = (f32x4){0.f, 0.f, 0.f, 0.f}; lB = lA; \
        mA = -3.0e38f; mB = -3.0e38f; \
        _Pragma("unroll 1") for (int st = 0; st < 8; ++st) { \
          if (st < 6) WAITBAR(8); else if (st == 6) WAITBAR(4); else WAITBAR(0); \
          if (st < 5) DMA_ST(st + 3, kh); \
          _Pragma("unroll 1") for (int u = 0; u < 4; ++u) { \
            bf16x8 k0, k1, v0, v1; \
            LDK(k0, k1, L + (st & 3) * AT_STAGE + koff + u * 2048); \
            LDV(v0, v1, L + (st & 3) * AT_STAGE + voffl + u * 2048); \
            SAFE_UNIT(qa0, qa1, mA, oA, lA); \
            SAFE_UNIT(qb0, qb1, mB, oB, lB); } } \
        FIN1(false); LGBAR(); } while (0)
    FIN1(true);

    {
      const int wm = w4 >> 1, wn = w4 & 1;
      const int fr = lane & 15, fq = lane >> 4, kq = ((fq & 1) << 1) | (fq >> 1), q4 = fr >> 2;
      const int aoff0 = (128 * wm + fr) * 128 + ((kq ^ (((fr >> 1) + 2) & 7)) * 16);
      const int boff0 = 32768 + (8 * kq + q4) * 256 + 8 * (lane & 3);
      const int bsw = q4 | ((fq & 1) << 2);
      lds_cptr pa[2], pb[4];
      pa[0] = L + aoff0; pa[1] = L + (aoff0 ^ 64); PIN(pa[0]); PIN(pa[1]);
#pragma unroll
      for (int n = 0; n < 4; ++n) { pb[n] = L + (boff0 + (((4 * wn + n) ^ bsw) * 32)); PIN(pb[n]); }
      bf16x8 fa0[8], fb0[4], fa1[8], fb1[4];
#define RD(i, KS, FA, FB) do { if ((i) < 4) FB[(i) & 3] = cat8(lds_tr(pb[(i) & 3] + (KS) * 8192), lds_tr(pb[(i) & 3] + (KS) * 8192 + 1024)); \
      else FA[((i) - 4) & 7] = lds_rd128(pa[KS] + (((i) - 4) & 7) * 2048); } while (0)
#define RD12(KS, FA, FB) do { RD(0, KS, FA, FB); RD(1, KS, FA, FB); RD(2, KS, FA, FB); RD(3, KS, FA, FB); RD(4, KS, FA, FB); RD(5, KS, FA, FB); \
      RD(6, KS, FA, FB); RD(7, KS, FA, FB); RD(8, KS, FA, FB); RD(9, KS, FA, FB); RD(10, KS, FA, FB); RD(11, KS, FA, FB); } while (0)
      if (g == 0) RD12(0, fa0, fb0);
      LGBAR();
      const bool ovf = *flag != 0u, fast = !ovf && *(volatile LAS unsigned*)(ctrl + 5) == 4u;
      if (!fast) {
        if (ovf) SAFE_PASS();
        if (g == 1) {
          G2_OFFS(lane);
          wait_flags(P.flags + FH_OFF + (8 * k4 + (lane & 7)) * 8 + rt2, tok);
          G2_DMA(0, 0); G2_DMA(1, 1);
        }
        VMBAR(12);
        if (g == 0) RD12(0, fa0, fb0);
        LGBAR();
      }
#undef RD12
      if (g == 1) {
#define FIN2(O, E, BL) do { \
      const float e2 = ew[(BL) * 64 + lane]; const u32x4 t0 = xw[(BL) * 128 + lane], t1 = xw[(BL) * 128 + 64 + lane]; \
      const float em = fmaxf(E, e2); float w1 = EXP2(E - em), w2 = EXP2(e2 - em); \
      const float rs = __builtin_amdgcn_rcpf(w1 + w2); w1 *= rs; w2 *= rs; u32x2 pk[4]; \
      _Pragma("unroll") for (int r = 0; r < 4; ++r) { \
        const unsigned ta = r < 2 ? t0[2 * r] : t1[2 * r - 4], tb2 = r < 2 ? t0[2 * r + 1] : t1[2 * r - 3]; \
        pk[r].x = cvtpk(O[4 * r] * w1 + bf_lo(ta) * w2, O[4 * r + 1] * w1 + bf_hi(ta) * w2); \
        pk[r].y = cvtpk(O[4 * r + 2] * w1 + bf_lo(tb2) * w2, O[4 * r + 3] * w1 + bf_hi(tb2) * w2); } \
      bf16_t* op = P.AH + (size_t)(qb * 256 + qg * 64 + (BL) * 32 + (lane & 31)) * KA + h * 32 + 8 * hi; \
      _Pragma("unroll") for (int k = 0; k < 4; k += 2) { \
        auto rx = __builtin_amdgcn_permlane32_swap(pk[k].x, pk[k + 1].x, false, false); \
        auto ry = __builtin_amdgcn_permlane32_swap(pk[k].y, pk[k + 1].y, false, false); \
        st16wt(op + 8 * k, (u32x4){rx[0], ry[0], rx[1], ry[1]}); } } while (0)
      __builtin_amdgcn_s_setprio(3);
      { G2_OFFS(lane); (void)bvo; G2_DMA_A(2, 2); }
      FIN2(oA, eA, 0); FIN2(oB, eB, 1);
#undef FIN2
      }
      SB();
      if (g == 1) {
      int tl = threadIdx.x; asm volatile("" : "+v"(tl));
      const int ll = tl & 63;
      G2_OFFS(ll);
      WAITBAR(12);
      G2_DMA_B(2, 2); G2_DMA(3, 0); VMBAR(12);
      if (w == 4 && ll == 0) set_flag(P.flags + FB_OFF + qb * 32 + h, tok);
      int sl = 1;
#pragma unroll 1
      for (int i = 2; i < G2_NCH - 2; ++i) {
        const u64* of = P.flags + FB_OFF + rt2 * 32 + 8 * k4;
        if (i == 10) glds4c((const unsigned*)(of + ((ll >> 1) & 7)) + (ll & 1), lds0 + AT_HF + w * 256);
        if (i == 12) {
          const unsigned hf = *(volatile LAS unsigned*)((LAS char*)smem + AT_HF + w * 256 + ll * 4);
          if (!__all(hf == ((ll & 1) ? (unsigned)(tok >> 32) : (unsigned)tok))) wait_flags(of + (ll & 7), tok);
        }
        G2_DMA(i + 2, sl); sl = sl == 2 ? 0 : sl + 1; VMBAR(12);
      }
      VMBAR(0);
      if (bid == 0 && w == 4) {
#pragma unroll
        for (int i = 0; i < 4; ++i) wait_flags(P.flags + FA_OFF + i * 64 + ll, tok);
        if (ll == 0) set_flag(P.flags, tok);
      }
      __builtin_amdgcn_endpgm();
      }
      f32x4 acc[8][4];
#pragma unroll
      for (int m = 0; m < 8; ++m)
#pragma unroll
        for (int n = 0; n < 4; ++n) { acc[m][n] = (f32x4){0.f, 0.f, 0.f, 0.f}; PIN(acc[m][n]); }
      asm volatile("s_nop 1" ::: "memory");
#define MM(i, FA, FB) MFMA16A(acc[(i) >> 2][(i) & 3], FB[(i) & 3], FA[(i) >> 2])
#define MR(i, KS, CA, CB, NA, NB) do { MM(i, CA, CB); RD(i, KS, NA, NB); SB(); } while (0)
#define MR4(i, KS, CA, CB, NA, NB) do { MR(i, KS, CA, CB, NA, NB); MR(i + 1, KS, CA, CB, NA, NB); MR(i + 2, KS, CA, CB, NA, NB); MR(i + 3, KS, CA, CB, NA, NB); } while (0)
#define MM4(i, CA, CB) do { MM(i, CA, CB); MM(i + 1, CA, CB); MM(i + 2, CA, CB); MM(i + 3, CA, CB); } while (0)
#define HALF(KS, CA, CB, NA, NB, END) do { \
      MR4(0, KS, CA, CB, NA, NB); MR4(4, KS, CA, CB, NA, NB); MR4(8, KS, CA, CB, NA, NB); \
      MM4(12, CA, CB); MM4(16, CA, CB); MM4(20, CA, CB); MM4(24, CA, CB); MM(28, CA, CB); MM(29, CA, CB); \
      END; MM(30, CA, CB); MM(31, CA, CB); SB(); } while (0)
#define LGW() do { SB(); asm volatile("s_waitcnt lgkmcnt(0)" ::: "memory"); SB(); } while (0)
      HALF(1, fa0, fb0, fa1, fb1, LGBAR());
      int sl = 0;
#pragma unroll 1
      for (int i = 1; i < G2_NCH; ++i) {
        const int dl = sl == 2 ? -2 * G2_SLOT : G2_SLOT; sl = sl == 2 ? 0 : sl + 1;
        pa[0] += dl; pa[1] += dl; pb[0] += dl; pb[1] += dl; pb[2] += dl; pb[3] += dl;
        PIN(pa[0]); PIN(pa[1]); PIN(pb[0]); PIN(pb[1]); PIN(pb[2]); PIN(pb[3]); SB();
        HALF(0, fa1, fb1, fa0, fb0, LGW());
        HALF(1, fa0, fb0, fa1, fb1, LGW(); if (i < G2_NCH - 1) BARRIER());
      }
      MM4(0, fa1, fb1); MM4(4, fa1, fb1); MM4(8, fa1, fb1); MM4(12, fa1, fb1); MM4(16, fa1, fb1); MM4(20, fa1, fb1); MM4(24, fa1, fb1); MM4(28, fa1, fb1);
#undef HALF
#undef LGW
#undef MR4
#undef MM4
#undef MR
#undef MM
#undef RD
      asm volatile("s_nop 7\n\ts_nop 7" ::: "memory");
      SB();
      int te = threadIdx.x; asm volatile("" : "+v"(te));
      const int le = te & 63;
      unsigned* mine = (unsigned*)P.part + (size_t)bid * 16384 + w4 * 1024 + le * 4;
      f32x4 keep[2][4];
#define PKP(A, B) (u32x4){cvtpk(A[0], A[1]), cvtpk(A[2], A[3]), cvtpk(B[0], B[1]), cvtpk(B[2], B[3])}
#pragma unroll
      for (int q = 0; q < 4; ++q) {
        if (q != k4) {
#pragma unroll
          for (int i = 0; i < 4; ++i) st16wt(mine + q * 4096 + i * 256, PKP(acc[2 * q + (i >> 1)][2 * (i & 1)], acc[2 * q + (i >> 1)][2 * (i & 1) + 1]));
        } else {
#pragma unroll
          for (int i = 0; i < 8; ++i) keep[i >> 2][i & 3] = acc[2 * q + (i >> 2)][i & 3];
        }
      }
#undef PKP
      asm volatile("s_waitcnt vmcnt(0)" ::: "memory");
      if (le == 0) set_flag(P.flags + FX_OFF + bid * 4 + w4, tok);
      const int orow = rt2 * 256 + 128 * wm + 32 * k4 + (le & 15), ocol = ct2 * 128 + 64 * wn + 4 * (le >> 4);
      u32x2 xv[2][4];
#pragma unroll
      for (int mm = 0; mm < 2; ++mm)
#pragma unroll
        for (int n = 0; n < 4; ++n) xv[mm][n] = *(const u32x2*)(P.x + (size_t)(orow + 16 * mm) * DM + ocol + 16 * n);
      wait_flags(P.flags + FX_OFF + (bid ^ (1 + le % 3)) * 4 + w4, tok);
      u32x4 pv[3][4];
      const unsigned* t1 = (const unsigned*)P.part + (size_t)(bid ^ 1) * 16384 + k4 * 4096 + w4 * 1024 + le * 4;
      const unsigned* t2 = (const unsigned*)P.part + (size_t)(bid ^ 2) * 16384 + k4 * 4096 + w4 * 1024 + le * 4;
      const unsigned* t3 = (const unsigned*)P.part + (size_t)(bid ^ 3) * 16384 + k4 * 4096 + w4 * 1024 + le * 4;
      asm volatile("global_load_dwordx4 %0, %12, off sc1\n\tglobal_load_dwordx4 %1, %12, off offset:1024 sc1\n\t"
                   "global_load_dwordx4 %2, %12, off offset:2048 sc1\n\tglobal_load_dwordx4 %3, %12, off offset:3072 sc1\n\t"
                   "global_load_dwordx4 %4, %13, off sc1\n\tglobal_load_dwordx4 %5, %13, off offset:1024 sc1\n\t"
                   "global_load_dwordx4 %6, %13, off offset:2048 sc1\n\tglobal_load_dwordx4 %7, %13, off offset:3072 sc1\n\t"
                   "global_load_dwordx4 %8, %14, off sc1\n\tglobal_load_dwordx4 %9, %14, off offset:1024 sc1\n\t"
                   "global_load_dwordx4 %10, %14, off offset:2048 sc1\n\tglobal_load_dwordx4 %11, %14, off offset:3072 sc1\n\t"
                   "s_waitcnt vmcnt(0)"
                   : "=&v"(pv[0][0]), "=&v"(pv[0][1]), "=&v"(pv[0][2]), "=&v"(pv[0][3]), "=&v"(pv[1][0]), "=&v"(pv[1][1]), "=&v"(pv[1][2]), "=&v"(pv[1][3]),
                     "=&v"(pv[2][0]), "=&v"(pv[2][1]), "=&v"(pv[2][2]), "=&v"(pv[2][3]) : "v"(t1), "v"(t2), "v"(t3) : "memory");
#pragma unroll
      for (int i = 0; i < 8; ++i) {
        f32x4 v = keep[i >> 2][i & 3];
#pragma unroll
        for (int q = 0; q < 3; ++q) {
          const unsigned p0 = pv[q][i >> 1][2 * (i & 1)], p1 = pv[q][i >> 1][2 * (i & 1) + 1];
          v += (f32x4){bf_lo(p0), bf_hi(p0), bf_lo(p1), bf_hi(p1)};
        }
        const u32x2 xx = xv[i >> 2][i & 3];
        u32x2 o = {cvtpk(bf_lo(xx.x) + v[0], bf_hi(xx.x) + v[1]), cvtpk(bf_lo(xx.y) + v[2], bf_hi(xx.y) + v[3])};
        *(u32x2*)(P.out + (size_t)(orow + 16 * (i >> 2)) * DM + ocol + 16 * (i & 3)) = o;
      }
    }
#undef LDK
#undef LDV
#undef BF
#undef FIN1
#undef SAFE_UNIT
#undef SAFE_PASS
  }
#undef G2_DMA
#undef G2_DMA_A
#undef G2_DMA_B
#undef G2_K
#undef G2_OFFS
#undef HSTORE
#undef DMA_ST
}

extern "C" void kernel_launch(void* const* d_in, const int* in_sizes, int n_in, void* d_out, int out_size, void* d_ws,
                              size_t ws_size, hipStream_t stream) {
  char* ws = (char*)d_ws;
  Params P;
  P.x = (const bf16_t*)d_in[0];
  P.wqkv = (const bf16_t*)d_in[1]; P.wo = (const bf16_t*)d_in[2];
  P.wg = (const bf16_t*)d_in[4]; P.wu = (const bf16_t*)d_in[5]; P.wd = (const bf16_t*)d_in[6];
  P.xn = (bf16_t*)(ws + WS_XN);
  P.Qb = (bf16_t*)(ws + WS_Q); P.Kb = (bf16_t*)(ws + WS_K); P.Vb = (bf16_t*)(ws + WS_V);
  P.AH = (bf16_t*)(ws + WS_AH); P.out = (bf16_t*)d_out;
  P.part = (float*)(ws + WS_PART); P.flags = (u64*)(ws + WS_FLAGS);
  (void)hipFuncSetAttribute((const void*)fused_kernel, hipFuncAttributeMaxDynamicSharedMemorySize, LDS_TOTAL);
  fused_kernel<<<256, 512, LDS_TOTAL, stream>>>(P);
}
```
